```python
import torch

def _softmax(x, axis=-1):
    return torch.softmax(x, dim=axis)
def _gelu(x):
    return torch.nn.functional.gelu(x, approximate="tanh")
def _silu(x):
    return torch.nn.functional.silu(x)
def _sigmoid(x):
    return torch.sigmoid(x)
def _layernorm(x, axis=-1, eps=1e-5):
    m = x.mean(dim=axis, keepdim=True)
    v = x.var(dim=axis, keepdim=True, unbiased=False)
    return (x - m) / torch.sqrt(v + eps)
def _scatter_add(tgt, idx, src, axis=0):
    return tgt.clone().index_add_(axis, idx, src)
def _segment_sum(src, seg, n):
    out = torch.zeros((n,) + src.shape[1:], dtype=src.dtype)
    return out.index_add_(0, seg, src)
def _pad(x, pw):
    flat = tuple(v for pair in reversed(pw) for v in pair)
    return torch.nn.functional.pad(x, flat)

kernel_name = 'standalone__gqa_block_cached__near_ridge'

def setup_inputs():

    _w = (lambda a: (lambda t: t.to(torch.bfloat16) if t.is_floating_point() else t)(torch.as_tensor(a)))
    B, Sq, D = 16, 224, 512
    Hq, Hkv, Sk, Dh = 32, 32, 512, 128
    assert Hq % Hkv == 0, (Hq, Hkv)
    return {
        "x": _w((lambda *s: torch.randn(*s, dtype=torch.bfloat16) * 0.8)(B, Sq, D)),
        "k_cache": _w((lambda *s: torch.randn(*s, dtype=torch.bfloat16) * 0.8)(B, Hkv, Sk, Dh)),
        "v_cache": _w((lambda *s: torch.randn(*s, dtype=torch.bfloat16) * 0.05)(B, Hkv, Sk, Dh)),
        "wq": _w((lambda *s: torch.randn(*s, dtype=torch.bfloat16) * 0.8)(D, Hq * Dh) * (D ** -0.5)),
        "wo": _w((lambda *s: torch.randn(*s, dtype=torch.bfloat16) * 0.05)(Hq * Dh, D) * ((Hq * Dh) ** -0.5)),
    }

def reference(x, k_cache, v_cache, wq, wo):
    B, Sq, D = x.shape
    Hq, Dh = 32, 128
    Hkv = k_cache.shape[1]
    rep = Hq // Hkv
    q = (lambda x, *d: x.permute(*d))((x @ wq).reshape(B, Sq, Hq, Dh), 0, 2, 1, 3)
    k = (lambda x, n, axis: torch.repeat_interleave(x, n, dim=axis))(k_cache, rep, axis=1)
    v = (lambda x, n, axis: torch.repeat_interleave(x, n, dim=axis))(v_cache, rep, axis=1)
    scores = torch.einsum("bhqd,bhkd->bhqk", q, k) * (Dh ** -0.5)
    p = _softmax(scores, axis=-1)
    o = torch.einsum("bhqk,bhkd->bhqd", p, v)
    return (lambda x, *d: x.permute(*d))(o, 0, 2, 1, 3).reshape(B, Sq, Hq * Dh) @ wo
```

```cpp
#include <hip/hip_runtime.h>
#include <hip/hip_bf16.h>
typedef __hip_bfloat16 bf16;
constexpr int B=16,SQ=224,DM=512,H=32,SK=512,DH=128;
__device__ inline float b2f(bf16 x){return __bfloat162float(x);}
__global__ void gemm_naive(const bf16* A,const bf16* W,bf16* C,int M,int N,int K){
  __shared__ float As[16][17], Ws[16][17];
  int r=blockIdx.y*16+threadIdx.y,c=blockIdx.x*16+threadIdx.x; float acc=0;
  for(int k=0;k<K;k+=16){
    As[threadIdx.y][threadIdx.x]=b2f(A[(long)r*K+k+threadIdx.x]);
    Ws[threadIdx.y][threadIdx.x]=b2f(W[(long)(k+threadIdx.y)*N+c]);
    __syncthreads();
    for(int i=0;i<16;i++)acc+=As[threadIdx.y][i]*Ws[i][threadIdx.x];
    __syncthreads();
  }
  C[(long)r*N+c]=__float2bfloat16(acc);
}
__global__ void attn_naive(const bf16* Q,const bf16* K,const bf16* V,bf16* O){
  int q=blockIdx.x,h=blockIdx.y,b=blockIdx.z,t=threadIdx.x;
  __shared__ float qs[128],p[512],red[128];
  qs[t]=b2f(Q[((long)(b*SQ+q))*(H*DH)+h*DH+t]); __syncthreads();
  const bf16* Kb=K+((long)(b*H+h))*SK*DH; const bf16* Vb=V+((long)(b*H+h))*SK*DH;
  float m=-1e30f;
  for(int j=t;j<SK;j+=128){float s=0;for(int d=0;d<DH;d++)s+=qs[d]*b2f(Kb[j*DH+d]); s*=0.08838834764831845f; p[j]=s; m=fmaxf(m,s);}
  red[t]=m;__syncthreads();
  for(int o=64;o>0;o>>=1){if(t<o)red[t]=fmaxf(red[t],red[t+o]);__syncthreads();}
  m=red[0];__syncthreads();
  float l=0;for(int j=t;j<SK;j+=128){float e=__expf(p[j]-m);p[j]=e;l+=e;}
  red[t]=l;__syncthreads();
  for(int o=64;o>0;o>>=1){if(t<o)red[t]+=red[t+o];__syncthreads();}
  l=red[0];
  float acc=0;for(int j=0;j<SK;j++)acc+=p[j]*b2f(Vb[j*DH+t]);
  O[((long)(b*SQ+q))*(H*DH)+h*DH+t]=__float2bfloat16(acc/l);
}
extern "C" void kernel_launch(void* const* d_in, const int* in_sizes, int n_in, void* d_out, int out_size, void* d_ws, size_t ws_size, hipStream_t stream) {
  const bf16* x=(const bf16*)d_in[0];const bf16* k=(const bf16*)d_in[1];const bf16* v=(const bf16*)d_in[2];const bf16* wq=(const bf16*)d_in[3];const bf16* wo=(const bf16*)d_in[4];
  bf16* Q=(bf16*)d_ws; bf16* O=Q+(long)B*SQ*H*DH;
  gemm_naive<<<dim3(H*DH/16,B*SQ/16),dim3(16,16),0,stream>>>(x,wq,Q,B*SQ,H*DH,DM);
  attn_naive<<<dim3(SQ,H,B),128,0,stream>>>(Q,k,v,O);
  gemm_naive<<<dim3(DM/16,B*SQ/16),dim3(16,16),0,stream>>>(O,wo,(bf16*)d_out,B*SQ,DM,H*DH);
}
```

```cpp
#include <hip/hip_runtime.h>
#include <hip/hip_bf16.h>
#include <utility>

typedef short    bf16x8 __attribute__((ext_vector_type(8)));
typedef short    s16x4  __attribute__((ext_vector_type(4)));
typedef float    f32x16 __attribute__((ext_vector_type(16)));
typedef float    f32x4  __attribute__((ext_vector_type(4)));
typedef float    f32x2  __attribute__((ext_vector_type(2)));
typedef __bf16   bf16x2 __attribute__((ext_vector_type(2)));
typedef unsigned u32x4  __attribute__((ext_vector_type(4)));
typedef unsigned u32x2  __attribute__((ext_vector_type(2)));
typedef __attribute__((address_space(3))) const char* lds_cptr;
typedef __amdgpu_buffer_rsrc_t rsrc_t;
#define DI __device__ __forceinline__
#define SBAR() __builtin_amdgcn_sched_barrier(0)
#define PIN(x) asm volatile("" : "+v"(x))
#define MFMA32(a, b, c) __builtin_amdgcn_mfma_f32_32x32x16_bf16(a, b, c, 0, 0, 0)
#define MFMA16(a, b, c) __builtin_amdgcn_mfma_f32_16x16x32_bf16(a, b, c, 0, 0, 0)
#define BARRIER() asm volatile("s_barrier" ::: "memory")
#define WAITV(n) asm volatile("s_waitcnt vmcnt(" #n ")" ::: "memory")

constexpr int NB = 16, SQ = 224, DM = 512, NH = 32, SK = 512, DH = 128;
constexpr unsigned X_BYTES = NB * SQ * DM * 2, KV_BYTES = (unsigned)NB * NH * SK * DH * 2, W_BYTES = DM * NH * DH * 2;
constexpr unsigned OF_BYTES = (unsigned)NB * SQ * NH * DH * 2;
constexpr size_t WS_OF = 0, WS_WT = OF_BYTES;
constexpr float QSCALE = 0.088388347648318440f * 1.4426950408889634f;
constexpr float THR = 8.0f;

constexpr unsigned KR = 0, VR = 65536, XR = 104 * 1024, LDS_BYTES = 160 * 1024;
DI constexpr unsigned wq_slot(int i) { return i == 0 ? 48u * 1024 : i == 1 ? 16u * 1024 : i == 2 ? 72u * 1024 : i == 3 ? 32u * 1024 : 88u * 1024; }

DI unsigned rfl(unsigned x) { return __builtin_amdgcn_readfirstlane(x); }
DI rsrc_t mk_rsrc(const void* p, unsigned bytes) { return __builtin_amdgcn_make_buffer_rsrc((void*)p, (short)0, (int)bytes, 0x00020000); }
DI void dma16(rsrc_t r, unsigned voff, unsigned soff, unsigned m0v) {
  asm volatile("s_mov_b32 m0, %0\n\ts_nop 0\n\tbuffer_load_dwordx4 %1, %2, %3 offen lds" :: "s"(m0v), "v"(voff), "s"(r), "s"(soff) : "memory");
}
DI bf16x8 lds128(lds_cptr p) { return *(const __attribute__((address_space(3))) bf16x8*)p; }
DI s16x4 ldstr(lds_cptr p) { return __builtin_bit_cast(s16x4, __builtin_amdgcn_ds_read_tr16_b64_v4i16((__attribute__((address_space(3))) s16x4*)p)); }
DI bf16x8 cat(s16x4 a, s16x4 b) { return __builtin_shufflevector(a, b, 0, 1, 2, 3, 4, 5, 6, 7); }
DI unsigned cvtpk(float lo, float hi) { f32x2 v = {lo, hi}; return __builtin_bit_cast(unsigned, __builtin_convertvector(v, bf16x2)); }
DI float max3f(float a, float b, float c) { float r; asm("v_max3_f32 %0, %1, %2, %3" : "=v"(r) : "v"(a), "v"(b), "v"(c)); return r; }
DI float fadd(float a, float b) { float r; asm("v_add_f32_e32 %0, %1, %2" : "=v"(r) : "v"(a), "v"(b)); return r; }
DI float swapmax(float m) { auto rr = __builtin_amdgcn_permlane32_swap(__float_as_uint(m), __float_as_uint(m), false, false); return __builtin_fmaxf(__uint_as_float(rr[0]), __uint_as_float(rr[1])); }
DI float swapsum(float m) { auto rr = __builtin_amdgcn_permlane32_swap(__float_as_uint(m), __float_as_uint(m), false, false); return __uint_as_float(rr[0]) + __uint_as_float(rr[1]); }

constexpr int FD = 4;

struct Att {
  f32x16 O[4], S[2], negm;
  bf16x8 Qa[8], Qb[8], F[FD];
  u32x4 P[2];
  float l0, l1, mhat, mx;
  lds_cptr ke, ko, vb;
};

template <int U, int G> DI void frag_read(Att& a) {
  constexpr int u = U + (G >> 4), g = G & 15, slot = G % FD;
  if constexpr (g < 8) {
    constexpr unsigned off = (unsigned)((u + 1) & 7) * 8192u + 512u * (g >> 1);
    a.F[slot] = lds128(((g & 1) ? a.ko : a.ke) + off);
  } else {
    constexpr int ks = (g - 8) >> 2, c = (g - 8) & 3;
    constexpr unsigned off = (unsigned)(u & 7) * 8192u + 4096u * ks + 512u * c;
    a.F[slot] = cat(ldstr(a.vb + off), ldstr(a.vb + off + 2048));
  }
}
#define EXPE(i) do { a.S[X][i] = __builtin_amdgcn_exp2f(a.S[X][i]); } while (0)
#define ADDE(i) do { if ((i) & 1) a.l1 = fadd(a.l1, a.S[X][i]); else a.l0 = fadd(a.l0, a.S[X][i]); } while (0)
#define CVTE(i) do { a.P[(i) >> 3][((i) >> 1) & 3] = cvtpk(a.S[X][i], a.S[X][(i) + 1]); } while (0)
template <int U, int MODE, int G> DI void gap(Att& a) {
  constexpr int X = U & 1, Y = X ^ 1, slot = G % FD;
  if constexpr (G == 16 - FD && (U & 1) == 0) BARRIER();
  if constexpr (G < 8) {
    if constexpr (MODE == 0) a.S[Y] = MFMA32(a.F[slot], a.Qa[G], G == 0 ? a.negm : a.S[Y]);
    if constexpr (MODE == 1) a.S[Y] = MFMA32(a.F[slot], a.Qb[G], G == 0 ? f32x16{} : a.S[Y]);
  } else {
    constexpr int ks = (G - 8) >> 2, c = (G - 8) & 3;
    a.O[c] = MFMA32(a.F[slot], __builtin_bit_cast(bf16x8, a.P[ks]), (U == 0 && ks == 0) ? f32x16{} : a.O[c]);
  }
  constexpr int GN = G + FD;
  if constexpr (!((MODE == 2 && (GN < 8 || GN >= 16)))) frag_read<U, GN>(a);
  if constexpr (G == 0) { EXPE(0); EXPE(1); }
  if constexpr (G == 1) { EXPE(2); EXPE(3); }
  if constexpr (G == 2) { EXPE(4); ADDE(0); CVTE(0); }
  if constexpr (G == 3) { EXPE(5); ADDE(1); CVTE(2); }
  if constexpr (G == 4) { EXPE(6); EXPE(7); }
  if constexpr (G == 5) { EXPE(8); CVTE(4); ADDE(2); }
  if constexpr (G == 6) { EXPE(9); CVTE(6); ADDE(3); }
  if constexpr (G == 7) { EXPE(10); EXPE(11); }
  if constexpr (G == 8) { EXPE(12); EXPE(13); CVTE(8); }
  if constexpr (G == 9) { EXPE(14); EXPE(15); CVTE(10); }
  if constexpr (G == 10) { CVTE(12); CVTE(14); ADDE(4); ADDE(5); }
  if constexpr (G >= 11) { ADDE(2 * G - 16); ADDE(2 * G - 15); }
  if constexpr (MODE != 2) {
    if constexpr (G == 11) { a.mx = max3f(a.S[Y][0], a.S[Y][1], a.S[Y][2]); a.mx = max3f(a.mx, a.S[Y][3], a.S[Y][4]); }
    if constexpr (G == 12) { a.mx = max3f(a.mx, a.S[Y][5], a.S[Y][6]); a.mx = max3f(a.mx, a.S[Y][7], a.S[Y][8]); }
    if constexpr (G == 13) { a.mx = max3f(a.mx, a.S[Y][9], a.S[Y][10]); a.mx = max3f(a.mx, a.S[Y][11], a.S[Y][12]); }
    if constexpr (G == 14) { a.mx = max3f(a.mx, a.S[Y][13], a.S[Y][14]); a.mx = max3f(a.mx, a.mx, a.S[Y][15]); }
    if constexpr (G >= 11 && G <= 14) PIN(a.mx);
  }
  if constexpr (G <= 9) PIN(a.S[X]);
  if constexpr (G == 2 || G == 3 || G == 5 || G == 6) PIN(a.P[0]);
  if constexpr (G >= 8 && G <= 10) PIN(a.P[1]);
  if constexpr (G == 2 || G == 5 || G >= 10) PIN(a.l0);
  if constexpr (G == 3 || G == 6 || G >= 10) PIN(a.l1);
  SBAR();
}
template <int U, int MODE, int... Gs> DI void period_(Att& a, std::integer_sequence<int, Gs...>) { (gap<U, MODE, Gs>(a), ...); }
template <int U, int MODE> DI void period(Att& a) {
  period_<U, MODE>(a, std::make_integer_sequence<int, 16>{});
  if constexpr (MODE == 0) {
    constexpr int Y = (U & 1) ^ 1;
    if (__builtin_expect(__any(a.mx > THR), 0)) {
      const float dl = __builtin_fmaxf(swapmax(a.mx), 0.f), f = __builtin_amdgcn_exp2f(-dl);
      a.mhat += dl; a.l0 *= f; a.l1 *= f;
#pragma unroll
      for (int r = 0; r < 16; ++r) { a.S[Y][r] -= dl; a.negm[r] = -a.mhat; }
#pragma unroll
      for (int c = 0; c < 4; ++c)
#pragma unroll
        for (int r = 0; r < 16; ++r) a.O[c][r] *= f;
      PIN(a.negm); PIN(a.mhat); PIN(a.S[Y]); PIN(a.O[0]); PIN(a.O[1]); PIN(a.O[2]); PIN(a.O[3]);
    }
    SBAR();
  }
}
DI void head_init(Att& a) {
  a.mhat = swapmax(a.mx); a.l0 = 0.f; a.l1 = 0.f;
#pragma unroll
  for (int r = 0; r < 16; ++r) { a.S[0][r] -= a.mhat; a.negm[r] = -a.mhat; }
  PIN(a.negm); PIN(a.mhat); PIN(a.S[0]); PIN(a.l0); PIN(a.l1);
}
template <int... Us> DI void periods(Att& a, std::integer_sequence<int, Us...>) { (period<Us, 0>(a), ...); }

__global__ __launch_bounds__(512, 2) void attn_kernel(const void* __restrict__ xg, const void* __restrict__ kg, const void* __restrict__ vg,
                                                      const void* __restrict__ wqg, const unsigned short* __restrict__ wog, char* __restrict__ ws) {
  extern __shared__ __attribute__((aligned(16))) char lds[];
  const unsigned tid = threadIdx.x, lane = tid & 63u, wid = rfl(tid >> 6);
  const unsigned xcd = blockIdx.x & 7u, jj = blockIdx.x >> 3;
  const unsigned b = (xcd & 3u) * 4u + (jj & 3u), hp = (xcd >> 2) * 8u + (jj >> 2);
  const unsigned lds0 = (unsigned)(size_t)lds;
  const unsigned h = lane >> 5, r32 = lane & 31u;

  if (wid == 7) {
    const rsrc_t rw = mk_rsrc(wqg, W_BYTES), rk = mk_rsrc(kg, KV_BYTES), rv = mk_rsrc(vg, KV_BYTES);
    const unsigned r8 = (lane >> 2) & 7u;
    const unsigned vw = r8 * 8192u + 64u * h + 16u * (lane & 3u);
    const unsigned vv = r8 * 256u + 64u * h + 16u * (lane & 3u);
    const unsigned vk0 = r8 * 256u + 64u * h + 16u * ((lane & 3u) ^ ((lane >> 4) & 1u));
    const unsigned vk1 = r8 * 256u + 64u * h + 16u * ((lane & 3u) ^ (2u + ((lane >> 4) & 1u)));
    const unsigned kvbase = (b * NH + 2u * hp) * (unsigned)(SK * DH * 2);
    auto ld_wq = [&](unsigned s, unsigned slot) {
#pragma unroll
      for (unsigned j = 0; j < 16; ++j) dma16(rw, vw, (32u * s + 8u * ((j >> 1) & 3u)) * 8192u + hp * 512u + 256u * (j >> 3) + 128u * (j & 1u), lds0 + slot + 1024u * j);
    };
    auto ld_k = [&](unsigned u) {
#pragma unroll
      for (unsigned j = 0; j < 8; ++j) dma16(rk, (j & 2u) ? vk1 : vk0, kvbase + u * 8192u + 2048u * (j >> 1) + 128u * (j & 1u), lds0 + KR + (u & 7u) * 8192u + 1024u * j);
    };
    auto ld_v = [&](unsigned u) {
#pragma unroll
      for (unsigned j = 0; j < 8; ++j) dma16(rv, vv, kvbase + u * 8192u + 2048u * (j >> 1) + 128u * (j & 1u), lds0 + VR + (u & 7u) * 8192u + 1024u * j);
    };
    ld_wq(0, wq_slot(0)); ld_wq(1, wq_slot(1)); ld_wq(2, wq_slot(2));
    WAITV(32); BARRIER();
    ld_wq(3, wq_slot(3)); ld_k(0); ld_k(1); ld_v(0);
    WAITV(56); BARRIER();
    ld_wq(4, wq_slot(4));
    WAITV(56); BARRIER();
    ld_wq(5, wq_slot(0));
    WAITV(56); BARRIER();
#pragma unroll 1
    for (unsigned s = 3; s < 13; ++s) { const unsigned i = (s + 3) % 5; ld_wq(s + 3, rfl(wq_slot(i))); WAITV(32); BARRIER(); }
    ld_k(2); ld_k(3); WAITV(32); BARRIER();
    ld_v(1); ld_v(2); WAITV(32); BARRIER();
    ld_k(4); ld_k(5); BARRIER();
    ld_v(3); ld_v(4); ld_k(6); ld_k(7); ld_v(5);
    WAITV(56); BARRIER();
    ld_v(6); ld_v(7); ld_k(8); ld_k(9);
    WAITV(56); BARRIER();
#pragma unroll 1
    for (unsigned t = 1; t < 12; ++t) {
      ld_v(2 * t + 6); ld_v(2 * t + 7); ld_k(2 * t + 8); ld_k(2 * t + 9);
      WAITV(56); BARRIER();
    }
    ld_v(30); ld_v(31);
    WAITV(40); BARRIER();
    WAITV(8); BARRIER();
    WAITV(0); BARRIER();
    return;
  }

  const rsrc_t rx = mk_rsrc(xg, X_BYTES);
  const unsigned xm0 = lds0 + XR + wid * 8192u;
  const unsigned vx0 = (lane >> 3) * 1024u + 16u * ((lane & 7u) ^ (lane >> 4)), vx1 = (lane >> 3) * 1024u + 16u * ((lane & 7u) ^ (4u + (lane >> 4)));
  const unsigned xsoff = (b * SQ + 32u * wid) * 1024u;
  auto ld_x = [&](unsigned c) {
#pragma unroll
    for (unsigned j = 0; j < 4; ++j) dma16(rx, (j & 1u) ? vx1 : vx0, xsoff + 8192u * j + 128u * c, xm0 + (c & 1u) * 4096u + 1024u * j);
  };
  ld_x(0); ld_x(1);
  {
    const unsigned ks = blockIdx.x >> 1;
    for (unsigned f = wid; f < 16; f += 7) {
      const unsigned n16 = (blockIdx.x & 1u) * 16u + f, cc = lane >> 4;
      const unsigned short* src = wog + (size_t)(32u * ks + 16u * (cc >> 1) + 4u * (cc & 1u)) * DM + n16 * 16u + (lane & 15u);
      unsigned e[8];
#pragma unroll
      for (int j = 0; j < 8; ++j) e[j] = src[(8 * (j >> 2) + (j & 3)) * DM];
      u32x4 w = {e[0] | (e[1] << 16), e[2] | (e[3] << 16), e[4] | (e[5] << 16), e[6] | (e[7] << 16)};
      *(u32x4*)(ws + WS_WT + ((size_t)((n16 >> 2) * 128u + ks) * 4u + (n16 & 3u)) * 1024u + lane * 16u) = w;
    }
  }
  Att a;
  {
    f32x16 acc[8];
    const unsigned q4 = (lane & 15u) >> 2, p = lane & 3u, blk = (lane >> 4) & 1u;
    const lds_cptr wbase = (lds_cptr)lds + 2048u * h + 64u * q4 + 32u * blk + 8u * (((p & 1u) << 1) | (p >> 1));
    lds_cptr xa[4];
#pragma unroll
    for (unsigned k = 0; k < 4; ++k) xa[k] = (lds_cptr)lds + XR + wid * 8192u + r32 * 128u + ((16u * (h ^ ((r32 >> 1) & 7u))) ^ (32u * k));
    bf16x8 F[FD], XF[2];
    auto wfrag = [&](lds_cptr base, int m) { const unsigned off = 8192u * ((m >> 2) & 1) + 4096u * (m >> 3) + 512u * (m & 3); return cat(ldstr(base + off), ldstr(base + off + 256)); };
    WAITV(4);
    BARRIER();
    lds_cptr wcur = wbase + wq_slot(0);
    XF[0] = lds128(xa[0]);
#pragma unroll
    for (int m = 0; m < FD; ++m) F[m] = wfrag(wcur, m);
#pragma unroll
    for (int s = 0; s < 16; ++s) {
      const lds_cptr wnext = wbase + wq_slot((s + 1) % 5);
#pragma unroll
      for (int m = 0; m < 16; ++m) {
        if (m == 16 - FD && s < 15) { if (s & 1) WAITV(4); BARRIER(); }
        acc[m & 7] = MFMA32(F[m % FD], XF[m >> 3], (s == 0 && m < 8) ? f32x16{} : acc[m & 7]);
        if (m + FD < 16) F[m % FD] = wfrag(wcur, m + FD); else if (s < 15) F[m % FD] = wfrag(wnext, m + FD - 16);
        if (m == 2) XF[1] = lds128(xa[2 * (s & 1) + 1] + ((s >> 1) & 1) * 4096u);
        if (m == 16 - FD && s < 15) XF[0] = lds128(xa[2 * ((s + 1) & 1)] + (((s + 1) >> 1) & 1) * 4096u);
        if (m == 9 && (s & 1)) { if ((s >> 1) + 2 < 8) ld_x((s >> 1) + 2); else { asm volatile("s_waitcnt vmcnt(0)" ::: "memory"); } }
        SBAR();
      }
      wcur = wnext;
    }
#pragma unroll
    for (int t = 0; t < 8; ++t)
#pragma unroll
      for (int s = 0; s < 2; ++s) {
        u32x4 w;
#pragma unroll
        for (int e = 0; e < 4; ++e) w[e] = cvtpk(acc[t][8 * s + 2 * e] * QSCALE, acc[t][8 * s + 2 * e + 1] * QSCALE);
        if (t < 4) a.Qa[2 * t + s] = __builtin_bit_cast(bf16x8, w); else a.Qb[2 * (t - 4) + s] = __builtin_bit_cast(bf16x8, w);
      }
  }
  {
    const unsigned kb = 2048u * (r32 >> 3) + 64u * (r32 & 7u) + 16u * (h ^ ((r32 >> 2) & 3u));
    a.ke = (lds_cptr)lds + KR + kb; a.ko = (lds_cptr)lds + KR + (kb ^ 32u);
    a.vb = (lds_cptr)lds + VR + 256u * h + 64u * ((lane & 15u) >> 2) + 32u * ((lane >> 4) & 1u) + 8u * (lane & 3u);
  }
  BARRIER();
  {
    bf16x8 kf[8];
#pragma unroll
    for (int g = 0; g < 8; ++g) kf[g] = lds128(((g & 1) ? a.ko : a.ke) + 512u * (g >> 1));
#pragma unroll
    for (int g = 0; g < 8; ++g) a.S[0] = MFMA32(kf[g], a.Qa[g], g == 0 ? f32x16{} : a.S[0]);
    frag_read<0, 0>(a); frag_read<0, 1>(a); frag_read<0, 2>(a); frag_read<0, 3>(a);
    float m = __builtin_fmaxf(a.S[0][0], a.S[0][1]);
#pragma unroll
    for (int r = 2; r < 16; ++r) m = __builtin_fmaxf(m, a.S[0][r]);
    a.mx = m;
  }
  const rsrc_t ro = mk_rsrc(ws + WS_OF, OF_BYTES);
  const unsigned q16 = 2u * wid + (r32 >> 4), mh = q16 >= 7u ? 1u : 0u, qb = q16 - 7u * mh;
  const unsigned ovoff = mh * (128u * 7168u) + qb * 1024u + (h * 16u + (lane & 15u)) * 16u;
  for (unsigned hd = 0; hd < 2; ++hd) {
    head_init(a);
    SBAR();
    periods(a, std::make_integer_sequence<int, 15>{});
    period<15, 1>(a);
    const float inv = __builtin_amdgcn_rcpf(swapsum(a.l0 + a.l1));
    const unsigned osoff = (b * 256u + (2u * hp + hd) * 4u) * 7168u;
#pragma unroll
    for (int c = 0; c < 4; ++c)
#pragma unroll
      for (int s = 0; s < 2; ++s) {
        u32x4 w;
#pragma unroll
        for (int e = 0; e < 4; ++e) w[e] = cvtpk(a.O[c][8 * s + 2 * e] * inv, a.O[c][8 * s + 2 * e + 1] * inv);
        __builtin_amdgcn_raw_buffer_store_b128(w, ro, ovoff + osoff + 7168u * c + 512u * s, 0, 0);
      }
#pragma unroll
    for (int g = 0; g < 8; ++g) a.Qa[g] = a.Qb[g];
  }
}

constexpr int PD = 4;
__global__ __launch_bounds__(256, 1) void oproj_kernel(const char* __restrict__ ws, unsigned short* __restrict__ out) {
  extern __shared__ __attribute__((aligned(16))) char lds[];
  const unsigned tid = threadIdx.x, lane = tid & 63u, wid = rfl(tid >> 6);
  const unsigned xcd = blockIdx.x & 7u, jj = blockIdx.x >> 3;
  const unsigned bm = xcd * 4u + (jj >> 3), nblk = jj & 7u;
  const rsrc_t ro = mk_rsrc(ws + WS_OF, OF_BYTES), rw = mk_rsrc(ws + WS_WT, W_BYTES);
  const unsigned vo = lane * 16u;
  const unsigned so = bm * (128u * 7168u) + wid * 7168u, sw = nblk * (128u * 4096u) + wid * 4096u;
  f32x4 acc[7][4];
#pragma unroll
  for (int i = 0; i < 7; ++i)
#pragma unroll
    for (int j = 0; j < 4; ++j) acc[i][j] = f32x4{};
  u32x4 A[PD][4], Bq[PD][7];
  auto load = [&](int st, unsigned it) {
#pragma unroll
    for (int j = 0; j < 4; ++j) A[st][j] = __builtin_amdgcn_raw_buffer_load_b128(rw, vo + 1024u * j, sw + it * (4u * 4096u), 0);
#pragma unroll
    for (int i = 0; i < 7; ++i) Bq[st][i] = __builtin_amdgcn_raw_buffer_load_b128(ro, vo + (i < 4 ? 1024u * i : 0u), so + it * (4u * 7168u) + (i < 4 ? 0u : 1024u * i), 0);
  };
#pragma unroll
  for (int st = 0; st < PD; ++st) load(st, st);
  for (unsigned it = 0; it < 32; it += PD) {
#pragma unroll
    for (int st = 0; st < PD; ++st) {
#pragma unroll
      for (int i = 0; i < 7; ++i)
#pragma unroll
        for (int j = 0; j < 4; ++j) acc[i][j] = MFMA16(__builtin_bit_cast(bf16x8, A[st][j]), __builtin_bit_cast(bf16x8, Bq[st][i]), acc[i][j]);
      SBAR();
      if (it + PD < 32) load(st, it + st + PD);
      SBAR();
    }
  }
  constexpr unsigned TS = 1024 + 16;
#pragma unroll
  for (int i = 0; i < 7; ++i)
#pragma unroll
    for (int j = 0; j < 4; ++j) *(f32x4*)(lds + (wid * 28u + i * 4 + j) * TS + lane * 16u) = acc[i][j];
  __syncthreads();
  const rsrc_t rout = mk_rsrc(out, X_BYTES);
  for (unsigned idx = wid; idx < 14; idx += 4) {
    const unsigned row = idx * 8u + (lane >> 3), cg = lane & 7u;
    const unsigned base = ((row >> 4) * 4u + (cg >> 1)) * TS + ((cg & 1u) * 32u + (row & 15u)) * 16u;
    f32x4 s0 = {}, s1 = {};
#pragma unroll
    for (int w = 0; w < 4; ++w) { s0 += *(const f32x4*)(lds + w * 28u * TS + base); s1 += *(const f32x4*)(lds + w * 28u * TS + base + 256u); }
    u32x4 w = {cvtpk(s0[0], s0[1]), cvtpk(s0[2], s0[3]), cvtpk(s1[0], s1[1]), cvtpk(s1[2], s1[3])};
    __builtin_amdgcn_raw_buffer_store_b128(w, rout, (bm * 112u + row) * 1024u + nblk * 128u + cg * 16u, 0, 0);
  }
}

extern "C" void kernel_launch(void* const* d_in, const int* in_sizes, int n_in, void* d_out, int out_size, void* d_ws, size_t ws_size, hipStream_t stream) {
  constexpr unsigned OP_LDS = 4 * 28 * (1024 + 16);
  (void)hipFuncSetAttribute((const void*)attn_kernel, hipFuncAttributeMaxDynamicSharedMemorySize, (int)LDS_BYTES);
  (void)hipFuncSetAttribute((const void*)oproj_kernel, hipFuncAttributeMaxDynamicSharedMemorySize, (int)OP_LDS);
  hipLaunchKernelGGL(attn_kernel, dim3(256), dim3(512), LDS_BYTES, stream, d_in[0], d_in[1], d_in[2], d_in[3], (const unsigned short*)d_in[4], (char*)d_ws);
  hipLaunchKernelGGL(oproj_kernel, dim3(256), dim3(256), OP_LDS, stream, (const char*)d_ws, (unsigned short*)d_out);
}
```

```cpp
#include <hip/hip_runtime.h>


__attribute__((aligned(8192)))
__global__ __launch_bounds__(256, 1) void gqa_fused(const char* __restrict__ x, const char* __restrict__ k, const char* __restrict__ v, const char* __restrict__ wq,
                                                    const char* __restrict__ wo, char* __restrict__ out, char* __restrict__ ws) {
  const unsigned wid = __builtin_amdgcn_readfirstlane(threadIdx.x >> 6), bid = blockIdx.x;
  const unsigned xcd = bid & 7u, jj = bid >> 3;
  const unsigned b = (xcd & 3u) * 4u + (jj & 3u), hp = (xcd >> 2) * 8u + (jj >> 2), g = hp >> 2, j = hp & 3u;
  const size_t hoff = (size_t)(b * 32u + 2u * hp + (wid >> 1)) * 131072u;
  const unsigned flip = (g >> 1) & (g ^ 1u) & 1u, mx = wid >> 1;
  const unsigned xr = (0x05420642u >> (4u * g + 16u * mx)) & 15u;
  const unsigned x0 = 7u * (wid & 1u) + 2u * xr, xlo = mx * 114688u, xb = xlo + ((x0 >= 14u ? x0 - 14u : x0) << 13);
  const unsigned long long ttab = g < 2u ? (g ? 0xa3928170d6c5b4ull : 0x8170d6c5b4a392ull) : (g == 2u ? 0x5b4a3928170d6cull : 0xd6c5b4a3928170ull);
  const unsigned rot = (bid * 5u + (wid >> 1) * 3u + (bid >> 4)) & 15u, kr = 0u;
  const char *px = x + (size_t)b * 229376u, *pwq = wq + hp * 512u, *pk = k + hoff, *pv = v + hoff, *pwo = wo + (size_t)g * 1048576u + j * 256u;
  char* pout = out + (size_t)b * 229376u;
  asm volatile(
      ".p2align 6\n"
      "s_mov_b64 s[48:49], %[pwq]\n"
      "s_mov_b32 s50, 4186624\n"
      "s_mov_b32 s51, 0x20000\n"
      "v_mbcnt_lo_u32_b32 v250, -1, 0\n"
      "v_mbcnt_hi_u32_b32 v250, -1, v250\n"
      "s_lshr_b32 s64, %[wid], 1\n"
      "s_and_b32 s65, %[wid], 1\n"
      "s_mul_i32 s66, s64, 49152\n"
      "s_lshl_b32 s70, %[wid], 12\n"
      "s_add_u32 s70, s70, 98304\n"
      "s_lshl_b32 s71, %[wid], 16\n"
      "s_lshl_b32 s67, %[kr], 19\n"
      "s_add_u32 s71, s71, s67\n"
      "v_lshrrev_b32 v251, 3, v250\n"
      "v_and_b32 v252, 7, v250\n"
      "v_lshrrev_b32 v253, 4, v250\n"
      "v_and_b32_e64 v254, 1, v253\n"
      "v_lshlrev_b32 v254, 2, v254\n"
      "v_xor_b32_e64 v254, v254, v252\n"
      "v_lshlrev_b32_e64 v254, 4, v254\n"
      "v_lshl_add_u32 v235, v251, 13, v254\n"
      "s_add_u32 m0, s70, 0\n"
      "s_nop 0\n"
      "buffer_load_dwordx4 v235, s[48:51], s71 offen offset:0 lds\n"
      "s_add_u32 m0, s70, 896\n"
      "s_nop 0\n"
      "buffer_load_dwordx4 v235, s[48:51], s71 offen offset:128 lds\n"
      "s_add_u32 m0, s70, 1792\n"
      "s_nop 0\n"
      "buffer_load_dwordx4 v235, s[48:51], s71 offen offset:256 lds\n"
      "s_add_u32 m0, s70, 2688\n"
      "s_nop 0\n"
      "buffer_load_dwordx4 v235, s[48:51], s71 offen offset:384 lds\n"
      "s_xor_b32 s71, s71, 262144\n"
      "s_memtime s[90:91]\n"
      "s_memrealtime s[92:93]\n"
      "s_mov_b64 s[44:45], %[px]\n"
      "s_mov_b32 s46, 229376\n"
      "s_mov_b32 s47, 0x20000\n"
      "v_lshlrev_b32 v254, 1, v253\n"
      "v_xor_b32 v254, v254, v252\n"
      "v_lshlrev_b32 v254, 4, v254\n"
      "v_lshl_add_u32 v234, v251, 10, v254\n"
      "v_lshl_add_u32 v231, v251, 8, v254\n"
      "v_add_u32 v230, 896, v231\n"
      "s_lshl_b32 s67, %[kr], 7\n"
      "v_add_u32 v234, s67, v234\n"
      "s_mul_i32 s72, s65, 7168\n"
      "s_add_u32 s72, s72, s66\n"
      "s_mov_b32 s73, %[xb]\n"
      "s_add_u32 s67, %[xlo], 114688\n"
      "s_add_u32 s74, s73, 8192\n"
      "s_cmp_eq_u32 s74, s67\n"
      "s_cselect_b32 s74, %[xlo], s74\n"
      "s_add_u32 s75, s74, 8192\n"
      "s_cmp_eq_u32 s75, s67\n"
      "s_cselect_b32 s75, %[xlo], s75\n"
      "s_add_u32 s76, s75, 8192\n"
      "s_cmp_eq_u32 s76, s67\n"
      "s_cselect_b32 s76, %[xlo], s76\n"
      "s_add_u32 s77, s76, 8192\n"
      "s_cmp_eq_u32 s77, s67\n"
      "s_cselect_b32 s77, %[xlo], s77\n"
      "s_add_u32 s78, s77, 8192\n"
      "s_cmp_eq_u32 s78, s67\n"
      "s_cselect_b32 s78, %[xlo], s78\n"
      "s_add_u32 s79, s78, 8192\n"
      "s_cmp_eq_u32 s79, s67\n"
      "s_cselect_b32 s79, %[xlo], s79\n"
      "s_add_u32 m0, s72, 16384\n"
      "s_nop 0\n"
      "buffer_load_dwordx4 v234, s[44:47], s73 offen lds\n"
      "s_add_u32 m0, s72, 17408\n"
      "s_nop 0\n"
      "buffer_load_dwordx4 v234, s[44:47], s74 offen lds\n"
      "s_add_u32 m0, s72, 18432\n"
      "s_nop 0\n"
      "buffer_load_dwordx4 v234, s[44:47], s75 offen lds\n"
      "s_add_u32 m0, s72, 19456\n"
      "s_nop 0\n"
      "buffer_load_dwordx4 v234, s[44:47], s76 offen lds\n"
      "s_add_u32 m0, s72, 20480\n"
      "s_nop 0\n"
      "s_nop 0\n"
      "buffer_load_dwordx4 v234, s[44:47], s77 offen lds\n"
      "s_add_u32 m0, s72, 21504\n"
      "s_nop 0\n"
      "buffer_load_dwordx4 v234, s[44:47], s78 offen lds\n"
      "s_add_u32 m0, s72, 22528\n"
      "s_nop 0\n"
      "buffer_load_dwordx4 v234, s[44:47], s79 offen lds\n"
      "v_xor_b32 v234, 128, v234\n"
      "s_getpc_b64 s[98:99]\n"
      "IPF_%=:\n"
      "s_lshr_b32 s97, %[bid], 3\n"
      "s_lshl_b32 s97, s97, 2\n"
      "s_add_u32 s97, s97, %[wid]\n"
      "s_lshl_b32 s97, s97, 9\n"
      "v_lshlrev_b32 v246, 3, v250\n"
      "v_add_u32 v246, s97, v246\n"
      "s_mov_b32 s97, OOLEND_%=-IPF_%=\n"
      "v_min_u32 v246, s97, v246\n"
      "global_load_dwordx2 v[244:245], v246, s[98:99]\n"
      "v_and_b32 v251, 15, v250\n"
      "v_lshrrev_b32 v252, 1, v251\n"
      "v_and_b32 v252, 3, v252\n"
      "v_lshlrev_b32 v252, 1, v252\n"
      "v_xor_b32 v252, v252, v253\n"
      "v_lshlrev_b32 v252, 4, v252\n"
      "v_and_b32_e64 v254, 7, v251\n"
      "v_lshl_add_u32 v252, v254, 7, v252\n"
      "v_lshrrev_b32_e64 v254, 3, v251\n"
      "s_lshr_b32 s64, %[wid], 1\n"
      "s_and_b32 s65, %[wid], 1\n"
      "s_mul_i32 s66, s65, 49152\n"
      "s_mul_i32 s67, s64, 49152\n"
      "v_lshl_add_u32 v236, v254, 10, v252\n"
      "v_add_u32 v236, s66, v236\n"
      "v_xor_b32 v237, 64, v236\n"
      "v_lshl_add_u32 v224, v254, 11, v252\n"
      "v_add_u32 v224, s67, v224\n"
      "v_xor_b32 v225, 64, v224\n"
      "v_and_b32_e64 v252, 3, v250\n"
      "v_lshlrev_b32_e64 v254, 2, v254\n"
      "v_xor_b32_e64 v254, v254, v252\n"
      "v_lshlrev_b32 v254, 4, v254\n"
      "v_lshrrev_b32_e64 v249, 2, v251\n"
      "v_lshl_add_u32 v254, v249, 7, v254\n"
      "v_lshl_add_u32 v238, v253, 12, v254\n"
      "s_lshl_b32 s66, s64, 11\n"
      "s_add_u32 s66, s66, 98304\n"
      "v_add_u32_e64 v238, s66, v238\n"
      "v_xor_b32_e64 v239, 64, v238\n"
      "s_add_u32 m0, s70, 16384\n"
      "s_nop 0\n"
      "buffer_load_dwordx4 v235, s[48:51], s71 offen offset:0 lds\n"
      "s_add_u32 m0, s70, 17280\n"
      "s_nop 0\n"
      "buffer_load_dwordx4 v235, s[48:51], s71 offen offset:128 lds\n"
      "s_add_u32 m0, s70, 18176\n"
      "s_nop 0\n"
      "buffer_load_dwordx4 v235, s[48:51], s71 offen offset:256 lds\n"
      "s_add_u32 m0, s70, 19072\n"
      "s_nop 0\n"
      "buffer_load_dwordx4 v235, s[48:51], s71 offen offset:384 lds\n"
      "s_xor_b32 s71, s71, 786432\n"
      "s_add_u32 m0, s70, 32768\n"
      "s_nop 0\n"
      "buffer_load_dwordx4 v235, s[48:51], s71 offen offset:0 lds\n"
      "s_add_u32 m0, s70, 33664\n"
      "s_nop 0\n"
      "buffer_load_dwordx4 v235, s[48:51], s71 offen offset:128 lds\n"
      "s_add_u32 m0, s70, 34560\n"
      "s_nop 0\n"
      "buffer_load_dwordx4 v235, s[48:51], s71 offen offset:256 lds\n"
      "s_add_u32 m0, s70, 35456\n"
      "s_nop 0\n"
      "buffer_load_dwordx4 v235, s[48:51], s71 offen offset:384 lds\n"
      "s_xor_b32 s71, s71, 262144\n"
      "s_add_u32 m0, s72, 32768\n"
      "s_nop 0\n"
      "s_nop 0\n"
      "buffer_load_dwordx4 v234, s[44:47], s73 offen lds\n"
      "s_add_u32 m0, s72, 33792\n"
      "s_nop 0\n"
      "buffer_load_dwordx4 v234, s[44:47], s74 offen lds\n"
      "s_add_u32 m0, s72, 34816\n"
      "s_nop 0\n"
      "buffer_load_dwordx4 v234, s[44:47], s75 offen lds\n"
      "s_waitcnt vmcnt(12) lgkmcnt(0)\n"
      "s_memtime s[82:83]\n"
      "s_memrealtime s[94:95]\n"
      "s_barrier\n"
      "ds_read_b64_tr_b16 a[28:29], v238 offset:0\n"
      "ds_read_b64_tr_b16 a[30:31], v238 offset:512\n"
      "ds_read_b128 a[0:3], v236 offset:16384\n"
      "ds_read_b128 a[4:7], v236 offset:18432\n"
      "ds_read_b128 a[8:11], v236 offset:20480\n"
      "ds_read_b128 a[12:15], v236 offset:22528\n"
      "ds_read_b128 a[16:19], v236 offset:24576\n"
      "ds_read_b128 a[20:23], v236 offset:26624\n"
      "ds_read_b128 a[24:27], v236 offset:28672\n"
      "ds_read_b64_tr_b16 a[32:33], v238 offset:8\n"
      "ds_read_b64_tr_b16 a[34:35], v238 offset:520\n"
      "s_waitcnt lgkmcnt(8)\n"
      "v_mfma_f32_16x16x32_bf16 v[0:3], a[28:31], a[0:3], 0\n"
      "ds_read_b64_tr_b16 a[36:37], v239 offset:0\n"
      "s_waitcnt lgkmcnt(8)\n"
      "v_mfma_f32_16x16x32_bf16 v[32:35], a[28:31], a[4:7], 0\n"
      "ds_read_b64_tr_b16 a[38:39], v239 offset:512\n"
      "s_add_u32 m0, s72, 35840\n"
      "s_lshl_b32 s66, s65, 13\n"
      "s_waitcnt lgkmcnt(8)\n"
      "v_mfma_f32_16x16x32_bf16 v[64:67], a[28:31], a[8:11], 0\n"
      "ds_read_b64_tr_b16 a[40:41], v239 offset:8\n"
      "s_add_u32 s68, s67, s66\n"
      "s_waitcnt lgkmcnt(8)\n"
      "v_mfma_f32_16x16x32_bf16 v[96:99], a[28:31], a[12:15], 0\n"
      "ds_read_b64_tr_b16 a[42:43], v239 offset:520\n"
      "buffer_load_dwordx4 v234, s[44:47], s76 offen lds\n"
      "s_lshl_b32 s69, %[wid], 13\n"
      "s_waitcnt lgkmcnt(8)\n"
      "v_mfma_f32_16x16x32_bf16 v[128:131], a[28:31], a[16:19], 0\n"
      "ds_read_b64_tr_b16 a[44:45], v238 offset:1024\n"
      "s_add_u32 s69, s69, 98304\n"
      "s_waitcnt lgkmcnt(8)\n"
      "v_mfma_f32_16x16x32_bf16 v[160:163], a[28:31], a[20:23], 0\n"
      "ds_read_b64_tr_b16 a[46:47], v238 offset:1536\n"
      "s_add_u32 m0, s72, 36864\n"
      "s_mul_i32 s80, %[rot], 8192\n"
      "s_waitcnt lgkmcnt(8)\n"
      "v_mfma_f32_16x16x32_bf16 v[192:195], a[28:31], a[24:27], 0\n"
      "ds_read_b64_tr_b16 a[48:49], v238 offset:1032\n"
      "s_add_u32 s81, s80, 0\n"
      "s_waitcnt lgkmcnt(7)\n"
      "v_mfma_f32_16x16x32_bf16 v[4:7], a[32:35], a[0:3], 0\n"
      "ds_read_b64_tr_b16 a[50:51], v238 offset:1544\n"
      "buffer_load_dwordx4 v234, s[44:47], s77 offen lds\n"
      "v_mfma_f32_16x16x32_bf16 v[36:39], a[32:35], a[4:7], 0\n"
      "ds_read_b64_tr_b16 a[52:53], v239 offset:1024\n"
      "s_and_b32 s81, s81, 0x1e000\n"
      "v_mfma_f32_16x16x32_bf16 v[68:71], a[32:35], a[8:11], 0\n"
      "ds_read_b64_tr_b16 a[54:55], v239 offset:1536\n"
      "s_add_u32 m0, s72, 37888\n"
      "v_and_b32_e64 v254, 1, v253\n"
      "v_mfma_f32_16x16x32_bf16 v[100:103], a[32:35], a[12:15], 0\n"
      "ds_read_b64_tr_b16 a[56:57], v239 offset:1032\n"
      "v_lshl_add_u32 v254, v254, 2, v249\n"
      "v_mfma_f32_16x16x32_bf16 v[132:135], a[32:35], a[16:19], 0\n"
      "ds_read_b64_tr_b16 a[58:59], v239 offset:1544\n"
      "buffer_load_dwordx4 v234, s[44:47], s78 offen lds\n"
      "v_lshrrev_b32 v248, 1, v254\n"
      "v_mfma_f32_16x16x32_bf16 v[164:167], a[32:35], a[20:23], 0\n"
      "v_lshlrev_b32_e64 v248, 5, v248\n"
      "v_mfma_f32_16x16x32_bf16 v[196:199], a[32:35], a[24:27], 0\n"
      "s_add_u32 m0, s72, 38912\n"
      "s_waitcnt lgkmcnt(10)\n"
      "v_mfma_f32_16x16x32_bf16 v[8:11], a[36:39], a[0:3], 0\n"
      "v_lshl_add_u32 v254, v254, 7, v248\n"
      "v_mfma_f32_16x16x32_bf16 v[40:43], a[36:39], a[4:7], 0\n"
      "buffer_load_dwordx4 v234, s[44:47], s79 offen lds\n"
      "v_lshl_add_u32 v254, v252, 3, v254\n"
      "v_mfma_f32_16x16x32_bf16 v[72:75], a[36:39], a[8:11], 0\n"
      "v_lshrrev_b32_e64 v248, 1, v253\n"
      "v_mfma_f32_16x16x32_bf16 v[104:107], a[36:39], a[12:15], 0\n"
      "v_xor_b32 v234, 384, v234\n"
      "s_add_u32 m0, s70, 49152\n"
      "v_lshl_add_u32 v254, v248, 11, v254\n"
      "v_mfma_f32_16x16x32_bf16 v[136:139], a[36:39], a[16:19], 0\n"
      "s_add_u32 s67, s67, 8192\n"
      "v_mfma_f32_16x16x32_bf16 v[168:171], a[36:39], a[20:23], 0\n"
      "buffer_load_dwordx4 v235, s[48:51], s71 offen offset:0 lds\n"
      "v_add_u32 v226, s67, v254\n"
      "v_mfma_f32_16x16x32_bf16 v[200:203], a[36:39], a[24:27], 0\n"
      "s_waitcnt lgkmcnt(8)\n"
      "v_mfma_f32_16x16x32_bf16 v[12:15], a[40:43], a[0:3], 0\n"
      "s_add_u32 m0, s70, 50048\n"
      "v_xor_b32_e64 v227, 32, v226\n"
      "v_mfma_f32_16x16x32_bf16 v[44:47], a[40:43], a[4:7], 0\n"
      "v_xor_b32_e64 v228, 64, v226\n"
      "v_mfma_f32_16x16x32_bf16 v[76:79], a[40:43], a[8:11], 0\n"
      "s_waitcnt vmcnt(12) lgkmcnt(0)\n"
      "s_barrier\n"
      "buffer_load_dwordx4 v235, s[48:51], s71 offen offset:128 lds\n"
      "v_xor_b32 v229, 96, v226\n"
      "v_mfma_f32_16x16x32_bf16 v[108:111], a[40:43], a[12:15], 0\n"
      "ds_read_b64_tr_b16 a[88:89], v238 offset:16384\n"
      "v_min_u32_e64 v247, 13, v250\n"
      "v_mfma_f32_16x16x32_bf16 v[140:143], a[40:43], a[16:19], 0\n"
      "ds_read_b64_tr_b16 a[90:91], v238 offset:16896\n"
      "s_add_u32 m0, s70, 50944\n"
      "v_lshlrev_b32 v247, 10, v247\n"
      "v_mfma_f32_16x16x32_bf16 v[172:175], a[40:43], a[20:23], 0\n"
      "ds_read_b128 a[60:63], v237 offset:16384\n"
      "v_mfma_f32_16x16x32_bf16 v[204:207], a[40:43], a[24:27], 0\n"
      "ds_read_b128 a[64:67], v237 offset:18432\n"
      "buffer_load_dwordx4 v235, s[48:51], s71 offen offset:256 lds\n"
      "s_and_b32 s97, %[g], 1\n"
      "v_mfma_f32_16x16x32_bf16 v[16:19], a[44:47], a[0:3], 0\n"
      "ds_read_b128 a[68:71], v237 offset:20480\n"
      "s_lshl_b32 s97, s97, 2\n"
      "v_mfma_f32_16x16x32_bf16 v[48:51], a[44:47], a[4:7], 0\n"
      "ds_read_b128 a[72:75], v237 offset:22528\n"
      "s_add_u32 m0, s70, 51840\n"
      "s_add_u32 s97, s97, %[j]\n"
      "v_mfma_f32_16x16x32_bf16 v[80:83], a[44:47], a[8:11], 0\n"
      "ds_read_b128 a[76:79], v237 offset:24576\n"
      "s_lshr_b32 s97, s97, 1\n"
      "v_mfma_f32_16x16x32_bf16 v[112:115], a[44:47], a[12:15], 0\n"
      "ds_read_b128 a[80:83], v237 offset:26624\n"
      "buffer_load_dwordx4 v235, s[48:51], s71 offen offset:384 lds\n"
      "s_lshl_b32 s97, s97, 2\n"
      "v_mfma_f32_16x16x32_bf16 v[144:147], a[44:47], a[16:19], 0\n"
      "ds_read_b128 a[84:87], v237 offset:28672\n"
      "v_mfma_f32_16x16x32_bf16 v[176:179], a[44:47], a[20:23], 0\n"
      "ds_read_b64_tr_b16 a[92:93], v238 offset:16392\n"
      "s_xor_b32 s71, s71, 1835008\n"
      "s_add_u32 m0, s70, 0\n"
      "s_add_u32 s97, s97, %[wid]\n"
      "v_mfma_f32_16x16x32_bf16 v[208:211], a[44:47], a[24:27], 0\n"
      "ds_read_b64_tr_b16 a[94:95], v238 offset:16904\n"
      "s_mul_i32 s97, s97, 14336\n"
      "v_mfma_f32_16x16x32_bf16 v[20:23], a[48:51], a[0:3], 0\n"
      "ds_read_b64_tr_b16 a[96:97], v239 offset:16384\n"
      "buffer_load_dwordx4 v235, s[48:51], s71 offen offset:0 lds\n"
      "s_add_u32 s97, s97, 384\n"
      "v_mfma_f32_16x16x32_bf16 v[52:55], a[48:51], a[4:7], 0\n"
      "ds_read_b64_tr_b16 a[98:99], v239 offset:16896\n"
      "s_bitcmp1_b32 %[j], 0\n"
      "s_cselect_b32 s97, s97, 1879048192\n"
      "v_mfma_f32_16x16x32_bf16 v[84:87], a[48:51], a[8:11], 0\n"
      "ds_read_b64_tr_b16 a[100:101], v239 offset:16392\n"
      "s_add_u32 m0, s70, 896\n"
      "v_mfma_f32_16x16x32_bf16 v[116:119], a[48:51], a[12:15], 0\n"
      "ds_read_b64_tr_b16 a[102:103], v239 offset:16904\n"
      "v_add_u32_e64 v247, s97, v247\n"
      "v_mfma_f32_16x16x32_bf16 v[148:151], a[48:51], a[16:19], 0\n"
      "ds_read_b64_tr_b16 a[104:105], v238 offset:17408\n"
      "buffer_load_dwordx4 v235, s[48:51], s71 offen offset:128 lds\n"
      "v_mfma_f32_16x16x32_bf16 v[180:183], a[48:51], a[20:23], 0\n"
      "ds_read_b64_tr_b16 a[106:107], v238 offset:17920\n"
      "s_bitcmp1_b32 %[wid], 0\n"
      "s_cselect_b64 s[52:53], %[pv], %[pk]\n"
      "v_mfma_f32_16x16x32_bf16 v[212:215], a[48:51], a[24:27], 0\n"
      "ds_read_b64_tr_b16 a[108:109], v238 offset:17416\n"
      "s_add_u32 m0, s70, 1792\n"
      "v_mfma_f32_16x16x32_bf16 v[24:27], a[52:55], a[0:3], 0\n"
      "ds_read_b64_tr_b16 a[110:111], v238 offset:17928\n"
      "s_sub_u32 s52, s52, 896\n"
      "s_subb_u32 s53, s53, 0\n"
      "v_mfma_f32_16x16x32_bf16 v[56:59], a[52:55], a[4:7], 0\n"
      "ds_read_b64_tr_b16 a[112:113], v239 offset:17408\n"
      "buffer_load_dwordx4 v235, s[48:51], s71 offen offset:256 lds\n"
      "v_mfma_f32_16x16x32_bf16 v[88:91], a[52:55], a[8:11], 0\n"
      "ds_read_b64_tr_b16 a[114:115], v239 offset:17920\n"
      "s_mov_b32 s54, 131968\n"
      "v_mfma_f32_16x16x32_bf16 v[120:123], a[52:55], a[12:15], 0\n"
      "ds_read_b64_tr_b16 a[116:117], v239 offset:17416\n"
      "s_add_u32 m0, s70, 2688\n"
      "v_mfma_f32_16x16x32_bf16 v[152:155], a[52:55], a[16:19], 0\n"
      "ds_read_b64_tr_b16 a[118:119], v239 offset:17928\n"
      "s_mov_b32 s55, 0x20000\n"
      "v_mfma_f32_16x16x32_bf16 v[184:187], a[52:55], a[20:23], 0\n"
      "buffer_load_dwordx4 v235, s[48:51], s71 offen offset:384 lds\n"
      "s_mov_b64 s[56:57], %[pwo]\n"
      "v_mfma_f32_16x16x32_bf16 v[216:219], a[52:55], a[24:27], 0\n"
      "s_mov_b32 s58, 1047808\n"
      "v_mfma_f32_16x16x32_bf16 v[28:31], a[56:59], a[0:3], 0\n"
      "s_xor_b32 s71, s71, 262144\n"
      "s_mov_b32 s59, 0x20000\n"
      "v_mfma_f32_16x16x32_bf16 v[60:63], a[56:59], a[4:7], 0\n"
      "s_mov_b64 s[60:61], %[pws]\n"
      "v_mfma_f32_16x16x32_bf16 v[92:95], a[56:59], a[8:11], 0\n"
      "v_mfma_f32_16x16x32_bf16 v[124:127], a[56:59], a[12:15], 0\n"
      "s_mov_b32 s62, 59768832\n"
      "v_mfma_f32_16x16x32_bf16 v[156:159], a[56:59], a[16:19], 0\n"
      "s_mov_b32 s63, 0x20000\n"
      "v_mfma_f32_16x16x32_bf16 v[188:191], a[56:59], a[20:23], 0\n"
      "s_mov_b32 s84, 0x3e0293ee\n"
      "v_mfma_f32_16x16x32_bf16 v[220:223], a[56:59], a[24:27], 0\n"
      "s_waitcnt vmcnt(8) lgkmcnt(0)\n"
      "s_barrier\n"
      "v_mfma_f32_16x16x32_bf16 v[0:3], a[88:91], a[60:63], v[0:3]\n"
      "ds_read_b64_tr_b16 a[28:29], v238 offset:32768\n"
      "v_mfma_f32_16x16x32_bf16 v[32:35], a[88:91], a[64:67], v[32:35]\n"
      "s_add_u32 m0, s72, 16384\n"
      "v_mfma_f32_16x16x32_bf16 v[64:67], a[88:91], a[68:71], v[64:67]\n"
      "ds_read_b64_tr_b16 a[30:31], v238 offset:33280\n"
      "v_mfma_f32_16x16x32_bf16 v[96:99], a[88:91], a[72:75], v[96:99]\n"
      "buffer_load_dwordx4 v234, s[44:47], s73 offen lds\n"
      "v_mfma_f32_16x16x32_bf16 v[128:131], a[88:91], a[76:79], v[128:131]\n"
      "ds_read_b128 a[0:3], v236 offset:32768\n"
      "v_mfma_f32_16x16x32_bf16 v[160:163], a[88:91], a[80:83], v[160:163]\n"
      "s_add_u32 m0, s72, 17408\n"
      "v_mfma_f32_16x16x32_bf16 v[192:195], a[88:91], a[84:87], v[192:195]\n"
      "ds_read_b128 a[4:7], v236 offset:34816\n"
      "v_mfma_f32_16x16x32_bf16 v[4:7], a[92:95], a[60:63], v[4:7]\n"
      "buffer_load_dwordx4 v234, s[44:47], s74 offen lds\n"
      "v_mfma_f32_16x16x32_bf16 v[36:39], a[92:95], a[64:67], v[36:39]\n"
      "ds_read_b128 a[8:11], v236 offset:36864\n"
      "v_mfma_f32_16x16x32_bf16 v[68:71], a[92:95], a[68:71], v[68:71]\n"
      "s_add_u32 m0, s72, 18432\n"
      "v_mfma_f32_16x16x32_bf16 v[100:103], a[92:95], a[72:75], v[100:103]\n"
      "ds_read_b128 a[12:15], v236 offset:38912\n"
      "v_mfma_f32_16x16x32_bf16 v[132:135], a[92:95], a[76:79], v[132:135]\n"
      "buffer_load_dwordx4 v234, s[44:47], s75 offen lds\n"
      "v_mfma_f32_16x16x32_bf16 v[164:167], a[92:95], a[80:83], v[164:167]\n"
      "ds_read_b128 a[16:19], v236 offset:40960\n"
      "v_mfma_f32_16x16x32_bf16 v[196:199], a[92:95], a[84:87], v[196:199]\n"
      "s_add_u32 m0, s72, 19456\n"
      "v_mfma_f32_16x16x32_bf16 v[8:11], a[96:99], a[60:63], v[8:11]\n"
      "ds_read_b128 a[20:23], v236 offset:43008\n"
      "v_mfma_f32_16x16x32_bf16 v[40:43], a[96:99], a[64:67], v[40:43]\n"
      "buffer_load_dwordx4 v234, s[44:47], s76 offen lds\n"
      "v_mfma_f32_16x16x32_bf16 v[72:75], a[96:99], a[68:71], v[72:75]\n"
      "ds_read_b128 a[24:27], v236 offset:45056\n"
      "v_mfma_f32_16x16x32_bf16 v[104:107], a[96:99], a[72:75], v[104:107]\n"
      "s_add_u32 m0, s72, 20480\n"
      "v_mfma_f32_16x16x32_bf16 v[136:139], a[96:99], a[76:79], v[136:139]\n"
      "ds_read_b64_tr_b16 a[32:33], v238 offset:32776\n"
      "v_mfma_f32_16x16x32_bf16 v[168:171], a[96:99], a[80:83], v[168:171]\n"
      "buffer_load_dwordx4 v234, s[44:47], s77 offen lds\n"
      "v_mfma_f32_16x16x32_bf16 v[200:203], a[96:99], a[84:87], v[200:203]\n"
      "ds_read_b64_tr_b16 a[34:35], v238 offset:33288\n"
      "v_mfma_f32_16x16x32_bf16 v[12:15], a[100:103], a[60:63], v[12:15]\n"
      "s_add_u32 m0, s72, 21504\n"
      "v_mfma_f32_16x16x32_bf16 v[44:47], a[100:103], a[64:67], v[44:47]\n"
      "ds_read_b64_tr_b16 a[36:37], v239 offset:32768\n"
      "v_mfma_f32_16x16x32_bf16 v[76:79], a[100:103], a[68:71], v[76:79]\n"
      "buffer_load_dwordx4 v234, s[44:47], s78 offen lds\n"
      "v_mfma_f32_16x16x32_bf16 v[108:111], a[100:103], a[72:75], v[108:111]\n"
      "ds_read_b64_tr_b16 a[38:39], v239 offset:33280\n"
      "v_mfma_f32_16x16x32_bf16 v[140:143], a[100:103], a[76:79], v[140:143]\n"
      "s_add_u32 m0, s72, 22528\n"
      "v_mfma_f32_16x16x32_bf16 v[172:175], a[100:103], a[80:83], v[172:175]\n"
      "ds_read_b64_tr_b16 a[40:41], v239 offset:32776\n"
      "v_mfma_f32_16x16x32_bf16 v[204:207], a[100:103], a[84:87], v[204:207]\n"
      "buffer_load_dwordx4 v234, s[44:47], s79 offen lds\n"
      "v_mfma_f32_16x16x32_bf16 v[16:19], a[104:107], a[60:63], v[16:19]\n"
      "ds_read_b64_tr_b16 a[42:43], v239 offset:33288\n"
      "v_mfma_f32_16x16x32_bf16 v[48:51], a[104:107], a[64:67], v[48:51]\n"
      "v_xor_b32 v234, 128, v234\n"
      "s_add_u32 m0, s70, 16384\n"
      "v_mfma_f32_16x16x32_bf16 v[80:83], a[104:107], a[68:71], v[80:83]\n"
      "ds_read_b64_tr_b16 a[44:45], v238 offset:33792\n"
      "s_sub_u32 s82, s82, s90\n"
      "s_sub_u32 s94, s94, s92\n"
      "v_mfma_f32_16x16x32_bf16 v[112:115], a[104:107], a[72:75], v[112:115]\n"
      "buffer_load_dwordx4 v235, s[48:51], s71 offen offset:0 lds\n"
      "v_mfma_f32_16x16x32_bf16 v[144:147], a[104:107], a[76:79], v[144:147]\n"
      "ds_read_b64_tr_b16 a[46:47], v238 offset:34304\n"
      "s_mul_i32 s83, s82, 100\n"
      "s_mul_i32 s95, s94, 2180\n"
      "v_mfma_f32_16x16x32_bf16 v[176:179], a[104:107], a[80:83], v[176:179]\n"
      "s_add_u32 m0, s70, 17280\n"
      "v_mfma_f32_16x16x32_bf16 v[208:211], a[104:107], a[84:87], v[208:211]\n"
      "ds_read_b64_tr_b16 a[48:49], v238 offset:33800\n"
      "s_cmp_lt_u32 s83, s95\n"
      "s_cselect_b32 s82, 0, s82\n"
      "s_cselect_b32 s94, 0, s94\n"
      "v_mfma_f32_16x16x32_bf16 v[20:23], a[108:111], a[60:63], v[20:23]\n"
      "buffer_load_dwordx4 v235, s[48:51], s71 offen offset:128 lds\n"
      "v_mfma_f32_16x16x32_bf16 v[52:55], a[108:111], a[64:67], v[52:55]\n"
      "ds_read_b64_tr_b16 a[50:51], v238 offset:34312\n"
      "s_mul_i32 s83, s94, 1000\n"
      "v_mfma_f32_16x16x32_bf16 v[84:87], a[108:111], a[68:71], v[84:87]\n"
      "s_add_u32 m0, s70, 18176\n"
      "v_mfma_f32_16x16x32_bf16 v[116:119], a[108:111], a[72:75], v[116:119]\n"
      "ds_read_b64_tr_b16 a[52:53], v239 offset:33792\n"
      "s_mul_i32 s95, s82, 673\n"
      "v_mfma_f32_16x16x32_bf16 v[148:151], a[108:111], a[76:79], v[148:151]\n"
      "buffer_load_dwordx4 v235, s[48:51], s71 offen offset:256 lds\n"
      "v_mfma_f32_16x16x32_bf16 v[180:183], a[108:111], a[80:83], v[180:183]\n"
      "ds_read_b64_tr_b16 a[54:55], v239 offset:34304\n"
      "v_mfma_f32_16x16x32_bf16 v[212:215], a[108:111], a[84:87], v[212:215]\n"
      "s_add_u32 m0, s70, 19072\n"
      "v_mfma_f32_16x16x32_bf16 v[24:27], a[112:115], a[60:63], v[24:27]\n"
      "ds_read_b64_tr_b16 a[56:57], v239 offset:33800\n"
      "s_cmp_eq_u32 s83, 0\n"
      "s_cbranch_scc1 QS142_%=\n"
      "s_sleep 11\n"
      "QS142_%=:\n"
      "v_mfma_f32_16x16x32_bf16 v[56:59], a[112:115], a[64:67], v[56:59]\n"
      "buffer_load_dwordx4 v235, s[48:51], s71 offen offset:384 lds\n"
      "v_mfma_f32_16x16x32_bf16 v[88:91], a[112:115], a[68:71], v[88:91]\n"
      "ds_read_b64_tr_b16 a[58:59], v239 offset:34312\n"
      "v_mfma_f32_16x16x32_bf16 v[120:123], a[112:115], a[72:75], v[120:123]\n"
      "s_xor_b32 s71, s71, 786432\n"
      "v_mfma_f32_16x16x32_bf16 v[152:155], a[112:115], a[76:79], v[152:155]\n"
      "v_mfma_f32_16x16x32_bf16 v[184:187], a[112:115], a[80:83], v[184:187]\n"
      "buffer_load_dword v233, v247, s[44:47], 0 offen\n"
      "v_mfma_f32_16x16x32_bf16 v[216:219], a[112:115], a[84:87], v[216:219]\n"
      "v_mfma_f32_16x16x32_bf16 v[28:31], a[116:119], a[60:63], v[28:31]\n"
      "v_mfma_f32_16x16x32_bf16 v[60:63], a[116:119], a[64:67], v[60:63]\n"
      "v_mfma_f32_16x16x32_bf16 v[92:95], a[116:119], a[68:71], v[92:95]\n"
      "v_mfma_f32_16x16x32_bf16 v[124:127], a[116:119], a[72:75], v[124:127]\n"
      "v_mfma_f32_16x16x32_bf16 v[156:159], a[116:119], a[76:79], v[156:159]\n"
      "v_mfma_f32_16x16x32_bf16 v[188:191], a[116:119], a[80:83], v[188:191]\n"
      "v_mfma_f32_16x16x32_bf16 v[220:223], a[116:119], a[84:87], v[220:223]\n"
      "s_waitcnt vmcnt(16) lgkmcnt(0)\n"
      "s_barrier\n"
      "v_mfma_f32_16x16x32_bf16 v[0:3], a[28:31], a[0:3], v[0:3]\n"
      "ds_read_b64_tr_b16 a[88:89], v238 offset:49152\n"
      "v_mfma_f32_16x16x32_bf16 v[32:35], a[28:31], a[4:7], v[32:35]\n"
      "s_add_u32 m0, s70, 32768\n"
      "s_mov_b32 s85, 0x3e0293ee\n"
      "v_mfma_f32_16x16x32_bf16 v[64:67], a[28:31], a[8:11], v[64:67]\n"
      "ds_read_b64_tr_b16 a[90:91], v238 offset:49664\n"
      "s_lshl_b32 s86, %[wid], 15\n"
      "v_mfma_f32_16x16x32_bf16 v[96:99], a[28:31], a[12:15], v[96:99]\n"
      "buffer_load_dwordx4 v235, s[48:51], s71 offen offset:0 lds\n"
      "s_lshl_b32 s66, %[j], 18\n"
      "v_mfma_f32_16x16x32_bf16 v[128:131], a[28:31], a[16:19], v[128:131]\n"
      "ds_read_b128 a[60:63], v237 offset:32768\n"
      "v_mfma_f32_16x16x32_bf16 v[160:163], a[28:31], a[20:23], v[160:163]\n"
      "s_add_u32 s86, s86, s66\n"
      "s_nop 0\n"
      "v_mfma_f32_16x16x32_bf16 v[192:195], a[28:31], a[24:27], v[192:195]\n"
      "ds_read_b128 a[64:67], v237 offset:34816\n"
      "s_lshl_b32 s88, %[wid], 11\n"
      "v_mfma_f32_16x16x32_bf16 v[4:7], a[32:35], a[0:3], v[4:7]\n"
      "s_add_u32 m0, s70, 33664\n"
      "s_lshr_b32 s64, %[wid], 1\n"
      "v_mfma_f32_16x16x32_bf16 v[36:39], a[32:35], a[4:7], v[36:39]\n"
      "ds_read_b128 a[68:71], v237 offset:36864\n"
      "s_and_b32 s65, %[wid], 1\n"
      "v_mfma_f32_16x16x32_bf16 v[68:71], a[32:35], a[8:11], v[68:71]\n"
      "buffer_load_dwordx4 v235, s[48:51], s71 offen offset:128 lds\n"
      "s_xor_b32 s65, s65, %[flip]\n"
      "v_mfma_f32_16x16x32_bf16 v[100:103], a[32:35], a[12:15], v[100:103]\n"
      "ds_read_b128 a[72:75], v237 offset:38912\n"
      "v_mfma_f32_16x16x32_bf16 v[132:135], a[32:35], a[16:19], v[132:135]\n"
      "s_lshl_b32 s87, s65, 15\n"
      "v_mfma_f32_16x16x32_bf16 v[164:167], a[32:35], a[20:23], v[164:167]\n"
      "ds_read_b128 a[76:79], v237 offset:40960\n"
      "s_lshl_b32 s66, %[j], 13\n"
      "v_mfma_f32_16x16x32_bf16 v[196:199], a[32:35], a[24:27], v[196:199]\n"
      "s_add_u32 m0, s70, 34560\n"
      "s_add_u32 s87, s87, s66\n"
      "v_mfma_f32_16x16x32_bf16 v[8:11], a[36:39], a[0:3], v[8:11]\n"
      "ds_read_b128 a[80:83], v237 offset:43008\n"
      "s_lshl_b32 s66, s64, 10\n"
      "v_mfma_f32_16x16x32_bf16 v[40:43], a[36:39], a[4:7], v[40:43]\n"
      "buffer_load_dwordx4 v235, s[48:51], s71 offen offset:256 lds\n"
      "s_add_u32 s87, s87, s66\n"
      "v_mfma_f32_16x16x32_bf16 v[72:75], a[36:39], a[8:11], v[72:75]\n"
      "ds_read_b128 a[84:87], v237 offset:45056\n"
      "v_mfma_f32_16x16x32_bf16 v[104:107], a[36:39], a[12:15], v[104:107]\n"
      "s_lshl_b32 s89, %[wid], 10\n"
      "v_mfma_f32_16x16x32_bf16 v[136:139], a[36:39], a[16:19], v[136:139]\n"
      "ds_read_b64_tr_b16 a[92:93], v238 offset:49160\n"
      "s_lshl_b32 s66, %[g], 12\n"
      "v_mfma_f32_16x16x32_bf16 v[168:171], a[36:39], a[20:23], v[168:171]\n"
      "s_add_u32 m0, s70, 35456\n"
      "s_add_u32 s89, s89, s66\n"
      "v_mfma_f32_16x16x32_bf16 v[200:203], a[36:39], a[24:27], v[200:203]\n"
      "ds_read_b64_tr_b16 a[94:95], v238 offset:49672\n"
      "s_lshl_b32 s90, %[j], 8\n"
      "v_mfma_f32_16x16x32_bf16 v[12:15], a[40:43], a[0:3], v[12:15]\n"
      "buffer_load_dwordx4 v235, s[48:51], s71 offen offset:384 lds\n"
      "s_lshl_b32 s66, %[wid], 6\n"
      "v_mfma_f32_16x16x32_bf16 v[44:47], a[40:43], a[4:7], v[44:47]\n"
      "ds_read_b64_tr_b16 a[96:97], v239 offset:49152\n"
      "s_add_u32 s90, s90, s66\n"
      "v_mfma_f32_16x16x32_bf16 v[76:79], a[40:43], a[8:11], v[76:79]\n"
      "s_xor_b32 s71, s71, 262144\n"
      "v_mfma_f32_16x16x32_bf16 v[108:111], a[40:43], a[12:15], v[108:111]\n"
      "ds_read_b64_tr_b16 a[98:99], v239 offset:49664\n"
      "s_lshl_b32 s66, %[b], 2\n"
      "v_mfma_f32_16x16x32_bf16 v[140:143], a[40:43], a[16:19], v[140:143]\n"
      "s_add_u32 s92, s66, %[g]\n"
      "v_mfma_f32_16x16x32_bf16 v[172:175], a[40:43], a[20:23], v[172:175]\n"
      "ds_read_b64_tr_b16 a[100:101], v239 offset:49160\n"
      "s_lshl_b32 s92, s92, 7\n"
      "v_mfma_f32_16x16x32_bf16 v[204:207], a[40:43], a[24:27], v[204:207]\n"
      "s_lshl_b32 s67, %[j], 2\n"
      "v_mfma_f32_16x16x32_bf16 v[16:19], a[44:47], a[0:3], v[16:19]\n"
      "ds_read_b64_tr_b16 a[102:103], v239 offset:49672\n"
      "s_add_u32 s67, s67, %[wid]\n"
      "v_mfma_f32_16x16x32_bf16 v[48:51], a[44:47], a[4:7], v[48:51]\n"
      "v_mfma_f32_16x16x32_bf16 v[80:83], a[44:47], a[8:11], v[80:83]\n"
      "ds_read_b64_tr_b16 a[104:105], v238 offset:50176\n"
      "s_lshl_b32 s67, s67, 2\n"
      "v_mfma_f32_16x16x32_bf16 v[112:115], a[44:47], a[12:15], v[112:115]\n"
      "s_add_u32 s91, s92, s67\n"
      "v_mfma_f32_16x16x32_bf16 v[144:147], a[44:47], a[16:19], v[144:147]\n"
      "ds_read_b64_tr_b16 a[106:107], v238 offset:50688\n"
      "s_add_u32 s94, s66, %[j]\n"
      "v_mfma_f32_16x16x32_bf16 v[176:179], a[44:47], a[20:23], v[176:179]\n"
      "s_lshl_b32 s94, s94, 7\n"
      "v_mfma_f32_16x16x32_bf16 v[208:211], a[44:47], a[24:27], v[208:211]\n"
      "ds_read_b64_tr_b16 a[108:109], v238 offset:50184\n"
      "s_add_u32 s94, s94, 8192\n"
      "v_mfma_f32_16x16x32_bf16 v[20:23], a[48:51], a[0:3], v[20:23]\n"
      "v_mfma_f32_16x16x32_bf16 v[52:55], a[48:51], a[4:7], v[52:55]\n"
      "ds_read_b64_tr_b16 a[110:111], v238 offset:50696\n"
      "s_lshl_b32 s67, %[wid], 4\n"
      "v_mfma_f32_16x16x32_bf16 v[84:87], a[48:51], a[8:11], v[84:87]\n"
      "s_add_u32 s94, s94, s67\n"
      "v_mfma_f32_16x16x32_bf16 v[116:119], a[48:51], a[12:15], v[116:119]\n"
      "ds_read_b64_tr_b16 a[112:113], v239 offset:50176\n"
      "s_lshl_b32 s67, %[g], 2\n"
      "v_mfma_f32_16x16x32_bf16 v[148:151], a[48:51], a[16:19], v[148:151]\n"
      "s_add_u32 s93, s94, s67\n"
      "v_mfma_f32_16x16x32_bf16 v[180:183], a[48:51], a[20:23], v[180:183]\n"
      "ds_read_b64_tr_b16 a[114:115], v239 offset:50688\n"
      "s_add_u32 s64, s66, %[g]\n"
      "v_mfma_f32_16x16x32_bf16 v[212:215], a[48:51], a[24:27], v[212:215]\n"
      "s_mul_i32 s64, s64, 458752\n"
      "v_mfma_f32_16x16x32_bf16 v[24:27], a[52:55], a[0:3], v[24:27]\n"
      "ds_read_b64_tr_b16 a[116:117], v239 offset:50184\n"
      "v_mfma_f32_16x16x32_bf16 v[56:59], a[52:55], a[4:7], v[56:59]\n"
      "s_add_u32 s64, s64, 1048576\n"
      "v_mfma_f32_16x16x32_bf16 v[88:91], a[52:55], a[8:11], v[88:91]\n"
      "ds_read_b64_tr_b16 a[118:119], v239 offset:50696\n"
      "s_add_u32 s87, s87, s64\n"
      "v_mfma_f32_16x16x32_bf16 v[120:123], a[52:55], a[12:15], v[120:123]\n"
      "s_add_u32 s88, s88, s64\n"
      "v_mfma_f32_16x16x32_bf16 v[152:155], a[52:55], a[16:19], v[152:155]\n"
      "s_add_u32 s64, s66, %[j]\n"
      "v_mfma_f32_16x16x32_bf16 v[184:187], a[52:55], a[20:23], v[184:187]\n"
      "s_mul_i32 s64, s64, 229376\n"
      "v_mfma_f32_16x16x32_bf16 v[216:219], a[52:55], a[24:27], v[216:219]\n"
      "v_mfma_f32_16x16x32_bf16 v[28:31], a[56:59], a[0:3], v[28:31]\n"
      "s_add_u32 s64, s64, 30408704\n"
      "v_mfma_f32_16x16x32_bf16 v[60:63], a[56:59], a[4:7], v[60:63]\n"
      "s_add_u32 s89, s89, s64\n"
      "s_memrealtime s[96:97]\n"
      "v_mfma_f32_16x16x32_bf16 v[92:95], a[56:59], a[8:11], v[92:95]\n"
      "s_lshl_b32 s64, %[b], 7\n"
      "v_mfma_f32_16x16x32_bf16 v[124:127], a[56:59], a[12:15], v[124:127]\n"
      "s_add_u32 s64, s64, 16384\n"
      "s_memtime s[98:99]\n"
      "v_mfma_f32_16x16x32_bf16 v[156:159], a[56:59], a[16:19], v[156:159]\n"
      "buffer_load_dword v254, off, s[60:63], s64 sc1\n"
      "v_mfma_f32_16x16x32_bf16 v[188:191], a[56:59], a[20:23], v[188:191]\n"
      "v_mfma_f32_16x16x32_bf16 v[220:223], a[56:59], a[24:27], v[220:223]\n"
      "s_waitcnt vmcnt(10) lgkmcnt(0)\n"
      "s_barrier\n"
      "v_mfma_f32_16x16x32_bf16 v[0:3], a[88:91], a[60:63], v[0:3]\n"
      "ds_read_b64_tr_b16 a[28:29], v238 offset:0\n"
      "v_mfma_f32_16x16x32_bf16 v[32:35], a[88:91], a[64:67], v[32:35]\n"
      "s_add_u32 m0, s72, 32768\n"
      "v_mfma_f32_16x16x32_bf16 v[64:67], a[88:91], a[68:71], v[64:67]\n"
      "ds_read_b64_tr_b16 a[30:31], v238 offset:512\n"
      "v_mfma_f32_16x16x32_bf16 v[96:99], a[88:91], a[72:75], v[96:99]\n"
      "buffer_load_dwordx4 v234, s[44:47], s73 offen lds\n"
      "v_mfma_f32_16x16x32_bf16 v[128:131], a[88:91], a[76:79], v[128:131]\n"
      "ds_read_b128 a[0:3], v236 offset:16384\n"
      "v_mfma_f32_16x16x32_bf16 v[160:163], a[88:91], a[80:83], v[160:163]\n"
      "s_add_u32 m0, s72, 33792\n"
      "v_mfma_f32_16x16x32_bf16 v[192:195], a[88:91], a[84:87], v[192:195]\n"
      "ds_read_b128 a[4:7], v236 offset:18432\n"
      "v_mfma_f32_16x16x32_bf16 v[4:7], a[92:95], a[60:63], v[4:7]\n"
      "buffer_load_dwordx4 v234, s[44:47], s74 offen lds\n"
      "v_mfma_f32_16x16x32_bf16 v[36:39], a[92:95], a[64:67], v[36:39]\n"
      "ds_read_b128 a[8:11], v236 offset:20480\n"
      "v_mfma_f32_16x16x32_bf16 v[68:71], a[92:95], a[68:71], v[68:71]\n"
      "s_add_u32 m0, s72, 34816\n"
      "v_mfma_f32_16x16x32_bf16 v[100:103], a[92:95], a[72:75], v[100:103]\n"
      "ds_read_b128 a[12:15], v236 offset:22528\n"
      "v_mfma_f32_16x16x32_bf16 v[132:135], a[92:95], a[76:79], v[132:135]\n"
      "buffer_load_dwordx4 v234, s[44:47], s75 offen lds\n"
      "v_mfma_f32_16x16x32_bf16 v[164:167], a[92:95], a[80:83], v[164:167]\n"
      "ds_read_b128 a[16:19], v236 offset:24576\n"
      "v_mfma_f32_16x16x32_bf16 v[196:199], a[92:95], a[84:87], v[196:199]\n"
      "s_add_u32 m0, s72, 35840\n"
      "v_mfma_f32_16x16x32_bf16 v[8:11], a[96:99], a[60:63], v[8:11]\n"
      "ds_read_b128 a[20:23], v236 offset:26624\n"
      "v_mfma_f32_16x16x32_bf16 v[40:43], a[96:99], a[64:67], v[40:43]\n"
      "buffer_load_dwordx4 v234, s[44:47], s76 offen lds\n"
      "v_mfma_f32_16x16x32_bf16 v[72:75], a[96:99], a[68:71], v[72:75]\n"
      "ds_read_b128 a[24:27], v236 offset:28672\n"
      "v_mfma_f32_16x16x32_bf16 v[104:107], a[96:99], a[72:75], v[104:107]\n"
      "s_add_u32 m0, s72, 36864\n"
      "v_mfma_f32_16x16x32_bf16 v[136:139], a[96:99], a[76:79], v[136:139]\n"
      "ds_read_b64_tr_b16 a[32:33], v238 offset:8\n"
      "v_mfma_f32_16x16x32_bf16 v[168:171], a[96:99], a[80:83], v[168:171]\n"
      "buffer_load_dwordx4 v234, s[44:47], s77 offen lds\n"
      "v_mfma_f32_16x16x32_bf16 v[200:203], a[96:99], a[84:87], v[200:203]\n"
      "ds_read_b64_tr_b16 a[34:35], v238 offset:520\n"
      "v_mfma_f32_16x16x32_bf16 v[12:15], a[100:103], a[60:63], v[12:15]\n"
      "s_add_u32 m0, s72, 37888\n"
      "v_mfma_f32_16x16x32_bf16 v[44:47], a[100:103], a[64:67], v[44:47]\n"
      "ds_read_b64_tr_b16 a[36:37], v239 offset:0\n"
      "v_mfma_f32_16x16x32_bf16 v[76:79], a[100:103], a[68:71], v[76:79]\n"
      "buffer_load_dwordx4 v234, s[44:47], s78 offen lds\n"
      "v_mfma_f32_16x16x32_bf16 v[108:111], a[100:103], a[72:75], v[108:111]\n"
      "ds_read_b64_tr_b16 a[38:39], v239 offset:512\n"
      "v_mfma_f32_16x16x32_bf16 v[140:143], a[100:103], a[76:79], v[140:143]\n"
      "s_add_u32 m0, s72, 38912\n"
      "v_mfma_f32_16x16x32_bf16 v[172:175], a[100:103], a[80:83], v[172:175]\n"
      "ds_read_b64_tr_b16 a[40:41], v239 offset:8\n"
      "v_mfma_f32_16x16x32_bf16 v[204:207], a[100:103], a[84:87], v[204:207]\n"
      "buffer_load_dwordx4 v234, s[44:47], s79 offen lds\n"
      "v_mfma_f32_16x16x32_bf16 v[16:19], a[104:107], a[60:63], v[16:19]\n"
      "ds_read_b64_tr_b16 a[42:43], v239 offset:520\n"
      "v_mfma_f32_16x16x32_bf16 v[48:51], a[104:107], a[64:67], v[48:51]\n"
      "v_xor_b32 v234, 896, v234\n"
      "s_add_u32 m0, s70, 49152\n"
      "v_mfma_f32_16x16x32_bf16 v[80:83], a[104:107], a[68:71], v[80:83]\n"
      "ds_read_b64_tr_b16 a[44:45], v238 offset:1024\n"
      "v_mfma_f32_16x16x32_bf16 v[112:115], a[104:107], a[72:75], v[112:115]\n"
      "buffer_load_dwordx4 v235, s[48:51], s71 offen offset:0 lds\n"
      "v_mfma_f32_16x16x32_bf16 v[144:147], a[104:107], a[76:79], v[144:147]\n"
      "ds_read_b64_tr_b16 a[46:47], v238 offset:1536\n"
      "v_mfma_f32_16x16x32_bf16 v[176:179], a[104:107], a[80:83], v[176:179]\n"
      "s_add_u32 m0, s70, 50048\n"
      "v_mfma_f32_16x16x32_bf16 v[208:211], a[104:107], a[84:87], v[208:211]\n"
      "ds_read_b64_tr_b16 a[48:49], v238 offset:1032\n"
      "v_mfma_f32_16x16x32_bf16 v[20:23], a[108:111], a[60:63], v[20:23]\n"
      "buffer_load_dwordx4 v235, s[48:51], s71 offen offset:128 lds\n"
      "v_mfma_f32_16x16x32_bf16 v[52:55], a[108:111], a[64:67], v[52:55]\n"
      "ds_read_b64_tr_b16 a[50:51], v238 offset:1544\n"
      "v_mfma_f32_16x16x32_bf16 v[84:87], a[108:111], a[68:71], v[84:87]\n"
      "s_add_u32 m0, s70, 50944\n"
      "v_mfma_f32_16x16x32_bf16 v[116:119], a[108:111], a[72:75], v[116:119]\n"
      "ds_read_b64_tr_b16 a[52:53], v239 offset:1024\n"
      "v_mfma_f32_16x16x32_bf16 v[148:151], a[108:111], a[76:79], v[148:151]\n"
      "buffer_load_dwordx4 v235, s[48:51], s71 offen offset:256 lds\n"
      "v_mfma_f32_16x16x32_bf16 v[180:183], a[108:111], a[80:83], v[180:183]\n"
      "ds_read_b64_tr_b16 a[54:55], v239 offset:1536\n"
      "v_mfma_f32_16x16x32_bf16 v[212:215], a[108:111], a[84:87], v[212:215]\n"
      "s_add_u32 m0, s70, 51840\n"
      "v_mfma_f32_16x16x32_bf16 v[24:27], a[112:115], a[60:63], v[24:27]\n"
      "ds_read_b64_tr_b16 a[56:57], v239 offset:1032\n"
      "v_mfma_f32_16x16x32_bf16 v[56:59], a[112:115], a[64:67], v[56:59]\n"
      "buffer_load_dwordx4 v235, s[48:51], s71 offen offset:384 lds\n"
      "v_mfma_f32_16x16x32_bf16 v[88:91], a[112:115], a[68:71], v[88:91]\n"
      "ds_read_b64_tr_b16 a[58:59], v239 offset:1544\n"
      "v_mfma_f32_16x16x32_bf16 v[120:123], a[112:115], a[72:75], v[120:123]\n"
      "s_xor_b32 s71, s71, 3932160\n"
      "v_mfma_f32_16x16x32_bf16 v[152:155], a[112:115], a[76:79], v[152:155]\n"
      "s_mov_b32 s65, s96\n"
      "v_mfma_f32_16x16x32_bf16 v[184:187], a[112:115], a[80:83], v[184:187]\n"
      "v_mfma_f32_16x16x32_bf16 v[216:219], a[112:115], a[84:87], v[216:219]\n"
      "s_mov_b32 s67, s98\n"
      "v_mfma_f32_16x16x32_bf16 v[28:31], a[116:119], a[60:63], v[28:31]\n"
      "v_mfma_f32_16x16x32_bf16 v[60:63], a[116:119], a[64:67], v[60:63]\n"
      "s_lshr_b32 s98, s98, 4\n"
      "v_mfma_f32_16x16x32_bf16 v[92:95], a[116:119], a[68:71], v[92:95]\n"
      "v_mfma_f32_16x16x32_bf16 v[124:127], a[116:119], a[72:75], v[124:127]\n"
      "s_sub_u32 s96, s96, s98\n"
      "v_mfma_f32_16x16x32_bf16 v[156:159], a[116:119], a[76:79], v[156:159]\n"
      "v_mfma_f32_16x16x32_bf16 v[188:191], a[116:119], a[80:83], v[188:191]\n"
      "v_mfma_f32_16x16x32_bf16 v[220:223], a[116:119], a[84:87], v[220:223]\n"
      "s_waitcnt vmcnt(17) lgkmcnt(0)\n"
      "s_barrier\n"
      "v_mfma_f32_16x16x32_bf16 v[0:3], a[28:31], a[0:3], v[0:3]\n"
      "ds_read_b64_tr_b16 a[88:89], v238 offset:16384\n"
      "v_mfma_f32_16x16x32_bf16 v[32:35], a[28:31], a[4:7], v[32:35]\n"
      "s_add_u32 m0, s70, 0\n"
      "v_mfma_f32_16x16x32_bf16 v[64:67], a[28:31], a[8:11], v[64:67]\n"
      "ds_read_b64_tr_b16 a[90:91], v238 offset:16896\n"
      "v_mfma_f32_16x16x32_bf16 v[96:99], a[28:31], a[12:15], v[96:99]\n"
      "buffer_load_dwordx4 v235, s[48:51], s71 offen offset:0 lds\n"
      "v_mfma_f32_16x16x32_bf16 v[128:131], a[28:31], a[16:19], v[128:131]\n"
      "ds_read_b128 a[60:63], v237 offset:16384\n"
      "v_mfma_f32_16x16x32_bf16 v[160:163], a[28:31], a[20:23], v[160:163]\n"
      "v_mfma_f32_16x16x32_bf16 v[192:195], a[28:31], a[24:27], v[192:195]\n"
      "ds_read_b128 a[64:67], v237 offset:18432\n"
      "v_mfma_f32_16x16x32_bf16 v[4:7], a[32:35], a[0:3], v[4:7]\n"
      "s_add_u32 m0, s70, 896\n"
      "v_mfma_f32_16x16x32_bf16 v[36:39], a[32:35], a[4:7], v[36:39]\n"
      "ds_read_b128 a[68:71], v237 offset:20480\n"
      "v_mfma_f32_16x16x32_bf16 v[68:71], a[32:35], a[8:11], v[68:71]\n"
      "buffer_load_dwordx4 v235, s[48:51], s71 offen offset:128 lds\n"
      "v_mfma_f32_16x16x32_bf16 v[100:103], a[32:35], a[12:15], v[100:103]\n"
      "ds_read_b128 a[72:75], v237 offset:22528\n"
      "v_mfma_f32_16x16x32_bf16 v[132:135], a[32:35], a[16:19], v[132:135]\n"
      "v_mfma_f32_16x16x32_bf16 v[164:167], a[32:35], a[20:23], v[164:167]\n"
      "ds_read_b128 a[76:79], v237 offset:24576\n"
      "v_mfma_f32_16x16x32_bf16 v[196:199], a[32:35], a[24:27], v[196:199]\n"
      "s_add_u32 m0, s70, 1792\n"
      "v_mfma_f32_16x16x32_bf16 v[8:11], a[36:39], a[0:3], v[8:11]\n"
      "ds_read_b128 a[80:83], v237 offset:26624\n"
      "v_mfma_f32_16x16x32_bf16 v[40:43], a[36:39], a[4:7], v[40:43]\n"
      "buffer_load_dwordx4 v235, s[48:51], s71 offen offset:256 lds\n"
      "v_mfma_f32_16x16x32_bf16 v[72:75], a[36:39], a[8:11], v[72:75]\n"
      "ds_read_b128 a[84:87], v237 offset:28672\n"
      "v_mfma_f32_16x16x32_bf16 v[104:107], a[36:39], a[12:15], v[104:107]\n"
      "v_mfma_f32_16x16x32_bf16 v[136:139], a[36:39], a[16:19], v[136:139]\n"
      "ds_read_b64_tr_b16 a[92:93], v238 offset:16392\n"
      "v_mfma_f32_16x16x32_bf16 v[168:171], a[36:39], a[20:23], v[168:171]\n"
      "s_add_u32 m0, s70, 2688\n"
      "v_mfma_f32_16x16x32_bf16 v[200:203], a[36:39], a[24:27], v[200:203]\n"
      "ds_read_b64_tr_b16 a[94:95], v238 offset:16904\n"
      "v_mfma_f32_16x16x32_bf16 v[12:15], a[40:43], a[0:3], v[12:15]\n"
      "buffer_load_dwordx4 v235, s[48:51], s71 offen offset:384 lds\n"
      "v_mfma_f32_16x16x32_bf16 v[44:47], a[40:43], a[4:7], v[44:47]\n"
      "ds_read_b64_tr_b16 a[96:97], v239 offset:16384\n"
      "v_mfma_f32_16x16x32_bf16 v[76:79], a[40:43], a[8:11], v[76:79]\n"
      "s_xor_b32 s71, s71, 262144\n"
      "v_mfma_f32_16x16x32_bf16 v[108:111], a[40:43], a[12:15], v[108:111]\n"
      "ds_read_b64_tr_b16 a[98:99], v239 offset:16896\n"
      "v_mfma_f32_16x16x32_bf16 v[140:143], a[40:43], a[16:19], v[140:143]\n"
      "s_add_u32 m0, s68, 0\n"
      "v_mfma_f32_16x16x32_bf16 v[172:175], a[40:43], a[20:23], v[172:175]\n"
      "ds_read_b64_tr_b16 a[100:101], v239 offset:16392\n"
      "v_mfma_f32_16x16x32_bf16 v[204:207], a[40:43], a[24:27], v[204:207]\n"
      "buffer_load_dwordx4 v230, s[52:55], s80 offen offset:0 nt lds\n"
      "v_mfma_f32_16x16x32_bf16 v[16:19], a[44:47], a[0:3], v[16:19]\n"
      "ds_read_b64_tr_b16 a[102:103], v239 offset:16904\n"
      "v_mfma_f32_16x16x32_bf16 v[48:51], a[44:47], a[4:7], v[48:51]\n"
      "v_mfma_f32_16x16x32_bf16 v[80:83], a[44:47], a[8:11], v[80:83]\n"
      "ds_read_b64_tr_b16 a[104:105], v238 offset:17408\n"
      "v_mfma_f32_16x16x32_bf16 v[112:115], a[44:47], a[12:15], v[112:115]\n"
      "v_mfma_f32_16x16x32_bf16 v[144:147], a[44:47], a[16:19], v[144:147]\n"
      "ds_read_b64_tr_b16 a[106:107], v238 offset:17920\n"
      "v_mfma_f32_16x16x32_bf16 v[176:179], a[44:47], a[20:23], v[176:179]\n"
      "v_mfma_f32_16x16x32_bf16 v[208:211], a[44:47], a[24:27], v[208:211]\n"
      "ds_read_b64_tr_b16 a[108:109], v238 offset:17416\n"
      "v_mfma_f32_16x16x32_bf16 v[20:23], a[48:51], a[0:3], v[20:23]\n"
      "v_mfma_f32_16x16x32_bf16 v[52:55], a[48:51], a[4:7], v[52:55]\n"
      "ds_read_b64_tr_b16 a[110:111], v238 offset:17928\n"
      "v_mfma_f32_16x16x32_bf16 v[84:87], a[48:51], a[8:11], v[84:87]\n"
      "v_mfma_f32_16x16x32_bf16 v[116:119], a[48:51], a[12:15], v[116:119]\n"
      "ds_read_b64_tr_b16 a[112:113], v239 offset:17408\n"
      "v_mfma_f32_16x16x32_bf16 v[148:151], a[48:51], a[16:19], v[148:151]\n"
      "v_mfma_f32_16x16x32_bf16 v[180:183], a[48:51], a[20:23], v[180:183]\n"
      "ds_read_b64_tr_b16 a[114:115], v239 offset:17920\n"
      "v_mfma_f32_16x16x32_bf16 v[212:215], a[48:51], a[24:27], v[212:215]\n"
      "v_mfma_f32_16x16x32_bf16 v[24:27], a[52:55], a[0:3], v[24:27]\n"
      "ds_read_b64_tr_b16 a[116:117], v239 offset:17416\n"
      "v_mfma_f32_16x16x32_bf16 v[56:59], a[52:55], a[4:7], v[56:59]\n"
      "v_mfma_f32_16x16x32_bf16 v[88:91], a[52:55], a[8:11], v[88:91]\n"
      "ds_read_b64_tr_b16 a[118:119], v239 offset:17928\n"
      "v_mfma_f32_16x16x32_bf16 v[120:123], a[52:55], a[12:15], v[120:123]\n"
      "v_mfma_f32_16x16x32_bf16 v[152:155], a[52:55], a[16:19], v[152:155]\n"
      "v_mfma_f32_16x16x32_bf16 v[184:187], a[52:55], a[20:23], v[184:187]\n"
      "v_mfma_f32_16x16x32_bf16 v[216:219], a[52:55], a[24:27], v[216:219]\n"
      "v_mfma_f32_16x16x32_bf16 v[28:31], a[56:59], a[0:3], v[28:31]\n"
      "v_mfma_f32_16x16x32_bf16 v[60:63], a[56:59], a[4:7], v[60:63]\n"
      "v_mfma_f32_16x16x32_bf16 v[92:95], a[56:59], a[8:11], v[92:95]\n"
      "v_mfma_f32_16x16x32_bf16 v[124:127], a[56:59], a[12:15], v[124:127]\n"
      "v_mfma_f32_16x16x32_bf16 v[156:159], a[56:59], a[16:19], v[156:159]\n"
      "v_mfma_f32_16x16x32_bf16 v[188:191], a[56:59], a[20:23], v[188:191]\n"
      "v_mfma_f32_16x16x32_bf16 v[220:223], a[56:59], a[24:27], v[220:223]\n"
      "s_waitcnt vmcnt(9) lgkmcnt(0)\n"
      "s_barrier\n"
      "v_mfma_f32_16x16x32_bf16 v[0:3], a[88:91], a[60:63], v[0:3]\n"
      "ds_read_b64_tr_b16 a[28:29], v238 offset:32768\n"
      "v_mfma_f32_16x16x32_bf16 v[32:35], a[88:91], a[64:67], v[32:35]\n"
      "s_add_u32 m0, s72, 16384\n"
      "v_mfma_f32_16x16x32_bf16 v[64:67], a[88:91], a[68:71], v[64:67]\n"
      "ds_read_b64_tr_b16 a[30:31], v238 offset:33280\n"
      "v_mfma_f32_16x16x32_bf16 v[96:99], a[88:91], a[72:75], v[96:99]\n"
      "buffer_load_dwordx4 v234, s[44:47], s73 offen lds\n"
      "v_mfma_f32_16x16x32_bf16 v[128:131], a[88:91], a[76:79], v[128:131]\n"
      "ds_read_b128 a[0:3], v236 offset:32768\n"
      "v_mfma_f32_16x16x32_bf16 v[160:163], a[88:91], a[80:83], v[160:163]\n"
      "s_add_u32 m0, s72, 17408\n"
      "v_mfma_f32_16x16x32_bf16 v[192:195], a[88:91], a[84:87], v[192:195]\n"
      "ds_read_b128 a[4:7], v236 offset:34816\n"
      "v_mfma_f32_16x16x32_bf16 v[4:7], a[92:95], a[60:63], v[4:7]\n"
      "buffer_load_dwordx4 v234, s[44:47], s74 offen lds\n"
      "v_mfma_f32_16x16x32_bf16 v[36:39], a[92:95], a[64:67], v[36:39]\n"
      "ds_read_b128 a[8:11], v236 offset:36864\n"
      "v_mfma_f32_16x16x32_bf16 v[68:71], a[92:95], a[68:71], v[68:71]\n"
      "s_add_u32 m0, s72, 18432\n"
      "v_mfma_f32_16x16x32_bf16 v[100:103], a[92:95], a[72:75], v[100:103]\n"
      "ds_read_b128 a[12:15], v236 offset:38912\n"
      "v_mfma_f32_16x16x32_bf16 v[132:135], a[92:95], a[76:79], v[132:135]\n"
      "buffer_load_dwordx4 v234, s[44:47], s75 offen lds\n"
      "v_mfma_f32_16x16x32_bf16 v[164:167], a[92:95], a[80:83], v[164:167]\n"
      "ds_read_b128 a[16:19], v236 offset:40960\n"
      "v_mfma_f32_16x16x32_bf16 v[196:199], a[92:95], a[84:87], v[196:199]\n"
      "s_add_u32 m0, s72, 19456\n"
      "v_mfma_f32_16x16x32_bf16 v[8:11], a[96:99], a[60:63], v[8:11]\n"
      "ds_read_b128 a[20:23], v236 offset:43008\n"
      "v_mfma_f32_16x16x32_bf16 v[40:43], a[96:99], a[64:67], v[40:43]\n"
      "buffer_load_dwordx4 v234, s[44:47], s76 offen lds\n"
      "v_mfma_f32_16x16x32_bf16 v[72:75], a[96:99], a[68:71], v[72:75]\n"
      "ds_read_b128 a[24:27], v236 offset:45056\n"
      "v_mfma_f32_16x16x32_bf16 v[104:107], a[96:99], a[72:75], v[104:107]\n"
      "s_add_u32 m0, s72, 20480\n"
      "v_mfma_f32_16x16x32_bf16 v[136:139], a[96:99], a[76:79], v[136:139]\n"
      "ds_read_b64_tr_b16 a[32:33], v238 offset:32776\n"
      "v_mfma_f32_16x16x32_bf16 v[168:171], a[96:99], a[80:83], v[168:171]\n"
      "buffer_load_dwordx4 v234, s[44:47], s77 offen lds\n"
      "v_mfma_f32_16x16x32_bf16 v[200:203], a[96:99], a[84:87], v[200:203]\n"
      "ds_read_b64_tr_b16 a[34:35], v238 offset:33288\n"
      "v_mfma_f32_16x16x32_bf16 v[12:15], a[100:103], a[60:63], v[12:15]\n"
      "s_add_u32 m0, s72, 21504\n"
      "v_mfma_f32_16x16x32_bf16 v[44:47], a[100:103], a[64:67], v[44:47]\n"
      "ds_read_b64_tr_b16 a[36:37], v239 offset:32768\n"
      "v_mfma_f32_16x16x32_bf16 v[76:79], a[100:103], a[68:71], v[76:79]\n"
      "buffer_load_dwordx4 v234, s[44:47], s78 offen lds\n"
      "v_mfma_f32_16x16x32_bf16 v[108:111], a[100:103], a[72:75], v[108:111]\n"
      "ds_read_b64_tr_b16 a[38:39], v239 offset:33280\n"
      "v_mfma_f32_16x16x32_bf16 v[140:143], a[100:103], a[76:79], v[140:143]\n"
      "s_add_u32 m0, s72, 22528\n"
      "v_mfma_f32_16x16x32_bf16 v[172:175], a[100:103], a[80:83], v[172:175]\n"
      "ds_read_b64_tr_b16 a[40:41], v239 offset:32776\n"
      "v_mfma_f32_16x16x32_bf16 v[204:207], a[100:103], a[84:87], v[204:207]\n"
      "buffer_load_dwordx4 v234, s[44:47], s79 offen lds\n"
      "v_mfma_f32_16x16x32_bf16 v[16:19], a[104:107], a[60:63], v[16:19]\n"
      "ds_read_b64_tr_b16 a[42:43], v239 offset:33288\n"
      "v_mfma_f32_16x16x32_bf16 v[48:51], a[104:107], a[64:67], v[48:51]\n"
      "v_xor_b32 v234, 128, v234\n"
      "s_add_u32 m0, s70, 16384\n"
      "v_mfma_f32_16x16x32_bf16 v[80:83], a[104:107], a[68:71], v[80:83]\n"
      "ds_read_b64_tr_b16 a[44:45], v238 offset:33792\n"
      "v_mfma_f32_16x16x32_bf16 v[112:115], a[104:107], a[72:75], v[112:115]\n"
      "buffer_load_dwordx4 v235, s[48:51], s71 offen offset:0 lds\n"
      "v_mfma_f32_16x16x32_bf16 v[144:147], a[104:107], a[76:79], v[144:147]\n"
      "ds_read_b64_tr_b16 a[46:47], v238 offset:34304\n"
      "v_mfma_f32_16x16x32_bf16 v[176:179], a[104:107], a[80:83], v[176:179]\n"
      "s_add_u32 m0, s70, 17280\n"
      "v_mfma_f32_16x16x32_bf16 v[208:211], a[104:107], a[84:87], v[208:211]\n"
      "ds_read_b64_tr_b16 a[48:49], v238 offset:33800\n"
      "v_mfma_f32_16x16x32_bf16 v[20:23], a[108:111], a[60:63], v[20:23]\n"
      "buffer_load_dwordx4 v235, s[48:51], s71 offen offset:128 lds\n"
      "v_mfma_f32_16x16x32_bf16 v[52:55], a[108:111], a[64:67], v[52:55]\n"
      "ds_read_b64_tr_b16 a[50:51], v238 offset:34312\n"
      "v_mfma_f32_16x16x32_bf16 v[84:87], a[108:111], a[68:71], v[84:87]\n"
      "s_add_u32 m0, s70, 18176\n"
      "v_mfma_f32_16x16x32_bf16 v[116:119], a[108:111], a[72:75], v[116:119]\n"
      "ds_read_b64_tr_b16 a[52:53], v239 offset:33792\n"
      "v_mfma_f32_16x16x32_bf16 v[148:151], a[108:111], a[76:79], v[148:151]\n"
      "buffer_load_dwordx4 v235, s[48:51], s71 offen offset:256 lds\n"
      "v_mfma_f32_16x16x32_bf16 v[180:183], a[108:111], a[80:83], v[180:183]\n"
      "ds_read_b64_tr_b16 a[54:55], v239 offset:34304\n"
      "v_mfma_f32_16x16x32_bf16 v[212:215], a[108:111], a[84:87], v[212:215]\n"
      "s_add_u32 m0, s70, 19072\n"
      "v_mfma_f32_16x16x32_bf16 v[24:27], a[112:115], a[60:63], v[24:27]\n"
      "ds_read_b64_tr_b16 a[56:57], v239 offset:33800\n"
      "v_mfma_f32_16x16x32_bf16 v[56:59], a[112:115], a[64:67], v[56:59]\n"
      "buffer_load_dwordx4 v235, s[48:51], s71 offen offset:384 lds\n"
      "v_mfma_f32_16x16x32_bf16 v[88:91], a[112:115], a[68:71], v[88:91]\n"
      "ds_read_b64_tr_b16 a[58:59], v239 offset:34312\n"
      "v_mfma_f32_16x16x32_bf16 v[120:123], a[112:115], a[72:75], v[120:123]\n"
      "s_xor_b32 s71, s71, 786432\n"
      "v_mfma_f32_16x16x32_bf16 v[152:155], a[112:115], a[76:79], v[152:155]\n"
      "v_mfma_f32_16x16x32_bf16 v[184:187], a[112:115], a[80:83], v[184:187]\n"
      "v_mfma_f32_16x16x32_bf16 v[216:219], a[112:115], a[84:87], v[216:219]\n"
      "v_mfma_f32_16x16x32_bf16 v[28:31], a[116:119], a[60:63], v[28:31]\n"
      "v_mfma_f32_16x16x32_bf16 v[60:63], a[116:119], a[64:67], v[60:63]\n"
      "v_mfma_f32_16x16x32_bf16 v[92:95], a[116:119], a[68:71], v[92:95]\n"
      "v_mfma_f32_16x16x32_bf16 v[124:127], a[116:119], a[72:75], v[124:127]\n"
      "v_mfma_f32_16x16x32_bf16 v[156:159], a[116:119], a[76:79], v[156:159]\n"
      "v_mfma_f32_16x16x32_bf16 v[188:191], a[116:119], a[80:83], v[188:191]\n"
      "v_mfma_f32_16x16x32_bf16 v[220:223], a[116:119], a[84:87], v[220:223]\n"
      "s_waitcnt vmcnt(16) lgkmcnt(0)\n"
      "s_barrier\n"
      "v_mfma_f32_16x16x32_bf16 v[0:3], a[28:31], a[0:3], v[0:3]\n"
      "ds_read_b64_tr_b16 a[88:89], v238 offset:49152\n"
      "v_mfma_f32_16x16x32_bf16 v[32:35], a[28:31], a[4:7], v[32:35]\n"
      "s_add_u32 m0, s70, 32768\n"
      "v_mfma_f32_16x16x32_bf16 v[64:67], a[28:31], a[8:11], v[64:67]\n"
      "ds_read_b64_tr_b16 a[90:91], v238 offset:49664\n"
      "v_mfma_f32_16x16x32_bf16 v[96:99], a[28:31], a[12:15], v[96:99]\n"
      "buffer_load_dwordx4 v235, s[48:51], s71 offen offset:0 lds\n"
      "v_mfma_f32_16x16x32_bf16 v[128:131], a[28:31], a[16:19], v[128:131]\n"
      "ds_read_b128 a[60:63], v237 offset:32768\n"
      "v_mfma_f32_16x16x32_bf16 v[160:163], a[28:31], a[20:23], v[160:163]\n"
      "v_mfma_f32_16x16x32_bf16 v[192:195], a[28:31], a[24:27], v[192:195]\n"
      "ds_read_b128 a[64:67], v237 offset:34816\n"
      "v_mfma_f32_16x16x32_bf16 v[4:7], a[32:35], a[0:3], v[4:7]\n"
      "s_add_u32 m0, s70, 33664\n"
      "v_mfma_f32_16x16x32_bf16 v[36:39], a[32:35], a[4:7], v[36:39]\n"
      "ds_read_b128 a[68:71], v237 offset:36864\n"
      "v_mfma_f32_16x16x32_bf16 v[68:71], a[32:35], a[8:11], v[68:71]\n"
      "buffer_load_dwordx4 v235, s[48:51], s71 offen offset:128 lds\n"
      "v_mfma_f32_16x16x32_bf16 v[100:103], a[32:35], a[12:15], v[100:103]\n"
      "ds_read_b128 a[72:75], v237 offset:38912\n"
      "v_mfma_f32_16x16x32_bf16 v[132:135], a[32:35], a[16:19], v[132:135]\n"
      "v_mfma_f32_16x16x32_bf16 v[164:167], a[32:35], a[20:23], v[164:167]\n"
      "ds_read_b128 a[76:79], v237 offset:40960\n"
      "v_mfma_f32_16x16x32_bf16 v[196:199], a[32:35], a[24:27], v[196:199]\n"
      "s_add_u32 m0, s70, 34560\n"
      "v_mfma_f32_16x16x32_bf16 v[8:11], a[36:39], a[0:3], v[8:11]\n"
      "ds_read_b128 a[80:83], v237 offset:43008\n"
      "v_mfma_f32_16x16x32_bf16 v[40:43], a[36:39], a[4:7], v[40:43]\n"
      "buffer_load_dwordx4 v235, s[48:51], s71 offen offset:256 lds\n"
      "v_mfma_f32_16x16x32_bf16 v[72:75], a[36:39], a[8:11], v[72:75]\n"
      "ds_read_b128 a[84:87], v237 offset:45056\n"
      "v_mfma_f32_16x16x32_bf16 v[104:107], a[36:39], a[12:15], v[104:107]\n"
      "v_mfma_f32_16x16x32_bf16 v[136:139], a[36:39], a[16:19], v[136:139]\n"
      "ds_read_b64_tr_b16 a[92:93], v238 offset:49160\n"
      "v_mfma_f32_16x16x32_bf16 v[168:171], a[36:39], a[20:23], v[168:171]\n"
      "s_add_u32 m0, s70, 35456\n"
      "v_mfma_f32_16x16x32_bf16 v[200:203], a[36:39], a[24:27], v[200:203]\n"
      "ds_read_b64_tr_b16 a[94:95], v238 offset:49672\n"
      "v_mfma_f32_16x16x32_bf16 v[12:15], a[40:43], a[0:3], v[12:15]\n"
      "buffer_load_dwordx4 v235, s[48:51], s71 offen offset:384 lds\n"
      "v_mfma_f32_16x16x32_bf16 v[44:47], a[40:43], a[4:7], v[44:47]\n"
      "ds_read_b64_tr_b16 a[96:97], v239 offset:49152\n"
      "v_mfma_f32_16x16x32_bf16 v[76:79], a[40:43], a[8:11], v[76:79]\n"
      "s_xor_b32 s71, s71, 262144\n"
      "v_mfma_f32_16x16x32_bf16 v[108:111], a[40:43], a[12:15], v[108:111]\n"
      "ds_read_b64_tr_b16 a[98:99], v239 offset:49664\n"
      "v_mfma_f32_16x16x32_bf16 v[140:143], a[40:43], a[16:19], v[140:143]\n"
      "s_add_u32 m0, s68, 0\n"
      "s_nop 0\n"
      "v_mfma_f32_16x16x32_bf16 v[172:175], a[40:43], a[20:23], v[172:175]\n"
      "ds_read_b64_tr_b16 a[100:101], v239 offset:49160\n"
      "v_mfma_f32_16x16x32_bf16 v[204:207], a[40:43], a[24:27], v[204:207]\n"
      "buffer_load_dwordx4 v231, s[52:55], s80 offen offset:1024 nt lds\n"
      "v_mfma_f32_16x16x32_bf16 v[16:19], a[44:47], a[0:3], v[16:19]\n"
      "ds_read_b64_tr_b16 a[102:103], v239 offset:49672\n"
      "v_mfma_f32_16x16x32_bf16 v[48:51], a[44:47], a[4:7], v[48:51]\n"
      "v_mfma_f32_16x16x32_bf16 v[80:83], a[44:47], a[8:11], v[80:83]\n"
      "ds_read_b64_tr_b16 a[104:105], v238 offset:50176\n"
      "v_mfma_f32_16x16x32_bf16 v[112:115], a[44:47], a[12:15], v[112:115]\n"
      "v_mfma_f32_16x16x32_bf16 v[144:147], a[44:47], a[16:19], v[144:147]\n"
      "ds_read_b64_tr_b16 a[106:107], v238 offset:50688\n"
      "v_mfma_f32_16x16x32_bf16 v[176:179], a[44:47], a[20:23], v[176:179]\n"
      "v_mfma_f32_16x16x32_bf16 v[208:211], a[44:47], a[24:27], v[208:211]\n"
      "ds_read_b64_tr_b16 a[108:109], v238 offset:50184\n"
      "v_mfma_f32_16x16x32_bf16 v[20:23], a[48:51], a[0:3], v[20:23]\n"
      "v_mfma_f32_16x16x32_bf16 v[52:55], a[48:51], a[4:7], v[52:55]\n"
      "ds_read_b64_tr_b16 a[110:111], v238 offset:50696\n"
      "v_mfma_f32_16x16x32_bf16 v[84:87], a[48:51], a[8:11], v[84:87]\n"
      "v_mfma_f32_16x16x32_bf16 v[116:119], a[48:51], a[12:15], v[116:119]\n"
      "ds_read_b64_tr_b16 a[112:113], v239 offset:50176\n"
      "v_mfma_f32_16x16x32_bf16 v[148:151], a[48:51], a[16:19], v[148:151]\n"
      "v_mfma_f32_16x16x32_bf16 v[180:183], a[48:51], a[20:23], v[180:183]\n"
      "ds_read_b64_tr_b16 a[114:115], v239 offset:50688\n"
      "v_mfma_f32_16x16x32_bf16 v[212:215], a[48:51], a[24:27], v[212:215]\n"
      "v_mfma_f32_16x16x32_bf16 v[24:27], a[52:55], a[0:3], v[24:27]\n"
      "ds_read_b64_tr_b16 a[116:117], v239 offset:50184\n"
      "v_mfma_f32_16x16x32_bf16 v[56:59], a[52:55], a[4:7], v[56:59]\n"
      "v_mfma_f32_16x16x32_bf16 v[88:91], a[52:55], a[8:11], v[88:91]\n"
      "ds_read_b64_tr_b16 a[118:119], v239 offset:50696\n"
      "v_mfma_f32_16x16x32_bf16 v[120:123], a[52:55], a[12:15], v[120:123]\n"
      "v_mfma_f32_16x16x32_bf16 v[152:155], a[52:55], a[16:19], v[152:155]\n"
      "v_mfma_f32_16x16x32_bf16 v[184:187], a[52:55], a[20:23], v[184:187]\n"
      "v_mfma_f32_16x16x32_bf16 v[216:219], a[52:55], a[24:27], v[216:219]\n"
      "v_mfma_f32_16x16x32_bf16 v[28:31], a[56:59], a[0:3], v[28:31]\n"
      "v_mfma_f32_16x16x32_bf16 v[60:63], a[56:59], a[4:7], v[60:63]\n"
      "v_mfma_f32_16x16x32_bf16 v[92:95], a[56:59], a[8:11], v[92:95]\n"
      "v_mfma_f32_16x16x32_bf16 v[124:127], a[56:59], a[12:15], v[124:127]\n"
      "v_mfma_f32_16x16x32_bf16 v[156:159], a[56:59], a[16:19], v[156:159]\n"
      "v_mfma_f32_16x16x32_bf16 v[188:191], a[56:59], a[20:23], v[188:191]\n"
      "v_mfma_f32_16x16x32_bf16 v[220:223], a[56:59], a[24:27], v[220:223]\n"
      "s_waitcnt vmcnt(9) lgkmcnt(0)\n"
      "s_barrier\n"
      "v_mfma_f32_16x16x32_bf16 v[0:3], a[88:91], a[60:63], v[0:3]\n"
      "ds_read_b64_tr_b16 a[28:29], v238 offset:0\n"
      "v_mfma_f32_16x16x32_bf16 v[32:35], a[88:91], a[64:67], v[32:35]\n"
      "s_add_u32 m0, s72, 32768\n"
      "v_mfma_f32_16x16x32_bf16 v[64:67], a[88:91], a[68:71], v[64:67]\n"
      "ds_read_b64_tr_b16 a[30:31], v238 offset:512\n"
      "v_mfma_f32_16x16x32_bf16 v[96:99], a[88:91], a[72:75], v[96:99]\n"
      "buffer_load_dwordx4 v234, s[44:47], s73 offen lds\n"
      "v_mfma_f32_16x16x32_bf16 v[128:131], a[88:91], a[76:79], v[128:131]\n"
      "ds_read_b128 a[0:3], v236 offset:16384\n"
      "v_mfma_f32_16x16x32_bf16 v[160:163], a[88:91], a[80:83], v[160:163]\n"
      "s_add_u32 m0, s72, 33792\n"
      "v_mfma_f32_16x16x32_bf16 v[192:195], a[88:91], a[84:87], v[192:195]\n"
      "ds_read_b128 a[4:7], v236 offset:18432\n"
      "v_mfma_f32_16x16x32_bf16 v[4:7], a[92:95], a[60:63], v[4:7]\n"
      "buffer_load_dwordx4 v234, s[44:47], s74 offen lds\n"
      "v_mfma_f32_16x16x32_bf16 v[36:39], a[92:95], a[64:67], v[36:39]\n"
      "ds_read_b128 a[8:11], v236 offset:20480\n"
      "v_mfma_f32_16x16x32_bf16 v[68:71], a[92:95], a[68:71], v[68:71]\n"
      "s_add_u32 m0, s72, 34816\n"
      "v_mfma_f32_16x16x32_bf16 v[100:103], a[92:95], a[72:75], v[100:103]\n"
      "ds_read_b128 a[12:15], v236 offset:22528\n"
      "v_mfma_f32_16x16x32_bf16 v[132:135], a[92:95], a[76:79], v[132:135]\n"
      "buffer_load_dwordx4 v234, s[44:47], s75 offen lds\n"
      "v_mfma_f32_16x16x32_bf16 v[164:167], a[92:95], a[80:83], v[164:167]\n"
      "ds_read_b128 a[16:19], v236 offset:24576\n"
      "v_mfma_f32_16x16x32_bf16 v[196:199], a[92:95], a[84:87], v[196:199]\n"
      "s_add_u32 m0, s72, 35840\n"
      "v_mfma_f32_16x16x32_bf16 v[8:11], a[96:99], a[60:63], v[8:11]\n"
      "ds_read_b128 a[20:23], v236 offset:26624\n"
      "v_mfma_f32_16x16x32_bf16 v[40:43], a[96:99], a[64:67], v[40:43]\n"
      "buffer_load_dwordx4 v234, s[44:47], s76 offen lds\n"
      "v_mfma_f32_16x16x32_bf16 v[72:75], a[96:99], a[68:71], v[72:75]\n"
      "ds_read_b128 a[24:27], v236 offset:28672\n"
      "v_mfma_f32_16x16x32_bf16 v[104:107], a[96:99], a[72:75], v[104:107]\n"
      "s_add_u32 m0, s72, 36864\n"
      "v_mfma_f32_16x16x32_bf16 v[136:139], a[96:99], a[76:79], v[136:139]\n"
      "ds_read_b64_tr_b16 a[32:33], v238 offset:8\n"
      "v_mfma_f32_16x16x32_bf16 v[168:171], a[96:99], a[80:83], v[168:171]\n"
      "buffer_load_dwordx4 v234, s[44:47], s77 offen lds\n"
      "v_mfma_f32_16x16x32_bf16 v[200:203], a[96:99], a[84:87], v[200:203]\n"
      "ds_read_b64_tr_b16 a[34:35], v238 offset:520\n"
      "v_mfma_f32_16x16x32_bf16 v[12:15], a[100:103], a[60:63], v[12:15]\n"
      "s_add_u32 m0, s72, 37888\n"
      "v_mfma_f32_16x16x32_bf16 v[44:47], a[100:103], a[64:67], v[44:47]\n"
      "ds_read_b64_tr_b16 a[36:37], v239 offset:0\n"
      "v_mfma_f32_16x16x32_bf16 v[76:79], a[100:103], a[68:71], v[76:79]\n"
      "buffer_load_dwordx4 v234, s[44:47], s78 offen lds\n"
      "v_mfma_f32_16x16x32_bf16 v[108:111], a[100:103], a[72:75], v[108:111]\n"
      "ds_read_b64_tr_b16 a[38:39], v239 offset:512\n"
      "v_mfma_f32_16x16x32_bf16 v[140:143], a[100:103], a[76:79], v[140:143]\n"
      "s_add_u32 m0, s72, 38912\n"
      "v_mfma_f32_16x16x32_bf16 v[172:175], a[100:103], a[80:83], v[172:175]\n"
      "ds_read_b64_tr_b16 a[40:41], v239 offset:8\n"
      "v_mfma_f32_16x16x32_bf16 v[204:207], a[100:103], a[84:87], v[204:207]\n"
      "buffer_load_dwordx4 v234, s[44:47], s79 offen lds\n"
      "v_mfma_f32_16x16x32_bf16 v[16:19], a[104:107], a[60:63], v[16:19]\n"
      "ds_read_b64_tr_b16 a[42:43], v239 offset:520\n"
      "v_mfma_f32_16x16x32_bf16 v[48:51], a[104:107], a[64:67], v[48:51]\n"
      "v_xor_b32 v234, 384, v234\n"
      "s_add_u32 m0, s70, 49152\n"
      "v_mfma_f32_16x16x32_bf16 v[80:83], a[104:107], a[68:71], v[80:83]\n"
      "ds_read_b64_tr_b16 a[44:45], v238 offset:1024\n"
      "v_mfma_f32_16x16x32_bf16 v[112:115], a[104:107], a[72:75], v[112:115]\n"
      "buffer_load_dwordx4 v235, s[48:51], s71 offen offset:0 lds\n"
      "v_mfma_f32_16x16x32_bf16 v[144:147], a[104:107], a[76:79], v[144:147]\n"
      "ds_read_b64_tr_b16 a[46:47], v238 offset:1536\n"
      "v_mfma_f32_16x16x32_bf16 v[176:179], a[104:107], a[80:83], v[176:179]\n"
      "s_add_u32 m0, s70, 50048\n"
      "v_mfma_f32_16x16x32_bf16 v[208:211], a[104:107], a[84:87], v[208:211]\n"
      "ds_read_b64_tr_b16 a[48:49], v238 offset:1032\n"
      "v_mfma_f32_16x16x32_bf16 v[20:23], a[108:111], a[60:63], v[20:23]\n"
      "buffer_load_dwordx4 v235, s[48:51], s71 offen offset:128 lds\n"
      "v_mfma_f32_16x16x32_bf16 v[52:55], a[108:111], a[64:67], v[52:55]\n"
      "ds_read_b64_tr_b16 a[50:51], v238 offset:1544\n"
      "v_mfma_f32_16x16x32_bf16 v[84:87], a[108:111], a[68:71], v[84:87]\n"
      "s_add_u32 m0, s70, 50944\n"
      "v_mfma_f32_16x16x32_bf16 v[116:119], a[108:111], a[72:75], v[116:119]\n"
      "ds_read_b64_tr_b16 a[52:53], v239 offset:1024\n"
      "v_mfma_f32_16x16x32_bf16 v[148:151], a[108:111], a[76:79], v[148:151]\n"
      "buffer_load_dwordx4 v235, s[48:51], s71 offen offset:256 lds\n"
      "v_mfma_f32_16x16x32_bf16 v[180:183], a[108:111], a[80:83], v[180:183]\n"
      "ds_read_b64_tr_b16 a[54:55], v239 offset:1536\n"
      "v_mfma_f32_16x16x32_bf16 v[212:215], a[108:111], a[84:87], v[212:215]\n"
      "s_add_u32 m0, s70, 51840\n"
      "v_mfma_f32_16x16x32_bf16 v[24:27], a[112:115], a[60:63], v[24:27]\n"
      "ds_read_b64_tr_b16 a[56:57], v239 offset:1032\n"
      "v_mfma_f32_16x16x32_bf16 v[56:59], a[112:115], a[64:67], v[56:59]\n"
      "buffer_load_dwordx4 v235, s[48:51], s71 offen offset:384 lds\n"
      "v_mfma_f32_16x16x32_bf16 v[88:91], a[112:115], a[68:71], v[88:91]\n"
      "ds_read_b64_tr_b16 a[58:59], v239 offset:1544\n"
      "v_mfma_f32_16x16x32_bf16 v[120:123], a[112:115], a[72:75], v[120:123]\n"
      "s_xor_b32 s71, s71, 1835008\n"
      "v_mfma_f32_16x16x32_bf16 v[152:155], a[112:115], a[76:79], v[152:155]\n"
      "v_mfma_f32_16x16x32_bf16 v[184:187], a[112:115], a[80:83], v[184:187]\n"
      "v_mfma_f32_16x16x32_bf16 v[216:219], a[112:115], a[84:87], v[216:219]\n"
      "v_mfma_f32_16x16x32_bf16 v[28:31], a[116:119], a[60:63], v[28:31]\n"
      "v_mfma_f32_16x16x32_bf16 v[60:63], a[116:119], a[64:67], v[60:63]\n"
      "v_mfma_f32_16x16x32_bf16 v[92:95], a[116:119], a[68:71], v[92:95]\n"
      "v_mfma_f32_16x16x32_bf16 v[124:127], a[116:119], a[72:75], v[124:127]\n"
      "v_mfma_f32_16x16x32_bf16 v[156:159], a[116:119], a[76:79], v[156:159]\n"
      "v_mfma_f32_16x16x32_bf16 v[188:191], a[116:119], a[80:83], v[188:191]\n"
      "v_mfma_f32_16x16x32_bf16 v[220:223], a[116:119], a[84:87], v[220:223]\n"
      "s_waitcnt vmcnt(16) lgkmcnt(0)\n"
      "s_barrier\n"
      "v_mfma_f32_16x16x32_bf16 v[0:3], a[28:31], a[0:3], v[0:3]\n"
      "ds_read_b64_tr_b16 a[88:89], v238 offset:16384\n"
      "v_mfma_f32_16x16x32_bf16 v[32:35], a[28:31], a[4:7], v[32:35]\n"
      "s_add_u32 m0, s70, 0\n"
      "v_mfma_f32_16x16x32_bf16 v[64:67], a[28:31], a[8:11], v[64:67]\n"
      "ds_read_b64_tr_b16 a[90:91], v238 offset:16896\n"
      "v_mfma_f32_16x16x32_bf16 v[96:99], a[28:31], a[12:15], v[96:99]\n"
      "buffer_load_dwordx4 v235, s[48:51], s71 offen offset:0 lds\n"
      "v_mfma_f32_16x16x32_bf16 v[128:131], a[28:31], a[16:19], v[128:131]\n"
      "ds_read_b128 a[60:63], v237 offset:16384\n"
      "v_mfma_f32_16x16x32_bf16 v[160:163], a[28:31], a[20:23], v[160:163]\n"
      "v_mfma_f32_16x16x32_bf16 v[192:195], a[28:31], a[24:27], v[192:195]\n"
      "ds_read_b128 a[64:67], v237 offset:18432\n"
      "v_mfma_f32_16x16x32_bf16 v[4:7], a[32:35], a[0:3], v[4:7]\n"
      "s_add_u32 m0, s70, 896\n"
      "v_mfma_f32_16x16x32_bf16 v[36:39], a[32:35], a[4:7], v[36:39]\n"
      "ds_read_b128 a[68:71], v237 offset:20480\n"
      "v_mfma_f32_16x16x32_bf16 v[68:71], a[32:35], a[8:11], v[68:71]\n"
      "buffer_load_dwordx4 v235, s[48:51], s71 offen offset:128 lds\n"
      "v_mfma_f32_16x16x32_bf16 v[100:103], a[32:35], a[12:15], v[100:103]\n"
      "ds_read_b128 a[72:75], v237 offset:22528\n"
      "v_mfma_f32_16x16x32_bf16 v[132:135], a[32:35], a[16:19], v[132:135]\n"
      "v_mfma_f32_16x16x32_bf16 v[164:167], a[32:35], a[20:23], v[164:167]\n"
      "ds_read_b128 a[76:79], v237 offset:24576\n"
      "v_mfma_f32_16x16x32_bf16 v[196:199], a[32:35], a[24:27], v[196:199]\n"
      "s_add_u32 m0, s70, 1792\n"
      "v_mfma_f32_16x16x32_bf16 v[8:11], a[36:39], a[0:3], v[8:11]\n"
      "ds_read_b128 a[80:83], v237 offset:26624\n"
      "v_mfma_f32_16x16x32_bf16 v[40:43], a[36:39], a[4:7], v[40:43]\n"
      "buffer_load_dwordx4 v235, s[48:51], s71 offen offset:256 lds\n"
      "v_mfma_f32_16x16x32_bf16 v[72:75], a[36:39], a[8:11], v[72:75]\n"
      "ds_read_b128 a[84:87], v237 offset:28672\n"
      "v_mfma_f32_16x16x32_bf16 v[104:107], a[36:39], a[12:15], v[104:107]\n"
      "v_mfma_f32_16x16x32_bf16 v[136:139], a[36:39], a[16:19], v[136:139]\n"
      "ds_read_b64_tr_b16 a[92:93], v238 offset:16392\n"
      "v_mfma_f32_16x16x32_bf16 v[168:171], a[36:39], a[20:23], v[168:171]\n"
      "s_add_u32 m0, s70, 2688\n"
      "v_mfma_f32_16x16x32_bf16 v[200:203], a[36:39], a[24:27], v[200:203]\n"
      "ds_read_b64_tr_b16 a[94:95], v238 offset:16904\n"
      "v_mfma_f32_16x16x32_bf16 v[12:15], a[40:43], a[0:3], v[12:15]\n"
      "buffer_load_dwordx4 v235, s[48:51], s71 offen offset:384 lds\n"
      "v_mfma_f32_16x16x32_bf16 v[44:47], a[40:43], a[4:7], v[44:47]\n"
      "ds_read_b64_tr_b16 a[96:97], v239 offset:16384\n"
      "v_mfma_f32_16x16x32_bf16 v[76:79], a[40:43], a[8:11], v[76:79]\n"
      "s_xor_b32 s71, s71, 262144\n"
      "v_mfma_f32_16x16x32_bf16 v[108:111], a[40:43], a[12:15], v[108:111]\n"
      "ds_read_b64_tr_b16 a[98:99], v239 offset:16896\n"
      "v_mfma_f32_16x16x32_bf16 v[140:143], a[40:43], a[16:19], v[140:143]\n"
      "s_add_u32 m0, s68, 0\n"
      "v_mfma_f32_16x16x32_bf16 v[172:175], a[40:43], a[20:23], v[172:175]\n"
      "ds_read_b64_tr_b16 a[100:101], v239 offset:16392\n"
      "v_mfma_f32_16x16x32_bf16 v[204:207], a[40:43], a[24:27], v[204:207]\n"
      "buffer_load_dwordx4 v230, s[52:55], s80 offen offset:2048 nt lds\n"
      "v_mfma_f32_16x16x32_bf16 v[16:19], a[44:47], a[0:3], v[16:19]\n"
      "ds_read_b64_tr_b16 a[102:103], v239 offset:16904\n"
      "v_mfma_f32_16x16x32_bf16 v[48:51], a[44:47], a[4:7], v[48:51]\n"
      "v_mfma_f32_16x16x32_bf16 v[80:83], a[44:47], a[8:11], v[80:83]\n"
      "ds_read_b64_tr_b16 a[104:105], v238 offset:17408\n"
      "v_mfma_f32_16x16x32_bf16 v[112:115], a[44:47], a[12:15], v[112:115]\n"
      "v_mfma_f32_16x16x32_bf16 v[144:147], a[44:47], a[16:19], v[144:147]\n"
      "ds_read_b64_tr_b16 a[106:107], v238 offset:17920\n"
      "v_mfma_f32_16x16x32_bf16 v[176:179], a[44:47], a[20:23], v[176:179]\n"
      "v_mfma_f32_16x16x32_bf16 v[208:211], a[44:47], a[24:27], v[208:211]\n"
      "ds_read_b64_tr_b16 a[108:109], v238 offset:17416\n"
      "v_mfma_f32_16x16x32_bf16 v[20:23], a[48:51], a[0:3], v[20:23]\n"
      "v_mfma_f32_16x16x32_bf16 v[52:55], a[48:51], a[4:7], v[52:55]\n"
      "ds_read_b64_tr_b16 a[110:111], v238 offset:17928\n"
      "v_mfma_f32_16x16x32_bf16 v[84:87], a[48:51], a[8:11], v[84:87]\n"
      "v_mfma_f32_16x16x32_bf16 v[116:119], a[48:51], a[12:15], v[116:119]\n"
      "ds_read_b64_tr_b16 a[112:113], v239 offset:17408\n"
      "v_mfma_f32_16x16x32_bf16 v[148:151], a[48:51], a[16:19], v[148:151]\n"
      "v_mfma_f32_16x16x32_bf16 v[180:183], a[48:51], a[20:23], v[180:183]\n"
      "ds_read_b64_tr_b16 a[114:115], v239 offset:17920\n"
      "v_mfma_f32_16x16x32_bf16 v[212:215], a[48:51], a[24:27], v[212:215]\n"
      "v_mfma_f32_16x16x32_bf16 v[24:27], a[52:55], a[0:3], v[24:27]\n"
      "ds_read_b64_tr_b16 a[116:117], v239 offset:17416\n"
      "v_mfma_f32_16x16x32_bf16 v[56:59], a[52:55], a[4:7], v[56:59]\n"
      "v_mfma_f32_16x16x32_bf16 v[88:91], a[52:55], a[8:11], v[88:91]\n"
      "ds_read_b64_tr_b16 a[118:119], v239 offset:17928\n"
      "v_mfma_f32_16x16x32_bf16 v[120:123], a[52:55], a[12:15], v[120:123]\n"
      "v_mfma_f32_16x16x32_bf16 v[152:155], a[52:55], a[16:19], v[152:155]\n"
      "v_mfma_f32_16x16x32_bf16 v[184:187], a[52:55], a[20:23], v[184:187]\n"
      "v_mfma_f32_16x16x32_bf16 v[216:219], a[52:55], a[24:27], v[216:219]\n"
      "v_mfma_f32_16x16x32_bf16 v[28:31], a[56:59], a[0:3], v[28:31]\n"
      "v_mfma_f32_16x16x32_bf16 v[60:63], a[56:59], a[4:7], v[60:63]\n"
      "v_mfma_f32_16x16x32_bf16 v[92:95], a[56:59], a[8:11], v[92:95]\n"
      "v_mfma_f32_16x16x32_bf16 v[124:127], a[56:59], a[12:15], v[124:127]\n"
      "v_mfma_f32_16x16x32_bf16 v[156:159], a[56:59], a[16:19], v[156:159]\n"
      "v_mfma_f32_16x16x32_bf16 v[188:191], a[56:59], a[20:23], v[188:191]\n"
      "v_mfma_f32_16x16x32_bf16 v[220:223], a[56:59], a[24:27], v[220:223]\n"
      "s_waitcnt vmcnt(9) lgkmcnt(0)\n"
      "s_barrier\n"
      "v_mfma_f32_16x16x32_bf16 v[0:3], a[88:91], a[60:63], v[0:3]\n"
      "ds_read_b64_tr_b16 a[28:29], v238 offset:32768\n"
      "v_mfma_f32_16x16x32_bf16 v[32:35], a[88:91], a[64:67], v[32:35]\n"
      "s_add_u32 m0, s72, 16384\n"
      "v_mfma_f32_16x16x32_bf16 v[64:67], a[88:91], a[68:71], v[64:67]\n"
      "ds_read_b64_tr_b16 a[30:31], v238 offset:33280\n"
      "v_mfma_f32_16x16x32_bf16 v[96:99], a[88:91], a[72:75], v[96:99]\n"
      "buffer_load_dwordx4 v234, s[44:47], s73 offen lds\n"
      "v_mfma_f32_16x16x32_bf16 v[128:131], a[88:91], a[76:79], v[128:131]\n"
      "ds_read_b128 a[0:3], v236 offset:32768\n"
      "v_mfma_f32_16x16x32_bf16 v[160:163], a[88:91], a[80:83], v[160:163]\n"
      "s_add_u32 m0, s72, 17408\n"
      "v_mfma_f32_16x16x32_bf16 v[192:195], a[88:91], a[84:87], v[192:195]\n"
      "ds_read_b128 a[4:7], v236 offset:34816\n"
      "v_mfma_f32_16x16x32_bf16 v[4:7], a[92:95], a[60:63], v[4:7]\n"
      "buffer_load_dwordx4 v234, s[44:47], s74 offen lds\n"
      "v_mfma_f32_16x16x32_bf16 v[36:39], a[92:95], a[64:67], v[36:39]\n"
      "ds_read_b128 a[8:11], v236 offset:36864\n"
      "v_mfma_f32_16x16x32_bf16 v[68:71], a[92:95], a[68:71], v[68:71]\n"
      "s_add_u32 m0, s72, 18432\n"
      "v_mfma_f32_16x16x32_bf16 v[100:103], a[92:95], a[72:75], v[100:103]\n"
      "ds_read_b128 a[12:15], v236 offset:38912\n"
      "v_mfma_f32_16x16x32_bf16 v[132:135], a[92:95], a[76:79], v[132:135]\n"
      "buffer_load_dwordx4 v234, s[44:47], s75 offen lds\n"
      "v_mfma_f32_16x16x32_bf16 v[164:167], a[92:95], a[80:83], v[164:167]\n"
      "ds_read_b128 a[16:19], v236 offset:40960\n"
      "v_mfma_f32_16x16x32_bf16 v[196:199], a[92:95], a[84:87], v[196:199]\n"
      "s_add_u32 m0, s72, 19456\n"
      "v_mfma_f32_16x16x32_bf16 v[8:11], a[96:99], a[60:63], v[8:11]\n"
      "ds_read_b128 a[20:23], v236 offset:43008\n"
      "v_mfma_f32_16x16x32_bf16 v[40:43], a[96:99], a[64:67], v[40:43]\n"
      "buffer_load_dwordx4 v234, s[44:47], s76 offen lds\n"
      "v_mfma_f32_16x16x32_bf16 v[72:75], a[96:99], a[68:71], v[72:75]\n"
      "ds_read_b128 a[24:27], v236 offset:45056\n"
      "v_mfma_f32_16x16x32_bf16 v[104:107], a[96:99], a[72:75], v[104:107]\n"
      "s_add_u32 m0, s72, 20480\n"
      "v_mfma_f32_16x16x32_bf16 v[136:139], a[96:99], a[76:79], v[136:139]\n"
      "ds_read_b64_tr_b16 a[32:33], v238 offset:32776\n"
      "v_mfma_f32_16x16x32_bf16 v[168:171], a[96:99], a[80:83], v[168:171]\n"
      "buffer_load_dwordx4 v234, s[44:47], s77 offen lds\n"
      "v_mfma_f32_16x16x32_bf16 v[200:203], a[96:99], a[84:87], v[200:203]\n"
      "ds_read_b64_tr_b16 a[34:35], v238 offset:33288\n"
      "v_mfma_f32_16x16x32_bf16 v[12:15], a[100:103], a[60:63], v[12:15]\n"
      "s_add_u32 m0, s72, 21504\n"
      "v_mfma_f32_16x16x32_bf16 v[44:47], a[100:103], a[64:67], v[44:47]\n"
      "ds_read_b64_tr_b16 a[36:37], v239 offset:32768\n"
      "v_mfma_f32_16x16x32_bf16 v[76:79], a[100:103], a[68:71], v[76:79]\n"
      "buffer_load_dwordx4 v234, s[44:47], s78 offen lds\n"
      "v_mfma_f32_16x16x32_bf16 v[108:111], a[100:103], a[72:75], v[108:111]\n"
      "ds_read_b64_tr_b16 a[38:39], v239 offset:33280\n"
      "v_mfma_f32_16x16x32_bf16 v[140:143], a[100:103], a[76:79], v[140:143]\n"
      "s_add_u32 m0, s72, 22528\n"
      "v_mfma_f32_16x16x32_bf16 v[172:175], a[100:103], a[80:83], v[172:175]\n"
      "ds_read_b64_tr_b16 a[40:41], v239 offset:32776\n"
      "v_mfma_f32_16x16x32_bf16 v[204:207], a[100:103], a[84:87], v[204:207]\n"
      "buffer_load_dwordx4 v234, s[44:47], s79 offen lds\n"
      "v_mfma_f32_16x16x32_bf16 v[16:19], a[104:107], a[60:63], v[16:19]\n"
      "ds_read_b64_tr_b16 a[42:43], v239 offset:33288\n"
      "v_mfma_f32_16x16x32_bf16 v[48:51], a[104:107], a[64:67], v[48:51]\n"
      "v_xor_b32 v234, 128, v234\n"
      "s_add_u32 m0, s70, 16384\n"
      "v_mfma_f32_16x16x32_bf16 v[80:83], a[104:107], a[68:71], v[80:83]\n"
      "ds_read_b64_tr_b16 a[44:45], v238 offset:33792\n"
      "v_mfma_f32_16x16x32_bf16 v[112:115], a[104:107], a[72:75], v[112:115]\n"
      "buffer_load_dwordx4 v235, s[48:51], s71 offen offset:0 lds\n"
      "v_mfma_f32_16x16x32_bf16 v[144:147], a[104:107], a[76:79], v[144:147]\n"
      "ds_read_b64_tr_b16 a[46:47], v238 offset:34304\n"
      "v_mfma_f32_16x16x32_bf16 v[176:179], a[104:107], a[80:83], v[176:179]\n"
      "s_add_u32 m0, s70, 17280\n"
      "v_mfma_f32_16x16x32_bf16 v[208:211], a[104:107], a[84:87], v[208:211]\n"
      "ds_read_b64_tr_b16 a[48:49], v238 offset:33800\n"
      "v_mfma_f32_16x16x32_bf16 v[20:23], a[108:111], a[60:63], v[20:23]\n"
      "buffer_load_dwordx4 v235, s[48:51], s71 offen offset:128 lds\n"
      "v_mfma_f32_16x16x32_bf16 v[52:55], a[108:111], a[64:67], v[52:55]\n"
      "ds_read_b64_tr_b16 a[50:51], v238 offset:34312\n"
      "v_mfma_f32_16x16x32_bf16 v[84:87], a[108:111], a[68:71], v[84:87]\n"
      "s_add_u32 m0, s70, 18176\n"
      "v_mfma_f32_16x16x32_bf16 v[116:119], a[108:111], a[72:75], v[116:119]\n"
      "ds_read_b64_tr_b16 a[52:53], v239 offset:33792\n"
      "v_mfma_f32_16x16x32_bf16 v[148:151], a[108:111], a[76:79], v[148:151]\n"
      "buffer_load_dwordx4 v235, s[48:51], s71 offen offset:256 lds\n"
      "v_mfma_f32_16x16x32_bf16 v[180:183], a[108:111], a[80:83], v[180:183]\n"
      "ds_read_b64_tr_b16 a[54:55], v239 offset:34304\n"
      "v_mfma_f32_16x16x32_bf16 v[212:215], a[108:111], a[84:87], v[212:215]\n"
      "s_add_u32 m0, s70, 19072\n"
      "v_mfma_f32_16x16x32_bf16 v[24:27], a[112:115], a[60:63], v[24:27]\n"
      "ds_read_b64_tr_b16 a[56:57], v239 offset:33800\n"
      "v_mfma_f32_16x16x32_bf16 v[56:59], a[112:115], a[64:67], v[56:59]\n"
      "buffer_load_dwordx4 v235, s[48:51], s71 offen offset:384 lds\n"
      "v_mfma_f32_16x16x32_bf16 v[88:91], a[112:115], a[68:71], v[88:91]\n"
      "ds_read_b64_tr_b16 a[58:59], v239 offset:34312\n"
      "v_mfma_f32_16x16x32_bf16 v[120:123], a[112:115], a[72:75], v[120:123]\n"
      "s_xor_b32 s71, s71, 786432\n"
      "v_mfma_f32_16x16x32_bf16 v[152:155], a[112:115], a[76:79], v[152:155]\n"
      "v_mfma_f32_16x16x32_bf16 v[184:187], a[112:115], a[80:83], v[184:187]\n"
      "v_mfma_f32_16x16x32_bf16 v[216:219], a[112:115], a[84:87], v[216:219]\n"
      "v_mfma_f32_16x16x32_bf16 v[28:31], a[116:119], a[60:63], v[28:31]\n"
      "v_mfma_f32_16x16x32_bf16 v[60:63], a[116:119], a[64:67], v[60:63]\n"
      "v_mfma_f32_16x16x32_bf16 v[92:95], a[116:119], a[68:71], v[92:95]\n"
      "v_mfma_f32_16x16x32_bf16 v[124:127], a[116:119], a[72:75], v[124:127]\n"
      "v_mfma_f32_16x16x32_bf16 v[156:159], a[116:119], a[76:79], v[156:159]\n"
      "v_mfma_f32_16x16x32_bf16 v[188:191], a[116:119], a[80:83], v[188:191]\n"
      "v_mfma_f32_16x16x32_bf16 v[220:223], a[116:119], a[84:87], v[220:223]\n"
      "s_waitcnt vmcnt(16) lgkmcnt(0)\n"
      "s_barrier\n"
      "v_mfma_f32_16x16x32_bf16 v[0:3], a[28:31], a[0:3], v[0:3]\n"
      "ds_read_b64_tr_b16 a[88:89], v238 offset:49152\n"
      "v_mfma_f32_16x16x32_bf16 v[32:35], a[28:31], a[4:7], v[32:35]\n"
      "s_add_u32 m0, s70, 32768\n"
      "v_mfma_f32_16x16x32_bf16 v[64:67], a[28:31], a[8:11], v[64:67]\n"
      "ds_read_b64_tr_b16 a[90:91], v238 offset:49664\n"
      "v_mfma_f32_16x16x32_bf16 v[96:99], a[28:31], a[12:15], v[96:99]\n"
      "buffer_load_dwordx4 v235, s[48:51], s71 offen offset:0 lds\n"
      "v_mfma_f32_16x16x32_bf16 v[128:131], a[28:31], a[16:19], v[128:131]\n"
      "ds_read_b128 a[60:63], v237 offset:32768\n"
      "v_mfma_f32_16x16x32_bf16 v[160:163], a[28:31], a[20:23], v[160:163]\n"
      "v_mfma_f32_16x16x32_bf16 v[192:195], a[28:31], a[24:27], v[192:195]\n"
      "ds_read_b128 a[64:67], v237 offset:34816\n"
      "v_mfma_f32_16x16x32_bf16 v[4:7], a[32:35], a[0:3], v[4:7]\n"
      "s_add_u32 m0, s70, 33664\n"
      "v_mfma_f32_16x16x32_bf16 v[36:39], a[32:35], a[4:7], v[36:39]\n"
      "ds_read_b128 a[68:71], v237 offset:36864\n"
      "v_mfma_f32_16x16x32_bf16 v[68:71], a[32:35], a[8:11], v[68:71]\n"
      "buffer_load_dwordx4 v235, s[48:51], s71 offen offset:128 lds\n"
      "v_mfma_f32_16x16x32_bf16 v[100:103], a[32:35], a[12:15], v[100:103]\n"
      "ds_read_b128 a[72:75], v237 offset:38912\n"
      "v_mfma_f32_16x16x32_bf16 v[132:135], a[32:35], a[16:19], v[132:135]\n"
      "v_mfma_f32_16x16x32_bf16 v[164:167], a[32:35], a[20:23], v[164:167]\n"
      "ds_read_b128 a[76:79], v237 offset:40960\n"
      "v_mfma_f32_16x16x32_bf16 v[196:199], a[32:35], a[24:27], v[196:199]\n"
      "s_add_u32 m0, s70, 34560\n"
      "v_mfma_f32_16x16x32_bf16 v[8:11], a[36:39], a[0:3], v[8:11]\n"
      "ds_read_b128 a[80:83], v237 offset:43008\n"
      "v_mfma_f32_16x16x32_bf16 v[40:43], a[36:39], a[4:7], v[40:43]\n"
      "buffer_load_dwordx4 v235, s[48:51], s71 offen offset:256 lds\n"
      "v_mfma_f32_16x16x32_bf16 v[72:75], a[36:39], a[8:11], v[72:75]\n"
      "ds_read_b128 a[84:87], v237 offset:45056\n"
      "v_mfma_f32_16x16x32_bf16 v[104:107], a[36:39], a[12:15], v[104:107]\n"
      "v_mfma_f32_16x16x32_bf16 v[136:139], a[36:39], a[16:19], v[136:139]\n"
      "ds_read_b64_tr_b16 a[92:93], v238 offset:49160\n"
      "v_mfma_f32_16x16x32_bf16 v[168:171], a[36:39], a[20:23], v[168:171]\n"
      "s_add_u32 m0, s70, 35456\n"
      "v_mfma_f32_16x16x32_bf16 v[200:203], a[36:39], a[24:27], v[200:203]\n"
      "ds_read_b64_tr_b16 a[94:95], v238 offset:49672\n"
      "v_mfma_f32_16x16x32_bf16 v[12:15], a[40:43], a[0:3], v[12:15]\n"
      "buffer_load_dwordx4 v235, s[48:51], s71 offen offset:384 lds\n"
      "v_mfma_f32_16x16x32_bf16 v[44:47], a[40:43], a[4:7], v[44:47]\n"
      "ds_read_b64_tr_b16 a[96:97], v239 offset:49152\n"
      "v_mfma_f32_16x16x32_bf16 v[76:79], a[40:43], a[8:11], v[76:79]\n"
      "s_xor_b32 s71, s71, 262144\n"
      "v_mfma_f32_16x16x32_bf16 v[108:111], a[40:43], a[12:15], v[108:111]\n"
      "ds_read_b64_tr_b16 a[98:99], v239 offset:49664\n"
      "v_mfma_f32_16x16x32_bf16 v[140:143], a[40:43], a[16:19], v[140:143]\n"
      "s_add_u32 m0, s68, 0\n"
      "s_nop 0\n"
      "v_mfma_f32_16x16x32_bf16 v[172:175], a[40:43], a[20:23], v[172:175]\n"
      "ds_read_b64_tr_b16 a[100:101], v239 offset:49160\n"
      "v_mfma_f32_16x16x32_bf16 v[204:207], a[40:43], a[24:27], v[204:207]\n"
      "buffer_load_dwordx4 v231, s[52:55], s80 offen offset:3072 nt lds\n"
      "v_mfma_f32_16x16x32_bf16 v[16:19], a[44:47], a[0:3], v[16:19]\n"
      "ds_read_b64_tr_b16 a[102:103], v239 offset:49672\n"
      "v_mfma_f32_16x16x32_bf16 v[48:51], a[44:47], a[4:7], v[48:51]\n"
      "v_mfma_f32_16x16x32_bf16 v[80:83], a[44:47], a[8:11], v[80:83]\n"
      "ds_read_b64_tr_b16 a[104:105], v238 offset:50176\n"
      "v_mfma_f32_16x16x32_bf16 v[112:115], a[44:47], a[12:15], v[112:115]\n"
      "v_mfma_f32_16x16x32_bf16 v[144:147], a[44:47], a[16:19], v[144:147]\n"
      "ds_read_b64_tr_b16 a[106:107], v238 offset:50688\n"
      "v_mfma_f32_16x16x32_bf16 v[176:179], a[44:47], a[20:23], v[176:179]\n"
      "v_mfma_f32_16x16x32_bf16 v[208:211], a[44:47], a[24:27], v[208:211]\n"
      "ds_read_b64_tr_b16 a[108:109], v238 offset:50184\n"
      "v_mfma_f32_16x16x32_bf16 v[20:23], a[48:51], a[0:3], v[20:23]\n"
      "v_mfma_f32_16x16x32_bf16 v[52:55], a[48:51], a[4:7], v[52:55]\n"
      "ds_read_b64_tr_b16 a[110:111], v238 offset:50696\n"
      "v_mfma_f32_16x16x32_bf16 v[84:87], a[48:51], a[8:11], v[84:87]\n"
      "v_mfma_f32_16x16x32_bf16 v[116:119], a[48:51], a[12:15], v[116:119]\n"
      "ds_read_b64_tr_b16 a[112:113], v239 offset:50176\n"
      "v_mfma_f32_16x16x32_bf16 v[148:151], a[48:51], a[16:19], v[148:151]\n"
      "v_mfma_f32_16x16x32_bf16 v[180:183], a[48:51], a[20:23], v[180:183]\n"
      "ds_read_b64_tr_b16 a[114:115], v239 offset:50688\n"
      "v_mfma_f32_16x16x32_bf16 v[212:215], a[48:51], a[24:27], v[212:215]\n"
      "v_mfma_f32_16x16x32_bf16 v[24:27], a[52:55], a[0:3], v[24:27]\n"
      "ds_read_b64_tr_b16 a[116:117], v239 offset:50184\n"
      "v_mfma_f32_16x16x32_bf16 v[56:59], a[52:55], a[4:7], v[56:59]\n"
      "v_mfma_f32_16x16x32_bf16 v[88:91], a[52:55], a[8:11], v[88:91]\n"
      "ds_read_b64_tr_b16 a[118:119], v239 offset:50696\n"
      "v_mfma_f32_16x16x32_bf16 v[120:123], a[52:55], a[12:15], v[120:123]\n"
      "v_mfma_f32_16x16x32_bf16 v[152:155], a[52:55], a[16:19], v[152:155]\n"
      "v_mfma_f32_16x16x32_bf16 v[184:187], a[52:55], a[20:23], v[184:187]\n"
      "v_mfma_f32_16x16x32_bf16 v[216:219], a[52:55], a[24:27], v[216:219]\n"
      "v_mfma_f32_16x16x32_bf16 v[28:31], a[56:59], a[0:3], v[28:31]\n"
      "v_mfma_f32_16x16x32_bf16 v[60:63], a[56:59], a[4:7], v[60:63]\n"
      "v_mfma_f32_16x16x32_bf16 v[92:95], a[56:59], a[8:11], v[92:95]\n"
      "v_mfma_f32_16x16x32_bf16 v[124:127], a[56:59], a[12:15], v[124:127]\n"
      "v_mfma_f32_16x16x32_bf16 v[156:159], a[56:59], a[16:19], v[156:159]\n"
      "v_mfma_f32_16x16x32_bf16 v[188:191], a[56:59], a[20:23], v[188:191]\n"
      "v_mfma_f32_16x16x32_bf16 v[220:223], a[56:59], a[24:27], v[220:223]\n"
      "s_waitcnt vmcnt(9) lgkmcnt(0)\n"
      "s_barrier\n"
      "v_mfma_f32_16x16x32_bf16 v[0:3], a[88:91], a[60:63], v[0:3]\n"
      "ds_read_b64_tr_b16 a[28:29], v238 offset:0\n"
      "v_mfma_f32_16x16x32_bf16 v[32:35], a[88:91], a[64:67], v[32:35]\n"
      "s_add_u32 m0, s72, 32768\n"
      "v_mfma_f32_16x16x32_bf16 v[64:67], a[88:91], a[68:71], v[64:67]\n"
      "ds_read_b64_tr_b16 a[30:31], v238 offset:512\n"
      "v_mfma_f32_16x16x32_bf16 v[96:99], a[88:91], a[72:75], v[96:99]\n"
      "buffer_load_dwordx4 v234, s[44:47], s73 offen lds\n"
      "v_mfma_f32_16x16x32_bf16 v[128:131], a[88:91], a[76:79], v[128:131]\n"
      "ds_read_b128 a[0:3], v236 offset:16384\n"
      "v_mfma_f32_16x16x32_bf16 v[160:163], a[88:91], a[80:83], v[160:163]\n"
      "s_add_u32 m0, s72, 33792\n"
      "v_mfma_f32_16x16x32_bf16 v[192:195], a[88:91], a[84:87], v[192:195]\n"
      "ds_read_b128 a[4:7], v236 offset:18432\n"
      "v_mfma_f32_16x16x32_bf16 v[4:7], a[92:95], a[60:63], v[4:7]\n"
      "buffer_load_dwordx4 v234, s[44:47], s74 offen lds\n"
      "v_mfma_f32_16x16x32_bf16 v[36:39], a[92:95], a[64:67], v[36:39]\n"
      "ds_read_b128 a[8:11], v236 offset:20480\n"
      "v_mfma_f32_16x16x32_bf16 v[68:71], a[92:95], a[68:71], v[68:71]\n"
      "s_add_u32 m0, s72, 34816\n"
      "v_mfma_f32_16x16x32_bf16 v[100:103], a[92:95], a[72:75], v[100:103]\n"
      "ds_read_b128 a[12:15], v236 offset:22528\n"
      "v_mfma_f32_16x16x32_bf16 v[132:135], a[92:95], a[76:79], v[132:135]\n"
      "buffer_load_dwordx4 v234, s[44:47], s75 offen lds\n"
      "v_mfma_f32_16x16x32_bf16 v[164:167], a[92:95], a[80:83], v[164:167]\n"
      "ds_read_b128 a[16:19], v236 offset:24576\n"
      "v_mfma_f32_16x16x32_bf16 v[196:199], a[92:95], a[84:87], v[196:199]\n"
      "s_add_u32 m0, s72, 35840\n"
      "v_mfma_f32_16x16x32_bf16 v[8:11], a[96:99], a[60:63], v[8:11]\n"
      "ds_read_b128 a[20:23], v236 offset:26624\n"
      "v_mfma_f32_16x16x32_bf16 v[40:43], a[96:99], a[64:67], v[40:43]\n"
      "buffer_load_dwordx4 v234, s[44:47], s76 offen lds\n"
      "v_mfma_f32_16x16x32_bf16 v[72:75], a[96:99], a[68:71], v[72:75]\n"
      "ds_read_b128 a[24:27], v236 offset:28672\n"
      "v_mfma_f32_16x16x32_bf16 v[104:107], a[96:99], a[72:75], v[104:107]\n"
      "s_add_u32 m0, s72, 36864\n"
      "v_mfma_f32_16x16x32_bf16 v[136:139], a[96:99], a[76:79], v[136:139]\n"
      "ds_read_b64_tr_b16 a[32:33], v238 offset:8\n"
      "v_mfma_f32_16x16x32_bf16 v[168:171], a[96:99], a[80:83], v[168:171]\n"
      "buffer_load_dwordx4 v234, s[44:47], s77 offen lds\n"
      "v_mfma_f32_16x16x32_bf16 v[200:203], a[96:99], a[84:87], v[200:203]\n"
      "ds_read_b64_tr_b16 a[34:35], v238 offset:520\n"
      "v_mfma_f32_16x16x32_bf16 v[12:15], a[100:103], a[60:63], v[12:15]\n"
      "s_add_u32 m0, s72, 37888\n"
      "v_mfma_f32_16x16x32_bf16 v[44:47], a[100:103], a[64:67], v[44:47]\n"
      "ds_read_b64_tr_b16 a[36:37], v239 offset:0\n"
      "v_mfma_f32_16x16x32_bf16 v[76:79], a[100:103], a[68:71], v[76:79]\n"
      "buffer_load_dwordx4 v234, s[44:47], s78 offen lds\n"
      "v_mfma_f32_16x16x32_bf16 v[108:111], a[100:103], a[72:75], v[108:111]\n"
      "ds_read_b64_tr_b16 a[38:39], v239 offset:512\n"
      "v_mfma_f32_16x16x32_bf16 v[140:143], a[100:103], a[76:79], v[140:143]\n"
      "s_add_u32 m0, s72, 38912\n"
      "v_mfma_f32_16x16x32_bf16 v[172:175], a[100:103], a[80:83], v[172:175]\n"
      "ds_read_b64_tr_b16 a[40:41], v239 offset:8\n"
      "v_mfma_f32_16x16x32_bf16 v[204:207], a[100:103], a[84:87], v[204:207]\n"
      "buffer_load_dwordx4 v234, s[44:47], s79 offen lds\n"
      "v_mfma_f32_16x16x32_bf16 v[16:19], a[104:107], a[60:63], v[16:19]\n"
      "ds_read_b64_tr_b16 a[42:43], v239 offset:520\n"
      "v_mfma_f32_16x16x32_bf16 v[48:51], a[104:107], a[64:67], v[48:51]\n"
      "v_and_b32 v234, 0xfffffc7f, v234\n"
      "s_add_u32 m0, s70, 49152\n"
      "v_mfma_f32_16x16x32_bf16 v[80:83], a[104:107], a[68:71], v[80:83]\n"
      "ds_read_b64_tr_b16 a[44:45], v238 offset:1024\n"
      "v_mfma_f32_16x16x32_bf16 v[112:115], a[104:107], a[72:75], v[112:115]\n"
      "buffer_load_dwordx4 v235, s[48:51], s71 offen offset:0 lds\n"
      "v_mfma_f32_16x16x32_bf16 v[144:147], a[104:107], a[76:79], v[144:147]\n"
      "ds_read_b64_tr_b16 a[46:47], v238 offset:1536\n"
      "v_mfma_f32_16x16x32_bf16 v[176:179], a[104:107], a[80:83], v[176:179]\n"
      "s_add_u32 m0, s70, 50048\n"
      "v_mfma_f32_16x16x32_bf16 v[208:211], a[104:107], a[84:87], v[208:211]\n"
      "ds_read_b64_tr_b16 a[48:49], v238 offset:1032\n"
      "v_mfma_f32_16x16x32_bf16 v[20:23], a[108:111], a[60:63], v[20:23]\n"
      "buffer_load_dwordx4 v235, s[48:51], s71 offen offset:128 lds\n"
      "v_mfma_f32_16x16x32_bf16 v[52:55], a[108:111], a[64:67], v[52:55]\n"
      "ds_read_b64_tr_b16 a[50:51], v238 offset:1544\n"
      "v_mfma_f32_16x16x32_bf16 v[84:87], a[108:111], a[68:71], v[84:87]\n"
      "s_add_u32 m0, s70, 50944\n"
      "v_mfma_f32_16x16x32_bf16 v[116:119], a[108:111], a[72:75], v[116:119]\n"
      "ds_read_b64_tr_b16 a[52:53], v239 offset:1024\n"
      "v_mfma_f32_16x16x32_bf16 v[148:151], a[108:111], a[76:79], v[148:151]\n"
      "buffer_load_dwordx4 v235, s[48:51], s71 offen offset:256 lds\n"
      "v_mfma_f32_16x16x32_bf16 v[180:183], a[108:111], a[80:83], v[180:183]\n"
      "ds_read_b64_tr_b16 a[54:55], v239 offset:1536\n"
      "v_mfma_f32_16x16x32_bf16 v[212:215], a[108:111], a[84:87], v[212:215]\n"
      "s_add_u32 m0, s70, 51840\n"
      "v_mfma_f32_16x16x32_bf16 v[24:27], a[112:115], a[60:63], v[24:27]\n"
      "ds_read_b64_tr_b16 a[56:57], v239 offset:1032\n"
      "v_mfma_f32_16x16x32_bf16 v[56:59], a[112:115], a[64:67], v[56:59]\n"
      "buffer_load_dwordx4 v235, s[48:51], s71 offen offset:384 lds\n"
      "v_mfma_f32_16x16x32_bf16 v[88:91], a[112:115], a[68:71], v[88:91]\n"
      "ds_read_b64_tr_b16 a[58:59], v239 offset:1544\n"
      "v_mfma_f32_16x16x32_bf16 v[120:123], a[112:115], a[72:75], v[120:123]\n"
      "s_xor_b32 s71, s71, 3932160\n"
      "s_add_u32 m0, s68, 4096\n"
      "s_add_u32 s64, s80, 4096\n"
      "v_mfma_f32_16x16x32_bf16 v[152:155], a[112:115], a[76:79], v[152:155]\n"
      "v_mfma_f32_16x16x32_bf16 v[184:187], a[112:115], a[80:83], v[184:187]\n"
      "buffer_load_dwordx4 v230, s[52:55], s64 offen offset:0 nt lds\n"
      "v_mfma_f32_16x16x32_bf16 v[216:219], a[112:115], a[84:87], v[216:219]\n"
      "v_mfma_f32_16x16x32_bf16 v[28:31], a[116:119], a[60:63], v[28:31]\n"
      "s_add_u32 m0, s68, 4096\n"
      "s_add_u32 s64, s80, 4096\n"
      "v_mfma_f32_16x16x32_bf16 v[60:63], a[116:119], a[64:67], v[60:63]\n"
      "v_mfma_f32_16x16x32_bf16 v[92:95], a[116:119], a[68:71], v[92:95]\n"
      "buffer_load_dwordx4 v231, s[52:55], s64 offen offset:1024 nt lds\n"
      "v_mfma_f32_16x16x32_bf16 v[124:127], a[116:119], a[72:75], v[124:127]\n"
      "v_mfma_f32_16x16x32_bf16 v[156:159], a[116:119], a[76:79], v[156:159]\n"
      "v_mfma_f32_16x16x32_bf16 v[188:191], a[116:119], a[80:83], v[188:191]\n"
      "v_mfma_f32_16x16x32_bf16 v[220:223], a[116:119], a[84:87], v[220:223]\n"
      "s_waitcnt vmcnt(18) lgkmcnt(0)\n"
      "s_barrier\n"
      "v_mfma_f32_16x16x32_bf16 v[0:3], a[28:31], a[0:3], v[0:3]\n"
      "ds_read_b64_tr_b16 a[88:89], v238 offset:16384\n"
      "s_lshl_b32 s66, %[wid], 6\n"
      "v_xor_b32 v234, s66, v234\n"
      "v_xor_b32 v235, 0x80, v234\n"
      "v_mfma_f32_16x16x32_bf16 v[32:35], a[28:31], a[4:7], v[32:35]\n"
      "s_add_u32 m0, s68, 4096\n"
      "s_add_u32 s64, s80, 4096\n"
      "v_mfma_f32_16x16x32_bf16 v[64:67], a[28:31], a[8:11], v[64:67]\n"
      "ds_read_b64_tr_b16 a[90:91], v238 offset:16896\n"
      "v_mfma_f32_16x16x32_bf16 v[96:99], a[28:31], a[12:15], v[96:99]\n"
      "buffer_load_dwordx4 v230, s[52:55], s64 offen offset:2048 nt lds\n"
      "v_mfma_f32_16x16x32_bf16 v[128:131], a[28:31], a[16:19], v[128:131]\n"
      "ds_read_b128 a[60:63], v237 offset:16384\n"
      "v_mfma_f32_16x16x32_bf16 v[160:163], a[28:31], a[20:23], v[160:163]\n"
      "v_mfma_f32_16x16x32_bf16 v[192:195], a[28:31], a[24:27], v[192:195]\n"
      "ds_read_b128 a[64:67], v237 offset:18432\n"
      "v_mfma_f32_16x16x32_bf16 v[4:7], a[32:35], a[0:3], v[4:7]\n"
      "v_mfma_f32_16x16x32_bf16 v[36:39], a[32:35], a[4:7], v[36:39]\n"
      "ds_read_b128 a[68:71], v237 offset:20480\n"
      "v_mfma_f32_16x16x32_bf16 v[68:71], a[32:35], a[8:11], v[68:71]\n"
      "v_mfma_f32_16x16x32_bf16 v[100:103], a[32:35], a[12:15], v[100:103]\n"
      "ds_read_b128 a[72:75], v237 offset:22528\n"
      "v_mfma_f32_16x16x32_bf16 v[132:135], a[32:35], a[16:19], v[132:135]\n"
      "v_mfma_f32_16x16x32_bf16 v[164:167], a[32:35], a[20:23], v[164:167]\n"
      "ds_read_b128 a[76:79], v237 offset:24576\n"
      "v_mfma_f32_16x16x32_bf16 v[196:199], a[32:35], a[24:27], v[196:199]\n"
      "s_add_u32 m0, s68, 4096\n"
      "s_add_u32 s64, s80, 4096\n"
      "v_mfma_f32_16x16x32_bf16 v[8:11], a[36:39], a[0:3], v[8:11]\n"
      "ds_read_b128 a[80:83], v237 offset:26624\n"
      "v_mfma_f32_16x16x32_bf16 v[40:43], a[36:39], a[4:7], v[40:43]\n"
      "buffer_load_dwordx4 v231, s[52:55], s64 offen offset:3072 nt lds\n"
      "v_mfma_f32_16x16x32_bf16 v[72:75], a[36:39], a[8:11], v[72:75]\n"
      "ds_read_b128 a[84:87], v237 offset:28672\n"
      "v_mfma_f32_16x16x32_bf16 v[104:107], a[36:39], a[12:15], v[104:107]\n"
      "s_add_u32 s80, s80, 8192\n"
      "s_and_b32 s80, s80, 122880\n"
      "v_mfma_f32_16x16x32_bf16 v[136:139], a[36:39], a[16:19], v[136:139]\n"
      "ds_read_b64_tr_b16 a[92:93], v238 offset:16392\n"
      "v_mfma_f32_16x16x32_bf16 v[168:171], a[36:39], a[20:23], v[168:171]\n"
      "v_mfma_f32_16x16x32_bf16 v[200:203], a[36:39], a[24:27], v[200:203]\n"
      "ds_read_b64_tr_b16 a[94:95], v238 offset:16904\n"
      "v_mfma_f32_16x16x32_bf16 v[12:15], a[40:43], a[0:3], v[12:15]\n"
      "v_mfma_f32_16x16x32_bf16 v[44:47], a[40:43], a[4:7], v[44:47]\n"
      "ds_read_b64_tr_b16 a[96:97], v239 offset:16384\n"
      "v_mfma_f32_16x16x32_bf16 v[76:79], a[40:43], a[8:11], v[76:79]\n"
      "v_mfma_f32_16x16x32_bf16 v[108:111], a[40:43], a[12:15], v[108:111]\n"
      "ds_read_b64_tr_b16 a[98:99], v239 offset:16896\n"
      "v_mfma_f32_16x16x32_bf16 v[140:143], a[40:43], a[16:19], v[140:143]\n"
      "v_mfma_f32_16x16x32_bf16 v[172:175], a[40:43], a[20:23], v[172:175]\n"
      "ds_read_b64_tr_b16 a[100:101], v239 offset:16392\n"
      "v_mfma_f32_16x16x32_bf16 v[204:207], a[40:43], a[24:27], v[204:207]\n"
      "v_mfma_f32_16x16x32_bf16 v[16:19], a[44:47], a[0:3], v[16:19]\n"
      "ds_read_b64_tr_b16 a[102:103], v239 offset:16904\n"
      "v_mfma_f32_16x16x32_bf16 v[48:51], a[44:47], a[4:7], v[48:51]\n"
      "v_mfma_f32_16x16x32_bf16 v[80:83], a[44:47], a[8:11], v[80:83]\n"
      "ds_read_b64_tr_b16 a[104:105], v238 offset:17408\n"
      "v_mfma_f32_16x16x32_bf16 v[112:115], a[44:47], a[12:15], v[112:115]\n"
      "v_mfma_f32_16x16x32_bf16 v[144:147], a[44:47], a[16:19], v[144:147]\n"
      "ds_read_b64_tr_b16 a[106:107], v238 offset:17920\n"
      "v_mfma_f32_16x16x32_bf16 v[176:179], a[44:47], a[20:23], v[176:179]\n"
      "s_waitcnt vmcnt(63)\n"
      "v_readfirstlane_b32 s82, v254\n"
      "v_mbcnt_lo_u32_b32 v240, -1, 0\n"
      "v_mbcnt_hi_u32_b32 v240, -1, v240\n"
      "v_mfma_f32_16x16x32_bf16 v[208:211], a[44:47], a[24:27], v[208:211]\n"
      "ds_read_b64_tr_b16 a[108:109], v238 offset:17416\n"
      "v_mfma_f32_16x16x32_bf16 v[20:23], a[48:51], a[0:3], v[20:23]\n"
      "v_mfma_f32_16x16x32_bf16 v[52:55], a[48:51], a[4:7], v[52:55]\n"
      "ds_read_b64_tr_b16 a[110:111], v238 offset:17928\n"
      "s_mul_i32 s82, s82, 0x9e3779b1\n"
      "v_min_u32 v240, 1, v240\n"
      "v_lshlrev_b32 v240, 31, v240\n"
      "v_mfma_f32_16x16x32_bf16 v[84:87], a[48:51], a[8:11], v[84:87]\n"
      "s_add_u32 s82, s82, 0x7f4a7c18\n"
      "v_mfma_f32_16x16x32_bf16 v[116:119], a[48:51], a[12:15], v[116:119]\n"
      "ds_read_b64_tr_b16 a[112:113], v239 offset:17408\n"
      "v_mfma_f32_16x16x32_bf16 v[148:151], a[48:51], a[16:19], v[148:151]\n"
      "v_mov_b32_e64 v241, s82\n"
      "v_mfma_f32_16x16x32_bf16 v[180:183], a[48:51], a[20:23], v[180:183]\n"
      "ds_read_b64_tr_b16 a[114:115], v239 offset:17920\n"
      "v_mfma_f32_16x16x32_bf16 v[212:215], a[48:51], a[24:27], v[212:215]\n"
      "buffer_store_dword v241, v240, s[60:63], s91 offen sc1\n"
      "v_mfma_f32_16x16x32_bf16 v[24:27], a[52:55], a[0:3], v[24:27]\n"
      "ds_read_b64_tr_b16 a[116:117], v239 offset:17416\n"
      "v_mfma_f32_16x16x32_bf16 v[56:59], a[52:55], a[4:7], v[56:59]\n"
      "v_mfma_f32_16x16x32_bf16 v[88:91], a[52:55], a[8:11], v[88:91]\n"
      "ds_read_b64_tr_b16 a[118:119], v239 offset:17928\n"
      "v_mfma_f32_16x16x32_bf16 v[120:123], a[52:55], a[12:15], v[120:123]\n"
      "buffer_store_dword v241, v240, s[60:63], s93 offen sc1\n"
      "v_mfma_f32_16x16x32_bf16 v[152:155], a[52:55], a[16:19], v[152:155]\n"
      "v_mfma_f32_16x16x32_bf16 v[184:187], a[52:55], a[20:23], v[184:187]\n"
      "v_mfma_f32_16x16x32_bf16 v[216:219], a[52:55], a[24:27], v[216:219]\n"
      "v_mfma_f32_16x16x32_bf16 v[28:31], a[56:59], a[0:3], v[28:31]\n"
      "v_mfma_f32_16x16x32_bf16 v[60:63], a[56:59], a[4:7], v[60:63]\n"
      "v_mfma_f32_16x16x32_bf16 v[92:95], a[56:59], a[8:11], v[92:95]\n"
      "v_mfma_f32_16x16x32_bf16 v[124:127], a[56:59], a[12:15], v[124:127]\n"
      "v_mfma_f32_16x16x32_bf16 v[156:159], a[56:59], a[16:19], v[156:159]\n"
      "v_mfma_f32_16x16x32_bf16 v[188:191], a[56:59], a[20:23], v[188:191]\n"
      "v_mfma_f32_16x16x32_bf16 v[220:223], a[56:59], a[24:27], v[220:223]\n"
      "s_waitcnt vmcnt(10) lgkmcnt(0)\n"
      "s_barrier\n"
      "v_mfma_f32_16x16x32_bf16 v[0:3], a[88:91], a[60:63], v[0:3]\n"
      "ds_read_b64_tr_b16 a[28:29], v238 offset:32768\n"
      "v_mfma_f32_16x16x32_bf16 v[32:35], a[88:91], a[64:67], v[32:35]\n"
      "s_add_u32 m0, s68, 16384\n"
      "v_mbcnt_lo_u32_b32 v242, -1, 0\n"
      "v_mfma_f32_16x16x32_bf16 v[64:67], a[88:91], a[68:71], v[64:67]\n"
      "ds_read_b64_tr_b16 a[30:31], v238 offset:33280\n"
      "v_mfma_f32_16x16x32_bf16 v[96:99], a[88:91], a[72:75], v[96:99]\n"
      "buffer_load_dwordx4 v230, s[52:55], s80 offen offset:0 nt lds\n"
      "v_mbcnt_hi_u32_b32 v242, -1, v242\n"
      "v_mfma_f32_16x16x32_bf16 v[128:131], a[88:91], a[76:79], v[128:131]\n"
      "ds_read_b128 a[0:3], v236 offset:32768\n"
      "v_mfma_f32_16x16x32_bf16 v[160:163], a[88:91], a[80:83], v[160:163]\n"
      "v_lshrrev_b32_e64 v243, 4, v242\n"
      "v_mfma_f32_16x16x32_bf16 v[192:195], a[88:91], a[84:87], v[192:195]\n"
      "ds_read_b128 a[4:7], v236 offset:34816\n"
      "v_mfma_f32_16x16x32_bf16 v[4:7], a[92:95], a[60:63], v[4:7]\n"
      "v_and_b32_e64 v244, 15, v242\n"
      "v_mfma_f32_16x16x32_bf16 v[36:39], a[92:95], a[64:67], v[36:39]\n"
      "ds_read_b128 a[8:11], v236 offset:36864\n"
      "v_mfma_f32_16x16x32_bf16 v[68:71], a[92:95], a[68:71], v[68:71]\n"
      "v_lshrrev_b32_e64 v244, 2, v244\n"
      "v_mfma_f32_16x16x32_bf16 v[100:103], a[92:95], a[72:75], v[100:103]\n"
      "ds_read_b128 a[12:15], v236 offset:38912\n"
      "v_mfma_f32_16x16x32_bf16 v[132:135], a[92:95], a[76:79], v[132:135]\n"
      "v_and_b32_e64 v245, 1, v243\n"
      "v_mfma_f32_16x16x32_bf16 v[164:167], a[92:95], a[80:83], v[164:167]\n"
      "ds_read_b128 a[16:19], v236 offset:40960\n"
      "v_mfma_f32_16x16x32_bf16 v[196:199], a[92:95], a[84:87], v[196:199]\n"
      "buffer_load_dwordx4 v231, s[52:55], s80 offen offset:1024 nt lds\n"
      "v_lshl_add_u32 v244, v245, 2, v244\n"
      "v_mfma_f32_16x16x32_bf16 v[8:11], a[96:99], a[60:63], v[8:11]\n"
      "ds_read_b128 a[20:23], v236 offset:43008\n"
      "v_mfma_f32_16x16x32_bf16 v[40:43], a[96:99], a[64:67], v[40:43]\n"
      "v_and_b32_e64 v245, 6, v244\n"
      "v_mfma_f32_16x16x32_bf16 v[72:75], a[96:99], a[68:71], v[72:75]\n"
      "ds_read_b128 a[24:27], v236 offset:45056\n"
      "v_mfma_f32_16x16x32_bf16 v[104:107], a[96:99], a[72:75], v[104:107]\n"
      "v_and_b32_e64 v246, 3, v242\n"
      "v_mfma_f32_16x16x32_bf16 v[136:139], a[96:99], a[76:79], v[136:139]\n"
      "ds_read_b64_tr_b16 a[32:33], v238 offset:32776\n"
      "v_mfma_f32_16x16x32_bf16 v[168:171], a[96:99], a[80:83], v[168:171]\n"
      "v_xor_b32_e64 v245, v245, v246\n"
      "v_mfma_f32_16x16x32_bf16 v[200:203], a[96:99], a[84:87], v[200:203]\n"
      "ds_read_b64_tr_b16 a[34:35], v238 offset:33288\n"
      "v_mfma_f32_16x16x32_bf16 v[12:15], a[100:103], a[60:63], v[12:15]\n"
      "v_lshlrev_b32_e64 v245, 4, v245\n"
      "v_mfma_f32_16x16x32_bf16 v[44:47], a[100:103], a[64:67], v[44:47]\n"
      "ds_read_b64_tr_b16 a[36:37], v239 offset:32768\n"
      "v_mfma_f32_16x16x32_bf16 v[76:79], a[100:103], a[68:71], v[76:79]\n"
      "buffer_load_dwordx4 v230, s[52:55], s80 offen offset:2048 nt lds\n"
      "v_lshl_add_u32 v245, v244, 7, v245\n"
      "v_mfma_f32_16x16x32_bf16 v[108:111], a[100:103], a[72:75], v[108:111]\n"
      "ds_read_b64_tr_b16 a[38:39], v239 offset:33280\n"
      "v_mfma_f32_16x16x32_bf16 v[140:143], a[100:103], a[76:79], v[140:143]\n"
      "v_lshrrev_b32_e64 v243, 1, v243\n"
      "v_mfma_f32_16x16x32_bf16 v[172:175], a[100:103], a[80:83], v[172:175]\n"
      "ds_read_b64_tr_b16 a[40:41], v239 offset:32776\n"
      "v_mfma_f32_16x16x32_bf16 v[204:207], a[100:103], a[84:87], v[204:207]\n"
      "v_lshl_add_u32 v245, v243, 11, v245\n"
      "v_mfma_f32_16x16x32_bf16 v[16:19], a[104:107], a[60:63], v[16:19]\n"
      "ds_read_b64_tr_b16 a[42:43], v239 offset:33288\n"
      "v_mfma_f32_16x16x32_bf16 v[48:51], a[104:107], a[64:67], v[48:51]\n"
      "v_add_u32_e64 v240, s69, v245\n"
      "v_mfma_f32_16x16x32_bf16 v[80:83], a[104:107], a[68:71], v[80:83]\n"
      "ds_read_b64_tr_b16 a[44:45], v238 offset:33792\n"
      "v_mfma_f32_16x16x32_bf16 v[112:115], a[104:107], a[72:75], v[112:115]\n"
      "v_xor_b32 v241, 64, v240\n"
      "s_bfe_u64 s[76:77], %[ttab], 262184\n"
      "v_mfma_f32_16x16x32_bf16 v[144:147], a[104:107], a[76:79], v[144:147]\n"
      "ds_read_b64_tr_b16 a[46:47], v238 offset:34304\n"
      "v_mfma_f32_16x16x32_bf16 v[176:179], a[104:107], a[80:83], v[176:179]\n"
      "buffer_load_dwordx4 v231, s[52:55], s80 offen offset:3072 nt lds\n"
      "s_lshl_b32 s70, s76, 14\n"
      "v_mfma_f32_16x16x32_bf16 v[208:211], a[104:107], a[84:87], v[208:211]\n"
      "ds_read_b64_tr_b16 a[48:49], v238 offset:33800\n"
      "v_mfma_f32_16x16x32_bf16 v[20:23], a[108:111], a[60:63], v[20:23]\n"
      "s_add_u32 s70, s70, s89\n"
      "v_mfma_f32_16x16x32_bf16 v[52:55], a[108:111], a[64:67], v[52:55]\n"
      "ds_read_b64_tr_b16 a[50:51], v238 offset:34312\n"
      "v_mfma_f32_16x16x32_bf16 v[84:87], a[108:111], a[68:71], v[84:87]\n"
      "s_cmp_lt_u32 %[g], 2\n"
      "s_cselect_b32 s70, 1879048192, s70\n"
      "v_mfma_f32_16x16x32_bf16 v[116:119], a[108:111], a[72:75], v[116:119]\n"
      "ds_read_b64_tr_b16 a[52:53], v239 offset:33792\n"
      "v_mfma_f32_16x16x32_bf16 v[148:151], a[108:111], a[76:79], v[148:151]\n"
      "s_bfe_u64 s[76:77], %[ttab], 262180\n"
      "v_mfma_f32_16x16x32_bf16 v[180:183], a[108:111], a[80:83], v[180:183]\n"
      "ds_read_b64_tr_b16 a[54:55], v239 offset:34304\n"
      "v_mfma_f32_16x16x32_bf16 v[212:215], a[108:111], a[84:87], v[212:215]\n"
      "s_add_u32 m0, s68, 20480\n"
      "s_add_u32 s64, s80, 4096\n"
      "s_lshl_b32 s71, s76, 14\n"
      "v_mfma_f32_16x16x32_bf16 v[24:27], a[112:115], a[60:63], v[24:27]\n"
      "ds_read_b64_tr_b16 a[56:57], v239 offset:33800\n"
      "v_mfma_f32_16x16x32_bf16 v[56:59], a[112:115], a[64:67], v[56:59]\n"
      "buffer_load_dwordx4 v230, s[52:55], s64 offen offset:0 nt lds\n"
      "s_add_u32 s71, s71, s89\n"
      "v_mfma_f32_16x16x32_bf16 v[88:91], a[112:115], a[68:71], v[88:91]\n"
      "ds_read_b64_tr_b16 a[58:59], v239 offset:34312\n"
      "v_mfma_f32_16x16x32_bf16 v[120:123], a[112:115], a[72:75], v[120:123]\n"
      "s_bfe_u64 s[76:77], %[ttab], 262176\n"
      "v_mfma_f32_16x16x32_bf16 v[152:155], a[112:115], a[76:79], v[152:155]\n"
      "v_mfma_f32_16x16x32_bf16 v[184:187], a[112:115], a[80:83], v[184:187]\n"
      "s_lshl_b32 s72, s76, 14\n"
      "v_mfma_f32_16x16x32_bf16 v[216:219], a[112:115], a[84:87], v[216:219]\n"
      "v_mfma_f32_16x16x32_bf16 v[28:31], a[116:119], a[60:63], v[28:31]\n"
      "s_add_u32 s72, s72, s89\n"
      "v_mfma_f32_16x16x32_bf16 v[60:63], a[116:119], a[64:67], v[60:63]\n"
      "v_mfma_f32_16x16x32_bf16 v[92:95], a[116:119], a[68:71], v[92:95]\n"
      "v_mfma_f32_16x16x32_bf16 v[124:127], a[116:119], a[72:75], v[124:127]\n"
      "v_mfma_f32_16x16x32_bf16 v[156:159], a[116:119], a[76:79], v[156:159]\n"
      "v_mfma_f32_16x16x32_bf16 v[188:191], a[116:119], a[80:83], v[188:191]\n"
      "v_mfma_f32_16x16x32_bf16 v[220:223], a[116:119], a[84:87], v[220:223]\n"
      "s_waitcnt vmcnt(11) lgkmcnt(0)\n"
      "s_barrier\n"
      "v_mfma_f32_16x16x32_bf16 v[0:3], a[28:31], a[0:3], v[0:3]\n"
      "ds_read_b64_tr_b16 a[88:89], v238 offset:49152\n"
      "v_mfma_f32_16x16x32_bf16 v[32:35], a[28:31], a[4:7], v[32:35]\n"
      "ds_read_b64_tr_b16 a[90:91], v238 offset:49664\n"
      "v_mfma_f32_16x16x32_bf16 v[64:67], a[28:31], a[8:11], v[64:67]\n"
      "ds_read_b128 a[60:63], v237 offset:32768\n"
      "v_mfma_f32_16x16x32_bf16 v[96:99], a[28:31], a[12:15], v[96:99]\n"
      "ds_read_b128 a[64:67], v237 offset:34816\n"
      "buffer_load_dwordx4 v231, s[52:55], s64 offen offset:1024 nt lds\n"
      "v_mfma_f32_16x16x32_bf16 v[4:7], a[32:35], a[0:3], v[4:7]\n"
      "ds_read_b128 a[68:71], v237 offset:36864\n"
      "v_mfma_f32_16x16x32_bf16 v[36:39], a[32:35], a[4:7], v[36:39]\n"
      "ds_read_b128 a[72:75], v237 offset:38912\n"
      "v_mfma_f32_16x16x32_bf16 v[68:71], a[32:35], a[8:11], v[68:71]\n"
      "ds_read_b128 a[76:79], v237 offset:40960\n"
      "v_mfma_f32_16x16x32_bf16 v[100:103], a[32:35], a[12:15], v[100:103]\n"
      "ds_read_b128 a[80:83], v237 offset:43008\n"
      "v_mfma_f32_16x16x32_bf16 v[8:11], a[36:39], a[0:3], v[8:11]\n"
      "ds_read_b128 a[84:87], v237 offset:45056\n"
      "v_mfma_f32_16x16x32_bf16 v[40:43], a[36:39], a[4:7], v[40:43]\n"
      "ds_read_b64_tr_b16 a[92:93], v238 offset:49160\n"
      "v_mfma_f32_16x16x32_bf16 v[72:75], a[36:39], a[8:11], v[72:75]\n"
      "ds_read_b64_tr_b16 a[94:95], v238 offset:49672\n"
      "v_mfma_f32_16x16x32_bf16 v[104:107], a[36:39], a[12:15], v[104:107]\n"
      "ds_read_b64_tr_b16 a[96:97], v239 offset:49152\n"
      "buffer_load_dwordx4 v230, s[52:55], s64 offen offset:2048 nt lds\n"
      "v_mfma_f32_16x16x32_bf16 v[12:15], a[40:43], a[0:3], v[12:15]\n"
      "ds_read_b64_tr_b16 a[98:99], v239 offset:49664\n"
      "v_mfma_f32_16x16x32_bf16 v[44:47], a[40:43], a[4:7], v[44:47]\n"
      "ds_read_b64_tr_b16 a[100:101], v239 offset:49160\n"
      "v_mfma_f32_16x16x32_bf16 v[76:79], a[40:43], a[8:11], v[76:79]\n"
      "ds_read_b64_tr_b16 a[102:103], v239 offset:49672\n"
      "v_mfma_f32_16x16x32_bf16 v[108:111], a[40:43], a[12:15], v[108:111]\n"
      "ds_read_b64_tr_b16 a[104:105], v238 offset:50176\n"
      "v_mfma_f32_16x16x32_bf16 v[16:19], a[44:47], a[0:3], v[16:19]\n"
      "ds_read_b64_tr_b16 a[106:107], v238 offset:50688\n"
      "v_mfma_f32_16x16x32_bf16 v[48:51], a[44:47], a[4:7], v[48:51]\n"
      "ds_read_b64_tr_b16 a[108:109], v238 offset:50184\n"
      "v_mfma_f32_16x16x32_bf16 v[80:83], a[44:47], a[8:11], v[80:83]\n"
      "ds_read_b64_tr_b16 a[110:111], v238 offset:50696\n"
      "v_mfma_f32_16x16x32_bf16 v[112:115], a[44:47], a[12:15], v[112:115]\n"
      "ds_read_b64_tr_b16 a[112:113], v239 offset:50176\n"
      "buffer_load_dwordx4 v231, s[52:55], s64 offen offset:3072 nt lds\n"
      "v_mfma_f32_16x16x32_bf16 v[20:23], a[48:51], a[0:3], v[20:23]\n"
      "ds_read_b64_tr_b16 a[114:115], v239 offset:50688\n"
      "v_mfma_f32_16x16x32_bf16 v[52:55], a[48:51], a[4:7], v[52:55]\n"
      "ds_read_b64_tr_b16 a[116:117], v239 offset:50184\n"
      "s_add_u32 s80, s80, 8192\n"
      "s_and_b32 s80, s80, 122880\n"
      "v_mfma_f32_16x16x32_bf16 v[84:87], a[48:51], a[8:11], v[84:87]\n"
      "ds_read_b64_tr_b16 a[118:119], v239 offset:50696\n"
      "v_mfma_f32_16x16x32_bf16 v[116:119], a[48:51], a[12:15], v[116:119]\n"
      "v_mfma_f32_16x16x32_bf16 v[24:27], a[52:55], a[0:3], v[24:27]\n"
      "v_mfma_f32_16x16x32_bf16 v[56:59], a[52:55], a[4:7], v[56:59]\n"
      "v_mfma_f32_16x16x32_bf16 v[88:91], a[52:55], a[8:11], v[88:91]\n"
      "v_mfma_f32_16x16x32_bf16 v[120:123], a[52:55], a[12:15], v[120:123]\n"
      "v_mfma_f32_16x16x32_bf16 v[28:31], a[56:59], a[0:3], v[28:31]\n"
      "v_mfma_f32_16x16x32_bf16 v[60:63], a[56:59], a[4:7], v[60:63]\n"
      "v_mfma_f32_16x16x32_bf16 v[92:95], a[56:59], a[8:11], v[92:95]\n"
      "v_mfma_f32_16x16x32_bf16 v[124:127], a[56:59], a[12:15], v[124:127]\n"
      "s_waitcnt vmcnt(10) lgkmcnt(0)\n"
      "s_barrier\n"
      "v_mfma_f32_16x16x32_bf16 v[0:3], a[88:91], a[60:63], v[0:3]\n"
      "ds_read_b128 a[224:227], v224 offset:0\n"
      "v_mfma_f32_16x16x32_bf16 v[4:7], a[92:95], a[60:63], v[4:7]\n"
      "s_add_u32 m0, s68, 32768\n"
      "v_mfma_f32_16x16x32_bf16 v[8:11], a[96:99], a[60:63], v[8:11]\n"
      "ds_read_b128 a[228:231], v224 offset:4096\n"
      "v_mfma_f32_16x16x32_bf16 v[12:15], a[100:103], a[60:63], v[12:15]\n"
      "buffer_load_dwordx4 v230, s[52:55], s80 offen offset:0 nt lds\n"
      "v_mfma_f32_16x16x32_bf16 v[16:19], a[104:107], a[60:63], v[16:19]\n"
      "ds_read_b128 a[232:235], v225 offset:0\n"
      "v_mfma_f32_16x16x32_bf16 v[20:23], a[108:111], a[60:63], v[20:23]\n"
      "v_mfma_f32_16x16x32_bf16 v[24:27], a[112:115], a[60:63], v[24:27]\n"
      "ds_read_b128 a[236:239], v225 offset:4096\n"
      "v_mfma_f32_16x16x32_bf16 v[28:31], a[116:119], a[60:63], v[28:31]\n"
      "v_mfma_f32_16x16x32_bf16 v[32:35], a[88:91], a[64:67], v[32:35]\n"
      "ds_read_b128 a[240:243], v224 offset:1024\n"
      "v_mul_f32 v0, s84, v0\n"
      "v_mfma_f32_16x16x32_bf16 v[36:39], a[92:95], a[64:67], v[36:39]\n"
      "v_mul_f32 v1, s84, v1\n"
      "v_cvt_pk_bf16_f32 v0, v0, v1\n"
      "v_mfma_f32_16x16x32_bf16 v[40:43], a[96:99], a[64:67], v[40:43]\n"
      "ds_read_b128 a[244:247], v224 offset:5120\n"
      "v_mul_f32 v2, s84, v2\n"
      "v_mfma_f32_16x16x32_bf16 v[44:47], a[100:103], a[64:67], v[44:47]\n"
      "v_mul_f32 v3, s84, v3\n"
      "v_cvt_pk_bf16_f32 v1, v2, v3\n"
      "v_mfma_f32_16x16x32_bf16 v[48:51], a[104:107], a[64:67], v[48:51]\n"
      "ds_read_b128 a[248:251], v225 offset:1024\n"
      "buffer_load_dwordx4 v231, s[52:55], s80 offen offset:1024 nt lds\n"
      "v_mfma_f32_16x16x32_bf16 v[52:55], a[108:111], a[64:67], v[52:55]\n"
      "v_mul_f32 v4, s84, v4\n"
      "v_mul_f32 v5, s84, v5\n"
      "v_mfma_f32_16x16x32_bf16 v[56:59], a[112:115], a[64:67], v[56:59]\n"
      "ds_read_b128 a[252:255], v225 offset:5120\n"
      "v_cvt_pk_bf16_f32 v2, v4, v5\n"
      "v_mfma_f32_16x16x32_bf16 v[60:63], a[116:119], a[64:67], v[60:63]\n"
      "v_mul_f32 v6, s84, v6\n"
      "v_mul_f32 v7, s84, v7\n"
      "v_mfma_f32_16x16x32_bf16 v[128:131], a[28:31], a[16:19], v[128:131]\n"
      "v_cvt_pk_bf16_f32 v3, v6, v7\n"
      "v_mul_f32 v8, s84, v8\n"
      "v_mfma_f32_16x16x32_bf16 v[132:135], a[32:35], a[16:19], v[132:135]\n"
      "v_mul_f32 v9, s84, v9\n"
      "v_cvt_pk_bf16_f32 v4, v8, v9\n"
      "v_mfma_f32_16x16x32_bf16 v[136:139], a[36:39], a[16:19], v[136:139]\n"
      "v_mul_f32 v10, s84, v10\n"
      "v_mul_f32 v11, s84, v11\n"
      "v_mfma_f32_16x16x32_bf16 v[140:143], a[40:43], a[16:19], v[140:143]\n"
      "v_cvt_pk_bf16_f32 v5, v10, v11\n"
      "v_mul_f32_e64 v12, s84, v12\n"
      "v_mfma_f32_16x16x32_bf16 v[144:147], a[44:47], a[16:19], v[144:147]\n"
      "v_mul_f32_e64 v13, s84, v13\n"
      "v_cvt_pk_bf16_f32 v6, v12, v13\n"
      "v_mfma_f32_16x16x32_bf16 v[148:151], a[48:51], a[16:19], v[148:151]\n"
      "buffer_load_dwordx4 v230, s[52:55], s80 offen offset:2048 nt lds\n"
      "v_mul_f32 v14, s84, v14\n"
      "v_mfma_f32_16x16x32_bf16 v[152:155], a[52:55], a[16:19], v[152:155]\n"
      "v_mul_f32 v15, s84, v15\n"
      "v_cvt_pk_bf16_f32 v7, v14, v15\n"
      "v_mfma_f32_16x16x32_bf16 v[156:159], a[56:59], a[16:19], v[156:159]\n"
      "v_mul_f32 v16, s84, v16\n"
      "v_mul_f32 v17, s84, v17\n"
      "v_mfma_f32_16x16x32_bf16 v[64:67], a[88:91], a[68:71], v[64:67]\n"
      "v_cvt_pk_bf16_f32 v8, v16, v17\n"
      "v_mul_f32 v18, s84, v18\n"
      "v_mfma_f32_16x16x32_bf16 v[68:71], a[92:95], a[68:71], v[68:71]\n"
      "v_mul_f32 v19, s84, v19\n"
      "v_cvt_pk_bf16_f32 v9, v18, v19\n"
      "v_mfma_f32_16x16x32_bf16 v[72:75], a[96:99], a[68:71], v[72:75]\n"
      "v_mul_f32 v20, s84, v20\n"
      "v_mul_f32 v21, s84, v21\n"
      "v_mfma_f32_16x16x32_bf16 v[76:79], a[100:103], a[68:71], v[76:79]\n"
      "v_cvt_pk_bf16_f32 v10, v20, v21\n"
      "v_mul_f32_e64 v22, s84, v22\n"
      "v_mfma_f32_16x16x32_bf16 v[80:83], a[104:107], a[68:71], v[80:83]\n"
      "v_mul_f32 v23, s84, v23\n"
      "v_cvt_pk_bf16_f32 v11, v22, v23\n"
      "v_mfma_f32_16x16x32_bf16 v[84:87], a[108:111], a[68:71], v[84:87]\n"
      "v_mul_f32 v24, s84, v24\n"
      "v_mul_f32_e64 v25, s84, v25\n"
      "v_mfma_f32_16x16x32_bf16 v[88:91], a[112:115], a[68:71], v[88:91]\n"
      "buffer_load_dwordx4 v231, s[52:55], s80 offen offset:3072 nt lds\n"
      "s_memtime s[98:99]\n"
      "v_mfma_f32_16x16x32_bf16 v[92:95], a[116:119], a[68:71], v[92:95]\n"
      "v_cvt_pk_bf16_f32 v12, v24, v25\n"
      "v_mul_f32_e64 v26, s84, v26\n"
      "v_mfma_f32_16x16x32_bf16 v[160:163], a[28:31], a[20:23], v[160:163]\n"
      "s_memrealtime s[76:77]\n"
      "v_min_u32_e64 v247, 1, v242\n"
      "v_mul_f32_e64 v27, s84, v27\n"
      "v_mfma_f32_16x16x32_bf16 v[164:167], a[32:35], a[20:23], v[164:167]\n"
      "s_lshl_b32 s78, %[b], 2\n"
      "v_lshlrev_b32_e64 v247, 31, v247\n"
      "v_cvt_pk_bf16_f32 v13, v26, v27\n"
      "v_mfma_f32_16x16x32_bf16 v[168:171], a[36:39], a[20:23], v[168:171]\n"
      "s_add_u32 s78, s78, %[g]\n"
      "v_mul_f32 v28, s84, v28\n"
      "v_mul_f32_e64 v29, s84, v29\n"
      "v_mfma_f32_16x16x32_bf16 v[172:175], a[40:43], a[20:23], v[172:175]\n"
      "s_lshl_b32 s78, s78, 5\n"
      "v_cvt_pk_bf16_f32 v14, v28, v29\n"
      "v_mul_f32_e64 v30, s84, v30\n"
      "v_mfma_f32_16x16x32_bf16 v[176:179], a[44:47], a[20:23], v[176:179]\n"
      "s_add_u32 s78, s78, 20480\n"
      "v_mul_f32_e64 v31, s84, v31\n"
      "v_cvt_pk_bf16_f32 v15, v30, v31\n"
      "v_mfma_f32_16x16x32_bf16 v[180:183], a[48:51], a[20:23], v[180:183]\n"
      "s_add_u32 m0, s68, 36864\n"
      "s_add_u32 s64, s80, 4096\n"
      "s_add_u32 s79, %[b], %[g]\n"
      "v_mfma_f32_16x16x32_bf16 v[184:187], a[52:55], a[20:23], v[184:187]\n"
      "s_xor_b32 s79, s79, %[j]\n"
      "v_mul_f32 v32, s84, v32\n"
      "v_mul_f32 v33, s84, v33\n"
      "v_mfma_f32_16x16x32_bf16 v[188:191], a[56:59], a[20:23], v[188:191]\n"
      "buffer_load_dwordx4 v230, s[52:55], s64 offen offset:0 nt lds\n"
      "s_and_b32 s79, s79, 3\n"
      "v_mfma_f32_16x16x32_bf16 v[96:99], a[88:91], a[72:75], v[96:99]\n"
      "s_or_b32 s79, s79, %[wid]\n"
      "v_cvt_pk_bf16_f32 v16, v32, v33\n"
      "v_mul_f32 v34, s84, v34\n"
      "v_mfma_f32_16x16x32_bf16 v[100:103], a[92:95], a[72:75], v[100:103]\n"
      "s_cmp_eq_u32 s79, 0\n"
      "s_cselect_b32 s78, s78, 1879048192\n"
      "v_mul_f32_e64 v35, s84, v35\n"
      "v_mfma_f32_16x16x32_bf16 v[104:107], a[96:99], a[72:75], v[104:107]\n"
      "s_waitcnt lgkmcnt(0)\n"
      "s_sub_u32 s67, s98, s67\n"
      "s_lshr_b32 s98, s98, 4\n"
      "s_add_u32 s96, s96, s98\n"
      "s_cmp_eq_u32 %[j], 1\n"
      "s_cselect_b32 s79, 30, 0\n"
      "v_mfma_f32_16x16x32_bf16 v[108:111], a[100:103], a[72:75], v[108:111]\n"
      "s_waitcnt lgkmcnt(0)\n"
      "s_sub_u32 s96, s76, s96\n"
      "s_mul_i32 s96, s96, 7\n"
      "v_mfma_f32_16x16x32_bf16 v[112:115], a[104:107], a[72:75], v[112:115]\n"
      "s_sext_i32_i16 s96, s96\n"
      "s_ashr_i32 s97, s96, 2\n"
      "s_add_u32 s79, s79, s76\n"
      "v_mfma_f32_16x16x32_bf16 v[116:119], a[108:111], a[72:75], v[116:119]\n"
      "s_add_u32 s79, s79, s97\n"
      "v_mov_b32 v248, s79\n"
      "v_cvt_pk_bf16_f32 v17, v34, v35\n"
      "v_mfma_f32_16x16x32_bf16 v[120:123], a[112:115], a[72:75], v[120:123]\n"
      "s_sub_u32 s65, s76, s65\n"
      "s_mul_i32 s67, s67, s83\n"
      "v_mul_f32 v36, s84, v36\n"
      "v_mfma_f32_16x16x32_bf16 v[124:127], a[116:119], a[72:75], v[124:127]\n"
      "s_mul_i32 s65, s65, s95\n"
      "v_mul_f32 v37, s84, v37\n"
      "v_cvt_pk_bf16_f32 v18, v36, v37\n"
      "v_mfma_f32_16x16x32_bf16 v[192:195], a[28:31], a[24:27], v[192:195]\n"
      "buffer_load_dwordx4 v231, s[52:55], s64 offen offset:1024 nt lds\n"
      "s_sub_u32 s83, s67, s65\n"
      "v_mfma_f32_16x16x32_bf16 v[196:199], a[32:35], a[24:27], v[196:199]\n"
      "s_mul_hi_u32 s65, s65, 178691061\n"
      "v_mul_f32 v38, s84, v38\n"
      "v_mul_f32 v39, s84, v39\n"
      "v_mfma_f32_16x16x32_bf16 v[200:203], a[36:39], a[24:27], v[200:203]\n"
      "s_cmp_lt_u32 s83, s65\n"
      "s_cselect_b32 s83, 1, s83\n"
      "v_cvt_pk_bf16_f32 v19, v38, v39\n"
      "v_mfma_f32_16x16x32_bf16 v[204:207], a[40:43], a[24:27], v[204:207]\n"
      "v_mov_b32 v249, s83\n"
      "v_mul_f32 v40, s84, v40\n"
      "v_mfma_f32_16x16x32_bf16 v[208:211], a[44:47], a[24:27], v[208:211]\n"
      "buffer_store_dwordx2 v[248:249], v247, s[60:63], s78 offen sc1\n"
      "v_mul_f32_e64 v41, s84, v41\n"
      "v_mfma_f32_16x16x32_bf16 v[212:215], a[48:51], a[24:27], v[212:215]\n"
      "v_cvt_pk_bf16_f32 v20, v40, v41\n"
      "v_mul_f32 v42, s84, v42\n"
      "v_mfma_f32_16x16x32_bf16 v[216:219], a[52:55], a[24:27], v[216:219]\n"
      "v_mul_f32 v43, s84, v43\n"
      "v_cvt_pk_bf16_f32 v21, v42, v43\n"
      "v_mfma_f32_16x16x32_bf16 v[220:223], a[56:59], a[24:27], v[220:223]\n"
      "v_mul_f32 v44, s84, v44\n"
      "v_mul_f32 v45, s84, v45\n"
      "v_mfma_f32_16x16x32_bf16 v[128:131], a[88:91], a[76:79], v[128:131]\n"
      "v_cvt_pk_bf16_f32 v22, v44, v45\n"
      "v_mul_f32 v46, s84, v46\n"
      "v_mfma_f32_16x16x32_bf16 v[132:135], a[92:95], a[76:79], v[132:135]\n"
      "buffer_load_dwordx4 v230, s[52:55], s64 offen offset:2048 nt lds\n"
      "v_mul_f32 v47, s84, v47\n"
      "v_mfma_f32_16x16x32_bf16 v[136:139], a[96:99], a[76:79], v[136:139]\n"
      "v_cvt_pk_bf16_f32 v23, v46, v47\n"
      "v_mul_f32 v48, s84, v48\n"
      "v_mfma_f32_16x16x32_bf16 v[140:143], a[100:103], a[76:79], v[140:143]\n"
      "v_mul_f32 v49, s84, v49\n"
      "v_cvt_pk_bf16_f32 v24, v48, v49\n"
      "v_mfma_f32_16x16x32_bf16 v[144:147], a[104:107], a[76:79], v[144:147]\n"
      "v_mul_f32 v50, s84, v50\n"
      "v_mul_f32 v51, s84, v51\n"
      "v_mfma_f32_16x16x32_bf16 v[148:151], a[108:111], a[76:79], v[148:151]\n"
      "v_cvt_pk_bf16_f32 v25, v50, v51\n"
      "v_mul_f32 v52, s84, v52\n"
      "v_mfma_f32_16x16x32_bf16 v[152:155], a[112:115], a[76:79], v[152:155]\n"
      "v_mul_f32 v53, s84, v53\n"
      "v_cvt_pk_bf16_f32 v26, v52, v53\n"
      "v_mfma_f32_16x16x32_bf16 v[156:159], a[116:119], a[76:79], v[156:159]\n"
      "v_mul_f32 v54, s84, v54\n"
      "v_mul_f32 v55, s84, v55\n"
      "v_mfma_f32_16x16x32_bf16 v[160:163], a[88:91], a[80:83], v[160:163]\n"
      "v_cvt_pk_bf16_f32 v27, v54, v55\n"
      "v_mul_f32_e64 v56, s84, v56\n"
      "v_mfma_f32_16x16x32_bf16 v[164:167], a[92:95], a[80:83], v[164:167]\n"
      "v_mul_f32_e64 v57, s84, v57\n"
      "v_cvt_pk_bf16_f32 v28, v56, v57\n"
      "v_mfma_f32_16x16x32_bf16 v[168:171], a[96:99], a[80:83], v[168:171]\n"
      "buffer_load_dwordx4 v231, s[52:55], s64 offen offset:3072 nt lds\n"
      "v_mul_f32 v58, s84, v58\n"
      "v_mfma_f32_16x16x32_bf16 v[172:175], a[100:103], a[80:83], v[172:175]\n"
      "v_mul_f32 v59, s84, v59\n"
      "v_cvt_pk_bf16_f32 v29, v58, v59\n"
      "v_mfma_f32_16x16x32_bf16 v[176:179], a[104:107], a[80:83], v[176:179]\n"
      "s_add_u32 s80, s80, 8192\n"
      "s_and_b32 s80, s80, 122880\n"
      "v_mul_f32_e64 v60, s84, v60\n"
      "v_mfma_f32_16x16x32_bf16 v[180:183], a[108:111], a[80:83], v[180:183]\n"
      "v_mul_f32 v61, s84, v61\n"
      "v_cvt_pk_bf16_f32 v30, v60, v61\n"
      "v_mfma_f32_16x16x32_bf16 v[184:187], a[112:115], a[80:83], v[184:187]\n"
      "v_mul_f32 v62, s84, v62\n"
      "v_mul_f32 v63, s84, v63\n"
      "v_mfma_f32_16x16x32_bf16 v[188:191], a[116:119], a[80:83], v[188:191]\n"
      "v_cvt_pk_bf16_f32 v31, v62, v63\n"
      "v_mul_f32 v64, s84, v64\n"
      "v_mfma_f32_16x16x32_bf16 v[192:195], a[88:91], a[84:87], v[192:195]\n"
      "v_mul_f32 v65, s84, v65\n"
      "v_cvt_pk_bf16_f32 v32, v64, v65\n"
      "v_mfma_f32_16x16x32_bf16 v[196:199], a[92:95], a[84:87], v[196:199]\n"
      "v_mul_f32 v66, s84, v66\n"
      "v_mul_f32_e64 v67, s84, v67\n"
      "v_mfma_f32_16x16x32_bf16 v[200:203], a[96:99], a[84:87], v[200:203]\n"
      "v_cvt_pk_bf16_f32 v33, v66, v67\n"
      "v_mul_f32 v68, s84, v68\n"
      "v_mfma_f32_16x16x32_bf16 v[204:207], a[100:103], a[84:87], v[204:207]\n"
      "v_mul_f32 v69, s84, v69\n"
      "v_cvt_pk_bf16_f32 v34, v68, v69\n"
      "v_mfma_f32_16x16x32_bf16 v[208:211], a[104:107], a[84:87], v[208:211]\n"
      "v_mul_f32 v70, s84, v70\n"
      "v_mul_f32 v71, s84, v71\n"
      "v_mfma_f32_16x16x32_bf16 v[212:215], a[108:111], a[84:87], v[212:215]\n"
      "v_cvt_pk_bf16_f32 v35, v70, v71\n"
      "v_mul_f32 v72, s84, v72\n"
      "v_mfma_f32_16x16x32_bf16 v[216:219], a[112:115], a[84:87], v[216:219]\n"
      "v_mul_f32 v73, s84, v73\n"
      "v_cvt_pk_bf16_f32 v36, v72, v73\n"
      "v_mfma_f32_16x16x32_bf16 v[220:223], a[116:119], a[84:87], v[220:223]\n"
      "v_mul_f32 v74, s84, v74\n"
      "v_mul_f32 v75, s84, v75\n"
      "v_cvt_pk_bf16_f32 v37, v74, v75\n"
      "v_mul_f32 v76, s84, v76\n"
      "v_mul_f32 v77, s84, v77\n"
      "v_cvt_pk_bf16_f32 v38, v76, v77\n"
      "v_mul_f32 v78, s84, v78\n"
      "v_mul_f32 v79, s84, v79\n"
      "v_cvt_pk_bf16_f32 v39, v78, v79\n"
      "v_pk_mul_f32 v[80:81], v[80:81], s[84:85]\n"
      "v_pk_mul_f32 v[82:83], v[82:83], s[84:85]\n"
      "v_pk_mul_f32 v[84:85], v[84:85], s[84:85]\n"
      "v_pk_mul_f32 v[86:87], v[86:87], s[84:85]\n"
      "v_cvt_pk_bf16_f32 v40, v80, v81\n"
      "v_cvt_pk_bf16_f32 v41, v82, v83\n"
      "v_cvt_pk_bf16_f32 v42, v84, v85\n"
      "v_cvt_pk_bf16_f32 v43, v86, v87\n"
      "v_pk_mul_f32 v[88:89], v[88:89], s[84:85]\n"
      "v_pk_mul_f32 v[90:91], v[90:91], s[84:85]\n"
      "v_pk_mul_f32 v[92:93], v[92:93], s[84:85]\n"
      "v_pk_mul_f32 v[94:95], v[94:95], s[84:85]\n"
      "v_cvt_pk_bf16_f32 v44, v88, v89\n"
      "v_cvt_pk_bf16_f32 v45, v90, v91\n"
      "v_cvt_pk_bf16_f32 v46, v92, v93\n"
      "v_cvt_pk_bf16_f32 v47, v94, v95\n"
      "v_pk_mul_f32 v[96:97], v[96:97], s[84:85]\n"
      "v_pk_mul_f32 v[98:99], v[98:99], s[84:85]\n"
      "v_pk_mul_f32 v[100:101], v[100:101], s[84:85]\n"
      "v_pk_mul_f32 v[102:103], v[102:103], s[84:85]\n"
      "v_cvt_pk_bf16_f32 v48, v96, v97\n"
      "v_cvt_pk_bf16_f32 v49, v98, v99\n"
      "v_cvt_pk_bf16_f32 v50, v100, v101\n"
      "v_cvt_pk_bf16_f32 v51, v102, v103\n"
      "v_pk_mul_f32 v[104:105], v[104:105], s[84:85]\n"
      "v_pk_mul_f32 v[106:107], v[106:107], s[84:85]\n"
      "v_pk_mul_f32 v[108:109], v[108:109], s[84:85]\n"
      "v_pk_mul_f32 v[110:111], v[110:111], s[84:85]\n"
      "v_cvt_pk_bf16_f32 v52, v104, v105\n"
      "v_cvt_pk_bf16_f32 v53, v106, v107\n"
      "v_cvt_pk_bf16_f32 v54, v108, v109\n"
      "v_cvt_pk_bf16_f32 v55, v110, v111\n"
      "v_pk_mul_f32 v[112:113], v[112:113], s[84:85]\n"
      "v_pk_mul_f32 v[114:115], v[114:115], s[84:85]\n"
      "v_pk_mul_f32 v[116:117], v[116:117], s[84:85]\n"
      "v_pk_mul_f32 v[118:119], v[118:119], s[84:85]\n"
      "v_cvt_pk_bf16_f32 v56, v112, v113\n"
      "v_cvt_pk_bf16_f32 v57, v114, v115\n"
      "v_cvt_pk_bf16_f32 v58, v116, v117\n"
      "v_cvt_pk_bf16_f32 v59, v118, v119\n"
      "v_pk_mul_f32 v[120:121], v[120:121], s[84:85]\n"
      "v_pk_mul_f32 v[122:123], v[122:123], s[84:85]\n"
      "v_pk_mul_f32 v[124:125], v[124:125], s[84:85]\n"
      "v_pk_mul_f32 v[126:127], v[126:127], s[84:85]\n"
      "v_cvt_pk_bf16_f32 v60, v120, v121\n"
      "v_cvt_pk_bf16_f32 v61, v122, v123\n"
      "v_cvt_pk_bf16_f32 v62, v124, v125\n"
      "v_cvt_pk_bf16_f32 v63, v126, v127\n"
      "v_pk_mul_f32 v[128:129], v[128:129], s[84:85]\n"
      "v_pk_mul_f32 v[130:131], v[130:131], s[84:85]\n"
      "v_pk_mul_f32 v[132:133], v[132:133], s[84:85]\n"
      "v_pk_mul_f32 v[134:135], v[134:135], s[84:85]\n"
      "v_cvt_pk_bf16_f32 v64, v128, v129\n"
      "v_cvt_pk_bf16_f32 v65, v130, v131\n"
      "v_cvt_pk_bf16_f32 v66, v132, v133\n"
      "v_cvt_pk_bf16_f32 v67, v134, v135\n"
      "v_pk_mul_f32 v[136:137], v[136:137], s[84:85]\n"
      "v_pk_mul_f32 v[138:139], v[138:139], s[84:85]\n"
      "v_pk_mul_f32 v[140:141], v[140:141], s[84:85]\n"
      "v_pk_mul_f32 v[142:143], v[142:143], s[84:85]\n"
      "v_cvt_pk_bf16_f32 v68, v136, v137\n"
      "v_cvt_pk_bf16_f32 v69, v138, v139\n"
      "v_cvt_pk_bf16_f32 v70, v140, v141\n"
      "v_cvt_pk_bf16_f32 v71, v142, v143\n"
      "v_pk_mul_f32 v[144:145], v[144:145], s[84:85]\n"
      "v_pk_mul_f32 v[146:147], v[146:147], s[84:85]\n"
      "v_pk_mul_f32 v[148:149], v[148:149], s[84:85]\n"
      "v_pk_mul_f32 v[150:151], v[150:151], s[84:85]\n"
      "v_cvt_pk_bf16_f32 v72, v144, v145\n"
      "v_cvt_pk_bf16_f32 v73, v146, v147\n"
      "v_cvt_pk_bf16_f32 v74, v148, v149\n"
      "v_cvt_pk_bf16_f32 v75, v150, v151\n"
      "v_pk_mul_f32 v[152:153], v[152:153], s[84:85]\n"
      "v_pk_mul_f32 v[154:155], v[154:155], s[84:85]\n"
      "v_pk_mul_f32 v[156:157], v[156:157], s[84:85]\n"
      "v_pk_mul_f32 v[158:159], v[158:159], s[84:85]\n"
      "v_cvt_pk_bf16_f32 v76, v152, v153\n"
      "v_cvt_pk_bf16_f32 v77, v154, v155\n"
      "v_cvt_pk_bf16_f32 v78, v156, v157\n"
      "v_cvt_pk_bf16_f32 v79, v158, v159\n"
      "v_pk_mul_f32 v[160:161], v[160:161], s[84:85]\n"
      "v_pk_mul_f32 v[162:163], v[162:163], s[84:85]\n"
      "v_pk_mul_f32 v[164:165], v[164:165], s[84:85]\n"
      "v_pk_mul_f32 v[166:167], v[166:167], s[84:85]\n"
      "v_cvt_pk_bf16_f32 v80, v160, v161\n"
      "v_cvt_pk_bf16_f32 v81, v162, v163\n"
      "v_cvt_pk_bf16_f32 v82, v164, v165\n"
      "v_cvt_pk_bf16_f32 v83, v166, v167\n"
      "v_pk_mul_f32 v[168:169], v[168:169], s[84:85]\n"
      "v_pk_mul_f32 v[170:171], v[170:171], s[84:85]\n"
      "v_pk_mul_f32 v[172:173], v[172:173], s[84:85]\n"
      "v_pk_mul_f32 v[174:175], v[174:175], s[84:85]\n"
      "v_cvt_pk_bf16_f32 v84, v168, v169\n"
      "v_cvt_pk_bf16_f32 v85, v170, v171\n"
      "v_cvt_pk_bf16_f32 v86, v172, v173\n"
      "v_cvt_pk_bf16_f32 v87, v174, v175\n"
      "v_pk_mul_f32 v[176:177], v[176:177], s[84:85]\n"
      "v_pk_mul_f32 v[178:179], v[178:179], s[84:85]\n"
      "v_pk_mul_f32 v[180:181], v[180:181], s[84:85]\n"
      "v_pk_mul_f32 v[182:183], v[182:183], s[84:85]\n"
      "v_cvt_pk_bf16_f32 v88, v176, v177\n"
      "v_cvt_pk_bf16_f32 v89, v178, v179\n"
      "v_cvt_pk_bf16_f32 v90, v180, v181\n"
      "v_cvt_pk_bf16_f32 v91, v182, v183\n"
      "v_pk_mul_f32 v[184:185], v[184:185], s[84:85]\n"
      "v_pk_mul_f32 v[186:187], v[186:187], s[84:85]\n"
      "v_pk_mul_f32 v[188:189], v[188:189], s[84:85]\n"
      "v_pk_mul_f32 v[190:191], v[190:191], s[84:85]\n"
      "v_cvt_pk_bf16_f32 v92, v184, v185\n"
      "v_cvt_pk_bf16_f32 v93, v186, v187\n"
      "v_cvt_pk_bf16_f32 v94, v188, v189\n"
      "v_cvt_pk_bf16_f32 v95, v190, v191\n"
      "v_pk_mul_f32 v[192:193], v[192:193], s[84:85]\n"
      "v_pk_mul_f32 v[194:195], v[194:195], s[84:85]\n"
      "v_pk_mul_f32 v[196:197], v[196:197], s[84:85]\n"
      "v_pk_mul_f32 v[198:199], v[198:199], s[84:85]\n"
      "v_cvt_pk_bf16_f32 v96, v192, v193\n"
      "v_cvt_pk_bf16_f32 v97, v194, v195\n"
      "v_cvt_pk_bf16_f32 v98, v196, v197\n"
      "v_cvt_pk_bf16_f32 v99, v198, v199\n"
      "v_pk_mul_f32 v[200:201], v[200:201], s[84:85]\n"
      "v_pk_mul_f32 v[202:203], v[202:203], s[84:85]\n"
      "v_pk_mul_f32 v[204:205], v[204:205], s[84:85]\n"
      "v_pk_mul_f32 v[206:207], v[206:207], s[84:85]\n"
      "v_cvt_pk_bf16_f32 v100, v200, v201\n"
      "v_cvt_pk_bf16_f32 v101, v202, v203\n"
      "v_cvt_pk_bf16_f32 v102, v204, v205\n"
      "v_cvt_pk_bf16_f32 v103, v206, v207\n"
      "v_pk_mul_f32 v[208:209], v[208:209], s[84:85]\n"
      "v_pk_mul_f32 v[210:211], v[210:211], s[84:85]\n"
      "v_pk_mul_f32 v[212:213], v[212:213], s[84:85]\n"
      "v_pk_mul_f32 v[214:215], v[214:215], s[84:85]\n"
      "v_cvt_pk_bf16_f32 v104, v208, v209\n"
      "v_cvt_pk_bf16_f32 v105, v210, v211\n"
      "v_cvt_pk_bf16_f32 v106, v212, v213\n"
      "v_cvt_pk_bf16_f32 v107, v214, v215\n"
      "v_pk_mul_f32 v[216:217], v[216:217], s[84:85]\n"
      "v_pk_mul_f32 v[218:219], v[218:219], s[84:85]\n"
      "v_pk_mul_f32 v[220:221], v[220:221], s[84:85]\n"
      "v_pk_mul_f32 v[222:223], v[222:223], s[84:85]\n"
      "v_cvt_pk_bf16_f32 v108, v216, v217\n"
      "v_cvt_pk_bf16_f32 v109, v218, v219\n"
      "v_cvt_pk_bf16_f32 v110, v220, v221\n"
      "v_cvt_pk_bf16_f32 v111, v222, v223\n"
      "v_mov_b32 v216, 0\n"
      "v_mov_b32 v217, 0\n"
      "v_mov_b32 v218, 0\n"
      "v_mov_b32 v219, 0\n"
      "v_mov_b32 v220, 0\n"
      "v_mov_b32 v221, 0\n"
      "v_mov_b32 v222, 0\n"
      "s_waitcnt lgkmcnt(0)\n"
      "v_mfma_f32_16x16x32_bf16 v[144:147], a[224:227], v[0:3], 0\n"
      "v_mfma_f32_16x16x32_bf16 v[148:151], a[228:231], v[0:3], 0\n"
      "v_mbcnt_lo_u32_b32 v247, -1, 0\n"
      "v_mfma_f32_16x16x32_bf16 v[144:147], a[232:235], v[4:7], v[144:147]\n"
      "v_mbcnt_hi_u32_b32 v247, -1, v247\n"
      "v_and_b32_e64 v248, 1, v247\n"
      "v_mfma_f32_16x16x32_bf16 v[148:151], a[236:239], v[4:7], v[148:151]\n"
      "v_bfe_u32 v247, v247, 1, 4\n"
      "v_lshlrev_b32_e64 v247, 10, v247\n"
      "v_mfma_f32_16x16x32_bf16 v[144:147], a[240:243], v[8:11], v[144:147]\n"
      "v_lshl_add_u32 v247, v248, 7, v247\n"
      "s_lshl_b32 s66, %[b], 2\n"
      "v_mfma_f32_16x16x32_bf16 v[148:151], a[244:247], v[8:11], v[148:151]\n"
      "s_add_u32 s66, s66, %[wid]\n"
      "s_lshl_b32 s66, s66, 14\n"
      "v_mfma_f32_16x16x32_bf16 v[144:147], a[248:251], v[12:15], v[144:147]\n"
      "buffer_load_dword v248, v247, s[56:59], s66 offen\n"
      "v_mfma_f32_16x16x32_bf16 v[148:151], a[252:255], v[12:15], v[148:151]\n"
      "s_nop 5\n"
      "v_mfma_f32_16x16x32_bf16 v[152:155], a[224:227], v[16:19], 0\n"
      "s_bfe_u64 s[84:85], %[ttab], 262172\n"
      "v_exp_f32 v144, v144\n"
      "s_nop 0\n"
      "ds_read_b64_tr_b16 a[192:193], v226 offset:0\n"
      "v_add_f32 v216, v216, v144\n"
      "v_mfma_f32_16x16x32_bf16 v[156:159], a[228:231], v[16:19], 0\n"
      "s_lshl_b32 s48, s84, 14\n"
      "v_exp_f32 v145, v145\n"
      "s_nop 0\n"
      "ds_read_b64_tr_b16 a[194:195], v226 offset:4096\n"
      "v_cvt_pk_bf16_f32 v168, v144, v145\n"
      "v_mfma_f32_16x16x32_bf16 v[152:155], a[232:235], v[20:23], v[152:155]\n"
      "s_add_u32 s48, s48, s89\n"
      "v_exp_f32_e64 v146, v146\n"
      "s_nop 0\n"
      "ds_read_b64_tr_b16 a[196:197], v227 offset:0\n"
      "v_add_f32 v216, v216, v145\n"
      "v_mfma_f32_16x16x32_bf16 v[156:159], a[236:239], v[20:23], v[156:159]\n"
      "s_bfe_u64 s[84:85], %[ttab], 262168\n"
      "v_exp_f32 v147, v147\n"
      "s_nop 0\n"
      "ds_read_b64_tr_b16 a[198:199], v227 offset:4096\n"
      "v_cvt_pk_bf16_f32 v169, v146, v147\n"
      "v_mfma_f32_16x16x32_bf16 v[152:155], a[240:243], v[24:27], v[152:155]\n"
      "s_lshl_b32 s49, s84, 14\n"
      "v_exp_f32 v148, v148\n"
      "s_nop 0\n"
      "ds_read_b64_tr_b16 a[200:201], v228 offset:0\n"
      "v_add_f32 v216, v216, v146\n"
      "v_mfma_f32_16x16x32_bf16 v[156:159], a[244:247], v[24:27], v[156:159]\n"
      "s_add_u32 s49, s49, s89\n"
      "v_exp_f32 v149, v149\n"
      "s_nop 0\n"
      "ds_read_b64_tr_b16 a[202:203], v228 offset:4096\n"
      "v_cvt_pk_bf16_f32 v170, v148, v149\n"
      "v_mfma_f32_16x16x32_bf16 v[152:155], a[248:251], v[28:31], v[152:155]\n"
      "v_exp_f32 v150, v150\n"
      "s_nop 0\n"
      "ds_read_b64_tr_b16 a[204:205], v229 offset:0\n"
      "v_add_f32 v216, v216, v147\n"
      "v_mfma_f32_16x16x32_bf16 v[156:159], a[252:255], v[28:31], v[156:159]\n"
      "v_exp_f32 v151, v151\n"
      "s_nop 0\n"
      "ds_read_b64_tr_b16 a[206:207], v229 offset:4096\n"
      "v_cvt_pk_bf16_f32 v171, v150, v151\n"
      "s_nop 5\n"
      "v_mfma_f32_16x16x32_bf16 v[160:163], a[224:227], v[32:35], 0\n"
      "v_exp_f32 v152, v152\n"
      "s_nop 0\n"
      "ds_read_b64_tr_b16 a[208:209], v226 offset:1024\n"
      "v_add_f32 v216, v216, v148\n"
      "v_mfma_f32_16x16x32_bf16 v[164:167], a[228:231], v[32:35], 0\n"
      "v_exp_f32 v153, v153\n"
      "s_nop 0\n"
      "ds_read_b64_tr_b16 a[210:211], v226 offset:5120\n"
      "v_cvt_pk_bf16_f32 v172, v152, v153\n"
      "v_mfma_f32_16x16x32_bf16 v[160:163], a[232:235], v[36:39], v[160:163]\n"
      "v_exp_f32 v154, v154\n"
      "s_nop 0\n"
      "ds_read_b64_tr_b16 a[212:213], v227 offset:1024\n"
      "v_add_f32 v216, v216, v149\n"
      "v_mfma_f32_16x16x32_bf16 v[164:167], a[236:239], v[36:39], v[164:167]\n"
      "v_exp_f32 v155, v155\n"
      "s_nop 0\n"
      "ds_read_b64_tr_b16 a[214:215], v227 offset:5120\n"
      "v_cvt_pk_bf16_f32 v173, v154, v155\n"
      "v_mfma_f32_16x16x32_bf16 v[160:163], a[240:243], v[40:43], v[160:163]\n"
      "s_waitcnt lgkmcnt(4)\n"
      "v_exp_f32 v156, v156\n"
      "v_mfma_f32_16x16x32_bf16 a[0:3], a[192:195], v[168:171], 0\n"
      "ds_read_b64_tr_b16 a[216:217], v228 offset:1024\n"
      "v_add_f32 v216, v216, v150\n"
      "v_mfma_f32_16x16x32_bf16 v[164:167], a[244:247], v[40:43], v[164:167]\n"
      "v_exp_f32 v157, v157\n"
      "v_mfma_f32_16x16x32_bf16 a[4:7], a[196:199], v[168:171], 0\n"
      "ds_read_b64_tr_b16 a[218:219], v228 offset:5120\n"
      "v_cvt_pk_bf16_f32 v174, v156, v157\n"
      "v_mfma_f32_16x16x32_bf16 v[160:163], a[248:251], v[44:47], v[160:163]\n"
      "v_exp_f32 v158, v158\n"
      "v_mfma_f32_16x16x32_bf16 a[8:11], a[200:203], v[168:171], 0\n"
      "ds_read_b64_tr_b16 a[220:221], v229 offset:1024\n"
      "v_add_f32 v216, v216, v151\n"
      "v_mfma_f32_16x16x32_bf16 v[164:167], a[252:255], v[44:47], v[164:167]\n"
      "v_exp_f32_e64 v159, v159\n"
      "v_mfma_f32_16x16x32_bf16 a[12:15], a[204:207], v[168:171], 0\n"
      "ds_read_b64_tr_b16 a[222:223], v229 offset:5120\n"
      "v_cvt_pk_bf16_f32 v175, v158, v159\n"
      "v_mfma_f32_16x16x32_bf16 v[144:147], a[224:227], v[48:51], 0\n"
      "v_exp_f32 v160, v160\n"
      "v_mfma_f32_16x16x32_bf16 a[32:35], a[192:195], v[172:175], 0\n"
      "v_add_f32 v217, v217, v153\n"
      "v_add_f32_e64 v217, v217, v152\n"
      "v_mfma_f32_16x16x32_bf16 v[148:151], a[228:231], v[48:51], 0\n"
      "v_exp_f32 v161, v161\n"
      "v_mfma_f32_16x16x32_bf16 a[36:39], a[196:199], v[172:175], 0\n"
      "v_add_f32 v217, v217, v154\n"
      "v_cvt_pk_bf16_f32 v176, v160, v161\n"
      "v_mfma_f32_16x16x32_bf16 v[144:147], a[232:235], v[52:55], v[144:147]\n"
      "s_memrealtime s[76:77]\n"
      "v_exp_f32 v162, v162\n"
      "v_mfma_f32_16x16x32_bf16 a[40:43], a[200:203], v[172:175], 0\n"
      "v_add_f32 v217, v217, v156\n"
      "v_add_f32 v217, v217, v155\n"
      "v_mfma_f32_16x16x32_bf16 v[148:151], a[236:239], v[52:55], v[148:151]\n"
      "v_exp_f32 v163, v163\n"
      "v_mfma_f32_16x16x32_bf16 a[44:47], a[204:207], v[172:175], 0\n"
      "v_add_f32_e64 v217, v217, v157\n"
      "v_cvt_pk_bf16_f32 v177, v162, v163\n"
      "v_mfma_f32_16x16x32_bf16 v[144:147], a[240:243], v[56:59], v[144:147]\n"
      "s_waitcnt vmcnt(10) lgkmcnt(0)\n"
      "v_exp_f32 v164, v164\n"
      "v_mfma_f32_16x16x32_bf16 a[16:19], a[208:211], v[168:171], 0\n"
      "s_add_u32 s96, s76, 122\n"
      "v_add_f32 v217, v217, v159\n"
      "v_add_f32 v217, v217, v158\n"
      "v_mfma_f32_16x16x32_bf16 v[148:151], a[244:247], v[56:59], v[148:151]\n"
      "s_barrier\n"
      "v_exp_f32 v165, v165\n"
      "v_mfma_f32_16x16x32_bf16 a[20:23], a[212:215], v[168:171], 0\n"
      "v_add_f32 v218, v218, v160\n"
      "v_cvt_pk_bf16_f32 v178, v164, v165\n"
      "v_mfma_f32_16x16x32_bf16 v[144:147], a[248:251], v[60:63], v[144:147]\n"
      "v_exp_f32 v166, v166\n"
      "v_mfma_f32_16x16x32_bf16 a[24:27], a[216:219], v[168:171], 0\n"
      "s_add_u32 m0, s68, 0\n"
      "v_add_f32 v218, v218, v162\n"
      "v_add_f32_e64 v218, v218, v161\n"
      "v_mfma_f32_16x16x32_bf16 v[148:151], a[252:255], v[60:63], v[148:151]\n"
      "buffer_load_dwordx4 v230, s[52:55], s80 offen offset:0 nt lds\n"
      "v_exp_f32 v167, v167\n"
      "v_mfma_f32_16x16x32_bf16 a[28:31], a[220:223], v[168:171], 0\n"
      "v_add_f32 v218, v218, v163\n"
      "v_cvt_pk_bf16_f32 v179, v166, v167\n"
      "v_mfma_f32_16x16x32_bf16 v[152:155], a[224:227], v[64:67], 0\n"
      "v_exp_f32 v144, v144\n"
      "v_mfma_f32_16x16x32_bf16 a[64:67], a[192:195], v[176:179], 0\n"
      "v_add_f32 v218, v218, v165\n"
      "v_add_f32_e64 v218, v218, v164\n"
      "v_mfma_f32_16x16x32_bf16 v[156:159], a[228:231], v[64:67], 0\n"
      "v_exp_f32 v145, v145\n"
      "v_mfma_f32_16x16x32_bf16 a[68:71], a[196:199], v[176:179], 0\n"
      "v_add_f32 v218, v218, v166\n"
      "v_cvt_pk_bf16_f32 v168, v144, v145\n"
      "v_mfma_f32_16x16x32_bf16 v[152:155], a[232:235], v[68:71], v[152:155]\n"
      "v_exp_f32 v146, v146\n"
      "v_mfma_f32_16x16x32_bf16 a[72:75], a[200:203], v[176:179], 0\n"
      "v_add_f32 v219, v219, v144\n"
      "v_add_f32_e64 v218, v218, v167\n"
      "v_mfma_f32_16x16x32_bf16 v[156:159], a[236:239], v[68:71], v[156:159]\n"
      "v_exp_f32 v147, v147\n"
      "v_mfma_f32_16x16x32_bf16 a[76:79], a[204:207], v[176:179], 0\n"
      "v_add_f32 v219, v219, v145\n"
      "v_cvt_pk_bf16_f32 v169, v146, v147\n"
      "v_mfma_f32_16x16x32_bf16 v[152:155], a[240:243], v[72:75], v[152:155]\n"
      "v_exp_f32 v148, v148\n"
      "v_mfma_f32_16x16x32_bf16 a[48:51], a[208:211], v[172:175], 0\n"
      "v_add_f32 v219, v219, v147\n"
      "v_add_f32_e64 v219, v219, v146\n"
      "v_mfma_f32_16x16x32_bf16 v[156:159], a[244:247], v[72:75], v[156:159]\n"
      "buffer_load_dwordx4 v231, s[52:55], s80 offen offset:1024 nt lds\n"
      "v_exp_f32 v149, v149\n"
      "v_mfma_f32_16x16x32_bf16 a[52:55], a[212:215], v[172:175], 0\n"
      "v_add_f32 v219, v219, v148\n"
      "v_cvt_pk_bf16_f32 v170, v148, v149\n"
      "v_mfma_f32_16x16x32_bf16 v[152:155], a[248:251], v[76:79], v[152:155]\n"
      "v_exp_f32 v150, v150\n"
      "v_mfma_f32_16x16x32_bf16 a[56:59], a[216:219], v[172:175], 0\n"
      "v_add_f32 v219, v219, v150\n"
      "v_add_f32 v219, v219, v149\n"
      "v_mfma_f32_16x16x32_bf16 v[156:159], a[252:255], v[76:79], v[156:159]\n"
      "v_exp_f32 v151, v151\n"
      "v_mfma_f32_16x16x32_bf16 a[60:63], a[220:223], v[172:175], 0\n"
      "v_add_f32_e64 v219, v219, v151\n"
      "v_cvt_pk_bf16_f32 v171, v150, v151\n"
      "v_mfma_f32_16x16x32_bf16 v[160:163], a[224:227], v[80:83], 0\n"
      "v_exp_f32 v152, v152\n"
      "v_mfma_f32_16x16x32_bf16 a[96:99], a[192:195], v[168:171], 0\n"
      "v_add_f32 v220, v220, v152\n"
      "v_mfma_f32_16x16x32_bf16 v[164:167], a[228:231], v[80:83], 0\n"
      "v_exp_f32 v153, v153\n"
      "v_mfma_f32_16x16x32_bf16 a[100:103], a[196:199], v[168:171], 0\n"
      "v_add_f32 v220, v220, v153\n"
      "v_cvt_pk_bf16_f32 v172, v152, v153\n"
      "v_mfma_f32_16x16x32_bf16 v[160:163], a[232:235], v[84:87], v[160:163]\n"
      "v_exp_f32 v154, v154\n"
      "v_mfma_f32_16x16x32_bf16 a[104:107], a[200:203], v[168:171], 0\n"
      "v_add_f32 v220, v220, v154\n"
      "v_mfma_f32_16x16x32_bf16 v[164:167], a[236:239], v[84:87], v[164:167]\n"
      "buffer_load_dwordx4 v230, s[52:55], s80 offen offset:2048 nt lds\n"
      "v_exp_f32 v155, v155\n"
      "v_mfma_f32_16x16x32_bf16 a[108:111], a[204:207], v[168:171], 0\n"
      "v_add_f32 v220, v220, v155\n"
      "v_cvt_pk_bf16_f32 v173, v154, v155\n"
      "v_mfma_f32_16x16x32_bf16 v[160:163], a[240:243], v[88:91], v[160:163]\n"
      "v_exp_f32 v156, v156\n"
      "v_mfma_f32_16x16x32_bf16 a[80:83], a[208:211], v[176:179], 0\n"
      "v_add_f32 v220, v220, v156\n"
      "v_mfma_f32_16x16x32_bf16 v[164:167], a[244:247], v[88:91], v[164:167]\n"
      "v_exp_f32 v157, v157\n"
      "v_mfma_f32_16x16x32_bf16 a[84:87], a[212:215], v[176:179], 0\n"
      "v_add_f32 v220, v220, v157\n"
      "v_cvt_pk_bf16_f32 v174, v156, v157\n"
      "v_mfma_f32_16x16x32_bf16 v[160:163], a[248:251], v[92:95], v[160:163]\n"
      "v_exp_f32 v158, v158\n"
      "v_mfma_f32_16x16x32_bf16 a[88:91], a[216:219], v[176:179], 0\n"
      "v_add_f32 v220, v220, v158\n"
      "v_mfma_f32_16x16x32_bf16 v[164:167], a[252:255], v[92:95], v[164:167]\n"
      "v_exp_f32_e64 v159, v159\n"
      "v_mfma_f32_16x16x32_bf16 a[92:95], a[220:223], v[176:179], 0\n"
      "v_add_f32 v220, v220, v159\n"
      "v_cvt_pk_bf16_f32 v175, v158, v159\n"
      "v_mfma_f32_16x16x32_bf16 v[144:147], a[224:227], v[96:99], 0\n"
      "v_exp_f32 v160, v160\n"
      "v_mfma_f32_16x16x32_bf16 a[128:131], a[192:195], v[172:175], 0\n"
      "ds_read_b128 a[224:227], v224 offset:16384\n"
      "v_add_f32_e64 v221, v221, v160\n"
      "v_mfma_f32_16x16x32_bf16 v[148:151], a[228:231], v[96:99], 0\n"
      "buffer_load_dwordx4 v231, s[52:55], s80 offen offset:3072 nt lds\n"
      "v_exp_f32_e64 v161, v161\n"
      "v_mfma_f32_16x16x32_bf16 a[132:135], a[196:199], v[172:175], 0\n"
      "ds_read_b128 a[228:231], v224 offset:20480\n"
      "v_cvt_pk_bf16_f32 v176, v160, v161\n"
      "v_mfma_f32_16x16x32_bf16 v[144:147], a[232:235], v[100:103], v[144:147]\n"
      "v_exp_f32_e64 v162, v162\n"
      "v_mfma_f32_16x16x32_bf16 a[136:139], a[200:203], v[172:175], 0\n"
      "ds_read_b128 a[232:235], v225 offset:16384\n"
      "v_add_f32 v221, v221, v161\n"
      "v_mfma_f32_16x16x32_bf16 v[148:151], a[236:239], v[100:103], v[148:151]\n"
      "v_exp_f32 v163, v163\n"
      "v_mfma_f32_16x16x32_bf16 a[140:143], a[204:207], v[172:175], 0\n"
      "ds_read_b128 a[236:239], v225 offset:20480\n"
      "v_cvt_pk_bf16_f32 v177, v162, v163\n"
      "v_mfma_f32_16x16x32_bf16 v[144:147], a[240:243], v[104:107], v[144:147]\n"
      "v_exp_f32_e64 v164, v164\n"
      "v_mfma_f32_16x16x32_bf16 a[112:115], a[208:211], v[168:171], 0\n"
      "ds_read_b128 a[240:243], v224 offset:17408\n"
      "v_add_f32_e64 v221, v221, v162\n"
      "v_mfma_f32_16x16x32_bf16 v[148:151], a[244:247], v[104:107], v[148:151]\n"
      "s_add_u32 m0, s68, 4096\n"
      "v_exp_f32_e64 v165, v165\n"
      "v_mfma_f32_16x16x32_bf16 a[116:119], a[212:215], v[168:171], 0\n"
      "ds_read_b128 a[244:247], v224 offset:21504\n"
      "s_add_u32 s64, s80, 4096\n"
      "v_cvt_pk_bf16_f32 v178, v164, v165\n"
      "v_mfma_f32_16x16x32_bf16 v[144:147], a[248:251], v[108:111], v[144:147]\n"
      "buffer_load_dwordx4 v230, s[52:55], s64 offen offset:0 nt lds\n"
      "v_exp_f32 v166, v166\n"
      "v_mfma_f32_16x16x32_bf16 a[120:123], a[216:219], v[168:171], 0\n"
      "ds_read_b128 a[248:251], v225 offset:17408\n"
      "v_add_f32 v221, v221, v163\n"
      "v_mfma_f32_16x16x32_bf16 v[148:151], a[252:255], v[108:111], v[148:151]\n"
      "s_waitcnt lgkmcnt(3)\n"
      "v_exp_f32 v167, v167\n"
      "v_mfma_f32_16x16x32_bf16 a[124:127], a[220:223], v[168:171], 0\n"
      "ds_read_b128 a[252:255], v225 offset:21504\n"
      "v_cvt_pk_bf16_f32 v179, v166, v167\n"
      "v_mfma_f32_16x16x32_bf16 v[152:155], a[224:227], v[0:3], 0\n"
      "v_exp_f32 v144, v144\n"
      "v_mfma_f32_16x16x32_bf16 v[180:183], a[192:195], v[176:179], 0\n"
      "v_add_f32 v221, v221, v165\n"
      "v_add_f32_e64 v221, v221, v164\n"
      "v_mfma_f32_16x16x32_bf16 v[156:159], a[228:231], v[0:3], 0\n"
      "v_exp_f32 v145, v145\n"
      "v_mfma_f32_16x16x32_bf16 v[184:187], a[196:199], v[176:179], 0\n"
      "v_add_f32 v221, v221, v166\n"
      "v_cvt_pk_bf16_f32 v168, v144, v145\n"
      "v_mfma_f32_16x16x32_bf16 v[152:155], a[232:235], v[4:7], v[152:155]\n"
      "v_exp_f32 v146, v146\n"
      "v_mfma_f32_16x16x32_bf16 v[188:191], a[200:203], v[176:179], 0\n"
      "v_add_f32 v222, v222, v144\n"
      "v_add_f32_e64 v221, v221, v167\n"
      "v_mfma_f32_16x16x32_bf16 v[156:159], a[236:239], v[4:7], v[156:159]\n"
      "s_waitcnt lgkmcnt(2)\n"
      "v_exp_f32 v147, v147\n"
      "v_mfma_f32_16x16x32_bf16 v[192:195], a[204:207], v[176:179], 0\n"
      "v_add_f32_e64 v222, v222, v145\n"
      "v_cvt_pk_bf16_f32 v169, v146, v147\n"
      "v_mfma_f32_16x16x32_bf16 v[152:155], a[240:243], v[8:11], v[152:155]\n"
      "v_exp_f32 v148, v148\n"
      "v_mfma_f32_16x16x32_bf16 a[144:147], a[208:211], v[172:175], 0\n"
      "v_add_f32 v222, v222, v147\n"
      "v_add_f32 v222, v222, v146\n"
      "v_mfma_f32_16x16x32_bf16 v[156:159], a[244:247], v[8:11], v[156:159]\n"
      "s_waitcnt lgkmcnt(0)\n"
      "v_exp_f32 v149, v149\n"
      "v_mfma_f32_16x16x32_bf16 a[148:151], a[212:215], v[172:175], 0\n"
      "v_add_f32 v222, v222, v148\n"
      "v_cvt_pk_bf16_f32 v170, v148, v149\n"
      "v_mfma_f32_16x16x32_bf16 v[152:155], a[248:251], v[12:15], v[152:155]\n"
      "v_exp_f32_e64 v150, v150\n"
      "v_mfma_f32_16x16x32_bf16 a[152:155], a[216:219], v[172:175], 0\n"
      "s_add_u32 m0, s68, 4096\n"
      "v_add_f32 v222, v222, v150\n"
      "v_add_f32 v222, v222, v149\n"
      "v_mfma_f32_16x16x32_bf16 v[156:159], a[252:255], v[12:15], v[156:159]\n"
      "buffer_load_dwordx4 v231, s[52:55], s64 offen offset:1024 nt lds\n"
      "v_exp_f32 v151, v151\n"
      "v_mfma_f32_16x16x32_bf16 a[156:159], a[220:223], v[172:175], 0\n"
      "v_add_f32 v222, v222, v151\n"
      "v_cvt_pk_bf16_f32 v171, v150, v151\n"
      "v_mfma_f32_16x16x32_bf16 v[160:163], a[224:227], v[16:19], 0\n"
      "v_exp_f32 v152, v152\n"
      "v_mfma_f32_16x16x32_bf16 v[112:115], a[192:195], v[168:171], 0\n"
      "ds_read_b64_tr_b16 a[192:193], v226 offset:16384\n"
      "v_add_f32 v216, v216, v152\n"
      "v_mfma_f32_16x16x32_bf16 v[164:167], a[228:231], v[16:19], 0\n"
      "v_exp_f32_e64 v153, v153\n"
      "v_mfma_f32_16x16x32_bf16 v[116:119], a[196:199], v[168:171], 0\n"
      "ds_read_b64_tr_b16 a[194:195], v226 offset:20480\n"
      "v_cvt_pk_bf16_f32 v172, v152, v153\n"
      "v_mfma_f32_16x16x32_bf16 v[160:163], a[232:235], v[20:23], v[160:163]\n"
      "v_exp_f32 v154, v154\n"
      "v_mfma_f32_16x16x32_bf16 v[120:123], a[200:203], v[168:171], 0\n"
      "ds_read_b64_tr_b16 a[196:197], v227 offset:16384\n"
      "v_add_f32 v216, v216, v153\n"
      "v_mfma_f32_16x16x32_bf16 v[164:167], a[236:239], v[20:23], v[164:167]\n"
      "v_exp_f32_e64 v155, v155\n"
      "v_mfma_f32_16x16x32_bf16 v[124:127], a[204:207], v[168:171], 0\n"
      "ds_read_b64_tr_b16 a[198:199], v227 offset:20480\n"
      "v_cvt_pk_bf16_f32 v173, v154, v155\n"
      "v_mfma_f32_16x16x32_bf16 v[160:163], a[240:243], v[24:27], v[160:163]\n"
      "v_exp_f32_e64 v156, v156\n"
      "v_mfma_f32_16x16x32_bf16 v[196:199], a[208:211], v[176:179], 0\n"
      "ds_read_b64_tr_b16 a[200:201], v228 offset:16384\n"
      "s_add_u32 m0, s68, 4096\n"
      "v_add_f32_e64 v216, v216, v154\n"
      "v_mfma_f32_16x16x32_bf16 v[164:167], a[244:247], v[24:27], v[164:167]\n"
      "buffer_load_dwordx4 v230, s[52:55], s64 offen offset:2048 nt lds\n"
      "v_exp_f32_e64 v157, v157\n"
      "v_mfma_f32_16x16x32_bf16 v[200:203], a[212:215], v[176:179], 0\n"
      "ds_read_b64_tr_b16 a[202:203], v228 offset:20480\n"
      "v_cvt_pk_bf16_f32 v174, v156, v157\n"
      "v_mfma_f32_16x16x32_bf16 v[160:163], a[248:251], v[28:31], v[160:163]\n"
      "v_exp_f32_e64 v158, v158\n"
      "v_mfma_f32_16x16x32_bf16 v[204:207], a[216:219], v[176:179], 0\n"
      "ds_read_b64_tr_b16 a[204:205], v229 offset:16384\n"
      "v_add_f32 v216, v216, v155\n"
      "v_mfma_f32_16x16x32_bf16 v[164:167], a[252:255], v[28:31], v[164:167]\n"
      "v_exp_f32 v159, v159\n"
      "v_mfma_f32_16x16x32_bf16 v[208:211], a[220:223], v[176:179], 0\n"
      "ds_read_b64_tr_b16 a[206:207], v229 offset:20480\n"
      "v_cvt_pk_bf16_f32 v175, v158, v159\n"
      "v_mfma_f32_16x16x32_bf16 v[144:147], a[224:227], v[32:35], 0\n"
      "v_exp_f32_e64 v160, v160\n"
      "v_mfma_f32_16x16x32_bf16 v[128:131], a[208:211], v[168:171], 0\n"
      "ds_read_b64_tr_b16 a[208:209], v226 offset:17408\n"
      "v_add_f32 v216, v216, v156\n"
      "v_mfma_f32_16x16x32_bf16 v[148:151], a[228:231], v[32:35], 0\n"
      "s_movk_i32 s84, 20480\n"
      "v_lshlrev_b32 v213, 5, v242\n"
      "v_exp_f32 v161, v161\n"
      "v_mfma_f32_16x16x32_bf16 v[132:135], a[212:215], v[168:171], 0\n"
      "ds_read_b64_tr_b16 a[210:211], v226 offset:21504\n"
      "v_cvt_pk_bf16_f32 v176, v160, v161\n"
      "v_mfma_f32_16x16x32_bf16 v[144:147], a[232:235], v[36:39], v[144:147]\n"
      "v_exp_f32 v162, v162\n"
      "v_mfma_f32_16x16x32_bf16 v[136:139], a[216:219], v[168:171], 0\n"
      "ds_read_b64_tr_b16 a[212:213], v227 offset:17408\n"
      "v_add_f32 v216, v216, v157\n"
      "v_mfma_f32_16x16x32_bf16 v[148:151], a[236:239], v[36:39], v[148:151]\n"
      "buffer_load_dwordx2 v[212:213], v213, s[60:63], s84 offen sc1\n"
      "v_exp_f32_e64 v163, v163\n"
      "v_mfma_f32_16x16x32_bf16 v[140:143], a[220:223], v[168:171], 0\n"
      "ds_read_b64_tr_b16 a[214:215], v227 offset:21504\n"
      "v_cvt_pk_bf16_f32 v177, v162, v163\n"
      "v_mfma_f32_16x16x32_bf16 v[144:147], a[240:243], v[40:43], v[144:147]\n"
      "s_waitcnt lgkmcnt(4)\n"
      "v_exp_f32 v164, v164\n"
      "v_mfma_f32_16x16x32_bf16 a[0:3], a[192:195], v[172:175], a[0:3]\n"
      "ds_read_b64_tr_b16 a[216:217], v228 offset:17408\n"
      "v_add_f32 v216, v216, v158\n"
      "v_mfma_f32_16x16x32_bf16 v[148:151], a[244:247], v[40:43], v[148:151]\n"
      "v_exp_f32 v165, v165\n"
      "v_mfma_f32_16x16x32_bf16 a[4:7], a[196:199], v[172:175], a[4:7]\n"
      "ds_read_b64_tr_b16 a[218:219], v228 offset:21504\n"
      "s_add_u32 m0, s68, 4096\n"
      "v_cvt_pk_bf16_f32 v178, v164, v165\n"
      "v_mfma_f32_16x16x32_bf16 v[144:147], a[248:251], v[44:47], v[144:147]\n"
      "buffer_load_dwordx4 v231, s[52:55], s64 offen offset:3072 nt lds\n"
      "v_exp_f32_e64 v166, v166\n"
      "v_mfma_f32_16x16x32_bf16 a[8:11], a[200:203], v[172:175], a[8:11]\n"
      "ds_read_b64_tr_b16 a[220:221], v229 offset:17408\n"
      "s_add_u32 s80, s80, 8192\n"
      "v_add_f32_e64 v216, v216, v159\n"
      "v_mfma_f32_16x16x32_bf16 v[148:151], a[252:255], v[44:47], v[148:151]\n"
      "s_and_b32 s80, s80, 122880\n"
      "v_exp_f32_e64 v167, v167\n"
      "v_mfma_f32_16x16x32_bf16 a[12:15], a[204:207], v[172:175], a[12:15]\n"
      "ds_read_b64_tr_b16 a[222:223], v229 offset:21504\n"
      "v_cvt_pk_bf16_f32 v179, v166, v167\n"
      "s_mov_b32 s73, s69\n"
      "s_mov_b32 s74, 4096\n"
      "s_mov_b32 s75, 16384\n"
      "s_mov_b32 s83, 4\n"
      "s_mov_b32 s78, 122\n"
      "s_mov_b32 s81, 8814712\n"
      "ATT_%=:\n"
      "v_mfma_f32_16x16x32_bf16 v[152:155], a[224:227], v[48:51], 0\n"
      "v_exp_f32_e64 v144, v144\n"
      "v_mfma_f32_16x16x32_bf16 a[32:35], a[192:195], v[176:179], a[32:35]\n"
      "v_add_f32 v217, v217, v161\n"
      "v_add_f32 v217, v217, v160\n"
      "v_mfma_f32_16x16x32_bf16 v[156:159], a[228:231], v[48:51], 0\n"
      "v_exp_f32_e64 v145, v145\n"
      "v_mfma_f32_16x16x32_bf16 a[36:39], a[196:199], v[176:179], a[36:39]\n"
      "v_add_f32_e64 v217, v217, v162\n"
      "v_cvt_pk_bf16_f32 v168, v144, v145\n"
      "v_mfma_f32_16x16x32_bf16 v[152:155], a[232:235], v[52:55], v[152:155]\n"
      "s_memrealtime s[76:77]\n"
      "v_exp_f32 v146, v146\n"
      "v_mfma_f32_16x16x32_bf16 a[40:43], a[200:203], v[176:179], a[40:43]\n"
      "v_add_f32 v217, v217, v164\n"
      "v_add_f32_e64 v217, v217, v163\n"
      "v_mfma_f32_16x16x32_bf16 v[156:159], a[236:239], v[52:55], v[156:159]\n"
      "v_exp_f32 v147, v147\n"
      "v_mfma_f32_16x16x32_bf16 a[44:47], a[204:207], v[176:179], a[44:47]\n"
      "v_add_f32 v217, v217, v165\n"
      "v_cvt_pk_bf16_f32 v169, v146, v147\n"
      "v_mfma_f32_16x16x32_bf16 v[152:155], a[240:243], v[56:59], v[152:155]\n"
      "s_waitcnt vmcnt(8) lgkmcnt(0)\n"
      "v_exp_f32 v148, v148\n"
      "v_mfma_f32_16x16x32_bf16 a[16:19], a[208:211], v[172:175], a[16:19]\n"
      "s_sub_u32 s77, s76, s96\n"
      "s_cmp_lt_i32 s77, 0\n"
      "s_cbranch_scc1 PC1_%=\n"
      "PC1R_%=:\n"
      "v_add_f32 v217, v217, v167\n"
      "v_add_f32 v217, v217, v166\n"
      "v_mfma_f32_16x16x32_bf16 v[156:159], a[244:247], v[56:59], v[156:159]\n"
      "s_barrier\n"
      "v_exp_f32_e64 v149, v149\n"
      "v_mfma_f32_16x16x32_bf16 a[20:23], a[212:215], v[172:175], a[20:23]\n"
      "s_add_u32 s96, s76, s78\n"
      "v_add_f32 v218, v218, v144\n"
      "v_cvt_pk_bf16_f32 v170, v148, v149\n"
      "v_mfma_f32_16x16x32_bf16 v[152:155], a[248:251], v[60:63], v[152:155]\n"
      "v_exp_f32 v150, v150\n"
      "v_mfma_f32_16x16x32_bf16 a[24:27], a[216:219], v[172:175], a[24:27]\n"
      "s_add_u32 m0, s68, 16384\n"
      "v_add_f32 v218, v218, v146\n"
      "v_add_f32_e64 v218, v218, v145\n"
      "v_mfma_f32_16x16x32_bf16 v[156:159], a[252:255], v[60:63], v[156:159]\n"
      "buffer_load_dwordx4 v230, s[52:55], s80 offen offset:0 nt lds\n"
      "v_exp_f32 v151, v151\n"
      "v_mfma_f32_16x16x32_bf16 a[28:31], a[220:223], v[172:175], a[28:31]\n"
      "v_add_f32 v218, v218, v147\n"
      "v_cvt_pk_bf16_f32 v171, v150, v151\n"
      "v_mfma_f32_16x16x32_bf16 v[160:163], a[224:227], v[64:67], 0\n"
      "v_exp_f32 v152, v152\n"
      "v_mfma_f32_16x16x32_bf16 a[64:67], a[192:195], v[168:171], a[64:67]\n"
      "v_add_f32 v218, v218, v149\n"
      "v_add_f32 v218, v218, v148\n"
      "v_mfma_f32_16x16x32_bf16 v[164:167], a[228:231], v[64:67], 0\n"
      "v_exp_f32 v153, v153\n"
      "v_mfma_f32_16x16x32_bf16 a[68:71], a[196:199], v[168:171], a[68:71]\n"
      "v_add_f32_e64 v218, v218, v150\n"
      "v_cvt_pk_bf16_f32 v172, v152, v153\n"
      "v_mfma_f32_16x16x32_bf16 v[160:163], a[232:235], v[68:71], v[160:163]\n"
      "v_exp_f32 v154, v154\n"
      "v_mfma_f32_16x16x32_bf16 a[72:75], a[200:203], v[168:171], a[72:75]\n"
      "v_add_f32 v219, v219, v152\n"
      "v_add_f32 v218, v218, v151\n"
      "v_mfma_f32_16x16x32_bf16 v[164:167], a[236:239], v[68:71], v[164:167]\n"
      "v_exp_f32 v155, v155\n"
      "v_mfma_f32_16x16x32_bf16 a[76:79], a[204:207], v[168:171], a[76:79]\n"
      "v_add_f32_e64 v219, v219, v153\n"
      "v_cvt_pk_bf16_f32 v173, v154, v155\n"
      "v_mfma_f32_16x16x32_bf16 v[160:163], a[240:243], v[72:75], v[160:163]\n"
      "v_exp_f32 v156, v156\n"
      "v_mfma_f32_16x16x32_bf16 a[48:51], a[208:211], v[176:179], a[48:51]\n"
      "v_add_f32 v219, v219, v155\n"
      "v_add_f32 v219, v219, v154\n"
      "v_mfma_f32_16x16x32_bf16 v[164:167], a[244:247], v[72:75], v[164:167]\n"
      "buffer_load_dwordx4 v231, s[52:55], s80 offen offset:1024 nt lds\n"
      "v_exp_f32 v157, v157\n"
      "v_mfma_f32_16x16x32_bf16 a[52:55], a[212:215], v[176:179], a[52:55]\n"
      "v_add_f32 v219, v219, v156\n"
      "v_cvt_pk_bf16_f32 v174, v156, v157\n"
      "v_mfma_f32_16x16x32_bf16 v[160:163], a[248:251], v[76:79], v[160:163]\n"
      "v_exp_f32 v158, v158\n"
      "v_mfma_f32_16x16x32_bf16 a[56:59], a[216:219], v[176:179], a[56:59]\n"
      "v_add_f32 v219, v219, v158\n"
      "v_add_f32 v219, v219, v157\n"
      "v_mfma_f32_16x16x32_bf16 v[164:167], a[252:255], v[76:79], v[164:167]\n"
      "v_exp_f32 v159, v159\n"
      "v_mfma_f32_16x16x32_bf16 a[60:63], a[220:223], v[176:179], a[60:63]\n"
      "v_add_f32 v219, v219, v159\n"
      "v_cvt_pk_bf16_f32 v175, v158, v159\n"
      "v_mfma_f32_16x16x32_bf16 v[144:147], a[224:227], v[80:83], 0\n"
      "v_exp_f32 v160, v160\n"
      "v_mfma_f32_16x16x32_bf16 a[96:99], a[192:195], v[172:175], a[96:99]\n"
      "v_add_f32 v220, v220, v160\n"
      "v_mfma_f32_16x16x32_bf16 v[148:151], a[228:231], v[80:83], 0\n"
      "v_exp_f32 v161, v161\n"
      "v_mfma_f32_16x16x32_bf16 a[100:103], a[196:199], v[172:175], a[100:103]\n"
      "v_add_f32 v220, v220, v161\n"
      "v_cvt_pk_bf16_f32 v176, v160, v161\n"
      "v_mfma_f32_16x16x32_bf16 v[144:147], a[232:235], v[84:87], v[144:147]\n"
      "v_exp_f32 v162, v162\n"
      "v_mfma_f32_16x16x32_bf16 a[104:107], a[200:203], v[172:175], a[104:107]\n"
      "v_add_f32 v220, v220, v162\n"
      "v_mfma_f32_16x16x32_bf16 v[148:151], a[236:239], v[84:87], v[148:151]\n"
      "buffer_load_dwordx4 v230, s[52:55], s80 offen offset:2048 nt lds\n"
      "v_exp_f32 v163, v163\n"
      "v_mfma_f32_16x16x32_bf16 a[108:111], a[204:207], v[172:175], a[108:111]\n"
      "v_add_f32 v220, v220, v163\n"
      "v_cvt_pk_bf16_f32 v177, v162, v163\n"
      "v_mfma_f32_16x16x32_bf16 v[144:147], a[240:243], v[88:91], v[144:147]\n"
      "v_exp_f32 v164, v164\n"
      "v_mfma_f32_16x16x32_bf16 a[80:83], a[208:211], v[168:171], a[80:83]\n"
      "v_add_f32 v220, v220, v164\n"
      "v_mfma_f32_16x16x32_bf16 v[148:151], a[244:247], v[88:91], v[148:151]\n"
      "v_exp_f32 v165, v165\n"
      "v_mfma_f32_16x16x32_bf16 a[84:87], a[212:215], v[168:171], a[84:87]\n"
      "v_add_f32 v220, v220, v165\n"
      "v_cvt_pk_bf16_f32 v178, v164, v165\n"
      "v_mfma_f32_16x16x32_bf16 v[144:147], a[248:251], v[92:95], v[144:147]\n"
      "v_exp_f32 v166, v166\n"
      "v_mfma_f32_16x16x32_bf16 a[88:91], a[216:219], v[168:171], a[88:91]\n"
      "v_add_f32 v220, v220, v166\n"
      "v_mfma_f32_16x16x32_bf16 v[148:151], a[252:255], v[92:95], v[148:151]\n"
      "v_exp_f32_e64 v167, v167\n"
      "v_mfma_f32_16x16x32_bf16 a[92:95], a[220:223], v[168:171], a[92:95]\n"
      "v_add_f32 v220, v220, v167\n"
      "v_cvt_pk_bf16_f32 v179, v166, v167\n"
      "v_mfma_f32_16x16x32_bf16 v[152:155], a[224:227], v[96:99], 0\n"
      "v_exp_f32 v144, v144\n"
      "v_mfma_f32_16x16x32_bf16 a[128:131], a[192:195], v[176:179], a[128:131]\n"
      "ds_read_b128 a[224:227], v224 offset:32768\n"
      "v_add_f32_e64 v221, v221, v144\n"
      "v_mfma_f32_16x16x32_bf16 v[156:159], a[228:231], v[96:99], 0\n"
      "buffer_load_dwordx4 v231, s[52:55], s80 offen offset:3072 nt lds\n"
      "v_exp_f32_e64 v145, v145\n"
      "v_mfma_f32_16x16x32_bf16 a[132:135], a[196:199], v[176:179], a[132:135]\n"
      "ds_read_b128 a[228:231], v224 offset:36864\n"
      "v_cvt_pk_bf16_f32 v168, v144, v145\n"
      "v_mfma_f32_16x16x32_bf16 v[152:155], a[232:235], v[100:103], v[152:155]\n"
      "v_exp_f32_e64 v146, v146\n"
      "v_mfma_f32_16x16x32_bf16 a[136:139], a[200:203], v[176:179], a[136:139]\n"
      "ds_read_b128 a[232:235], v225 offset:32768\n"
      "v_add_f32 v221, v221, v145\n"
      "v_mfma_f32_16x16x32_bf16 v[156:159], a[236:239], v[100:103], v[156:159]\n"
      "v_exp_f32 v147, v147\n"
      "v_mfma_f32_16x16x32_bf16 a[140:143], a[204:207], v[176:179], a[140:143]\n"
      "ds_read_b128 a[236:239], v225 offset:36864\n"
      "v_cvt_pk_bf16_f32 v169, v146, v147\n"
      "v_mfma_f32_16x16x32_bf16 v[152:155], a[240:243], v[104:107], v[152:155]\n"
      "v_exp_f32_e64 v148, v148\n"
      "v_mfma_f32_16x16x32_bf16 a[112:115], a[208:211], v[172:175], a[112:115]\n"
      "ds_read_b128 a[240:243], v224 offset:33792\n"
      "v_add_f32_e64 v221, v221, v146\n"
      "v_mfma_f32_16x16x32_bf16 v[156:159], a[244:247], v[104:107], v[156:159]\n"
      "s_add_u32 m0, s68, 20480\n"
      "v_exp_f32_e64 v149, v149\n"
      "v_mfma_f32_16x16x32_bf16 a[116:119], a[212:215], v[172:175], a[116:119]\n"
      "ds_read_b128 a[244:247], v224 offset:37888\n"
      "s_add_u32 s64, s80, 4096\n"
      "v_cvt_pk_bf16_f32 v170, v148, v149\n"
      "v_mfma_f32_16x16x32_bf16 v[152:155], a[248:251], v[108:111], v[152:155]\n"
      "buffer_load_dwordx4 v230, s[52:55], s64 offen offset:0 nt lds\n"
      "v_exp_f32 v150, v150\n"
      "v_mfma_f32_16x16x32_bf16 a[120:123], a[216:219], v[172:175], a[120:123]\n"
      "ds_read_b128 a[248:251], v225 offset:33792\n"
      "v_add_f32 v221, v221, v147\n"
      "v_mfma_f32_16x16x32_bf16 v[156:159], a[252:255], v[108:111], v[156:159]\n"
      "s_waitcnt lgkmcnt(3)\n"
      "v_exp_f32 v151, v151\n"
      "v_mfma_f32_16x16x32_bf16 a[124:127], a[220:223], v[172:175], a[124:127]\n"
      "ds_read_b128 a[252:255], v225 offset:37888\n"
      "v_cvt_pk_bf16_f32 v171, v150, v151\n"
      "v_mfma_f32_16x16x32_bf16 v[160:163], a[224:227], v[0:3], 0\n"
      "v_exp_f32 v152, v152\n"
      "v_mfma_f32_16x16x32_bf16 v[180:183], a[192:195], v[168:171], v[180:183]\n"
      "v_add_f32 v221, v221, v149\n"
      "v_add_f32_e64 v221, v221, v148\n"
      "v_mfma_f32_16x16x32_bf16 v[164:167], a[228:231], v[0:3], 0\n"
      "v_exp_f32 v153, v153\n"
      "v_mfma_f32_16x16x32_bf16 v[184:187], a[196:199], v[168:171], v[184:187]\n"
      "v_add_f32 v221, v221, v150\n"
      "v_cvt_pk_bf16_f32 v172, v152, v153\n"
      "v_mfma_f32_16x16x32_bf16 v[160:163], a[232:235], v[4:7], v[160:163]\n"
      "v_exp_f32 v154, v154\n"
      "v_mfma_f32_16x16x32_bf16 v[188:191], a[200:203], v[168:171], v[188:191]\n"
      "v_add_f32 v222, v222, v152\n"
      "v_add_f32_e64 v221, v221, v151\n"
      "v_mfma_f32_16x16x32_bf16 v[164:167], a[236:239], v[4:7], v[164:167]\n"
      "s_waitcnt lgkmcnt(2)\n"
      "v_exp_f32 v155, v155\n"
      "v_mfma_f32_16x16x32_bf16 v[192:195], a[204:207], v[168:171], v[192:195]\n"
      "v_add_f32_e64 v222, v222, v153\n"
      "v_cvt_pk_bf16_f32 v173, v154, v155\n"
      "v_mfma_f32_16x16x32_bf16 v[160:163], a[240:243], v[8:11], v[160:163]\n"
      "v_exp_f32 v156, v156\n"
      "v_mfma_f32_16x16x32_bf16 a[144:147], a[208:211], v[176:179], a[144:147]\n"
      "v_add_f32 v222, v222, v155\n"
      "v_add_f32 v222, v222, v154\n"
      "v_mfma_f32_16x16x32_bf16 v[164:167], a[244:247], v[8:11], v[164:167]\n"
      "s_waitcnt lgkmcnt(0)\n"
      "v_exp_f32 v157, v157\n"
      "v_mfma_f32_16x16x32_bf16 a[148:151], a[212:215], v[176:179], a[148:151]\n"
      "v_add_f32 v222, v222, v156\n"
      "v_cvt_pk_bf16_f32 v174, v156, v157\n"
      "v_mfma_f32_16x16x32_bf16 v[160:163], a[248:251], v[12:15], v[160:163]\n"
      "v_exp_f32_e64 v158, v158\n"
      "v_mfma_f32_16x16x32_bf16 a[152:155], a[216:219], v[176:179], a[152:155]\n"
      "s_add_u32 m0, s68, 20480\n"
      "v_add_f32 v222, v222, v158\n"
      "v_add_f32 v222, v222, v157\n"
      "v_mfma_f32_16x16x32_bf16 v[164:167], a[252:255], v[12:15], v[164:167]\n"
      "buffer_load_dwordx4 v231, s[52:55], s64 offen offset:1024 nt lds\n"
      "v_exp_f32 v159, v159\n"
      "v_mfma_f32_16x16x32_bf16 a[156:159], a[220:223], v[176:179], a[156:159]\n"
      "v_add_f32 v222, v222, v159\n"
      "v_cvt_pk_bf16_f32 v175, v158, v159\n"
      "v_mfma_f32_16x16x32_bf16 v[144:147], a[224:227], v[16:19], 0\n"
      "v_exp_f32 v160, v160\n"
      "v_mfma_f32_16x16x32_bf16 v[112:115], a[192:195], v[172:175], v[112:115]\n"
      "ds_read_b64_tr_b16 a[192:193], v226 offset:32768\n"
      "v_add_f32 v216, v216, v160\n"
      "v_mfma_f32_16x16x32_bf16 v[148:151], a[228:231], v[16:19], 0\n"
      "v_exp_f32_e64 v161, v161\n"
      "v_mfma_f32_16x16x32_bf16 v[116:119], a[196:199], v[172:175], v[116:119]\n"
      "ds_read_b64_tr_b16 a[194:195], v226 offset:36864\n"
      "v_cvt_pk_bf16_f32 v176, v160, v161\n"
      "v_mfma_f32_16x16x32_bf16 v[144:147], a[232:235], v[20:23], v[144:147]\n"
      "v_exp_f32 v162, v162\n"
      "v_mfma_f32_16x16x32_bf16 v[120:123], a[200:203], v[172:175], v[120:123]\n"
      "ds_read_b64_tr_b16 a[196:197], v227 offset:32768\n"
      "v_add_f32 v216, v216, v161\n"
      "v_mfma_f32_16x16x32_bf16 v[148:151], a[236:239], v[20:23], v[148:151]\n"
      "v_exp_f32_e64 v163, v163\n"
      "v_mfma_f32_16x16x32_bf16 v[124:127], a[204:207], v[172:175], v[124:127]\n"
      "ds_read_b64_tr_b16 a[198:199], v227 offset:36864\n"
      "v_cvt_pk_bf16_f32 v177, v162, v163\n"
      "v_mfma_f32_16x16x32_bf16 v[144:147], a[240:243], v[24:27], v[144:147]\n"
      "v_exp_f32_e64 v164, v164\n"
      "v_mfma_f32_16x16x32_bf16 v[196:199], a[208:211], v[168:171], v[196:199]\n"
      "ds_read_b64_tr_b16 a[200:201], v228 offset:32768\n"
      "s_add_u32 m0, s68, 20480\n"
      "v_add_f32_e64 v216, v216, v162\n"
      "v_mfma_f32_16x16x32_bf16 v[148:151], a[244:247], v[24:27], v[148:151]\n"
      "buffer_load_dwordx4 v230, s[52:55], s64 offen offset:2048 nt lds\n"
      "v_exp_f32_e64 v165, v165\n"
      "v_mfma_f32_16x16x32_bf16 v[200:203], a[212:215], v[168:171], v[200:203]\n"
      "ds_read_b64_tr_b16 a[202:203], v228 offset:36864\n"
      "v_cvt_pk_bf16_f32 v178, v164, v165\n"
      "v_mfma_f32_16x16x32_bf16 v[144:147], a[248:251], v[28:31], v[144:147]\n"
      "v_exp_f32_e64 v166, v166\n"
      "v_mfma_f32_16x16x32_bf16 v[204:207], a[216:219], v[168:171], v[204:207]\n"
      "ds_read_b64_tr_b16 a[204:205], v229 offset:32768\n"
      "v_add_f32 v216, v216, v163\n"
      "v_mfma_f32_16x16x32_bf16 v[148:151], a[252:255], v[28:31], v[148:151]\n"
      "v_exp_f32 v167, v167\n"
      "v_mfma_f32_16x16x32_bf16 v[208:211], a[220:223], v[168:171], v[208:211]\n"
      "ds_read_b64_tr_b16 a[206:207], v229 offset:36864\n"
      "v_cvt_pk_bf16_f32 v179, v166, v167\n"
      "v_mfma_f32_16x16x32_bf16 v[152:155], a[224:227], v[32:35], 0\n"
      "v_exp_f32_e64 v144, v144\n"
      "v_mfma_f32_16x16x32_bf16 v[128:131], a[208:211], v[172:175], v[128:131]\n"
      "ds_read_b64_tr_b16 a[208:209], v226 offset:33792\n"
      "v_add_f32 v216, v216, v164\n"
      "v_mfma_f32_16x16x32_bf16 v[156:159], a[228:231], v[32:35], 0\n"
      "v_exp_f32 v145, v145\n"
      "v_mfma_f32_16x16x32_bf16 v[132:135], a[212:215], v[172:175], v[132:135]\n"
      "ds_read_b64_tr_b16 a[210:211], v226 offset:37888\n"
      "v_cvt_pk_bf16_f32 v168, v144, v145\n"
      "v_mfma_f32_16x16x32_bf16 v[152:155], a[232:235], v[36:39], v[152:155]\n"
      "v_exp_f32 v146, v146\n"
      "v_mfma_f32_16x16x32_bf16 v[136:139], a[216:219], v[172:175], v[136:139]\n"
      "ds_read_b64_tr_b16 a[212:213], v227 offset:33792\n"
      "v_add_f32 v216, v216, v165\n"
      "v_mfma_f32_16x16x32_bf16 v[156:159], a[236:239], v[36:39], v[156:159]\n"
      "v_exp_f32_e64 v147, v147\n"
      "v_mfma_f32_16x16x32_bf16 v[140:143], a[220:223], v[172:175], v[140:143]\n"
      "ds_read_b64_tr_b16 a[214:215], v227 offset:37888\n"
      "v_cvt_pk_bf16_f32 v169, v146, v147\n"
      "v_mfma_f32_16x16x32_bf16 v[152:155], a[240:243], v[40:43], v[152:155]\n"
      "s_waitcnt lgkmcnt(4)\n"
      "v_exp_f32 v148, v148\n"
      "v_mfma_f32_16x16x32_bf16 a[0:3], a[192:195], v[176:179], a[0:3]\n"
      "ds_read_b64_tr_b16 a[216:217], v228 offset:33792\n"
      "v_add_f32 v216, v216, v166\n"
      "v_mfma_f32_16x16x32_bf16 v[156:159], a[244:247], v[40:43], v[156:159]\n"
      "v_exp_f32 v149, v149\n"
      "v_mfma_f32_16x16x32_bf16 a[4:7], a[196:199], v[176:179], a[4:7]\n"
      "ds_read_b64_tr_b16 a[218:219], v228 offset:37888\n"
      "s_add_u32 m0, s68, 20480\n"
      "v_cvt_pk_bf16_f32 v170, v148, v149\n"
      "v_mfma_f32_16x16x32_bf16 v[152:155], a[248:251], v[44:47], v[152:155]\n"
      "buffer_load_dwordx4 v231, s[52:55], s64 offen offset:3072 nt lds\n"
      "v_exp_f32 v150, v150\n"
      "v_mfma_f32_16x16x32_bf16 a[8:11], a[200:203], v[176:179], a[8:11]\n"
      "ds_read_b64_tr_b16 a[220:221], v229 offset:33792\n"
      "s_add_u32 s80, s80, 8192\n"
      "v_add_f32 v216, v216, v167\n"
      "v_mfma_f32_16x16x32_bf16 v[156:159], a[252:255], v[44:47], v[156:159]\n"
      "s_and_b32 s80, s80, 122880\n"
      "v_exp_f32_e64 v151, v151\n"
      "v_mfma_f32_16x16x32_bf16 a[12:15], a[204:207], v[176:179], a[12:15]\n"
      "ds_read_b64_tr_b16 a[222:223], v229 offset:37888\n"
      "v_cvt_pk_bf16_f32 v171, v150, v151\n"
      "v_mfma_f32_16x16x32_bf16 v[160:163], a[224:227], v[48:51], 0\n"
      "v_exp_f32_e64 v152, v152\n"
      "v_mfma_f32_16x16x32_bf16 a[32:35], a[192:195], v[168:171], a[32:35]\n"
      "v_add_f32 v217, v217, v145\n"
      "v_add_f32 v217, v217, v144\n"
      "v_mfma_f32_16x16x32_bf16 v[164:167], a[228:231], v[48:51], 0\n"
      "v_exp_f32_e64 v153, v153\n"
      "v_mfma_f32_16x16x32_bf16 a[36:39], a[196:199], v[168:171], a[36:39]\n"
      "v_add_f32_e64 v217, v217, v146\n"
      "v_cvt_pk_bf16_f32 v172, v152, v153\n"
      "v_mfma_f32_16x16x32_bf16 v[160:163], a[232:235], v[52:55], v[160:163]\n"
      "s_memrealtime s[76:77]\n"
      "v_exp_f32_e64 v154, v154\n"
      "v_mfma_f32_16x16x32_bf16 a[40:43], a[200:203], v[168:171], a[40:43]\n"
      "v_add_f32_e64 v217, v217, v148\n"
      "v_add_f32_e64 v217, v217, v147\n"
      "v_mfma_f32_16x16x32_bf16 v[164:167], a[236:239], v[52:55], v[164:167]\n"
      "v_exp_f32_e64 v155, v155\n"
      "v_mfma_f32_16x16x32_bf16 a[44:47], a[204:207], v[168:171], a[44:47]\n"
      "v_add_f32 v217, v217, v149\n"
      "v_cvt_pk_bf16_f32 v173, v154, v155\n"
      "v_mfma_f32_16x16x32_bf16 v[160:163], a[240:243], v[56:59], v[160:163]\n"
      "s_waitcnt vmcnt(8) lgkmcnt(0)\n"
      "v_exp_f32_e64 v156, v156\n"
      "v_mfma_f32_16x16x32_bf16 a[16:19], a[208:211], v[176:179], a[16:19]\n"
      "s_sub_u32 s77, s76, s96\n"
      "s_cmp_lt_i32 s77, 0\n"
      "s_cbranch_scc1 PC2_%=\n"
      "PC2R_%=:\n"
      "v_add_f32 v217, v217, v151\n"
      "v_add_f32_e64 v217, v217, v150\n"
      "v_mfma_f32_16x16x32_bf16 v[164:167], a[244:247], v[56:59], v[164:167]\n"
      "s_barrier\n"
      "v_exp_f32_e64 v157, v157\n"
      "v_mfma_f32_16x16x32_bf16 a[20:23], a[212:215], v[176:179], a[20:23]\n"
      "s_add_u32 s96, s76, s78\n"
      "v_add_f32_e64 v218, v218, v152\n"
      "v_cvt_pk_bf16_f32 v174, v156, v157\n"
      "v_mfma_f32_16x16x32_bf16 v[160:163], a[248:251], v[60:63], v[160:163]\n"
      "s_cmp_eq_u32 s83, 4\n"
      "s_cbranch_scc0 SYR_%=\n"
      "v_subrev_u32_e64 v212, s79, v212\n"
      "v_add_u32 v214, 0x200, v212\n"
      "v_cmp_gt_u32 vcc, 0x400, v214\n"
      "v_cmp_eq_u32 s[42:43], 0, v213\n"
      "v_cmp_gt_i32 s[84:85], 0, v213\n"
      "v_cndmask_b32 v212, 0, v212, vcc\n"
      "v_cmp_ge_i32 s[66:67], 1, v213\n"
      "s_and_b64 s[42:43], s[42:43], vcc\n"
      "v_add_u32_dpp v212, v212, v212 row_ror:8 row_mask:0xf bank_mask:0xf\n"
      "s_and_b64 s[84:85], s[84:85], vcc\n"
      "s_and_b64 s[66:67], s[66:67], vcc\n"
      "v_add_u32_dpp v212, v212, v212 row_ror:4 row_mask:0xf bank_mask:0xf\n"
      "s_bcnt1_i32_b64 s42, s[42:43]\n"
      "s_bcnt1_i32_b64 s43, s[84:85]\n"
      "v_add_u32_dpp v212, v212, v212 row_ror:2 row_mask:0xf bank_mask:0xf\n"
      "s_bcnt1_i32_b64 s66, s[66:67]\n"
      "s_mov_b32 s85, 395275\n"
      "v_add_u32_dpp v212, v212, v212 row_ror:1 row_mask:0xf bank_mask:0xf\n"
      "s_cmp_gt_u32 s66, 31\n"
      "s_cselect_b32 s85, 1185306, s85\n"
      "v_add_u32_dpp v212, v212, v212 row_bcast:15 row_mask:0xa bank_mask:0xf\n"
      "s_cselect_b32 s84, 22, 4\n"
      "s_cselect_b32 s79, 2, 1\n"
      "v_add_u32_dpp v212, v212, v212 row_bcast:31 row_mask:0xc bank_mask:0xf\n"
      "s_cmp_lt_u32 s43, 20\n"
      "s_cselect_b32 s79, s79, 0\n"
      "s_cselect_b32 s43, s84, -5\n"
      "s_cselect_b32 s85, s85, 0\n"
      "s_cmp_gt_u32 s42, 31\n"
      "s_cselect_b32 s79, 0, s79\n"
      "s_cselect_b32 s43, -12, s43\n"
      "s_cselect_b32 s85, -1577482, s85\n"
      "s_add_i32 s96, s96, s43\n"
      "s_add_i32 s78, s78, s43\n"
      "v_readlane_b32 s84, v212, 63\n"
      "s_add_i32 s81, s81, s85\n"
      "s_min_i32 s84, s84, 7676\n"
      "s_max_i32 s84, s84, -7676\n"
      "s_add_i32 s84, s84, 144\n"
      "s_mul_hi_i32 s85, s84, 3735552\n"
      "s_add_u32 s81, s81, s85\n"
      "s_mul_hi_i32 s85, s84, 7454720\n"
      "s_lshl_b32 s85, s85, 8\n"
      "s_add_u32 s81, s81, s85\n"
      "s_mul_hi_i32 s85, s84, 11190272\n"
      "s_lshl_b32 s85, s85, 16\n"
      "s_add_u32 s81, s81, s85\n"
      "s_cmp_gt_u32 s42, 31\n"
      "s_cbranch_scc1 SYRB_%=\n"
      "SYR_%=:\n"
      "v_exp_f32 v158, v158\n"
      "v_mfma_f32_16x16x32_bf16 a[24:27], a[216:219], v[176:179], a[24:27]\n"
      "s_add_u32 m0, s68, 32768\n"
      "v_add_f32 v218, v218, v154\n"
      "v_add_f32 v218, v218, v153\n"
      "v_mfma_f32_16x16x32_bf16 v[164:167], a[252:255], v[60:63], v[164:167]\n"
      "buffer_load_dwordx4 v230, s[52:55], s80 offen offset:0 nt lds\n"
      "v_exp_f32 v159, v159\n"
      "v_mfma_f32_16x16x32_bf16 a[28:31], a[220:223], v[176:179], a[28:31]\n"
      "v_add_f32 v218, v218, v155\n"
      "v_cvt_pk_bf16_f32 v175, v158, v159\n"
      "v_mfma_f32_16x16x32_bf16 v[144:147], a[224:227], v[64:67], 0\n"
      "v_exp_f32 v160, v160\n"
      "v_mfma_f32_16x16x32_bf16 a[64:67], a[192:195], v[172:175], a[64:67]\n"
      "v_add_f32 v218, v218, v157\n"
      "v_add_f32_e64 v218, v218, v156\n"
      "v_mfma_f32_16x16x32_bf16 v[148:151], a[228:231], v[64:67], 0\n"
      "v_exp_f32 v161, v161\n"
      "v_mfma_f32_16x16x32_bf16 a[68:71], a[196:199], v[172:175], a[68:71]\n"
      "v_add_f32 v218, v218, v158\n"
      "v_cvt_pk_bf16_f32 v176, v160, v161\n"
      "v_mfma_f32_16x16x32_bf16 v[144:147], a[232:235], v[68:71], v[144:147]\n"
      "v_exp_f32 v162, v162\n"
      "v_mfma_f32_16x16x32_bf16 a[72:75], a[200:203], v[172:175], a[72:75]\n"
      "v_add_f32 v219, v219, v160\n"
      "v_add_f32_e64 v218, v218, v159\n"
      "v_mfma_f32_16x16x32_bf16 v[148:151], a[236:239], v[68:71], v[148:151]\n"
      "v_exp_f32 v163, v163\n"
      "v_mfma_f32_16x16x32_bf16 a[76:79], a[204:207], v[172:175], a[76:79]\n"
      "v_add_f32 v219, v219, v161\n"
      "v_cvt_pk_bf16_f32 v177, v162, v163\n"
      "v_mfma_f32_16x16x32_bf16 v[144:147], a[240:243], v[72:75], v[144:147]\n"
      "v_exp_f32 v164, v164\n"
      "v_mfma_f32_16x16x32_bf16 a[48:51], a[208:211], v[168:171], a[48:51]\n"
      "v_add_f32 v219, v219, v163\n"
      "v_add_f32_e64 v219, v219, v162\n"
      "v_mfma_f32_16x16x32_bf16 v[148:151], a[244:247], v[72:75], v[148:151]\n"
      "buffer_load_dwordx4 v231, s[52:55], s80 offen offset:1024 nt lds\n"
      "v_exp_f32 v165, v165\n"
      "v_mfma_f32_16x16x32_bf16 a[52:55], a[212:215], v[168:171], a[52:55]\n"
      "v_add_f32 v219, v219, v164\n"
      "v_cvt_pk_bf16_f32 v178, v164, v165\n"
      "v_mfma_f32_16x16x32_bf16 v[144:147], a[248:251], v[76:79], v[144:147]\n"
      "v_exp_f32 v166, v166\n"
      "v_mfma_f32_16x16x32_bf16 a[56:59], a[216:219], v[168:171], a[56:59]\n"
      "v_add_f32 v219, v219, v166\n"
      "v_add_f32 v219, v219, v165\n"
      "v_mfma_f32_16x16x32_bf16 v[148:151], a[252:255], v[76:79], v[148:151]\n"
      "v_exp_f32 v167, v167\n"
      "v_mfma_f32_16x16x32_bf16 a[60:63], a[220:223], v[168:171], a[60:63]\n"
      "v_add_f32_e64 v219, v219, v167\n"
      "v_cvt_pk_bf16_f32 v179, v166, v167\n"
      "v_mfma_f32_16x16x32_bf16 v[152:155], a[224:227], v[80:83], 0\n"
      "v_exp_f32 v144, v144\n"
      "v_mfma_f32_16x16x32_bf16 a[96:99], a[192:195], v[176:179], a[96:99]\n"
      "v_add_f32 v220, v220, v144\n"
      "v_mfma_f32_16x16x32_bf16 v[156:159], a[228:231], v[80:83], 0\n"
      "v_exp_f32 v145, v145\n"
      "v_mfma_f32_16x16x32_bf16 a[100:103], a[196:199], v[176:179], a[100:103]\n"
      "v_add_f32 v220, v220, v145\n"
      "v_cvt_pk_bf16_f32 v168, v144, v145\n"
      "v_mfma_f32_16x16x32_bf16 v[152:155], a[232:235], v[84:87], v[152:155]\n"
      "v_exp_f32 v146, v146\n"
      "v_mfma_f32_16x16x32_bf16 a[104:107], a[200:203], v[176:179], a[104:107]\n"
      "v_add_f32 v220, v220, v146\n"
      "v_mfma_f32_16x16x32_bf16 v[156:159], a[236:239], v[84:87], v[156:159]\n"
      "buffer_load_dwordx4 v230, s[52:55], s80 offen offset:2048 nt lds\n"
      "v_exp_f32 v147, v147\n"
      "v_mfma_f32_16x16x32_bf16 a[108:111], a[204:207], v[176:179], a[108:111]\n"
      "v_add_f32 v220, v220, v147\n"
      "v_cvt_pk_bf16_f32 v169, v146, v147\n"
      "v_mfma_f32_16x16x32_bf16 v[152:155], a[240:243], v[88:91], v[152:155]\n"
      "v_exp_f32 v148, v148\n"
      "v_mfma_f32_16x16x32_bf16 a[80:83], a[208:211], v[172:175], a[80:83]\n"
      "v_add_f32 v220, v220, v148\n"
      "v_mfma_f32_16x16x32_bf16 v[156:159], a[244:247], v[88:91], v[156:159]\n"
      "v_exp_f32 v149, v149\n"
      "v_mfma_f32_16x16x32_bf16 a[84:87], a[212:215], v[172:175], a[84:87]\n"
      "v_add_f32 v220, v220, v149\n"
      "v_cvt_pk_bf16_f32 v170, v148, v149\n"
      "v_mfma_f32_16x16x32_bf16 v[152:155], a[248:251], v[92:95], v[152:155]\n"
      "v_exp_f32 v150, v150\n"
      "v_mfma_f32_16x16x32_bf16 a[88:91], a[216:219], v[172:175], a[88:91]\n"
      "v_add_f32 v220, v220, v150\n"
      "v_mfma_f32_16x16x32_bf16 v[156:159], a[252:255], v[92:95], v[156:159]\n"
      "v_exp_f32_e64 v151, v151\n"
      "v_mfma_f32_16x16x32_bf16 a[92:95], a[220:223], v[172:175], a[92:95]\n"
      "v_add_f32 v220, v220, v151\n"
      "v_cvt_pk_bf16_f32 v171, v150, v151\n"
      "v_mfma_f32_16x16x32_bf16 v[160:163], a[224:227], v[96:99], 0\n"
      "v_exp_f32 v152, v152\n"
      "v_mfma_f32_16x16x32_bf16 a[128:131], a[192:195], v[168:171], a[128:131]\n"
      "ds_read_b128 a[224:227], v224 offset:0\n"
      "v_add_f32_e64 v221, v221, v152\n"
      "v_mfma_f32_16x16x32_bf16 v[164:167], a[228:231], v[96:99], 0\n"
      "buffer_load_dwordx4 v231, s[52:55], s80 offen offset:3072 nt lds\n"
      "v_exp_f32_e64 v153, v153\n"
      "v_mfma_f32_16x16x32_bf16 a[132:135], a[196:199], v[168:171], a[132:135]\n"
      "ds_read_b128 a[228:231], v224 offset:4096\n"
      "v_cvt_pk_bf16_f32 v172, v152, v153\n"
      "v_mfma_f32_16x16x32_bf16 v[160:163], a[232:235], v[100:103], v[160:163]\n"
      "v_exp_f32_e64 v154, v154\n"
      "v_mfma_f32_16x16x32_bf16 a[136:139], a[200:203], v[168:171], a[136:139]\n"
      "ds_read_b128 a[232:235], v225 offset:0\n"
      "v_add_f32 v221, v221, v153\n"
      "v_mfma_f32_16x16x32_bf16 v[164:167], a[236:239], v[100:103], v[164:167]\n"
      "v_exp_f32 v155, v155\n"
      "v_mfma_f32_16x16x32_bf16 a[140:143], a[204:207], v[168:171], a[140:143]\n"
      "ds_read_b128 a[236:239], v225 offset:4096\n"
      "v_cvt_pk_bf16_f32 v173, v154, v155\n"
      "v_mfma_f32_16x16x32_bf16 v[160:163], a[240:243], v[104:107], v[160:163]\n"
      "v_exp_f32_e64 v156, v156\n"
      "v_mfma_f32_16x16x32_bf16 a[112:115], a[208:211], v[176:179], a[112:115]\n"
      "ds_read_b128 a[240:243], v224 offset:1024\n"
      "v_add_f32_e64 v221, v221, v154\n"
      "v_mfma_f32_16x16x32_bf16 v[164:167], a[244:247], v[104:107], v[164:167]\n"
      "s_add_u32 m0, s68, 36864\n"
      "v_exp_f32_e64 v157, v157\n"
      "v_mfma_f32_16x16x32_bf16 a[116:119], a[212:215], v[176:179], a[116:119]\n"
      "ds_read_b128 a[244:247], v224 offset:5120\n"
      "s_add_u32 s64, s80, 4096\n"
      "v_cvt_pk_bf16_f32 v174, v156, v157\n"
      "v_mfma_f32_16x16x32_bf16 v[160:163], a[248:251], v[108:111], v[160:163]\n"
      "buffer_load_dwordx4 v230, s[52:55], s64 offen offset:0 nt lds\n"
      "v_exp_f32 v158, v158\n"
      "v_mfma_f32_16x16x32_bf16 a[120:123], a[216:219], v[176:179], a[120:123]\n"
      "ds_read_b128 a[248:251], v225 offset:1024\n"
      "v_add_f32 v221, v221, v155\n"
      "v_mfma_f32_16x16x32_bf16 v[164:167], a[252:255], v[108:111], v[164:167]\n"
      "s_waitcnt lgkmcnt(3)\n"
      "v_exp_f32 v159, v159\n"
      "v_mfma_f32_16x16x32_bf16 a[124:127], a[220:223], v[176:179], a[124:127]\n"
      "ds_read_b128 a[252:255], v225 offset:5120\n"
      "v_cvt_pk_bf16_f32 v175, v158, v159\n"
      "v_mfma_f32_16x16x32_bf16 v[144:147], a[224:227], v[0:3], 0\n"
      "v_exp_f32 v160, v160\n"
      "v_mfma_f32_16x16x32_bf16 v[180:183], a[192:195], v[172:175], v[180:183]\n"
      "s_add_u32 m0, s73, 0\n"
      "v_add_f32 v221, v221, v157\n"
      "v_add_f32 v221, v221, v156\n"
      "v_mfma_f32_16x16x32_bf16 v[148:151], a[228:231], v[0:3], 0\n"
      "buffer_load_dwordx4 v234, s[56:59], s86 offen lds\n"
      "v_exp_f32 v161, v161\n"
      "v_mfma_f32_16x16x32_bf16 v[184:187], a[196:199], v[172:175], v[184:187]\n"
      "v_add_f32 v221, v221, v158\n"
      "v_cvt_pk_bf16_f32 v176, v160, v161\n"
      "v_mfma_f32_16x16x32_bf16 v[144:147], a[232:235], v[4:7], v[144:147]\n"
      "v_exp_f32 v162, v162\n"
      "v_mfma_f32_16x16x32_bf16 v[188:191], a[200:203], v[172:175], v[188:191]\n"
      "v_add_f32 v222, v222, v160\n"
      "v_add_f32 v221, v221, v159\n"
      "v_mfma_f32_16x16x32_bf16 v[148:151], a[236:239], v[4:7], v[148:151]\n"
      "s_waitcnt lgkmcnt(2)\n"
      "v_exp_f32_e64 v163, v163\n"
      "v_mfma_f32_16x16x32_bf16 v[192:195], a[204:207], v[172:175], v[192:195]\n"
      "s_add_u32 m0, s73, 1024\n"
      "v_add_f32_e64 v222, v222, v161\n"
      "v_cvt_pk_bf16_f32 v177, v162, v163\n"
      "v_mfma_f32_16x16x32_bf16 v[144:147], a[240:243], v[8:11], v[144:147]\n"
      "buffer_load_dwordx4 v235, s[56:59], s86 offen lds\n"
      "v_exp_f32 v164, v164\n"
      "v_mfma_f32_16x16x32_bf16 a[144:147], a[208:211], v[168:171], a[144:147]\n"
      "v_add_f32 v222, v222, v163\n"
      "v_add_f32_e64 v222, v222, v162\n"
      "v_mfma_f32_16x16x32_bf16 v[148:151], a[244:247], v[8:11], v[148:151]\n"
      "s_waitcnt lgkmcnt(0)\n"
      "v_exp_f32 v165, v165\n"
      "v_mfma_f32_16x16x32_bf16 a[148:151], a[212:215], v[168:171], a[148:151]\n"
      "v_add_f32_e64 v222, v222, v164\n"
      "v_cvt_pk_bf16_f32 v178, v164, v165\n"
      "v_mfma_f32_16x16x32_bf16 v[144:147], a[248:251], v[12:15], v[144:147]\n"
      "v_exp_f32_e64 v166, v166\n"
      "v_mfma_f32_16x16x32_bf16 a[152:155], a[216:219], v[168:171], a[152:155]\n"
      "s_add_u32 m0, s68, 36864\n"
      "v_add_f32 v222, v222, v166\n"
      "v_add_f32 v222, v222, v165\n"
      "v_mfma_f32_16x16x32_bf16 v[148:151], a[252:255], v[12:15], v[148:151]\n"
      "buffer_load_dwordx4 v231, s[52:55], s64 offen offset:1024 nt lds\n"
      "v_exp_f32 v167, v167\n"
      "v_mfma_f32_16x16x32_bf16 a[156:159], a[220:223], v[168:171], a[156:159]\n"
      "v_add_f32 v222, v222, v167\n"
      "v_cvt_pk_bf16_f32 v179, v166, v167\n"
      "v_mfma_f32_16x16x32_bf16 v[152:155], a[224:227], v[16:19], 0\n"
      "v_exp_f32 v144, v144\n"
      "v_mfma_f32_16x16x32_bf16 v[112:115], a[192:195], v[176:179], v[112:115]\n"
      "ds_read_b64_tr_b16 a[192:193], v226 offset:0\n"
      "v_add_f32 v216, v216, v144\n"
      "v_mfma_f32_16x16x32_bf16 v[156:159], a[228:231], v[16:19], 0\n"
      "s_add_u32 m0, s73, 2048\n"
      "v_exp_f32_e64 v145, v145\n"
      "v_mfma_f32_16x16x32_bf16 v[116:119], a[196:199], v[176:179], v[116:119]\n"
      "ds_read_b64_tr_b16 a[194:195], v226 offset:4096\n"
      "s_add_u32 s65, s86, 8192\n"
      "v_cvt_pk_bf16_f32 v168, v144, v145\n"
      "v_mfma_f32_16x16x32_bf16 v[152:155], a[232:235], v[20:23], v[152:155]\n"
      "buffer_load_dwordx4 v234, s[56:59], s65 offen lds\n"
      "v_exp_f32 v146, v146\n"
      "v_mfma_f32_16x16x32_bf16 v[120:123], a[200:203], v[176:179], v[120:123]\n"
      "ds_read_b64_tr_b16 a[196:197], v227 offset:0\n"
      "v_add_f32 v216, v216, v145\n"
      "v_mfma_f32_16x16x32_bf16 v[156:159], a[236:239], v[20:23], v[156:159]\n"
      "v_exp_f32_e64 v147, v147\n"
      "v_mfma_f32_16x16x32_bf16 v[124:127], a[204:207], v[176:179], v[124:127]\n"
      "ds_read_b64_tr_b16 a[198:199], v227 offset:4096\n"
      "v_cvt_pk_bf16_f32 v169, v146, v147\n"
      "v_mfma_f32_16x16x32_bf16 v[152:155], a[240:243], v[24:27], v[152:155]\n"
      "v_exp_f32_e64 v148, v148\n"
      "v_mfma_f32_16x16x32_bf16 v[196:199], a[208:211], v[172:175], v[196:199]\n"
      "ds_read_b64_tr_b16 a[200:201], v228 offset:0\n"
      "s_add_u32 m0, s68, 36864\n"
      "v_add_f32 v216, v216, v146\n"
      "v_mfma_f32_16x16x32_bf16 v[156:159], a[244:247], v[24:27], v[156:159]\n"
      "buffer_load_dwordx4 v230, s[52:55], s64 offen offset:2048 nt lds\n"
      "v_exp_f32 v149, v149\n"
      "v_mfma_f32_16x16x32_bf16 v[200:203], a[212:215], v[172:175], v[200:203]\n"
      "ds_read_b64_tr_b16 a[202:203], v228 offset:4096\n"
      "v_cvt_pk_bf16_f32 v170, v148, v149\n"
      "v_mfma_f32_16x16x32_bf16 v[152:155], a[248:251], v[28:31], v[152:155]\n"
      "v_exp_f32 v150, v150\n"
      "v_mfma_f32_16x16x32_bf16 v[204:207], a[216:219], v[172:175], v[204:207]\n"
      "ds_read_b64_tr_b16 a[204:205], v229 offset:0\n"
      "v_add_f32 v216, v216, v147\n"
      "v_mfma_f32_16x16x32_bf16 v[156:159], a[252:255], v[28:31], v[156:159]\n"
      "v_exp_f32_e64 v151, v151\n"
      "v_mfma_f32_16x16x32_bf16 v[208:211], a[220:223], v[172:175], v[208:211]\n"
      "ds_read_b64_tr_b16 a[206:207], v229 offset:4096\n"
      "v_cvt_pk_bf16_f32 v171, v150, v151\n"
      "v_mfma_f32_16x16x32_bf16 v[160:163], a[224:227], v[32:35], 0\n"
      "v_exp_f32_e64 v152, v152\n"
      "v_mfma_f32_16x16x32_bf16 v[128:131], a[208:211], v[176:179], v[128:131]\n"
      "ds_read_b64_tr_b16 a[208:209], v226 offset:1024\n"
      "s_add_u32 m0, s73, 3072\n"
      "v_add_f32_e64 v216, v216, v148\n"
      "v_mfma_f32_16x16x32_bf16 v[164:167], a[228:231], v[32:35], 0\n"
      "buffer_load_dwordx4 v235, s[56:59], s65 offen lds\n"
      "v_exp_f32 v153, v153\n"
      "v_mfma_f32_16x16x32_bf16 v[132:135], a[212:215], v[176:179], v[132:135]\n"
      "ds_read_b64_tr_b16 a[210:211], v226 offset:5120\n"
      "s_add_u32 s86, s86, s75\n"
      "v_cvt_pk_bf16_f32 v172, v152, v153\n"
      "v_mfma_f32_16x16x32_bf16 v[160:163], a[232:235], v[36:39], v[160:163]\n"
      "s_and_b32 s86, s86, 0xfffff\n"
      "v_exp_f32_e64 v154, v154\n"
      "v_mfma_f32_16x16x32_bf16 v[136:139], a[216:219], v[176:179], v[136:139]\n"
      "ds_read_b64_tr_b16 a[212:213], v227 offset:1024\n"
      "s_xor_b32 s75, s75, 98304\n"
      "v_add_f32 v216, v216, v149\n"
      "v_mfma_f32_16x16x32_bf16 v[164:167], a[236:239], v[36:39], v[164:167]\n"
      "s_add_u32 s73, s73, s74\n"
      "v_exp_f32_e64 v155, v155\n"
      "v_mfma_f32_16x16x32_bf16 v[140:143], a[220:223], v[176:179], v[140:143]\n"
      "ds_read_b64_tr_b16 a[214:215], v227 offset:5120\n"
      "s_xor_b32 s74, s74, 24576\n"
      "v_cvt_pk_bf16_f32 v173, v154, v155\n"
      "v_mfma_f32_16x16x32_bf16 v[160:163], a[240:243], v[40:43], v[160:163]\n"
      "s_waitcnt lgkmcnt(4)\n"
      "v_exp_f32 v156, v156\n"
      "v_mfma_f32_16x16x32_bf16 a[0:3], a[192:195], v[168:171], a[0:3]\n"
      "ds_read_b64_tr_b16 a[216:217], v228 offset:1024\n"
      "v_add_f32 v216, v216, v150\n"
      "v_mfma_f32_16x16x32_bf16 v[164:167], a[244:247], v[40:43], v[164:167]\n"
      "v_exp_f32 v157, v157\n"
      "v_mfma_f32_16x16x32_bf16 a[4:7], a[196:199], v[168:171], a[4:7]\n"
      "ds_read_b64_tr_b16 a[218:219], v228 offset:5120\n"
      "s_add_u32 m0, s68, 36864\n"
      "v_cvt_pk_bf16_f32 v174, v156, v157\n"
      "v_mfma_f32_16x16x32_bf16 v[160:163], a[248:251], v[44:47], v[160:163]\n"
      "buffer_load_dwordx4 v231, s[52:55], s64 offen offset:3072 nt lds\n"
      "v_exp_f32_e64 v158, v158\n"
      "v_mfma_f32_16x16x32_bf16 a[8:11], a[200:203], v[168:171], a[8:11]\n"
      "ds_read_b64_tr_b16 a[220:221], v229 offset:1024\n"
      "s_add_u32 s80, s80, 8192\n"
      "v_add_f32 v216, v216, v151\n"
      "v_mfma_f32_16x16x32_bf16 v[164:167], a[252:255], v[44:47], v[164:167]\n"
      "s_and_b32 s80, s80, 122880\n"
      "v_exp_f32 v159, v159\n"
      "v_mfma_f32_16x16x32_bf16 a[12:15], a[204:207], v[168:171], a[12:15]\n"
      "ds_read_b64_tr_b16 a[222:223], v229 offset:5120\n"
      "v_cvt_pk_bf16_f32 v175, v158, v159\n"
      "v_mfma_f32_16x16x32_bf16 v[144:147], a[224:227], v[48:51], 0\n"
      "v_exp_f32 v160, v160\n"
      "v_mfma_f32_16x16x32_bf16 a[32:35], a[192:195], v[172:175], a[32:35]\n"
      "v_add_f32 v217, v217, v153\n"
      "v_add_f32 v217, v217, v152\n"
      "v_mfma_f32_16x16x32_bf16 v[148:151], a[228:231], v[48:51], 0\n"
      "v_exp_f32 v161, v161\n"
      "v_mfma_f32_16x16x32_bf16 a[36:39], a[196:199], v[172:175], a[36:39]\n"
      "v_add_f32_e64 v217, v217, v154\n"
      "v_cvt_pk_bf16_f32 v176, v160, v161\n"
      "v_mfma_f32_16x16x32_bf16 v[144:147], a[232:235], v[52:55], v[144:147]\n"
      "s_memrealtime s[76:77]\n"
      "v_exp_f32 v162, v162\n"
      "v_mfma_f32_16x16x32_bf16 a[40:43], a[200:203], v[172:175], a[40:43]\n"
      "v_add_f32 v217, v217, v156\n"
      "v_add_f32_e64 v217, v217, v155\n"
      "v_mfma_f32_16x16x32_bf16 v[148:151], a[236:239], v[52:55], v[148:151]\n"
      "v_exp_f32 v163, v163\n"
      "v_mfma_f32_16x16x32_bf16 a[44:47], a[204:207], v[172:175], a[44:47]\n"
      "v_add_f32 v217, v217, v157\n"
      "v_cvt_pk_bf16_f32 v177, v162, v163\n"
      "v_mfma_f32_16x16x32_bf16 v[144:147], a[240:243], v[56:59], v[144:147]\n"
      "s_waitcnt vmcnt(12) lgkmcnt(0)\n"
      "v_exp_f32 v164, v164\n"
      "v_mfma_f32_16x16x32_bf16 a[16:19], a[208:211], v[168:171], a[16:19]\n"
      "s_sub_u32 s77, s76, s96\n"
      "s_cmp_lt_i32 s77, 0\n"
      "s_cbranch_scc1 PC0_%=\n"
      "PC0R_%=:\n"
      "v_add_f32 v217, v217, v159\n"
      "v_add_f32 v217, v217, v158\n"
      "v_mfma_f32_16x16x32_bf16 v[148:151], a[244:247], v[56:59], v[148:151]\n"
      "s_barrier\n"
      "v_exp_f32_e64 v165, v165\n"
      "v_mfma_f32_16x16x32_bf16 a[20:23], a[212:215], v[168:171], a[20:23]\n"
      "s_add_u32 s96, s76, s78\n"
      "v_add_f32 v218, v218, v160\n"
      "v_cvt_pk_bf16_f32 v178, v164, v165\n"
      "v_mfma_f32_16x16x32_bf16 v[144:147], a[248:251], v[60:63], v[144:147]\n"
      "v_exp_f32 v166, v166\n"
      "v_mfma_f32_16x16x32_bf16 a[24:27], a[216:219], v[168:171], a[24:27]\n"
      "s_add_u32 m0, s68, 0\n"
      "v_add_f32 v218, v218, v162\n"
      "v_add_f32 v218, v218, v161\n"
      "v_mfma_f32_16x16x32_bf16 v[148:151], a[252:255], v[60:63], v[148:151]\n"
      "buffer_load_dwordx4 v230, s[52:55], s80 offen offset:0 nt lds\n"
      "v_exp_f32 v167, v167\n"
      "v_mfma_f32_16x16x32_bf16 a[28:31], a[220:223], v[168:171], a[28:31]\n"
      "v_add_f32 v218, v218, v163\n"
      "v_cvt_pk_bf16_f32 v179, v166, v167\n"
      "v_mfma_f32_16x16x32_bf16 v[152:155], a[224:227], v[64:67], 0\n"
      "v_exp_f32 v144, v144\n"
      "v_mfma_f32_16x16x32_bf16 a[64:67], a[192:195], v[176:179], a[64:67]\n"
      "v_add_f32 v218, v218, v165\n"
      "v_add_f32_e64 v218, v218, v164\n"
      "v_mfma_f32_16x16x32_bf16 v[156:159], a[228:231], v[64:67], 0\n"
      "v_exp_f32 v145, v145\n"
      "v_mfma_f32_16x16x32_bf16 a[68:71], a[196:199], v[176:179], a[68:71]\n"
      "v_add_f32 v218, v218, v166\n"
      "v_cvt_pk_bf16_f32 v168, v144, v145\n"
      "v_mfma_f32_16x16x32_bf16 v[152:155], a[232:235], v[68:71], v[152:155]\n"
      "v_exp_f32 v146, v146\n"
      "v_mfma_f32_16x16x32_bf16 a[72:75], a[200:203], v[176:179], a[72:75]\n"
      "v_add_f32 v219, v219, v144\n"
      "v_add_f32_e64 v218, v218, v167\n"
      "v_mfma_f32_16x16x32_bf16 v[156:159], a[236:239], v[68:71], v[156:159]\n"
      "v_exp_f32 v147, v147\n"
      "v_mfma_f32_16x16x32_bf16 a[76:79], a[204:207], v[176:179], a[76:79]\n"
      "v_add_f32 v219, v219, v145\n"
      "v_cvt_pk_bf16_f32 v169, v146, v147\n"
      "v_mfma_f32_16x16x32_bf16 v[152:155], a[240:243], v[72:75], v[152:155]\n"
      "v_exp_f32 v148, v148\n"
      "v_mfma_f32_16x16x32_bf16 a[48:51], a[208:211], v[172:175], a[48:51]\n"
      "v_add_f32 v219, v219, v147\n"
      "v_add_f32_e64 v219, v219, v146\n"
      "v_mfma_f32_16x16x32_bf16 v[156:159], a[244:247], v[72:75], v[156:159]\n"
      "buffer_load_dwordx4 v231, s[52:55], s80 offen offset:1024 nt lds\n"
      "v_exp_f32 v149, v149\n"
      "v_mfma_f32_16x16x32_bf16 a[52:55], a[212:215], v[172:175], a[52:55]\n"
      "v_add_f32 v219, v219, v148\n"
      "v_cvt_pk_bf16_f32 v170, v148, v149\n"
      "v_mfma_f32_16x16x32_bf16 v[152:155], a[248:251], v[76:79], v[152:155]\n"
      "v_exp_f32 v150, v150\n"
      "v_mfma_f32_16x16x32_bf16 a[56:59], a[216:219], v[172:175], a[56:59]\n"
      "v_add_f32 v219, v219, v150\n"
      "v_add_f32 v219, v219, v149\n"
      "v_mfma_f32_16x16x32_bf16 v[156:159], a[252:255], v[76:79], v[156:159]\n"
      "v_exp_f32 v151, v151\n"
      "v_mfma_f32_16x16x32_bf16 a[60:63], a[220:223], v[172:175], a[60:63]\n"
      "v_add_f32_e64 v219, v219, v151\n"
      "v_cvt_pk_bf16_f32 v171, v150, v151\n"
      "v_mfma_f32_16x16x32_bf16 v[160:163], a[224:227], v[80:83], 0\n"
      "v_exp_f32 v152, v152\n"
      "v_mfma_f32_16x16x32_bf16 a[96:99], a[192:195], v[168:171], a[96:99]\n"
      "v_add_f32 v220, v220, v152\n"
      "v_mfma_f32_16x16x32_bf16 v[164:167], a[228:231], v[80:83], 0\n"
      "v_exp_f32 v153, v153\n"
      "v_mfma_f32_16x16x32_bf16 a[100:103], a[196:199], v[168:171], a[100:103]\n"
      "v_add_f32 v220, v220, v153\n"
      "v_cvt_pk_bf16_f32 v172, v152, v153\n"
      "v_mfma_f32_16x16x32_bf16 v[160:163], a[232:235], v[84:87], v[160:163]\n"
      "v_exp_f32 v154, v154\n"
      "v_mfma_f32_16x16x32_bf16 a[104:107], a[200:203], v[168:171], a[104:107]\n"
      "v_add_f32 v220, v220, v154\n"
      "v_mfma_f32_16x16x32_bf16 v[164:167], a[236:239], v[84:87], v[164:167]\n"
      "buffer_load_dwordx4 v230, s[52:55], s80 offen offset:2048 nt lds\n"
      "v_exp_f32 v155, v155\n"
      "v_mfma_f32_16x16x32_bf16 a[108:111], a[204:207], v[168:171], a[108:111]\n"
      "v_add_f32 v220, v220, v155\n"
      "v_cvt_pk_bf16_f32 v173, v154, v155\n"
      "v_mfma_f32_16x16x32_bf16 v[160:163], a[240:243], v[88:91], v[160:163]\n"
      "v_exp_f32 v156, v156\n"
      "v_mfma_f32_16x16x32_bf16 a[80:83], a[208:211], v[176:179], a[80:83]\n"
      "v_add_f32 v220, v220, v156\n"
      "v_mfma_f32_16x16x32_bf16 v[164:167], a[244:247], v[88:91], v[164:167]\n"
      "v_exp_f32 v157, v157\n"
      "v_mfma_f32_16x16x32_bf16 a[84:87], a[212:215], v[176:179], a[84:87]\n"
      "v_add_f32 v220, v220, v157\n"
      "v_cvt_pk_bf16_f32 v174, v156, v157\n"
      "v_mfma_f32_16x16x32_bf16 v[160:163], a[248:251], v[92:95], v[160:163]\n"
      "v_exp_f32 v158, v158\n"
      "v_mfma_f32_16x16x32_bf16 a[88:91], a[216:219], v[176:179], a[88:91]\n"
      "v_add_f32 v220, v220, v158\n"
      "v_mfma_f32_16x16x32_bf16 v[164:167], a[252:255], v[92:95], v[164:167]\n"
      "v_exp_f32_e64 v159, v159\n"
      "v_mfma_f32_16x16x32_bf16 a[92:95], a[220:223], v[176:179], a[92:95]\n"
      "v_add_f32 v220, v220, v159\n"
      "v_cvt_pk_bf16_f32 v175, v158, v159\n"
      "v_mfma_f32_16x16x32_bf16 v[144:147], a[224:227], v[96:99], 0\n"
      "v_exp_f32 v160, v160\n"
      "v_mfma_f32_16x16x32_bf16 a[128:131], a[192:195], v[172:175], a[128:131]\n"
      "ds_read_b128 a[224:227], v224 offset:16384\n"
      "v_add_f32_e64 v221, v221, v160\n"
      "v_mfma_f32_16x16x32_bf16 v[148:151], a[228:231], v[96:99], 0\n"
      "buffer_load_dwordx4 v231, s[52:55], s80 offen offset:3072 nt lds\n"
      "v_exp_f32_e64 v161, v161\n"
      "v_mfma_f32_16x16x32_bf16 a[132:135], a[196:199], v[172:175], a[132:135]\n"
      "ds_read_b128 a[228:231], v224 offset:20480\n"
      "v_cvt_pk_bf16_f32 v176, v160, v161\n"
      "v_mfma_f32_16x16x32_bf16 v[144:147], a[232:235], v[100:103], v[144:147]\n"
      "v_exp_f32_e64 v162, v162\n"
      "v_mfma_f32_16x16x32_bf16 a[136:139], a[200:203], v[172:175], a[136:139]\n"
      "ds_read_b128 a[232:235], v225 offset:16384\n"
      "v_add_f32 v221, v221, v161\n"
      "v_mfma_f32_16x16x32_bf16 v[148:151], a[236:239], v[100:103], v[148:151]\n"
      "v_exp_f32 v163, v163\n"
      "v_mfma_f32_16x16x32_bf16 a[140:143], a[204:207], v[172:175], a[140:143]\n"
      "ds_read_b128 a[236:239], v225 offset:20480\n"
      "v_cvt_pk_bf16_f32 v177, v162, v163\n"
      "v_mfma_f32_16x16x32_bf16 v[144:147], a[240:243], v[104:107], v[144:147]\n"
      "v_exp_f32_e64 v164, v164\n"
      "v_mfma_f32_16x16x32_bf16 a[112:115], a[208:211], v[168:171], a[112:115]\n"
      "ds_read_b128 a[240:243], v224 offset:17408\n"
      "v_add_f32_e64 v221, v221, v162\n"
      "v_mfma_f32_16x16x32_bf16 v[148:151], a[244:247], v[104:107], v[148:151]\n"
      "s_add_u32 m0, s68, 4096\n"
      "v_exp_f32_e64 v165, v165\n"
      "v_mfma_f32_16x16x32_bf16 a[116:119], a[212:215], v[168:171], a[116:119]\n"
      "ds_read_b128 a[244:247], v224 offset:21504\n"
      "s_add_u32 s64, s80, 4096\n"
      "v_cvt_pk_bf16_f32 v178, v164, v165\n"
      "v_mfma_f32_16x16x32_bf16 v[144:147], a[248:251], v[108:111], v[144:147]\n"
      "buffer_load_dwordx4 v230, s[52:55], s64 offen offset:0 nt lds\n"
      "v_exp_f32 v166, v166\n"
      "v_mfma_f32_16x16x32_bf16 a[120:123], a[216:219], v[168:171], a[120:123]\n"
      "ds_read_b128 a[248:251], v225 offset:17408\n"
      "v_add_f32 v221, v221, v163\n"
      "v_mfma_f32_16x16x32_bf16 v[148:151], a[252:255], v[108:111], v[148:151]\n"
      "s_waitcnt lgkmcnt(3)\n"
      "v_exp_f32 v167, v167\n"
      "v_mfma_f32_16x16x32_bf16 a[124:127], a[220:223], v[168:171], a[124:127]\n"
      "ds_read_b128 a[252:255], v225 offset:21504\n"
      "v_cvt_pk_bf16_f32 v179, v166, v167\n"
      "v_mfma_f32_16x16x32_bf16 v[152:155], a[224:227], v[0:3], 0\n"
      "v_exp_f32 v144, v144\n"
      "v_mfma_f32_16x16x32_bf16 v[180:183], a[192:195], v[176:179], v[180:183]\n"
      "v_add_f32 v221, v221, v165\n"
      "v_add_f32_e64 v221, v221, v164\n"
      "v_mfma_f32_16x16x32_bf16 v[156:159], a[228:231], v[0:3], 0\n"
      "v_exp_f32 v145, v145\n"
      "v_mfma_f32_16x16x32_bf16 v[184:187], a[196:199], v[176:179], v[184:187]\n"
      "v_add_f32 v221, v221, v166\n"
      "v_cvt_pk_bf16_f32 v168, v144, v145\n"
      "v_mfma_f32_16x16x32_bf16 v[152:155], a[232:235], v[4:7], v[152:155]\n"
      "v_exp_f32 v146, v146\n"
      "v_mfma_f32_16x16x32_bf16 v[188:191], a[200:203], v[176:179], v[188:191]\n"
      "v_add_f32 v222, v222, v144\n"
      "v_add_f32_e64 v221, v221, v167\n"
      "v_mfma_f32_16x16x32_bf16 v[156:159], a[236:239], v[4:7], v[156:159]\n"
      "s_waitcnt lgkmcnt(2)\n"
      "v_exp_f32 v147, v147\n"
      "v_mfma_f32_16x16x32_bf16 v[192:195], a[204:207], v[176:179], v[192:195]\n"
      "v_add_f32_e64 v222, v222, v145\n"
      "v_cvt_pk_bf16_f32 v169, v146, v147\n"
      "v_mfma_f32_16x16x32_bf16 v[152:155], a[240:243], v[8:11], v[152:155]\n"
      "v_exp_f32 v148, v148\n"
      "v_mfma_f32_16x16x32_bf16 a[144:147], a[208:211], v[172:175], a[144:147]\n"
      "v_add_f32 v222, v222, v147\n"
      "v_add_f32 v222, v222, v146\n"
      "v_mfma_f32_16x16x32_bf16 v[156:159], a[244:247], v[8:11], v[156:159]\n"
      "s_waitcnt lgkmcnt(0)\n"
      "v_exp_f32 v149, v149\n"
      "v_mfma_f32_16x16x32_bf16 a[148:151], a[212:215], v[172:175], a[148:151]\n"
      "v_add_f32 v222, v222, v148\n"
      "v_cvt_pk_bf16_f32 v170, v148, v149\n"
      "v_mfma_f32_16x16x32_bf16 v[152:155], a[248:251], v[12:15], v[152:155]\n"
      "v_exp_f32_e64 v150, v150\n"
      "v_mfma_f32_16x16x32_bf16 a[152:155], a[216:219], v[172:175], a[152:155]\n"
      "s_add_u32 m0, s68, 4096\n"
      "v_add_f32 v222, v222, v150\n"
      "v_add_f32 v222, v222, v149\n"
      "v_mfma_f32_16x16x32_bf16 v[156:159], a[252:255], v[12:15], v[156:159]\n"
      "buffer_load_dwordx4 v231, s[52:55], s64 offen offset:1024 nt lds\n"
      "v_exp_f32 v151, v151\n"
      "v_mfma_f32_16x16x32_bf16 a[156:159], a[220:223], v[172:175], a[156:159]\n"
      "v_add_f32 v222, v222, v151\n"
      "v_cvt_pk_bf16_f32 v171, v150, v151\n"
      "v_mfma_f32_16x16x32_bf16 v[160:163], a[224:227], v[16:19], 0\n"
      "v_exp_f32 v152, v152\n"
      "v_mfma_f32_16x16x32_bf16 v[112:115], a[192:195], v[168:171], v[112:115]\n"
      "ds_read_b64_tr_b16 a[192:193], v226 offset:16384\n"
      "v_add_f32 v216, v216, v152\n"
      "v_mfma_f32_16x16x32_bf16 v[164:167], a[228:231], v[16:19], 0\n"
      "v_exp_f32_e64 v153, v153\n"
      "v_mfma_f32_16x16x32_bf16 v[116:119], a[196:199], v[168:171], v[116:119]\n"
      "ds_read_b64_tr_b16 a[194:195], v226 offset:20480\n"
      "v_cvt_pk_bf16_f32 v172, v152, v153\n"
      "v_mfma_f32_16x16x32_bf16 v[160:163], a[232:235], v[20:23], v[160:163]\n"
      "v_exp_f32 v154, v154\n"
      "v_mfma_f32_16x16x32_bf16 v[120:123], a[200:203], v[168:171], v[120:123]\n"
      "ds_read_b64_tr_b16 a[196:197], v227 offset:16384\n"
      "v_add_f32 v216, v216, v153\n"
      "v_mfma_f32_16x16x32_bf16 v[164:167], a[236:239], v[20:23], v[164:167]\n"
      "v_exp_f32_e64 v155, v155\n"
      "v_mfma_f32_16x16x32_bf16 v[124:127], a[204:207], v[168:171], v[124:127]\n"
      "ds_read_b64_tr_b16 a[198:199], v227 offset:20480\n"
      "v_cvt_pk_bf16_f32 v173, v154, v155\n"
      "v_mfma_f32_16x16x32_bf16 v[160:163], a[240:243], v[24:27], v[160:163]\n"
      "v_exp_f32_e64 v156, v156\n"
      "v_mfma_f32_16x16x32_bf16 v[196:199], a[208:211], v[176:179], v[196:199]\n"
      "ds_read_b64_tr_b16 a[200:201], v228 offset:16384\n"
      "s_add_u32 m0, s68, 4096\n"
      "v_add_f32_e64 v216, v216, v154\n"
      "v_mfma_f32_16x16x32_bf16 v[164:167], a[244:247], v[24:27], v[164:167]\n"
      "buffer_load_dwordx4 v230, s[52:55], s64 offen offset:2048 nt lds\n"
      "v_exp_f32_e64 v157, v157\n"
      "v_mfma_f32_16x16x32_bf16 v[200:203], a[212:215], v[176:179], v[200:203]\n"
      "ds_read_b64_tr_b16 a[202:203], v228 offset:20480\n"
      "v_cvt_pk_bf16_f32 v174, v156, v157\n"
      "v_mfma_f32_16x16x32_bf16 v[160:163], a[248:251], v[28:31], v[160:163]\n"
      "v_exp_f32_e64 v158, v158\n"
      "v_mfma_f32_16x16x32_bf16 v[204:207], a[216:219], v[176:179], v[204:207]\n"
      "ds_read_b64_tr_b16 a[204:205], v229 offset:16384\n"
      "v_add_f32 v216, v216, v155\n"
      "v_mfma_f32_16x16x32_bf16 v[164:167], a[252:255], v[28:31], v[164:167]\n"
      "v_exp_f32 v159, v159\n"
      "v_mfma_f32_16x16x32_bf16 v[208:211], a[220:223], v[176:179], v[208:211]\n"
      "ds_read_b64_tr_b16 a[206:207], v229 offset:20480\n"
      "v_cvt_pk_bf16_f32 v175, v158, v159\n"
      "v_mfma_f32_16x16x32_bf16 v[144:147], a[224:227], v[32:35], 0\n"
      "v_exp_f32_e64 v160, v160\n"
      "v_mfma_f32_16x16x32_bf16 v[128:131], a[208:211], v[168:171], v[128:131]\n"
      "ds_read_b64_tr_b16 a[208:209], v226 offset:17408\n"
      "v_add_f32 v216, v216, v156\n"
      "v_mfma_f32_16x16x32_bf16 v[148:151], a[228:231], v[32:35], 0\n"
      "v_exp_f32 v161, v161\n"
      "v_mfma_f32_16x16x32_bf16 v[132:135], a[212:215], v[168:171], v[132:135]\n"
      "ds_read_b64_tr_b16 a[210:211], v226 offset:21504\n"
      "v_cvt_pk_bf16_f32 v176, v160, v161\n"
      "v_mfma_f32_16x16x32_bf16 v[144:147], a[232:235], v[36:39], v[144:147]\n"
      "v_exp_f32 v162, v162\n"
      "v_mfma_f32_16x16x32_bf16 v[136:139], a[216:219], v[168:171], v[136:139]\n"
      "ds_read_b64_tr_b16 a[212:213], v227 offset:17408\n"
      "v_add_f32 v216, v216, v157\n"
      "v_mfma_f32_16x16x32_bf16 v[148:151], a[236:239], v[36:39], v[148:151]\n"
      "v_exp_f32_e64 v163, v163\n"
      "v_mfma_f32_16x16x32_bf16 v[140:143], a[220:223], v[168:171], v[140:143]\n"
      "ds_read_b64_tr_b16 a[214:215], v227 offset:21504\n"
      "v_cvt_pk_bf16_f32 v177, v162, v163\n"
      "v_mfma_f32_16x16x32_bf16 v[144:147], a[240:243], v[40:43], v[144:147]\n"
      "s_waitcnt lgkmcnt(4)\n"
      "v_exp_f32 v164, v164\n"
      "v_mfma_f32_16x16x32_bf16 a[0:3], a[192:195], v[172:175], a[0:3]\n"
      "ds_read_b64_tr_b16 a[216:217], v228 offset:17408\n"
      "v_add_f32 v216, v216, v158\n"
      "v_mfma_f32_16x16x32_bf16 v[148:151], a[244:247], v[40:43], v[148:151]\n"
      "v_exp_f32 v165, v165\n"
      "v_mfma_f32_16x16x32_bf16 a[4:7], a[196:199], v[172:175], a[4:7]\n"
      "ds_read_b64_tr_b16 a[218:219], v228 offset:21504\n"
      "s_add_u32 m0, s68, 4096\n"
      "v_cvt_pk_bf16_f32 v178, v164, v165\n"
      "v_mfma_f32_16x16x32_bf16 v[144:147], a[248:251], v[44:47], v[144:147]\n"
      "buffer_load_dwordx4 v231, s[52:55], s64 offen offset:3072 nt lds\n"
      "v_exp_f32 v166, v166\n"
      "v_mfma_f32_16x16x32_bf16 a[8:11], a[200:203], v[172:175], a[8:11]\n"
      "ds_read_b64_tr_b16 a[220:221], v229 offset:17408\n"
      "s_add_u32 s80, s80, 8192\n"
      "v_add_f32 v216, v216, v159\n"
      "v_mfma_f32_16x16x32_bf16 v[148:151], a[252:255], v[44:47], v[148:151]\n"
      "s_and_b32 s80, s80, 122880\n"
      "v_exp_f32_e64 v167, v167\n"
      "v_mfma_f32_16x16x32_bf16 a[12:15], a[204:207], v[172:175], a[12:15]\n"
      "ds_read_b64_tr_b16 a[222:223], v229 offset:21504\n"
      "v_cvt_pk_bf16_f32 v179, v166, v167\n"
      "s_and_b32 s78, s81, 255\n"
      "s_lshr_b32 s81, s81, 8\n"
      "s_sub_u32 s83, s83, 1\n"
      "s_cmp_lg_u32 s83, 0\n"
      "s_cbranch_scc1 ATT_%=\n"
      "ATTX_%=:\n"
      "s_waitcnt vmcnt(63)\n"
      "s_getpc_b64 s[42:43]\n"
      "s_lshr_b32 s97, %[bid], 3\n"
      "s_lshl_b32 s97, s97, 2\n"
      "s_add_u32 s97, s97, %[wid]\n"
      "s_lshl_b32 s97, s97, 8\n"
      "v_lshl_add_u32 v215, v242, 2, s97\n"
      "global_load_dword v215, v215, s[42:43]\n"
      "v_mov_b32 v248, -1\n"
      "v_mov_b32 v249, -1\n"
      "v_mov_b32 v250, -1\n"
      "v_mov_b32 v251, -1\n"
      "v_lshlrev_b32 v230, 4, v242\n"
      "v_mfma_f32_16x16x32_bf16 v[152:155], a[224:227], v[48:51], 0\n"
      "v_exp_f32 v144, v144\n"
      "v_mfma_f32_16x16x32_bf16 a[32:35], a[192:195], v[176:179], a[32:35]\n"
      "ds_read_b64_tr_b16 a[160:161], v240 offset:0\n"
      "v_add_f32 v217, v217, v160\n"
      "v_mfma_f32_16x16x32_bf16 v[156:159], a[228:231], v[48:51], 0\n"
      "v_exp_f32 v145, v145\n"
      "v_mfma_f32_16x16x32_bf16 a[36:39], a[196:199], v[176:179], a[36:39]\n"
      "ds_read_b64_tr_b16 a[162:163], v240 offset:4096\n"
      "v_cvt_pk_bf16_f32 v168, v144, v145\n"
      "v_mfma_f32_16x16x32_bf16 v[152:155], a[232:235], v[52:55], v[152:155]\n"
      "s_memrealtime s[76:77]\n"
      "v_exp_f32_e64 v146, v146\n"
      "v_mfma_f32_16x16x32_bf16 a[40:43], a[200:203], v[176:179], a[40:43]\n"
      "ds_read_b64_tr_b16 a[164:165], v240 offset:8\n"
      "v_add_f32 v217, v217, v161\n"
      "v_mfma_f32_16x16x32_bf16 v[156:159], a[236:239], v[52:55], v[156:159]\n"
      "v_exp_f32 v147, v147\n"
      "v_mfma_f32_16x16x32_bf16 a[44:47], a[204:207], v[176:179], a[44:47]\n"
      "ds_read_b64_tr_b16 a[166:167], v240 offset:4104\n"
      "v_cvt_pk_bf16_f32 v169, v146, v147\n"
      "v_mfma_f32_16x16x32_bf16 v[152:155], a[240:243], v[56:59], v[152:155]\n"
      "s_waitcnt vmcnt(9) lgkmcnt(0)\n"
      "v_exp_f32 v148, v148\n"
      "v_mfma_f32_16x16x32_bf16 a[16:19], a[208:211], v[172:175], a[16:19]\n"
      "s_sub_u32 s77, s76, s96\n"
      "s_cmp_lt_i32 s77, 0\n"
      "s_cbranch_scc1 PC33_%=\n"
      "PC33R_%=:\n"
      "v_add_f32 v217, v217, v163\n"
      "v_add_f32 v217, v217, v162\n"
      "v_mfma_f32_16x16x32_bf16 v[156:159], a[244:247], v[56:59], v[156:159]\n"
      "s_barrier\n"
      "v_exp_f32 v149, v149\n"
      "v_mfma_f32_16x16x32_bf16 a[20:23], a[212:215], v[172:175], a[20:23]\n"
      "s_add_u32 s96, s76, 0\n"
      "ds_read_b64_tr_b16 a[168:169], v241 offset:0\n"
      "v_cvt_pk_bf16_f32 v170, v148, v149\n"
      "v_mfma_f32_16x16x32_bf16 v[152:155], a[248:251], v[60:63], v[152:155]\n"
      "v_exp_f32_e64 v150, v150\n"
      "v_mfma_f32_16x16x32_bf16 a[24:27], a[216:219], v[172:175], a[24:27]\n"
      "ds_read_b64_tr_b16 a[170:171], v241 offset:4096\n"
      "v_add_f32 v217, v217, v165\n"
      "v_add_f32 v217, v217, v164\n"
      "v_mfma_f32_16x16x32_bf16 v[156:159], a[252:255], v[60:63], v[156:159]\n"
      "v_exp_f32 v151, v151\n"
      "v_mfma_f32_16x16x32_bf16 a[28:31], a[220:223], v[172:175], a[28:31]\n"
      "ds_read_b64_tr_b16 a[172:173], v241 offset:8\n"
      "v_add_f32 v217, v217, v167\n"
      "v_add_f32_e64 v217, v217, v166\n"
      "v_cvt_pk_bf16_f32 v171, v150, v151\n"
      "v_mfma_f32_16x16x32_bf16 v[160:163], a[224:227], v[64:67], 0\n"
      "ds_read_b64_tr_b16 a[174:175], v241 offset:4104\n"
      "v_exp_f32_e64 v152, v152\n"
      "v_mfma_f32_16x16x32_bf16 a[64:67], a[192:195], v[168:171], a[64:67]\n"
      "ds_read_b64_tr_b16 a[176:177], v240 offset:1024\n"
      "v_add_f32 v218, v218, v144\n"
      "v_mfma_f32_16x16x32_bf16 v[164:167], a[228:231], v[64:67], 0\n"
      "v_exp_f32 v153, v153\n"
      "v_mfma_f32_16x16x32_bf16 a[68:71], a[196:199], v[168:171], a[68:71]\n"
      "ds_read_b64_tr_b16 a[178:179], v240 offset:5120\n"
      "v_cvt_pk_bf16_f32 v172, v152, v153\n"
      "v_mfma_f32_16x16x32_bf16 v[160:163], a[232:235], v[68:71], v[160:163]\n"
      "v_exp_f32 v154, v154\n"
      "v_mfma_f32_16x16x32_bf16 a[72:75], a[200:203], v[168:171], a[72:75]\n"
      "ds_read_b64_tr_b16 a[180:181], v240 offset:1032\n"
      "v_add_f32 v218, v218, v145\n"
      "v_mfma_f32_16x16x32_bf16 v[164:167], a[236:239], v[68:71], v[164:167]\n"
      "v_exp_f32_e64 v155, v155\n"
      "v_mfma_f32_16x16x32_bf16 a[76:79], a[204:207], v[168:171], a[76:79]\n"
      "ds_read_b64_tr_b16 a[182:183], v240 offset:5128\n"
      "v_cvt_pk_bf16_f32 v173, v154, v155\n"
      "v_mfma_f32_16x16x32_bf16 v[160:163], a[240:243], v[72:75], v[160:163]\n"
      "v_exp_f32 v156, v156\n"
      "v_mfma_f32_16x16x32_bf16 a[48:51], a[208:211], v[176:179], a[48:51]\n"
      "ds_read_b64_tr_b16 a[184:185], v241 offset:1024\n"
      "v_add_f32 v218, v218, v146\n"
      "v_mfma_f32_16x16x32_bf16 v[164:167], a[244:247], v[72:75], v[164:167]\n"
      "v_exp_f32 v157, v157\n"
      "v_mfma_f32_16x16x32_bf16 a[52:55], a[212:215], v[176:179], a[52:55]\n"
      "ds_read_b64_tr_b16 a[186:187], v241 offset:5120\n"
      "v_add_f32 v218, v218, v147\n"
      "v_cvt_pk_bf16_f32 v174, v156, v157\n"
      "v_mfma_f32_16x16x32_bf16 v[160:163], a[248:251], v[76:79], v[160:163]\n"
      "ds_read_b64_tr_b16 a[188:189], v241 offset:1032\n"
      "v_exp_f32_e64 v158, v158\n"
      "v_mfma_f32_16x16x32_bf16 a[56:59], a[216:219], v[176:179], a[56:59]\n"
      "ds_read_b64_tr_b16 a[190:191], v241 offset:5128\n"
      "v_add_f32 v218, v218, v149\n"
      "v_add_f32 v218, v218, v148\n"
      "v_mfma_f32_16x16x32_bf16 v[164:167], a[252:255], v[76:79], v[164:167]\n"
      "buffer_store_dwordx4 v[248:251], v230, s[60:63], s49 offen sc1\n"
      "v_exp_f32 v159, v159\n"
      "v_mfma_f32_16x16x32_bf16 a[60:63], a[220:223], v[176:179], a[60:63]\n"
      "v_add_f32 v218, v218, v151\n"
      "v_add_f32_e64 v218, v218, v150\n"
      "v_cvt_pk_bf16_f32 v175, v158, v159\n"
      "v_mfma_f32_16x16x32_bf16 v[144:147], a[224:227], v[80:83], 0\n"
      "v_exp_f32 v160, v160\n"
      "v_mfma_f32_16x16x32_bf16 a[96:99], a[192:195], v[172:175], a[96:99]\n"
      "s_waitcnt lgkmcnt(0)\n"
      "v_add_f32 v219, v219, v153\n"
      "v_add_f32 v219, v219, v152\n"
      "v_mfma_f32_16x16x32_bf16 v[148:151], a[228:231], v[80:83], 0\n"
      "s_add_u32 m0, s69, 0\n"
      "v_exp_f32 v161, v161\n"
      "v_mfma_f32_16x16x32_bf16 a[100:103], a[196:199], v[172:175], a[100:103]\n"
      "s_add_u32 s64, s86, 0\n"
      "v_add_f32 v219, v219, v154\n"
      "v_cvt_pk_bf16_f32 v176, v160, v161\n"
      "v_mfma_f32_16x16x32_bf16 v[144:147], a[232:235], v[84:87], v[144:147]\n"
      "buffer_load_dwordx4 v234, s[56:59], s64 offen lds\n"
      "v_exp_f32 v162, v162\n"
      "v_mfma_f32_16x16x32_bf16 a[104:107], a[200:203], v[172:175], a[104:107]\n"
      "v_add_f32 v219, v219, v156\n"
      "v_add_f32 v219, v219, v155\n"
      "v_mfma_f32_16x16x32_bf16 v[148:151], a[236:239], v[84:87], v[148:151]\n"
      "v_exp_f32 v163, v163\n"
      "v_mfma_f32_16x16x32_bf16 a[108:111], a[204:207], v[172:175], a[108:111]\n"
      "s_add_u32 m0, s69, 1024\n"
      "v_add_f32_e64 v219, v219, v157\n"
      "v_cvt_pk_bf16_f32 v177, v162, v163\n"
      "v_mfma_f32_16x16x32_bf16 v[144:147], a[240:243], v[88:91], v[144:147]\n"
      "buffer_load_dwordx4 v235, s[56:59], s64 offen lds\n"
      "v_exp_f32 v164, v164\n"
      "v_mfma_f32_16x16x32_bf16 a[80:83], a[208:211], v[168:171], a[80:83]\n"
      "v_add_f32 v219, v219, v159\n"
      "v_add_f32 v219, v219, v158\n"
      "v_mfma_f32_16x16x32_bf16 v[148:151], a[244:247], v[88:91], v[148:151]\n"
      "s_add_u32 m0, s69, 2048\n"
      "v_exp_f32 v165, v165\n"
      "v_mfma_f32_16x16x32_bf16 a[84:87], a[212:215], v[168:171], a[84:87]\n"
      "s_add_u32 s64, s86, 8192\n"
      "v_add_f32_e64 v220, v220, v160\n"
      "v_cvt_pk_bf16_f32 v178, v164, v165\n"
      "v_mfma_f32_16x16x32_bf16 v[144:147], a[248:251], v[92:95], v[144:147]\n"
      "buffer_load_dwordx4 v234, s[56:59], s64 offen lds\n"
      "v_exp_f32 v166, v166\n"
      "v_mfma_f32_16x16x32_bf16 a[88:91], a[216:219], v[168:171], a[88:91]\n"
      "v_add_f32 v220, v220, v162\n"
      "v_add_f32_e64 v220, v220, v161\n"
      "v_mfma_f32_16x16x32_bf16 v[148:151], a[252:255], v[92:95], v[148:151]\n"
      "v_exp_f32 v167, v167\n"
      "v_mfma_f32_16x16x32_bf16 a[92:95], a[220:223], v[168:171], a[92:95]\n"
      "s_add_u32 m0, s69, 3072\n"
      "v_add_f32 v220, v220, v163\n"
      "v_cvt_pk_bf16_f32 v179, v166, v167\n"
      "v_mfma_f32_16x16x32_bf16 v[152:155], a[224:227], v[96:99], 0\n"
      "buffer_load_dwordx4 v235, s[56:59], s64 offen lds\n"
      "v_exp_f32_e64 v144, v144\n"
      "v_mfma_f32_16x16x32_bf16 a[128:131], a[192:195], v[176:179], a[128:131]\n"
      "ds_read_b128 a[224:227], v224 offset:32768\n"
      "v_add_f32_e64 v220, v220, v164\n"
      "v_mfma_f32_16x16x32_bf16 v[156:159], a[228:231], v[96:99], 0\n"
      "s_add_u32 m0, s69, 4096\n"
      "v_exp_f32_e64 v145, v145\n"
      "v_mfma_f32_16x16x32_bf16 a[132:135], a[196:199], v[176:179], a[132:135]\n"
      "ds_read_b128 a[228:231], v224 offset:36864\n"
      "s_add_u32 s64, s86, 16384\n"
      "v_cvt_pk_bf16_f32 v168, v144, v145\n"
      "v_mfma_f32_16x16x32_bf16 v[152:155], a[232:235], v[100:103], v[152:155]\n"
      "buffer_load_dwordx4 v234, s[56:59], s64 offen lds\n"
      "v_exp_f32 v146, v146\n"
      "v_mfma_f32_16x16x32_bf16 a[136:139], a[200:203], v[176:179], a[136:139]\n"
      "ds_read_b128 a[232:235], v225 offset:32768\n"
      "v_add_f32 v220, v220, v165\n"
      "v_mfma_f32_16x16x32_bf16 v[156:159], a[236:239], v[100:103], v[156:159]\n"
      "v_exp_f32_e64 v147, v147\n"
      "v_mfma_f32_16x16x32_bf16 a[140:143], a[204:207], v[176:179], a[140:143]\n"
      "ds_read_b128 a[236:239], v225 offset:36864\n"
      "s_add_u32 m0, s69, 5120\n"
      "v_cvt_pk_bf16_f32 v169, v146, v147\n"
      "v_mfma_f32_16x16x32_bf16 v[152:155], a[240:243], v[104:107], v[152:155]\n"
      "buffer_load_dwordx4 v235, s[56:59], s64 offen lds\n"
      "v_exp_f32_e64 v148, v148\n"
      "v_mfma_f32_16x16x32_bf16 a[112:115], a[208:211], v[172:175], a[112:115]\n"
      "ds_read_b128 a[240:243], v224 offset:33792\n"
      "v_add_f32_e64 v220, v220, v166\n"
      "v_mfma_f32_16x16x32_bf16 v[156:159], a[244:247], v[104:107], v[156:159]\n"
      "s_add_u32 m0, s69, 6144\n"
      "v_exp_f32_e64 v149, v149\n"
      "v_mfma_f32_16x16x32_bf16 a[116:119], a[212:215], v[172:175], a[116:119]\n"
      "ds_read_b128 a[244:247], v224 offset:37888\n"
      "s_add_u32 s64, s86, 24576\n"
      "v_cvt_pk_bf16_f32 v170, v148, v149\n"
      "v_mfma_f32_16x16x32_bf16 v[152:155], a[248:251], v[108:111], v[152:155]\n"
      "buffer_load_dwordx4 v234, s[56:59], s64 offen lds\n"
      "v_exp_f32 v150, v150\n"
      "v_mfma_f32_16x16x32_bf16 a[120:123], a[216:219], v[172:175], a[120:123]\n"
      "ds_read_b128 a[248:251], v225 offset:33792\n"
      "v_add_f32 v220, v220, v167\n"
      "v_mfma_f32_16x16x32_bf16 v[156:159], a[252:255], v[108:111], v[156:159]\n"
      "s_waitcnt lgkmcnt(3)\n"
      "v_exp_f32 v151, v151\n"
      "v_mfma_f32_16x16x32_bf16 a[124:127], a[220:223], v[172:175], a[124:127]\n"
      "ds_read_b128 a[252:255], v225 offset:37888\n"
      "v_cvt_pk_bf16_f32 v171, v150, v151\n"
      "v_mov_b32 v232, v240\n"
      "v_mov_b32 v233, v241\n"
      "v_min_u32 v231, 1, v242\n"
      "v_lshlrev_b32 v231, 31, v231\n"
      "v_lshlrev_b32 v214, 2, v242\n"
      "v_and_b32 v214, 60, v214\n"
      "v_mfma_f32_16x16x32_bf16 v[160:163], a[224:227], v[0:3], 0\n"
      "s_mov_b64 s[44:45], %[pout]\n"
      "v_exp_f32 v152, v152\n"
      "v_mfma_f32_16x16x32_bf16 v[180:183], a[192:195], v[168:171], v[180:183]\n"
      "s_add_u32 m0, s69, 7168\n"
      "v_add_f32 v221, v221, v145\n"
      "v_add_f32 v221, v221, v144\n"
      "v_mfma_f32_16x16x32_bf16 v[164:167], a[228:231], v[0:3], 0\n"
      "buffer_load_dwordx4 v235, s[56:59], s64 offen lds\n"
      "v_exp_f32_e64 v153, v153\n"
      "v_mfma_f32_16x16x32_bf16 v[184:187], a[196:199], v[168:171], v[184:187]\n"
      "s_add_u32 s86, s86, 0x20000\n"
      "v_add_f32_e64 v221, v221, v146\n"
      "v_cvt_pk_bf16_f32 v172, v152, v153\n"
      "v_mfma_f32_16x16x32_bf16 v[160:163], a[232:235], v[4:7], v[160:163]\n"
      "s_and_b32 s86, s86, 0xfffff\n"
      "v_exp_f32 v154, v154\n"
      "v_mfma_f32_16x16x32_bf16 v[188:191], a[200:203], v[168:171], v[188:191]\n"
      "s_add_u32 s73, %[g], 1\n"
      "v_add_f32 v221, v221, v148\n"
      "v_add_f32 v221, v221, v147\n"
      "v_mfma_f32_16x16x32_bf16 v[164:167], a[236:239], v[4:7], v[164:167]\n"
      "s_waitcnt lgkmcnt(2)\n"
      "buffer_store_dwordx4 v[248:251], v230, s[60:63], s48 offen sc1\n"
      "v_exp_f32 v155, v155\n"
      "v_mfma_f32_16x16x32_bf16 v[192:195], a[204:207], v[168:171], v[192:195]\n"
      "s_xor_b32 s73, s73, %[g]\n"
      "v_add_f32 v221, v221, v149\n"
      "v_cvt_pk_bf16_f32 v173, v154, v155\n"
      "v_mfma_f32_16x16x32_bf16 v[160:163], a[240:243], v[8:11], v[160:163]\n"
      "v_exp_f32_e64 v156, v156\n"
      "v_mfma_f32_16x16x32_bf16 a[144:147], a[208:211], v[176:179], a[144:147]\n"
      "s_and_b32 s73, s73, 3\n"
      "v_add_f32 v221, v221, v151\n"
      "v_add_f32 v221, v221, v150\n"
      "v_mfma_f32_16x16x32_bf16 v[164:167], a[244:247], v[8:11], v[164:167]\n"
      "s_waitcnt lgkmcnt(0)\n"
      "s_add_u32 m0, s68, 16384\n"
      "v_exp_f32 v157, v157\n"
      "v_mfma_f32_16x16x32_bf16 a[148:151], a[212:215], v[176:179], a[148:151]\n"
      "s_add_u32 s64, s86, 0\n"
      "v_add_f32_e64 v222, v222, v152\n"
      "v_cvt_pk_bf16_f32 v174, v156, v157\n"
      "v_mfma_f32_16x16x32_bf16 v[160:163], a[248:251], v[12:15], v[160:163]\n"
      "buffer_load_dwordx4 v234, s[56:59], s64 offen lds\n"
      "v_exp_f32_e64 v158, v158\n"
      "v_mfma_f32_16x16x32_bf16 a[152:155], a[216:219], v[176:179], a[152:155]\n"
      "s_lshl_b32 s73, s73, 12\n"
      "v_add_f32 v222, v222, v154\n"
      "v_add_f32 v222, v222, v153\n"
      "v_mfma_f32_16x16x32_bf16 v[164:167], a[252:255], v[12:15], v[164:167]\n"
      "s_xor_b32 s73, s73, s89\n"
      "v_exp_f32 v159, v159\n"
      "v_mfma_f32_16x16x32_bf16 a[156:159], a[220:223], v[176:179], a[156:159]\n"
      "s_add_u32 s76, %[j], 1\n"
      "v_add_f32_e64 v222, v222, v155\n"
      "v_cvt_pk_bf16_f32 v175, v158, v159\n"
      "v_mfma_f32_16x16x32_bf16 v[144:147], a[224:227], v[16:19], 0\n"
      "s_and_b32 s76, s76, 3\n"
      "v_exp_f32 v160, v160\n"
      "v_mfma_f32_16x16x32_bf16 v[112:115], a[192:195], v[172:175], v[112:115]\n"
      "ds_read_b64_tr_b16 a[192:193], v226 offset:32768\n"
      "s_add_u32 m0, s68, 17408\n"
      "v_add_f32 v222, v222, v156\n"
      "v_mfma_f32_16x16x32_bf16 v[148:151], a[228:231], v[16:19], 0\n"
      "buffer_load_dwordx4 v235, s[56:59], s64 offen lds\n"
      "v_exp_f32 v161, v161\n"
      "v_mfma_f32_16x16x32_bf16 v[116:119], a[196:199], v[172:175], v[116:119]\n"
      "ds_read_b64_tr_b16 a[194:195], v226 offset:36864\n"
      "v_cvt_pk_bf16_f32 v176, v160, v161\n"
      "v_mfma_f32_16x16x32_bf16 v[144:147], a[232:235], v[20:23], v[144:147]\n"
      "s_add_u32 m0, s68, 18432\n"
      "v_exp_f32_e64 v162, v162\n"
      "v_mfma_f32_16x16x32_bf16 v[120:123], a[200:203], v[172:175], v[120:123]\n"
      "ds_read_b64_tr_b16 a[196:197], v227 offset:32768\n"
      "s_add_u32 s64, s86, 8192\n"
      "v_add_f32 v222, v222, v157\n"
      "v_mfma_f32_16x16x32_bf16 v[148:151], a[236:239], v[20:23], v[148:151]\n"
      "buffer_load_dwordx4 v234, s[56:59], s64 offen lds\n"
      "v_exp_f32 v163, v163\n"
      "v_mfma_f32_16x16x32_bf16 v[124:127], a[204:207], v[172:175], v[124:127]\n"
      "ds_read_b64_tr_b16 a[198:199], v227 offset:36864\n"
      "v_cvt_pk_bf16_f32 v177, v162, v163\n"
      "v_mfma_f32_16x16x32_bf16 v[144:147], a[240:243], v[24:27], v[144:147]\n"
      "s_waitcnt vmcnt(14)\n"
      "v_exp_f32 v164, v164\n"
      "v_mfma_f32_16x16x32_bf16 v[196:199], a[208:211], v[168:171], v[196:199]\n"
      "ds_read_b64_tr_b16 a[200:201], v228 offset:32768\n"
      "v_add_f32_e64 v222, v222, v158\n"
      "v_mfma_f32_16x16x32_bf16 v[148:151], a[244:247], v[24:27], v[148:151]\n"
      "s_barrier\n"
      "v_exp_f32 v165, v165\n"
      "v_mfma_f32_16x16x32_bf16 v[200:203], a[212:215], v[168:171], v[200:203]\n"
      "ds_read_b64_tr_b16 a[202:203], v228 offset:36864\n"
      "v_cvt_pk_bf16_f32 v178, v164, v165\n"
      "v_mfma_f32_16x16x32_bf16 v[144:147], a[248:251], v[28:31], v[144:147]\n"
      "s_lshl_b32 s76, s76, 13\n"
      "v_exp_f32 v166, v166\n"
      "v_mfma_f32_16x16x32_bf16 v[204:207], a[216:219], v[168:171], v[204:207]\n"
      "ds_read_b64_tr_b16 a[204:205], v229 offset:32768\n"
      "s_add_u32 m0, s68, 19456\n"
      "v_add_f32_e64 v222, v222, v159\n"
      "v_mfma_f32_16x16x32_bf16 v[148:151], a[252:255], v[28:31], v[148:151]\n"
      "buffer_load_dwordx4 v235, s[56:59], s64 offen lds\n"
      "v_exp_f32_e64 v167, v167\n"
      "v_mfma_f32_16x16x32_bf16 v[208:211], a[220:223], v[168:171], v[208:211]\n"
      "ds_read_b64_tr_b16 a[206:207], v229 offset:36864\n"
      "v_cvt_pk_bf16_f32 v179, v166, v167\n"
      "v_mfma_f32_16x16x32_bf16 v[152:155], a[224:227], v[32:35], 0\n"
      "ds_read_b128 a[224:227], v224 offset:0\n"
      "v_exp_f32_e64 v144, v144\n"
      "v_mfma_f32_16x16x32_bf16 v[128:131], a[208:211], v[172:175], v[128:131]\n"
      "ds_read_b64_tr_b16 a[208:209], v226 offset:33792\n"
      "v_add_f32_e64 v216, v216, v160\n"
      "v_mfma_f32_16x16x32_bf16 v[156:159], a[228:231], v[32:35], 0\n"
      "ds_read_b128 a[228:231], v224 offset:4096\n"
      "v_exp_f32 v145, v145\n"
      "v_mfma_f32_16x16x32_bf16 v[132:135], a[212:215], v[172:175], v[132:135]\n"
      "ds_read_b64_tr_b16 a[210:211], v226 offset:37888\n"
      "v_add_f32 v216, v216, v161\n"
      "v_cvt_pk_bf16_f32 v168, v144, v145\n"
      "v_mfma_f32_16x16x32_bf16 v[152:155], a[232:235], v[36:39], v[152:155]\n"
      "ds_read_b128 a[232:235], v225 offset:0\n"
      "v_exp_f32_e64 v146, v146\n"
      "v_mfma_f32_16x16x32_bf16 v[136:139], a[216:219], v[172:175], v[136:139]\n"
      "ds_read_b64_tr_b16 a[212:213], v227 offset:33792\n"
      "v_add_f32_e64 v216, v216, v162\n"
      "v_mfma_f32_16x16x32_bf16 v[156:159], a[236:239], v[36:39], v[156:159]\n"
      "ds_read_b128 a[236:239], v225 offset:4096\n"
      "buffer_store_dwordx4 v[248:251], v230, s[60:63], s72 offen sc1\n"
      "v_exp_f32_e64 v147, v147\n"
      "v_mfma_f32_16x16x32_bf16 v[140:143], a[220:223], v[172:175], v[140:143]\n"
      "ds_read_b64_tr_b16 a[214:215], v227 offset:37888\n"
      "v_add_f32 v216, v216, v163\n"
      "v_cvt_pk_bf16_f32 v169, v146, v147\n"
      "v_mfma_f32_16x16x32_bf16 v[152:155], a[240:243], v[40:43], v[152:155]\n"
      "s_waitcnt lgkmcnt(8)\n"
      "ds_read_b128 a[240:243], v224 offset:1024\n"
      "v_exp_f32 v148, v148\n"
      "v_mfma_f32_16x16x32_bf16 a[0:3], a[192:195], v[176:179], a[0:3]\n"
      "ds_read_b64_tr_b16 a[216:217], v228 offset:33792\n"
      "v_add_f32 v216, v216, v164\n"
      "v_mfma_f32_16x16x32_bf16 v[156:159], a[244:247], v[40:43], v[156:159]\n"
      "ds_read_b128 a[244:247], v224 offset:5120\n"
      "v_exp_f32_e64 v149, v149\n"
      "v_mfma_f32_16x16x32_bf16 a[4:7], a[196:199], v[176:179], a[4:7]\n"
      "ds_read_b64_tr_b16 a[218:219], v228 offset:37888\n"
      "v_add_f32_e64 v216, v216, v165\n"
      "v_cvt_pk_bf16_f32 v170, v148, v149\n"
      "v_mfma_f32_16x16x32_bf16 v[152:155], a[248:251], v[44:47], v[152:155]\n"
      "ds_read_b128 a[248:251], v225 offset:1024\n"
      "v_exp_f32 v150, v150\n"
      "v_mfma_f32_16x16x32_bf16 a[8:11], a[200:203], v[176:179], a[8:11]\n"
      "ds_read_b64_tr_b16 a[220:221], v229 offset:33792\n"
      "v_add_f32 v216, v216, v166\n"
      "v_mfma_f32_16x16x32_bf16 v[156:159], a[252:255], v[44:47], v[156:159]\n"
      "ds_read_b128 a[252:255], v225 offset:5120\n"
      "s_waitcnt lgkmcnt(8)\n"
      "v_exp_f32 v151, v151\n"
      "v_mfma_f32_16x16x32_bf16 a[12:15], a[204:207], v[176:179], a[12:15]\n"
      "ds_read_b64_tr_b16 a[222:223], v229 offset:37888\n"
      "v_add_f32_e64 v216, v216, v167\n"
      "v_cvt_pk_bf16_f32 v171, v150, v151\n"
      "v_mfma_f32_16x16x32_bf16 v[160:163], a[224:227], v[0:3], 0\n"
      "s_add_u32 m0, s68, 20480\n"
      "v_exp_f32 v152, v152\n"
      "v_mfma_f32_16x16x32_bf16 a[32:35], a[192:195], v[168:171], a[32:35]\n"
      "s_add_u32 s64, s86, 16384\n"
      "v_add_f32 v217, v217, v145\n"
      "v_add_f32 v217, v217, v144\n"
      "v_mfma_f32_16x16x32_bf16 v[164:167], a[228:231], v[0:3], 0\n"
      "buffer_load_dwordx4 v234, s[56:59], s64 offen lds\n"
      "v_exp_f32 v153, v153\n"
      "v_mfma_f32_16x16x32_bf16 a[36:39], a[196:199], v[168:171], a[36:39]\n"
      "s_add_u32 s76, s76, s88\n"
      "v_add_f32 v217, v217, v146\n"
      "v_cvt_pk_bf16_f32 v172, v152, v153\n"
      "v_mfma_f32_16x16x32_bf16 v[160:163], a[232:235], v[4:7], v[160:163]\n"
      "s_add_u32 s74, %[g], 2\n"
      "v_exp_f32 v154, v154\n"
      "v_mfma_f32_16x16x32_bf16 a[40:43], a[200:203], v[168:171], a[40:43]\n"
      "s_xor_b32 s74, s74, %[g]\n"
      "v_add_f32 v217, v217, v148\n"
      "v_add_f32 v217, v217, v147\n"
      "v_mfma_f32_16x16x32_bf16 v[164:167], a[236:239], v[4:7], v[164:167]\n"
      "s_waitcnt lgkmcnt(5)\n"
      "v_exp_f32 v155, v155\n"
      "v_mfma_f32_16x16x32_bf16 a[44:47], a[204:207], v[168:171], a[44:47]\n"
      "s_and_b32 s74, s74, 3\n"
      "v_add_f32_e64 v217, v217, v149\n"
      "v_cvt_pk_bf16_f32 v173, v154, v155\n"
      "v_mfma_f32_16x16x32_bf16 v[160:163], a[240:243], v[8:11], v[160:163]\n"
      "s_waitcnt lgkmcnt(0)\n"
      "v_exp_f32 v156, v156\n"
      "v_mfma_f32_16x16x32_bf16 a[16:19], a[208:211], v[176:179], a[16:19]\n"
      "s_lshl_b32 s74, s74, 12\n"
      "v_add_f32 v217, v217, v151\n"
      "v_add_f32 v217, v217, v150\n"
      "v_mfma_f32_16x16x32_bf16 v[164:167], a[244:247], v[8:11], v[164:167]\n"
      "s_waitcnt lgkmcnt(1)\n"
      "v_exp_f32_e64 v157, v157\n"
      "v_mfma_f32_16x16x32_bf16 a[20:23], a[212:215], v[176:179], a[20:23]\n"
      "s_add_u32 m0, s68, 21504\n"
      "v_add_f32_e64 v218, v218, v152\n"
      "v_cvt_pk_bf16_f32 v174, v156, v157\n"
      "v_mfma_f32_16x16x32_bf16 v[160:163], a[248:251], v[12:15], v[160:163]\n"
      "buffer_load_dwordx4 v235, s[56:59], s64 offen lds\n"
      "v_exp_f32 v158, v158\n"
      "v_mfma_f32_16x16x32_bf16 a[24:27], a[216:219], v[176:179], a[24:27]\n"
      "s_xor_b32 s74, s74, s89\n"
      "v_add_f32 v218, v218, v154\n"
      "v_add_f32 v218, v218, v153\n"
      "v_mfma_f32_16x16x32_bf16 v[164:167], a[252:255], v[12:15], v[164:167]\n"
      "s_add_u32 s77, %[j], 2\n"
      "v_exp_f32 v159, v159\n"
      "v_mfma_f32_16x16x32_bf16 a[28:31], a[220:223], v[176:179], a[28:31]\n"
      "s_and_b32 s77, s77, 3\n"
      "v_add_f32 v218, v218, v155\n"
      "v_cvt_pk_bf16_f32 v175, v158, v159\n"
      "v_mfma_f32_16x16x32_bf16 v[144:147], a[224:227], v[16:19], 0\n"
      "s_lshl_b32 s77, s77, 13\n"
      "v_exp_f32 v160, v160\n"
      "v_mfma_f32_16x16x32_bf16 a[64:67], a[192:195], v[172:175], a[64:67]\n"
      "ds_read_b64_tr_b16 a[192:193], v226 offset:0\n"
      "v_add_f32 v218, v218, v156\n"
      "v_mfma_f32_16x16x32_bf16 v[148:151], a[228:231], v[16:19], 0\n"
      "s_add_u32 m0, s68, 22528\n"
      "v_exp_f32 v161, v161\n"
      "v_mfma_f32_16x16x32_bf16 a[68:71], a[196:199], v[172:175], a[68:71]\n"
      "ds_read_b64_tr_b16 a[194:195], v226 offset:4096\n"
      "s_add_u32 s64, s86, 24576\n"
      "v_cvt_pk_bf16_f32 v176, v160, v161\n"
      "v_mfma_f32_16x16x32_bf16 v[144:147], a[232:235], v[20:23], v[144:147]\n"
      "buffer_load_dwordx4 v234, s[56:59], s64 offen lds\n"
      "v_exp_f32_e64 v162, v162\n"
      "v_mfma_f32_16x16x32_bf16 a[72:75], a[200:203], v[172:175], a[72:75]\n"
      "ds_read_b64_tr_b16 a[196:197], v227 offset:0\n"
      "v_add_f32 v218, v218, v157\n"
      "v_mfma_f32_16x16x32_bf16 v[148:151], a[236:239], v[20:23], v[148:151]\n"
      "v_exp_f32 v163, v163\n"
      "v_mfma_f32_16x16x32_bf16 a[76:79], a[204:207], v[172:175], a[76:79]\n"
      "ds_read_b64_tr_b16 a[198:199], v227 offset:4096\n"
      "v_cvt_pk_bf16_f32 v177, v162, v163\n"
      "v_mfma_f32_16x16x32_bf16 v[144:147], a[240:243], v[24:27], v[144:147]\n"
      "buffer_store_dwordx4 v[248:251], v230, s[60:63], s71 offen sc1\n"
      "v_exp_f32 v164, v164\n"
      "v_mfma_f32_16x16x32_bf16 a[48:51], a[208:211], v[168:171], a[48:51]\n"
      "ds_read_b64_tr_b16 a[200:201], v228 offset:0\n"
      "v_add_f32 v218, v218, v158\n"
      "v_mfma_f32_16x16x32_bf16 v[148:151], a[244:247], v[24:27], v[148:151]\n"
      "v_exp_f32_e64 v165, v165\n"
      "v_mfma_f32_16x16x32_bf16 a[52:55], a[212:215], v[168:171], a[52:55]\n"
      "ds_read_b64_tr_b16 a[202:203], v228 offset:4096\n"
      "s_add_u32 m0, s68, 23552\n"
      "v_cvt_pk_bf16_f32 v178, v164, v165\n"
      "v_mfma_f32_16x16x32_bf16 v[144:147], a[248:251], v[28:31], v[144:147]\n"
      "buffer_load_dwordx4 v235, s[56:59], s64 offen lds\n"
      "v_exp_f32_e64 v166, v166\n"
      "v_mfma_f32_16x16x32_bf16 a[56:59], a[216:219], v[168:171], a[56:59]\n"
      "ds_read_b64_tr_b16 a[204:205], v229 offset:0\n"
      "s_add_u32 s86, s86, 0x20000\n"
      "v_add_f32_e64 v218, v218, v159\n"
      "v_mfma_f32_16x16x32_bf16 v[148:151], a[252:255], v[28:31], v[148:151]\n"
      "s_and_b32 s86, s86, 0xfffff\n"
      "v_exp_f32_e64 v167, v167\n"
      "v_mfma_f32_16x16x32_bf16 a[60:63], a[220:223], v[168:171], a[60:63]\n"
      "ds_read_b64_tr_b16 a[206:207], v229 offset:4096\n"
      "v_cvt_pk_bf16_f32 v179, v166, v167\n"
      "v_mfma_f32_16x16x32_bf16 v[152:155], a[224:227], v[32:35], 0\n"
      "ds_read_b128 a[224:227], v224 offset:32768\n"
      "s_add_u32 s77, s77, s88\n"
      "v_exp_f32 v144, v144\n"
      "v_mfma_f32_16x16x32_bf16 a[80:83], a[208:211], v[172:175], a[80:83]\n"
      "ds_read_b64_tr_b16 a[208:209], v226 offset:1024\n"
      "v_add_f32_e64 v216, v216, v160\n"
      "v_mfma_f32_16x16x32_bf16 v[156:159], a[228:231], v[32:35], 0\n"
      "ds_read_b128 a[228:231], v224 offset:36864\n"
      "s_add_u32 s75, %[g], 3\n"
      "v_exp_f32 v145, v145\n"
      "v_mfma_f32_16x16x32_bf16 a[84:87], a[212:215], v[172:175], a[84:87]\n"
      "ds_read_b64_tr_b16 a[210:211], v226 offset:5120\n"
      "v_add_f32_e64 v216, v216, v161\n"
      "v_cvt_pk_bf16_f32 v168, v144, v145\n"
      "v_mfma_f32_16x16x32_bf16 v[152:155], a[232:235], v[36:39], v[152:155]\n"
      "ds_read_b128 a[232:235], v225 offset:32768\n"
      "s_xor_b32 s75, s75, %[g]\n"
      "v_exp_f32 v146, v146\n"
      "v_mfma_f32_16x16x32_bf16 a[88:91], a[216:219], v[172:175], a[88:91]\n"
      "ds_read_b64_tr_b16 a[212:213], v227 offset:1024\n"
      "v_add_f32 v216, v216, v162\n"
      "v_mfma_f32_16x16x32_bf16 v[156:159], a[236:239], v[36:39], v[156:159]\n"
      "ds_read_b128 a[236:239], v225 offset:36864\n"
      "v_exp_f32 v147, v147\n"
      "v_mfma_f32_16x16x32_bf16 a[92:95], a[220:223], v[172:175], a[92:95]\n"
      "ds_read_b64_tr_b16 a[214:215], v227 offset:5120\n"
      "v_add_f32 v216, v216, v163\n"
      "v_cvt_pk_bf16_f32 v169, v146, v147\n"
      "v_mfma_f32_16x16x32_bf16 v[152:155], a[240:243], v[40:43], v[152:155]\n"
      "s_waitcnt lgkmcnt(8)\n"
      "ds_read_b128 a[240:243], v224 offset:33792\n"
      "v_exp_f32 v148, v148\n"
      "v_mfma_f32_16x16x32_bf16 a[0:3], a[192:195], v[176:179], a[0:3]\n"
      "ds_read_b64_tr_b16 a[216:217], v228 offset:1024\n"
      "v_add_f32 v216, v216, v164\n"
      "v_mfma_f32_16x16x32_bf16 v[156:159], a[244:247], v[40:43], v[156:159]\n"
      "ds_read_b128 a[244:247], v224 offset:37888\n"
      "v_exp_f32_e64 v149, v149\n"
      "v_mfma_f32_16x16x32_bf16 a[4:7], a[196:199], v[176:179], a[4:7]\n"
      "ds_read_b64_tr_b16 a[218:219], v228 offset:5120\n"
      "v_add_f32_e64 v216, v216, v165\n"
      "v_cvt_pk_bf16_f32 v170, v148, v149\n"
      "v_mfma_f32_16x16x32_bf16 v[152:155], a[248:251], v[44:47], v[152:155]\n"
      "ds_read_b128 a[248:251], v225 offset:33792\n"
      "s_and_b32 s75, s75, 3\n"
      "v_exp_f32 v150, v150\n"
      "v_mfma_f32_16x16x32_bf16 a[8:11], a[200:203], v[176:179], a[8:11]\n"
      "ds_read_b64_tr_b16 a[220:221], v229 offset:1024\n"
      "v_add_f32 v216, v216, v166\n"
      "v_mfma_f32_16x16x32_bf16 v[156:159], a[252:255], v[44:47], v[156:159]\n"
      "ds_read_b128 a[252:255], v225 offset:37888\n"
      "s_waitcnt lgkmcnt(8)\n"
      "s_lshl_b32 s75, s75, 12\n"
      "v_exp_f32 v151, v151\n"
      "v_mfma_f32_16x16x32_bf16 a[12:15], a[204:207], v[176:179], a[12:15]\n"
      "ds_read_b64_tr_b16 a[222:223], v229 offset:5120\n"
      "v_add_f32_e64 v216, v216, v167\n"
      "v_cvt_pk_bf16_f32 v171, v150, v151\n"
      "v_mfma_f32_16x16x32_bf16 v[160:163], a[224:227], v[48:51], 0\n"
      "s_xor_b32 s75, s75, s89\n"
      "v_exp_f32 v152, v152\n"
      "v_mfma_f32_16x16x32_bf16 a[32:35], a[192:195], v[168:171], a[32:35]\n"
      "s_add_u32 s78, %[j], 3\n"
      "v_add_f32 v217, v217, v145\n"
      "v_add_f32 v217, v217, v144\n"
      "v_mfma_f32_16x16x32_bf16 v[164:167], a[228:231], v[48:51], 0\n"
      "s_and_b32 s78, s78, 3\n"
      "v_exp_f32 v153, v153\n"
      "v_mfma_f32_16x16x32_bf16 a[36:39], a[196:199], v[168:171], a[36:39]\n"
      "s_lshl_b32 s78, s78, 13\n"
      "v_add_f32 v217, v217, v146\n"
      "v_cvt_pk_bf16_f32 v172, v152, v153\n"
      "v_mfma_f32_16x16x32_bf16 v[160:163], a[232:235], v[52:55], v[160:163]\n"
      "s_add_u32 s78, s78, s88\n"
      "v_exp_f32_e64 v154, v154\n"
      "v_mfma_f32_16x16x32_bf16 a[40:43], a[200:203], v[168:171], a[40:43]\n"
      "s_sub_u32 s66, s68, s69\n"
      "v_add_f32 v217, v217, v148\n"
      "v_add_f32 v217, v217, v147\n"
      "v_mfma_f32_16x16x32_bf16 v[164:167], a[236:239], v[52:55], v[164:167]\n"
      "s_waitcnt lgkmcnt(5)\n"
      "buffer_store_dwordx4 v[248:251], v230, s[60:63], s70 offen sc1\n"
      "v_exp_f32 v155, v155\n"
      "v_mfma_f32_16x16x32_bf16 a[44:47], a[204:207], v[168:171], a[44:47]\n"
      "s_lshl_b32 s65, %[wid], 10\n"
      "v_add_f32_e64 v217, v217, v149\n"
      "v_cvt_pk_bf16_f32 v173, v154, v155\n"
      "v_mfma_f32_16x16x32_bf16 v[160:163], a[240:243], v[56:59], v[160:163]\n"
      "s_waitcnt lgkmcnt(0)\n"
      "v_exp_f32 v156, v156\n"
      "v_mfma_f32_16x16x32_bf16 a[16:19], a[208:211], v[176:179], a[16:19]\n"
      "s_lshl_b32 s67, %[flip], 13\n"
      "v_add_f32 v217, v217, v151\n"
      "v_add_f32 v217, v217, v150\n"
      "v_mfma_f32_16x16x32_bf16 v[164:167], a[244:247], v[56:59], v[164:167]\n"
      "s_waitcnt lgkmcnt(1)\n"
      "v_exp_f32_e64 v157, v157\n"
      "v_mfma_f32_16x16x32_bf16 a[20:23], a[212:215], v[176:179], a[20:23]\n"
      "s_add_u32 s65, s65, s67\n"
      "v_add_f32 v218, v218, v152\n"
      "v_cvt_pk_bf16_f32 v174, v156, v157\n"
      "v_mfma_f32_16x16x32_bf16 v[160:163], a[248:251], v[60:63], v[160:163]\n"
      "s_add_u32 s65, s65, 16384\n"
      "v_exp_f32 v158, v158\n"
      "v_mfma_f32_16x16x32_bf16 a[24:27], a[216:219], v[176:179], a[24:27]\n"
      "v_add_f32 v218, v218, v154\n"
      "v_add_f32_e64 v218, v218, v153\n"
      "v_mfma_f32_16x16x32_bf16 v[164:167], a[252:255], v[60:63], v[164:167]\n"
      "v_exp_f32 v159, v159\n"
      "v_mfma_f32_16x16x32_bf16 a[28:31], a[220:223], v[176:179], a[28:31]\n"
      "v_add_f32 v218, v218, v155\n"
      "v_cvt_pk_bf16_f32 v175, v158, v159\n"
      "v_mov_b32 v223, v216\n"
      "s_nop 1\n"
      "v_permlane16_swap_b32 v216, v223\n"
      "v_add_f32 v216, v216, v223\n"
      "v_mov_b32 v223, v216\n"
      "s_nop 1\n"
      "v_permlane32_swap_b32 v216, v223\n"
      "v_add_f32 v216, v216, v223\n"
      "v_subrev_u32 v223, 0x21800000, v216\n"
      "v_cmp_le_u32 vcc, 0x3c000000, v223\n"
      "s_nop 0\n"
      "s_cbranch_vccnz SLOW0_%=\n"
      "RET0_%=:\n"
      "v_rcp_f32_e64 v252, v216\n"
      "v_accvgpr_read_b32 v236, a0\n"
      "v_accvgpr_read_b32 v237, a1\n"
      "v_accvgpr_read_b32 v238, a2\n"
      "v_accvgpr_read_b32 v239, a3\n"
      "v_accvgpr_read_b32 v240, a4\n"
      "v_accvgpr_read_b32 v241, a5\n"
      "v_accvgpr_read_b32 v242, a6\n"
      "v_accvgpr_read_b32 v243, a7\n"
      "v_pk_mul_f32 v[236:237], v[236:237], v[252:253] op_sel_hi:[1,0]\n"
      "v_pk_mul_f32 v[238:239], v[238:239], v[252:253] op_sel_hi:[1,0]\n"
      "v_pk_mul_f32 v[240:241], v[240:241], v[252:253] op_sel_hi:[1,0]\n"
      "v_pk_mul_f32 v[242:243], v[242:243], v[252:253] op_sel_hi:[1,0]\n"
      "v_cvt_pk_bf16_f32 v0, v236, v237\n"
      "v_cvt_pk_bf16_f32 v1, v238, v239\n"
      "v_cvt_pk_bf16_f32 v2, v240, v241\n"
      "v_cvt_pk_bf16_f32 v3, v242, v243\n"
      "v_accvgpr_read_b32 v244, a8\n"
      "v_accvgpr_read_b32 v245, a9\n"
      "v_accvgpr_read_b32 v246, a10\n"
      "v_accvgpr_read_b32 v247, a11\n"
      "v_accvgpr_read_b32 v248, a12\n"
      "v_accvgpr_read_b32 v249, a13\n"
      "v_accvgpr_read_b32 v250, a14\n"
      "v_accvgpr_read_b32 v251, a15\n"
      "s_add_u32 s70, s87, 0\n"
      "s_nop 0\n"
      "buffer_store_dwordx4 v[0:3], v230, s[60:63], s70 offen offset:0 sc1\n"
      "v_pk_mul_f32 v[244:245], v[244:245], v[252:253] op_sel_hi:[1,0]\n"
      "v_pk_mul_f32 v[246:247], v[246:247], v[252:253] op_sel_hi:[1,0]\n"
      "v_pk_mul_f32 v[248:249], v[248:249], v[252:253] op_sel_hi:[1,0]\n"
      "v_pk_mul_f32 v[250:251], v[250:251], v[252:253] op_sel_hi:[1,0]\n"
      "v_cvt_pk_bf16_f32 v4, v244, v245\n"
      "v_cvt_pk_bf16_f32 v5, v246, v247\n"
      "v_cvt_pk_bf16_f32 v6, v248, v249\n"
      "v_cvt_pk_bf16_f32 v7, v250, v251\n"
      "v_accvgpr_read_b32 v236, a16\n"
      "v_accvgpr_read_b32 v237, a17\n"
      "v_accvgpr_read_b32 v238, a18\n"
      "v_accvgpr_read_b32 v239, a19\n"
      "v_accvgpr_read_b32 v240, a20\n"
      "v_accvgpr_read_b32 v241, a21\n"
      "v_accvgpr_read_b32 v242, a22\n"
      "v_accvgpr_read_b32 v243, a23\n"
      "buffer_store_dwordx4 v[4:7], v230, s[60:63], s70 offen offset:2048 sc1\n"
      "v_pk_mul_f32 v[236:237], v[236:237], v[252:253] op_sel_hi:[1,0]\n"
      "v_pk_mul_f32 v[238:239], v[238:239], v[252:253] op_sel_hi:[1,0]\n"
      "v_pk_mul_f32 v[240:241], v[240:241], v[252:253] op_sel_hi:[1,0]\n"
      "v_pk_mul_f32 v[242:243], v[242:243], v[252:253] op_sel_hi:[1,0]\n"
      "v_cvt_pk_bf16_f32 v8, v236, v237\n"
      "v_cvt_pk_bf16_f32 v9, v238, v239\n"
      "v_cvt_pk_bf16_f32 v10, v240, v241\n"
      "v_cvt_pk_bf16_f32 v11, v242, v243\n"
      "v_accvgpr_read_b32 v244, a24\n"
      "v_accvgpr_read_b32 v245, a25\n"
      "v_accvgpr_read_b32 v246, a26\n"
      "v_accvgpr_read_b32 v247, a27\n"
      "v_accvgpr_read_b32 v248, a28\n"
      "v_accvgpr_read_b32 v249, a29\n"
      "v_accvgpr_read_b32 v250, a30\n"
      "v_accvgpr_read_b32 v251, a31\n"
      "s_add_u32 s70, s87, 4096\n"
      "buffer_store_dwordx4 v[8:11], v230, s[60:63], s70 offen offset:0 sc1\n"
      "v_pk_mul_f32 v[244:245], v[244:245], v[252:253] op_sel_hi:[1,0]\n"
      "v_pk_mul_f32 v[246:247], v[246:247], v[252:253] op_sel_hi:[1,0]\n"
      "v_pk_mul_f32 v[248:249], v[248:249], v[252:253] op_sel_hi:[1,0]\n"
      "v_pk_mul_f32 v[250:251], v[250:251], v[252:253] op_sel_hi:[1,0]\n"
      "v_cvt_pk_bf16_f32 v12, v244, v245\n"
      "v_cvt_pk_bf16_f32 v13, v246, v247\n"
      "v_cvt_pk_bf16_f32 v14, v248, v249\n"
      "v_cvt_pk_bf16_f32 v15, v250, v251\n"
      "buffer_store_dwordx4 v[12:15], v230, s[60:63], s70 offen offset:2048 sc1\n"
      "ds_read_b64_tr_b16 a[0:1], v232 offset:32768\n"
      "ds_read_b64_tr_b16 a[2:3], v232 offset:36864\n"
      "ds_read_b64_tr_b16 a[4:5], v232 offset:32776\n"
      "ds_read_b64_tr_b16 a[6:7], v232 offset:36872\n"
      "ds_read_b64_tr_b16 a[8:9], v233 offset:32768\n"
      "ds_read_b64_tr_b16 a[10:11], v233 offset:36864\n"
      "ds_read_b64_tr_b16 a[12:13], v233 offset:32776\n"
      "ds_read_b64_tr_b16 a[14:15], v233 offset:36872\n"
      "v_mfma_f32_16x16x32_bf16 v[144:147], a[224:227], v[64:67], 0\n"
      "ds_read_b64_tr_b16 a[16:17], v232 offset:33792\n"
      "v_exp_f32 v160, v160\n"
      "v_mfma_f32_16x16x32_bf16 a[64:67], a[192:195], v[172:175], a[64:67]\n"
      "ds_read_b64_tr_b16 a[192:193], v226 offset:32768\n"
      "v_add_f32 v218, v218, v156\n"
      "v_mfma_f32_16x16x32_bf16 v[148:151], a[228:231], v[64:67], 0\n"
      "ds_read_b64_tr_b16 a[18:19], v232 offset:37888\n"
      "v_exp_f32_e64 v161, v161\n"
      "v_mfma_f32_16x16x32_bf16 a[68:71], a[196:199], v[172:175], a[68:71]\n"
      "ds_read_b64_tr_b16 a[194:195], v226 offset:36864\n"
      "v_cvt_pk_bf16_f32 v176, v160, v161\n"
      "v_mfma_f32_16x16x32_bf16 v[144:147], a[232:235], v[68:71], v[144:147]\n"
      "ds_read_b64_tr_b16 a[20:21], v232 offset:33800\n"
      "v_exp_f32_e64 v162, v162\n"
      "v_mfma_f32_16x16x32_bf16 a[72:75], a[200:203], v[172:175], a[72:75]\n"
      "ds_read_b64_tr_b16 a[196:197], v227 offset:32768\n"
      "v_add_f32_e64 v218, v218, v157\n"
      "v_mfma_f32_16x16x32_bf16 v[148:151], a[236:239], v[68:71], v[148:151]\n"
      "ds_read_b64_tr_b16 a[22:23], v232 offset:37896\n"
      "v_exp_f32_e64 v163, v163\n"
      "v_mfma_f32_16x16x32_bf16 a[76:79], a[204:207], v[172:175], a[76:79]\n"
      "ds_read_b64_tr_b16 a[198:199], v227 offset:36864\n"
      "v_cvt_pk_bf16_f32 v177, v162, v163\n"
      "v_mfma_f32_16x16x32_bf16 v[144:147], a[240:243], v[72:75], v[144:147]\n"
      "ds_read_b64_tr_b16 a[24:25], v233 offset:33792\n"
      "v_exp_f32_e64 v164, v164\n"
      "v_mfma_f32_16x16x32_bf16 a[48:51], a[208:211], v[168:171], a[48:51]\n"
      "ds_read_b64_tr_b16 a[200:201], v228 offset:32768\n"
      "v_add_f32_e64 v218, v218, v158\n"
      "v_mfma_f32_16x16x32_bf16 v[148:151], a[244:247], v[72:75], v[148:151]\n"
      "ds_read_b64_tr_b16 a[26:27], v233 offset:37888\n"
      "v_exp_f32_e64 v165, v165\n"
      "v_mfma_f32_16x16x32_bf16 a[52:55], a[212:215], v[168:171], a[52:55]\n"
      "ds_read_b64_tr_b16 a[202:203], v228 offset:36864\n"
      "v_cvt_pk_bf16_f32 v178, v164, v165\n"
      "v_mfma_f32_16x16x32_bf16 v[144:147], a[248:251], v[76:79], v[144:147]\n"
      "ds_read_b64_tr_b16 a[28:29], v233 offset:33800\n"
      "v_exp_f32_e64 v166, v166\n"
      "v_mfma_f32_16x16x32_bf16 a[56:59], a[216:219], v[168:171], a[56:59]\n"
      "ds_read_b64_tr_b16 a[204:205], v229 offset:32768\n"
      "v_add_f32_e64 v218, v218, v159\n"
      "v_mfma_f32_16x16x32_bf16 v[148:151], a[252:255], v[76:79], v[148:151]\n"
      "ds_read_b64_tr_b16 a[30:31], v233 offset:37896\n"
      "v_exp_f32_e64 v167, v167\n"
      "v_mfma_f32_16x16x32_bf16 a[60:63], a[220:223], v[168:171], a[60:63]\n"
      "ds_read_b64_tr_b16 a[206:207], v229 offset:36864\n"
      "v_cvt_pk_bf16_f32 v179, v166, v167\n"
      "s_waitcnt lgkmcnt(1)\n"
      "v_mov_b32 v223, v217\n"
      "s_nop 1\n"
      "v_permlane16_swap_b32 v217, v223\n"
      "v_add_f32 v217, v217, v223\n"
      "v_mov_b32 v223, v217\n"
      "s_nop 1\n"
      "v_permlane32_swap_b32 v217, v223\n"
      "v_add_f32 v217, v217, v223\n"
      "v_subrev_u32 v223, 0x21800000, v217\n"
      "v_cmp_le_u32 vcc, 0x3c000000, v223\n"
      "s_nop 0\n"
      "s_cbranch_vccnz SLOW1_%=\n"
      "RET1_%=:\n"
      "v_rcp_f32 v252, v217\n"
      "v_accvgpr_read_b32 v236, a32\n"
      "v_accvgpr_read_b32 v237, a33\n"
      "v_accvgpr_read_b32 v238, a34\n"
      "v_accvgpr_read_b32 v239, a35\n"
      "v_accvgpr_read_b32 v240, a36\n"
      "v_accvgpr_read_b32 v241, a37\n"
      "v_accvgpr_read_b32 v242, a38\n"
      "v_accvgpr_read_b32 v243, a39\n"
      "v_pk_mul_f32 v[236:237], v[236:237], v[252:253] op_sel_hi:[1,0]\n"
      "v_pk_mul_f32 v[238:239], v[238:239], v[252:253] op_sel_hi:[1,0]\n"
      "v_pk_mul_f32 v[240:241], v[240:241], v[252:253] op_sel_hi:[1,0]\n"
      "v_pk_mul_f32 v[242:243], v[242:243], v[252:253] op_sel_hi:[1,0]\n"
      "v_cvt_pk_bf16_f32 v16, v236, v237\n"
      "v_cvt_pk_bf16_f32 v17, v238, v239\n"
      "v_cvt_pk_bf16_f32 v18, v240, v241\n"
      "v_cvt_pk_bf16_f32 v19, v242, v243\n"
      "v_accvgpr_read_b32 v244, a40\n"
      "v_accvgpr_read_b32 v245, a41\n"
      "s_add_u32 m0, s69, 32768\n"
      "s_add_u32 s64, s86, 0\n"
      "s_nop 0\n"
      "buffer_load_dwordx4 v234, s[56:59], s64 offen lds\n"
      "v_accvgpr_read_b32 v246, a42\n"
      "v_accvgpr_read_b32 v247, a43\n"
      "v_accvgpr_read_b32 v248, a44\n"
      "v_accvgpr_read_b32 v249, a45\n"
      "v_accvgpr_read_b32 v250, a46\n"
      "v_accvgpr_read_b32 v251, a47\n"
      "s_add_u32 s70, s87, 65536\n"
      "buffer_store_dwordx4 v[16:19], v230, s[60:63], s70 offen offset:0 sc1\n"
      "v_pk_mul_f32 v[244:245], v[244:245], v[252:253] op_sel_hi:[1,0]\n"
      "v_pk_mul_f32 v[246:247], v[246:247], v[252:253] op_sel_hi:[1,0]\n"
      "v_pk_mul_f32 v[248:249], v[248:249], v[252:253] op_sel_hi:[1,0]\n"
      "v_pk_mul_f32 v[250:251], v[250:251], v[252:253] op_sel_hi:[1,0]\n"
      "v_cvt_pk_bf16_f32 v20, v244, v245\n"
      "v_cvt_pk_bf16_f32 v21, v246, v247\n"
      "v_cvt_pk_bf16_f32 v22, v248, v249\n"
      "v_cvt_pk_bf16_f32 v23, v250, v251\n"
      "v_accvgpr_read_b32 v236, a48\n"
      "v_accvgpr_read_b32 v237, a49\n"
      "v_accvgpr_read_b32 v238, a50\n"
      "v_accvgpr_read_b32 v239, a51\n"
      "v_accvgpr_read_b32 v240, a52\n"
      "v_accvgpr_read_b32 v241, a53\n"
      "v_accvgpr_read_b32 v242, a54\n"
      "v_accvgpr_read_b32 v243, a55\n"
      "buffer_store_dwordx4 v[20:23], v230, s[60:63], s70 offen offset:2048 sc1\n"
      "v_pk_mul_f32 v[236:237], v[236:237], v[252:253] op_sel_hi:[1,0]\n"
      "v_pk_mul_f32 v[238:239], v[238:239], v[252:253] op_sel_hi:[1,0]\n"
      "v_pk_mul_f32 v[240:241], v[240:241], v[252:253] op_sel_hi:[1,0]\n"
      "v_pk_mul_f32 v[242:243], v[242:243], v[252:253] op_sel_hi:[1,0]\n"
      "v_cvt_pk_bf16_f32 v24, v236, v237\n"
      "v_cvt_pk_bf16_f32 v25, v238, v239\n"
      "v_cvt_pk_bf16_f32 v26, v240, v241\n"
      "v_cvt_pk_bf16_f32 v27, v242, v243\n"
      "v_accvgpr_read_b32 v244, a56\n"
      "s_add_u32 m0, s69, 33792\n"
      "s_nop 0\n"
      "s_nop 0\n"
      "buffer_load_dwordx4 v235, s[56:59], s64 offen lds\n"
      "v_accvgpr_read_b32 v245, a57\n"
      "v_accvgpr_read_b32 v246, a58\n"
      "v_accvgpr_read_b32 v247, a59\n"
      "v_accvgpr_read_b32 v248, a60\n"
      "v_accvgpr_read_b32 v249, a61\n"
      "v_accvgpr_read_b32 v250, a62\n"
      "v_accvgpr_read_b32 v251, a63\n"
      "s_add_u32 s70, s87, 69632\n"
      "buffer_store_dwordx4 v[24:27], v230, s[60:63], s70 offen offset:0 sc1\n"
      "v_pk_mul_f32 v[244:245], v[244:245], v[252:253] op_sel_hi:[1,0]\n"
      "v_pk_mul_f32 v[246:247], v[246:247], v[252:253] op_sel_hi:[1,0]\n"
      "v_pk_mul_f32 v[248:249], v[248:249], v[252:253] op_sel_hi:[1,0]\n"
      "v_pk_mul_f32 v[250:251], v[250:251], v[252:253] op_sel_hi:[1,0]\n"
      "v_cvt_pk_bf16_f32 v28, v244, v245\n"
      "v_cvt_pk_bf16_f32 v29, v246, v247\n"
      "v_cvt_pk_bf16_f32 v30, v248, v249\n"
      "v_cvt_pk_bf16_f32 v31, v250, v251\n"
      "buffer_store_dwordx4 v[28:31], v230, s[60:63], s70 offen offset:2048 sc1\n"
      "s_waitcnt vmcnt(22)\n"
      "s_nop 0\n"
      "ds_read_b64_tr_b16 a[32:33], v232 offset:0\n"
      "ds_read_b64_tr_b16 a[34:35], v232 offset:4096\n"
      "ds_read_b64_tr_b16 a[36:37], v232 offset:8\n"
      "ds_read_b64_tr_b16 a[38:39], v232 offset:4104\n"
      "ds_read_b64_tr_b16 a[40:41], v233 offset:0\n"
      "ds_read_b64_tr_b16 a[42:43], v233 offset:4096\n"
      "ds_read_b64_tr_b16 a[44:45], v233 offset:8\n"
      "ds_read_b64_tr_b16 a[46:47], v233 offset:4104\n"
      "v_add_u32_e64 v216, s65, v230\n"
      "v_xor_b32 v217, 0x2000, v216\n"
      "v_mfma_f32_16x16x32_bf16 v[152:155], a[224:227], v[80:83], 0\n"
      "s_add_u32 m0, s69, 34816\n"
      "ds_read_b64_tr_b16 a[48:49], v232 offset:1024\n"
      "v_exp_f32_e64 v144, v144\n"
      "v_mfma_f32_16x16x32_bf16 a[80:83], a[208:211], v[172:175], a[80:83]\n"
      "ds_read_b64_tr_b16 a[208:209], v226 offset:33792\n"
      "s_add_u32 s64, s86, 8192\n"
      "v_add_f32_e64 v219, v219, v160\n"
      "v_mfma_f32_16x16x32_bf16 v[156:159], a[228:231], v[80:83], 0\n"
      "buffer_load_dwordx4 v234, s[56:59], s64 offen lds\n"
      "ds_read_b64_tr_b16 a[50:51], v232 offset:5120\n"
      "v_exp_f32_e64 v145, v145\n"
      "v_mfma_f32_16x16x32_bf16 a[84:87], a[212:215], v[172:175], a[84:87]\n"
      "ds_read_b64_tr_b16 a[210:211], v226 offset:37888\n"
      "v_add_f32_e64 v219, v219, v161\n"
      "v_cvt_pk_bf16_f32 v168, v144, v145\n"
      "v_mfma_f32_16x16x32_bf16 v[152:155], a[232:235], v[84:87], v[152:155]\n"
      "ds_read_b64_tr_b16 a[52:53], v232 offset:1032\n"
      "v_exp_f32_e64 v146, v146\n"
      "v_mfma_f32_16x16x32_bf16 a[88:91], a[216:219], v[172:175], a[88:91]\n"
      "ds_read_b64_tr_b16 a[212:213], v227 offset:33792\n"
      "v_add_f32_e64 v219, v219, v162\n"
      "v_mfma_f32_16x16x32_bf16 v[156:159], a[236:239], v[84:87], v[156:159]\n"
      "s_add_u32 m0, s69, 35840\n"
      "ds_read_b64_tr_b16 a[54:55], v232 offset:5128\n"
      "v_exp_f32_e64 v147, v147\n"
      "v_mfma_f32_16x16x32_bf16 a[92:95], a[220:223], v[172:175], a[92:95]\n"
      "ds_read_b64_tr_b16 a[214:215], v227 offset:37888\n"
      "buffer_load_dwordx4 v235, s[56:59], s64 offen lds\n"
      "v_add_f32 v219, v219, v163\n"
      "v_cvt_pk_bf16_f32 v169, v146, v147\n"
      "v_mfma_f32_16x16x32_bf16 v[152:155], a[240:243], v[88:91], v[152:155]\n"
      "s_waitcnt lgkmcnt(15)\n"
      "ds_read_b64_tr_b16 a[56:57], v233 offset:1024\n"
      "v_exp_f32 v148, v148\n"
      "v_mfma_f32_16x16x32_bf16 a[96:99], a[192:195], v[176:179], a[96:99]\n"
      "ds_read_b64_tr_b16 a[216:217], v228 offset:33792\n"
      "v_add_f32 v219, v219, v164\n"
      "v_mfma_f32_16x16x32_bf16 v[156:159], a[244:247], v[88:91], v[156:159]\n"
      "s_add_u32 m0, s69, 36864\n"
      "ds_read_b64_tr_b16 a[58:59], v233 offset:5120\n"
      "v_exp_f32_e64 v149, v149\n"
      "v_mfma_f32_16x16x32_bf16 a[100:103], a[196:199], v[176:179], a[100:103]\n"
      "ds_read_b64_tr_b16 a[218:219], v228 offset:37888\n"
      "s_add_u32 s64, s86, 16384\n"
      "v_add_f32_e64 v219, v219, v165\n"
      "v_cvt_pk_bf16_f32 v170, v148, v149\n"
      "v_mfma_f32_16x16x32_bf16 v[152:155], a[248:251], v[92:95], v[152:155]\n"
      "buffer_load_dwordx4 v234, s[56:59], s64 offen lds\n"
      "ds_read_b64_tr_b16 a[60:61], v233 offset:1032\n"
      "v_exp_f32 v150, v150\n"
      "v_mfma_f32_16x16x32_bf16 a[104:107], a[200:203], v[176:179], a[104:107]\n"
      "ds_read_b64_tr_b16 a[220:221], v229 offset:33792\n"
      "v_add_f32 v219, v219, v166\n"
      "v_mfma_f32_16x16x32_bf16 v[156:159], a[252:255], v[92:95], v[156:159]\n"
      "ds_read_b64_tr_b16 a[62:63], v233 offset:5128\n"
      "v_exp_f32 v151, v151\n"
      "v_mfma_f32_16x16x32_bf16 a[108:111], a[204:207], v[176:179], a[108:111]\n"
      "ds_read_b64_tr_b16 a[222:223], v229 offset:37888\n"
      "v_add_f32 v219, v219, v167\n"
      "v_cvt_pk_bf16_f32 v171, v150, v151\n"
      "s_waitcnt lgkmcnt(1)\n"
      "v_mov_b32 v223, v218\n"
      "s_nop 1\n"
      "v_permlane16_swap_b32 v218, v223\n"
      "v_add_f32 v218, v218, v223\n"
      "v_mov_b32 v223, v218\n"
      "s_nop 1\n"
      "v_permlane32_swap_b32 v218, v223\n"
      "v_add_f32 v218, v218, v223\n"
      "v_subrev_u32 v223, 0x21800000, v218\n"
      "v_cmp_le_u32 vcc, 0x3c000000, v223\n"
      "s_nop 0\n"
      "s_cbranch_vccnz SLOW2_%=\n"
      "RET2_%=:\n"
      "v_rcp_f32 v252, v218\n"
      "v_accvgpr_read_b32 v236, a64\n"
      "v_accvgpr_read_b32 v237, a65\n"
      "v_accvgpr_read_b32 v238, a66\n"
      "v_accvgpr_read_b32 v239, a67\n"
      "v_accvgpr_read_b32 v240, a68\n"
      "v_accvgpr_read_b32 v241, a69\n"
      "v_accvgpr_read_b32 v242, a70\n"
      "v_accvgpr_read_b32 v243, a71\n"
      "s_add_u32 m0, s69, 37888\n"
      "s_nop 0\n"
      "s_nop 0\n"
      "buffer_load_dwordx4 v235, s[56:59], s64 offen lds\n"
      "v_pk_mul_f32 v[236:237], v[236:237], v[252:253] op_sel_hi:[1,0]\n"
      "v_pk_mul_f32 v[238:239], v[238:239], v[252:253] op_sel_hi:[1,0]\n"
      "v_pk_mul_f32 v[240:241], v[240:241], v[252:253] op_sel_hi:[1,0]\n"
      "v_pk_mul_f32 v[242:243], v[242:243], v[252:253] op_sel_hi:[1,0]\n"
      "v_cvt_pk_bf16_f32 v32, v236, v237\n"
      "v_cvt_pk_bf16_f32 v33, v238, v239\n"
      "v_cvt_pk_bf16_f32 v34, v240, v241\n"
      "v_cvt_pk_bf16_f32 v35, v242, v243\n"
      "v_accvgpr_read_b32 v244, a72\n"
      "v_accvgpr_read_b32 v245, a73\n"
      "v_accvgpr_read_b32 v246, a74\n"
      "v_accvgpr_read_b32 v247, a75\n"
      "v_accvgpr_read_b32 v248, a76\n"
      "v_accvgpr_read_b32 v249, a77\n"
      "v_accvgpr_read_b32 v250, a78\n"
      "v_accvgpr_read_b32 v251, a79\n"
      "s_add_u32 m0, s69, 38912\n"
      "s_add_u32 s64, s86, 24576\n"
      "buffer_load_dwordx4 v234, s[56:59], s64 offen lds\n"
      "v_pk_mul_f32 v[244:245], v[244:245], v[252:253] op_sel_hi:[1,0]\n"
      "v_pk_mul_f32 v[246:247], v[246:247], v[252:253] op_sel_hi:[1,0]\n"
      "v_pk_mul_f32 v[248:249], v[248:249], v[252:253] op_sel_hi:[1,0]\n"
      "v_pk_mul_f32 v[250:251], v[250:251], v[252:253] op_sel_hi:[1,0]\n"
      "v_cvt_pk_bf16_f32 v36, v244, v245\n"
      "v_cvt_pk_bf16_f32 v37, v246, v247\n"
      "v_cvt_pk_bf16_f32 v38, v248, v249\n"
      "v_cvt_pk_bf16_f32 v39, v250, v251\n"
      "v_accvgpr_read_b32 v236, a80\n"
      "v_accvgpr_read_b32 v237, a81\n"
      "v_accvgpr_read_b32 v238, a82\n"
      "v_accvgpr_read_b32 v239, a83\n"
      "v_accvgpr_read_b32 v240, a84\n"
      "v_accvgpr_read_b32 v241, a85\n"
      "v_accvgpr_read_b32 v242, a86\n"
      "v_accvgpr_read_b32 v243, a87\n"
      "s_add_u32 m0, s69, 39936\n"
      "s_nop 0\n"
      "s_nop 0\n"
      "buffer_load_dwordx4 v235, s[56:59], s64 offen lds\n"
      "s_add_u32 s86, s86, 0x20000\n"
      "s_and_b32 s86, s86, 0xfffff\n"
      "v_pk_mul_f32 v[236:237], v[236:237], v[252:253] op_sel_hi:[1,0]\n"
      "v_pk_mul_f32 v[238:239], v[238:239], v[252:253] op_sel_hi:[1,0]\n"
      "v_pk_mul_f32 v[240:241], v[240:241], v[252:253] op_sel_hi:[1,0]\n"
      "v_pk_mul_f32 v[242:243], v[242:243], v[252:253] op_sel_hi:[1,0]\n"
      "v_cvt_pk_bf16_f32 v40, v236, v237\n"
      "v_cvt_pk_bf16_f32 v41, v238, v239\n"
      "v_cvt_pk_bf16_f32 v42, v240, v241\n"
      "v_cvt_pk_bf16_f32 v43, v242, v243\n"
      "v_accvgpr_read_b32 v244, a88\n"
      "v_accvgpr_read_b32 v245, a89\n"
      "v_accvgpr_read_b32 v246, a90\n"
      "v_accvgpr_read_b32 v247, a91\n"
      "v_accvgpr_read_b32 v248, a92\n"
      "v_accvgpr_read_b32 v249, a93\n"
      "v_accvgpr_read_b32 v250, a94\n"
      "v_accvgpr_read_b32 v251, a95\n"
      "s_add_u32 m0, s69, 0\n"
      "s_add_u32 s64, s86, 0\n"
      "buffer_load_dwordx4 v234, s[56:59], s64 offen lds\n"
      "v_pk_mul_f32 v[244:245], v[244:245], v[252:253] op_sel_hi:[1,0]\n"
      "v_pk_mul_f32 v[246:247], v[246:247], v[252:253] op_sel_hi:[1,0]\n"
      "v_pk_mul_f32 v[248:249], v[248:249], v[252:253] op_sel_hi:[1,0]\n"
      "v_pk_mul_f32 v[250:251], v[250:251], v[252:253] op_sel_hi:[1,0]\n"
      "v_cvt_pk_bf16_f32 v44, v244, v245\n"
      "v_cvt_pk_bf16_f32 v45, v246, v247\n"
      "v_cvt_pk_bf16_f32 v46, v248, v249\n"
      "v_cvt_pk_bf16_f32 v47, v250, v251\n"
      "s_add_u32 s72, s82, 1\n"
      "v_mov_b32_e64 v223, s72\n"
      "s_waitcnt vmcnt(13)\n"
      "buffer_store_dword v223, v231, s[60:63], s91 offen sc1\n"
      "s_nop 1\n"
      "v_mfma_f32_16x16x32_bf16 v[160:163], a[224:227], v[96:99], 0\n"
      "v_exp_f32 v152, v152\n"
      "v_mfma_f32_16x16x32_bf16 a[128:131], a[192:195], v[168:171], a[128:131]\n"
      "ds_read_b128 a[224:227], v224 offset:0\n"
      "s_add_u32 m0, s69, 1024\n"
      "v_add_f32_e64 v220, v220, v144\n"
      "v_mfma_f32_16x16x32_bf16 v[164:167], a[228:231], v[96:99], 0\n"
      "buffer_load_dwordx4 v235, s[56:59], s64 offen lds\n"
      "v_exp_f32 v153, v153\n"
      "v_mfma_f32_16x16x32_bf16 a[132:135], a[196:199], v[168:171], a[132:135]\n"
      "ds_read_b128 a[228:231], v224 offset:4096\n"
      "v_add_f32 v220, v220, v145\n"
      "v_cvt_pk_bf16_f32 v172, v152, v153\n"
      "v_mfma_f32_16x16x32_bf16 v[160:163], a[232:235], v[100:103], v[160:163]\n"
      "s_add_u32 m0, s69, 2048\n"
      "v_exp_f32_e64 v154, v154\n"
      "v_mfma_f32_16x16x32_bf16 a[136:139], a[200:203], v[168:171], a[136:139]\n"
      "ds_read_b128 a[232:235], v225 offset:0\n"
      "s_add_u32 s64, s86, 8192\n"
      "v_add_f32_e64 v220, v220, v146\n"
      "v_mfma_f32_16x16x32_bf16 v[164:167], a[236:239], v[100:103], v[164:167]\n"
      "buffer_load_dwordx4 v234, s[56:59], s64 offen lds\n"
      "v_exp_f32_e64 v155, v155\n"
      "v_mfma_f32_16x16x32_bf16 a[140:143], a[204:207], v[168:171], a[140:143]\n"
      "ds_read_b128 a[236:239], v225 offset:4096\n"
      "v_add_f32 v220, v220, v147\n"
      "v_cvt_pk_bf16_f32 v173, v154, v155\n"
      "v_mfma_f32_16x16x32_bf16 v[160:163], a[240:243], v[104:107], v[160:163]\n"
      "s_waitcnt lgkmcnt(4)\n"
      "v_exp_f32 v156, v156\n"
      "v_mfma_f32_16x16x32_bf16 a[112:115], a[208:211], v[176:179], a[112:115]\n"
      "ds_read_b128 a[240:243], v224 offset:1024\n"
      "s_add_u32 m0, s69, 3072\n"
      "v_add_f32 v220, v220, v148\n"
      "v_mfma_f32_16x16x32_bf16 v[164:167], a[244:247], v[104:107], v[164:167]\n"
      "s_barrier\n"
      "buffer_load_dwordx4 v235, s[56:59], s64 offen lds\n"
      "v_exp_f32 v157, v157\n"
      "v_mfma_f32_16x16x32_bf16 a[116:119], a[212:215], v[176:179], a[116:119]\n"
      "ds_read_b128 a[244:247], v224 offset:5120\n"
      "s_add_u32 m0, s69, 4096\n"
      "v_add_f32_e64 v220, v220, v149\n"
      "v_cvt_pk_bf16_f32 v174, v156, v157\n"
      "v_mfma_f32_16x16x32_bf16 v[160:163], a[248:251], v[108:111], v[160:163]\n"
      "s_add_u32 s64, s86, 16384\n"
      "v_exp_f32_e64 v158, v158\n"
      "v_mfma_f32_16x16x32_bf16 a[120:123], a[216:219], v[176:179], a[120:123]\n"
      "ds_read_b128 a[248:251], v225 offset:1024\n"
      "buffer_load_dwordx4 v234, s[56:59], s64 offen lds\n"
      "v_add_f32 v220, v220, v150\n"
      "v_mfma_f32_16x16x32_bf16 v[164:167], a[252:255], v[108:111], v[164:167]\n"
      "s_waitcnt lgkmcnt(3)\n"
      "v_exp_f32 v159, v159\n"
      "v_mfma_f32_16x16x32_bf16 a[124:127], a[220:223], v[176:179], a[124:127]\n"
      "ds_read_b128 a[252:255], v225 offset:5120\n"
      "v_add_f32 v220, v220, v151\n"
      "v_cvt_pk_bf16_f32 v175, v158, v159\n"
      "v_mfma_f32_16x16x32_bf16 v[144:147], a[224:227], v[48:51], 0\n"
      "v_exp_f32 v160, v160\n"
      "v_mfma_f32_16x16x32_bf16 v[180:183], a[192:195], v[172:175], v[180:183]\n"
      "s_add_u32 m0, s69, 5120\n"
      "v_add_f32 v221, v221, v153\n"
      "v_add_f32 v221, v221, v152\n"
      "v_mfma_f32_16x16x32_bf16 v[148:151], a[228:231], v[48:51], 0\n"
      "buffer_load_dwordx4 v235, s[56:59], s64 offen lds\n"
      "v_exp_f32 v161, v161\n"
      "v_mfma_f32_16x16x32_bf16 v[184:187], a[196:199], v[172:175], v[184:187]\n"
      "v_add_f32 v221, v221, v154\n"
      "v_cvt_pk_bf16_f32 v176, v160, v161\n"
      "v_mfma_f32_16x16x32_bf16 v[144:147], a[232:235], v[52:55], v[144:147]\n"
      "s_add_u32 m0, s69, 6144\n"
      "v_exp_f32 v162, v162\n"
      "v_mfma_f32_16x16x32_bf16 v[188:191], a[200:203], v[172:175], v[188:191]\n"
      "s_add_u32 s64, s86, 24576\n"
      "v_add_f32 v221, v221, v156\n"
      "v_add_f32 v221, v221, v155\n"
      "v_mfma_f32_16x16x32_bf16 v[148:151], a[236:239], v[52:55], v[148:151]\n"
      "s_waitcnt lgkmcnt(2)\n"
      "buffer_load_dwordx4 v234, s[56:59], s64 offen lds\n"
      "v_exp_f32 v163, v163\n"
      "v_mfma_f32_16x16x32_bf16 v[192:195], a[204:207], v[172:175], v[192:195]\n"
      "v_add_f32_e64 v221, v221, v157\n"
      "v_cvt_pk_bf16_f32 v177, v162, v163\n"
      "v_mfma_f32_16x16x32_bf16 v[144:147], a[240:243], v[56:59], v[144:147]\n"
      "v_exp_f32_e64 v164, v164\n"
      "v_mfma_f32_16x16x32_bf16 a[144:147], a[208:211], v[168:171], a[144:147]\n"
      "s_add_u32 m0, s69, 7168\n"
      "v_add_f32 v221, v221, v159\n"
      "v_add_f32_e64 v221, v221, v158\n"
      "v_mfma_f32_16x16x32_bf16 v[148:151], a[244:247], v[56:59], v[148:151]\n"
      "s_waitcnt lgkmcnt(0)\n"
      "buffer_load_dwordx4 v235, s[56:59], s64 offen lds\n"
      "v_exp_f32_e64 v165, v165\n"
      "v_mfma_f32_16x16x32_bf16 a[148:151], a[212:215], v[168:171], a[148:151]\n"
      "s_add_u32 s86, s86, 0x20000\n"
      "s_add_u32 m0, s68, 32768\n"
      "v_add_f32_e64 v222, v222, v160\n"
      "v_cvt_pk_bf16_f32 v178, v164, v165\n"
      "v_mfma_f32_16x16x32_bf16 v[144:147], a[248:251], v[60:63], v[144:147]\n"
      "s_and_b32 s86, s86, 0xfffff\n"
      "s_add_u32 s64, s86, 0\n"
      "v_exp_f32 v166, v166\n"
      "v_mfma_f32_16x16x32_bf16 a[152:155], a[216:219], v[168:171], a[152:155]\n"
      "buffer_load_dwordx4 v234, s[56:59], s64 offen lds\n"
      "v_add_f32 v222, v222, v161\n"
      "v_mfma_f32_16x16x32_bf16 v[148:151], a[252:255], v[60:63], v[148:151]\n"
      "v_exp_f32 v167, v167\n"
      "v_mfma_f32_16x16x32_bf16 a[156:159], a[220:223], v[168:171], a[156:159]\n"
      "v_add_f32_e64 v222, v222, v162\n"
      "v_cvt_pk_bf16_f32 v179, v166, v167\n"
      "v_mfma_f32_16x16x32_bf16 v[152:155], a[224:227], v[64:67], 0\n"
      "v_exp_f32_e64 v144, v144\n"
      "v_mfma_f32_16x16x32_bf16 v[112:115], a[192:195], v[176:179], v[112:115]\n"
      "ds_read_b64_tr_b16 a[192:193], v226 offset:0\n"
      "s_add_u32 m0, s68, 33792\n"
      "v_add_f32_e64 v222, v222, v163\n"
      "v_mfma_f32_16x16x32_bf16 v[156:159], a[228:231], v[64:67], 0\n"
      "buffer_load_dwordx4 v235, s[56:59], s64 offen lds\n"
      "v_exp_f32_e64 v145, v145\n"
      "v_mfma_f32_16x16x32_bf16 v[116:119], a[196:199], v[176:179], v[116:119]\n"
      "ds_read_b64_tr_b16 a[194:195], v226 offset:4096\n"
      "v_cvt_pk_bf16_f32 v168, v144, v145\n"
      "v_mfma_f32_16x16x32_bf16 v[152:155], a[232:235], v[68:71], v[152:155]\n"
      "s_add_u32 m0, s68, 34816\n"
      "v_exp_f32_e64 v146, v146\n"
      "v_mfma_f32_16x16x32_bf16 v[120:123], a[200:203], v[176:179], v[120:123]\n"
      "ds_read_b64_tr_b16 a[196:197], v227 offset:0\n"
      "s_add_u32 s64, s86, 8192\n"
      "v_add_f32_e64 v222, v222, v164\n"
      "v_mfma_f32_16x16x32_bf16 v[156:159], a[236:239], v[68:71], v[156:159]\n"
      "buffer_load_dwordx4 v234, s[56:59], s64 offen lds\n"
      "v_exp_f32_e64 v147, v147\n"
      "v_mfma_f32_16x16x32_bf16 v[124:127], a[204:207], v[176:179], v[124:127]\n"
      "ds_read_b64_tr_b16 a[198:199], v227 offset:4096\n"
      "v_cvt_pk_bf16_f32 v169, v146, v147\n"
      "v_mfma_f32_16x16x32_bf16 v[152:155], a[240:243], v[72:75], v[152:155]\n"
      "v_exp_f32_e64 v148, v148\n"
      "v_mfma_f32_16x16x32_bf16 v[196:199], a[208:211], v[172:175], v[196:199]\n"
      "ds_read_b64_tr_b16 a[200:201], v228 offset:0\n"
      "s_add_u32 m0, s68, 35840\n"
      "v_add_f32_e64 v222, v222, v165\n"
      "v_mfma_f32_16x16x32_bf16 v[156:159], a[244:247], v[72:75], v[156:159]\n"
      "buffer_load_dwordx4 v235, s[56:59], s64 offen lds\n"
      "v_exp_f32_e64 v149, v149\n"
      "v_mfma_f32_16x16x32_bf16 v[200:203], a[212:215], v[172:175], v[200:203]\n"
      "ds_read_b64_tr_b16 a[202:203], v228 offset:4096\n"
      "s_add_u32 m0, s68, 36864\n"
      "v_cvt_pk_bf16_f32 v170, v148, v149\n"
      "v_mfma_f32_16x16x32_bf16 v[152:155], a[248:251], v[76:79], v[152:155]\n"
      "s_add_u32 s64, s86, 16384\n"
      "v_exp_f32_e64 v150, v150\n"
      "v_mfma_f32_16x16x32_bf16 v[204:207], a[216:219], v[172:175], v[204:207]\n"
      "ds_read_b64_tr_b16 a[204:205], v229 offset:0\n"
      "buffer_load_dwordx4 v234, s[56:59], s64 offen lds\n"
      "v_add_f32 v222, v222, v166\n"
      "v_mfma_f32_16x16x32_bf16 v[156:159], a[252:255], v[76:79], v[156:159]\n"
      "v_exp_f32 v151, v151\n"
      "v_mfma_f32_16x16x32_bf16 v[208:211], a[220:223], v[172:175], v[208:211]\n"
      "ds_read_b64_tr_b16 a[206:207], v229 offset:4096\n"
      "v_add_f32 v222, v222, v167\n"
      "v_cvt_pk_bf16_f32 v171, v150, v151\n"
      "v_mfma_f32_16x16x32_bf16 v[160:163], a[224:227], v[80:83], 0\n"
      "v_exp_f32 v152, v152\n"
      "v_mfma_f32_16x16x32_bf16 v[128:131], a[208:211], v[176:179], v[128:131]\n"
      "ds_read_b64_tr_b16 a[208:209], v226 offset:1024\n"
      "s_add_u32 m0, s68, 37888\n"
      "v_add_f32_e64 v219, v219, v144\n"
      "v_mfma_f32_16x16x32_bf16 v[164:167], a[228:231], v[80:83], 0\n"
      "buffer_load_dwordx4 v235, s[56:59], s64 offen lds\n"
      "v_exp_f32_e64 v153, v153\n"
      "v_mfma_f32_16x16x32_bf16 v[132:135], a[212:215], v[176:179], v[132:135]\n"
      "ds_read_b64_tr_b16 a[210:211], v226 offset:5120\n"
      "v_add_f32_e64 v219, v219, v145\n"
      "v_cvt_pk_bf16_f32 v172, v152, v153\n"
      "v_mfma_f32_16x16x32_bf16 v[160:163], a[232:235], v[84:87], v[160:163]\n"
      "s_add_u32 m0, s68, 38912\n"
      "v_exp_f32_e64 v154, v154\n"
      "v_mfma_f32_16x16x32_bf16 v[136:139], a[216:219], v[176:179], v[136:139]\n"
      "ds_read_b64_tr_b16 a[212:213], v227 offset:1024\n"
      "s_add_u32 s64, s86, 24576\n"
      "v_add_f32 v219, v219, v146\n"
      "v_mfma_f32_16x16x32_bf16 v[164:167], a[236:239], v[84:87], v[164:167]\n"
      "buffer_load_dwordx4 v234, s[56:59], s64 offen lds\n"
      "v_exp_f32 v155, v155\n"
      "v_mfma_f32_16x16x32_bf16 v[140:143], a[220:223], v[176:179], v[140:143]\n"
      "ds_read_b64_tr_b16 a[214:215], v227 offset:5120\n"
      "v_add_f32_e64 v219, v219, v147\n"
      "v_cvt_pk_bf16_f32 v173, v154, v155\n"
      "v_mfma_f32_16x16x32_bf16 v[160:163], a[240:243], v[88:91], v[160:163]\n"
      "s_waitcnt lgkmcnt(4)\n"
      "v_exp_f32 v156, v156\n"
      "v_mfma_f32_16x16x32_bf16 a[96:99], a[192:195], v[168:171], a[96:99]\n"
      "ds_read_b64_tr_b16 a[216:217], v228 offset:1024\n"
      "s_add_u32 m0, s68, 39936\n"
      "v_add_f32 v219, v219, v148\n"
      "v_mfma_f32_16x16x32_bf16 v[164:167], a[244:247], v[88:91], v[164:167]\n"
      "buffer_load_dwordx4 v235, s[56:59], s64 offen lds\n"
      "v_exp_f32 v157, v157\n"
      "v_mfma_f32_16x16x32_bf16 a[100:103], a[196:199], v[168:171], a[100:103]\n"
      "ds_read_b64_tr_b16 a[218:219], v228 offset:5120\n"
      "s_add_u32 s86, s86, 0x20000\n"
      "v_add_f32_e64 v219, v219, v149\n"
      "v_cvt_pk_bf16_f32 v174, v156, v157\n"
      "v_mfma_f32_16x16x32_bf16 v[160:163], a[248:251], v[92:95], v[160:163]\n"
      "s_and_b32 s86, s86, 0xfffff\n"
      "s_add_u32 s70, s87, 131072\n"
      "v_exp_f32 v158, v158\n"
      "v_mfma_f32_16x16x32_bf16 a[104:107], a[200:203], v[168:171], a[104:107]\n"
      "ds_read_b64_tr_b16 a[220:221], v229 offset:1024\n"
      "buffer_store_dwordx4 v[32:35], v230, s[60:63], s70 offen offset:0 sc1\n"
      "v_add_f32 v219, v219, v150\n"
      "v_mfma_f32_16x16x32_bf16 v[164:167], a[252:255], v[92:95], v[164:167]\n"
      "v_exp_f32 v159, v159\n"
      "v_mfma_f32_16x16x32_bf16 a[108:111], a[204:207], v[168:171], a[108:111]\n"
      "ds_read_b64_tr_b16 a[222:223], v229 offset:5120\n"
      "v_add_f32 v219, v219, v151\n"
      "v_cvt_pk_bf16_f32 v175, v158, v159\n"
      "v_mfma_f32_16x16x32_bf16 v[144:147], a[224:227], v[96:99], 0\n"
      "v_exp_f32 v160, v160\n"
      "v_mfma_f32_16x16x32_bf16 a[128:131], a[192:195], v[172:175], a[128:131]\n"
      "v_add_f32 v220, v220, v153\n"
      "v_add_f32 v220, v220, v152\n"
      "v_mfma_f32_16x16x32_bf16 v[148:151], a[228:231], v[96:99], 0\n"
      "buffer_store_dwordx4 v[36:39], v230, s[60:63], s70 offen offset:2048 sc1\n"
      "v_exp_f32 v161, v161\n"
      "v_mfma_f32_16x16x32_bf16 a[132:135], a[196:199], v[172:175], a[132:135]\n"
      "v_add_f32 v220, v220, v154\n"
      "v_cvt_pk_bf16_f32 v176, v160, v161\n"
      "v_mfma_f32_16x16x32_bf16 v[144:147], a[232:235], v[100:103], v[144:147]\n"
      "v_exp_f32 v162, v162\n"
      "v_mfma_f32_16x16x32_bf16 a[136:139], a[200:203], v[172:175], a[136:139]\n"
      "v_add_f32 v220, v220, v156\n"
      "v_add_f32 v220, v220, v155\n"
      "v_mfma_f32_16x16x32_bf16 v[148:151], a[236:239], v[100:103], v[148:151]\n"
      "s_add_u32 s70, s87, 135168\n"
      "v_exp_f32_e64 v163, v163\n"
      "v_mfma_f32_16x16x32_bf16 a[140:143], a[204:207], v[172:175], a[140:143]\n"
      "buffer_store_dwordx4 v[40:43], v230, s[60:63], s70 offen offset:0 sc1\n"
      "v_cvt_pk_bf16_f32 v177, v162, v163\n"
      "v_mfma_f32_16x16x32_bf16 v[144:147], a[240:243], v[104:107], v[144:147]\n"
      "s_waitcnt lgkmcnt(0)\n"
      "v_exp_f32 v164, v164\n"
      "v_mfma_f32_16x16x32_bf16 a[112:115], a[208:211], v[168:171], a[112:115]\n"
      "v_add_f32 v220, v220, v158\n"
      "v_add_f32 v220, v220, v157\n"
      "v_mfma_f32_16x16x32_bf16 v[148:151], a[244:247], v[104:107], v[148:151]\n"
      "s_barrier\n"
      "v_exp_f32 v165, v165\n"
      "v_mfma_f32_16x16x32_bf16 a[116:119], a[212:215], v[168:171], a[116:119]\n"
      "v_add_f32_e64 v220, v220, v159\n"
      "v_cvt_pk_bf16_f32 v178, v164, v165\n"
      "v_mfma_f32_16x16x32_bf16 v[144:147], a[248:251], v[108:111], v[144:147]\n"
      "buffer_store_dwordx4 v[44:47], v230, s[60:63], s70 offen offset:2048 sc1\n"
      "v_exp_f32 v166, v166\n"
      "v_mfma_f32_16x16x32_bf16 a[120:123], a[216:219], v[168:171], a[120:123]\n"
      "v_add_f32 v221, v221, v161\n"
      "v_add_f32 v221, v221, v160\n"
      "v_mfma_f32_16x16x32_bf16 v[148:151], a[252:255], v[108:111], v[148:151]\n"
      "v_exp_f32 v167, v167\n"
      "v_mfma_f32_16x16x32_bf16 a[124:127], a[220:223], v[168:171], a[124:127]\n"
      "v_add_f32_e64 v221, v221, v162\n"
      "v_cvt_pk_bf16_f32 v179, v166, v167\n"
      "v_add_u32 v224, s66, v232\n"
      "v_xor_b32 v225, 64, v224\n"
      "v_mov_b32 v226, v214\n"
      "v_add_u32 v227, s68, v230\n"
      "buffer_load_dword v254, v214, s[60:63], s92 offen sc1\n"
      "s_waitcnt vmcnt(39)\n"
      "v_mov_b32 v223, v219\n"
      "s_nop 1\n"
      "v_permlane16_swap_b32 v219, v223\n"
      "v_add_f32 v219, v219, v223\n"
      "v_mov_b32 v223, v219\n"
      "s_nop 1\n"
      "v_permlane32_swap_b32 v219, v223\n"
      "v_add_f32 v219, v219, v223\n"
      "v_subrev_u32 v223, 0x21800000, v219\n"
      "v_cmp_le_u32 vcc, 0x3c000000, v223\n"
      "s_nop 0\n"
      "s_cbranch_vccnz SLOW3_%=\n"
      "RET3_%=:\n"
      "v_rcp_f32 v252, v219\n"
      "v_accvgpr_read_b32 v236, a96\n"
      "v_accvgpr_read_b32 v237, a97\n"
      "ds_read_b64_tr_b16 a[224:225], v224 offset:16384\n"
      "v_accvgpr_read_b32 v238, a98\n"
      "v_accvgpr_read_b32 v239, a99\n"
      "v_accvgpr_read_b32 v240, a100\n"
      "s_add_u32 m0, s68, 0\n"
      "s_add_u32 s64, s86, 0\n"
      "buffer_load_dwordx4 v234, s[56:59], s64 offen lds\n"
      "v_accvgpr_read_b32 v241, a101\n"
      "v_accvgpr_read_b32 v242, a102\n"
      "v_accvgpr_read_b32 v243, a103\n"
      "ds_read_b64_tr_b16 a[226:227], v224 offset:20480\n"
      "v_pk_mul_f32 v[236:237], v[236:237], v[252:253] op_sel_hi:[1,0]\n"
      "v_pk_mul_f32 v[238:239], v[238:239], v[252:253] op_sel_hi:[1,0]\n"
      "v_pk_mul_f32 v[240:241], v[240:241], v[252:253] op_sel_hi:[1,0]\n"
      "s_add_u32 m0, s68, 1024\n"
      "s_nop 0\n"
      "s_nop 0\n"
      "buffer_load_dwordx4 v235, s[56:59], s64 offen lds\n"
      "v_pk_mul_f32 v[242:243], v[242:243], v[252:253] op_sel_hi:[1,0]\n"
      "v_cvt_pk_bf16_f32 v48, v236, v237\n"
      "v_cvt_pk_bf16_f32 v49, v238, v239\n"
      "ds_read_b64_tr_b16 a[228:229], v224 offset:16392\n"
      "v_cvt_pk_bf16_f32 v50, v240, v241\n"
      "v_cvt_pk_bf16_f32 v51, v242, v243\n"
      "v_accvgpr_read_b32 v244, a104\n"
      "s_add_u32 m0, s68, 2048\n"
      "s_add_u32 s64, s86, 8192\n"
      "buffer_load_dwordx4 v234, s[56:59], s64 offen lds\n"
      "v_accvgpr_read_b32 v245, a105\n"
      "v_accvgpr_read_b32 v246, a106\n"
      "v_accvgpr_read_b32 v247, a107\n"
      "ds_read_b64_tr_b16 a[230:231], v224 offset:20488\n"
      "v_accvgpr_read_b32 v248, a108\n"
      "v_accvgpr_read_b32 v249, a109\n"
      "v_accvgpr_read_b32 v250, a110\n"
      "s_add_u32 m0, s68, 3072\n"
      "s_nop 0\n"
      "s_nop 0\n"
      "buffer_load_dwordx4 v235, s[56:59], s64 offen lds\n"
      "v_accvgpr_read_b32 v251, a111\n"
      "v_pk_mul_f32 v[244:245], v[244:245], v[252:253] op_sel_hi:[1,0]\n"
      "v_pk_mul_f32 v[246:247], v[246:247], v[252:253] op_sel_hi:[1,0]\n"
      "ds_read_b64_tr_b16 a[232:233], v225 offset:16384\n"
      "v_pk_mul_f32 v[248:249], v[248:249], v[252:253] op_sel_hi:[1,0]\n"
      "v_pk_mul_f32 v[250:251], v[250:251], v[252:253] op_sel_hi:[1,0]\n"
      "v_cvt_pk_bf16_f32 v52, v244, v245\n"
      "s_add_u32 m0, s68, 4096\n"
      "s_add_u32 s64, s86, 16384\n"
      "buffer_load_dwordx4 v234, s[56:59], s64 offen lds\n"
      "v_cvt_pk_bf16_f32 v53, v246, v247\n"
      "v_cvt_pk_bf16_f32 v54, v248, v249\n"
      "v_cvt_pk_bf16_f32 v55, v250, v251\n"
      "ds_read_b64_tr_b16 a[234:235], v225 offset:20480\n"
      "v_accvgpr_read_b32 v236, a112\n"
      "v_accvgpr_read_b32 v237, a113\n"
      "v_accvgpr_read_b32 v238, a114\n"
      "v_accvgpr_read_b32 v239, a115\n"
      "ds_read_b64_tr_b16 a[236:237], v225 offset:16392\n"
      "v_accvgpr_read_b32 v240, a116\n"
      "v_accvgpr_read_b32 v241, a117\n"
      "v_accvgpr_read_b32 v242, a118\n"
      "ds_read_b64_tr_b16 a[238:239], v225 offset:20488\n"
      "v_accvgpr_read_b32 v243, a119\n"
      "v_pk_mul_f32 v[236:237], v[236:237], v[252:253] op_sel_hi:[1,0]\n"
      "v_pk_mul_f32 v[238:239], v[238:239], v[252:253] op_sel_hi:[1,0]\n"
      "ds_read_b64_tr_b16 a[240:241], v224 offset:17408\n"
      "v_pk_mul_f32 v[240:241], v[240:241], v[252:253] op_sel_hi:[1,0]\n"
      "v_pk_mul_f32 v[242:243], v[242:243], v[252:253] op_sel_hi:[1,0]\n"
      "v_cvt_pk_bf16_f32 v56, v236, v237\n"
      "ds_read_b64_tr_b16 a[242:243], v224 offset:21504\n"
      "v_cvt_pk_bf16_f32 v57, v238, v239\n"
      "v_cvt_pk_bf16_f32 v58, v240, v241\n"
      "v_cvt_pk_bf16_f32 v59, v242, v243\n"
      "ds_read_b64_tr_b16 a[244:245], v224 offset:17416\n"
      "v_accvgpr_read_b32 v244, a120\n"
      "v_accvgpr_read_b32 v245, a121\n"
      "v_accvgpr_read_b32 v246, a122\n"
      "ds_read_b64_tr_b16 a[246:247], v224 offset:21512\n"
      "v_accvgpr_read_b32 v247, a123\n"
      "v_accvgpr_read_b32 v248, a124\n"
      "v_accvgpr_read_b32 v249, a125\n"
      "ds_read_b64_tr_b16 a[248:249], v225 offset:17408\n"
      "v_accvgpr_read_b32 v250, a126\n"
      "v_accvgpr_read_b32 v251, a127\n"
      "v_pk_mul_f32 v[244:245], v[244:245], v[252:253] op_sel_hi:[1,0]\n"
      "ds_read_b64_tr_b16 a[250:251], v225 offset:21504\n"
      "v_pk_mul_f32 v[246:247], v[246:247], v[252:253] op_sel_hi:[1,0]\n"
      "v_pk_mul_f32 v[248:249], v[248:249], v[252:253] op_sel_hi:[1,0]\n"
      "v_pk_mul_f32 v[250:251], v[250:251], v[252:253] op_sel_hi:[1,0]\n"
      "ds_read_b64_tr_b16 a[252:253], v225 offset:17416\n"
      "v_cvt_pk_bf16_f32 v60, v244, v245\n"
      "v_cvt_pk_bf16_f32 v61, v246, v247\n"
      "v_cvt_pk_bf16_f32 v62, v248, v249\n"
      "ds_read_b64_tr_b16 a[254:255], v225 offset:21512\n"
      "v_cvt_pk_bf16_f32 v63, v250, v251\n"
      "s_add_u32 s72, s82, 3\n"
      "v_mov_b32_e64 v223, s72\n"
      "s_waitcnt vmcnt(6)\n"
      "buffer_store_dword v223, v231, s[60:63], s91 offen sc1\n"
      "s_nop 1\n"
      "s_waitcnt vmcnt(6)\n"
      "s_add_u32 s97, s82, 1\n"
      "v_subrev_u32 v254, s97, v254\n"
      "v_cmp_lt_u32 vcc, 6, v254\n"
      "s_nop 0\n"
      "s_cbranch_vccnz PA_%=\n"
      "PAR_%=:\n"
      "s_waitcnt vmcnt(28)\n"
      "ds_read_b64_tr_b16 a[96:97], v232 offset:32768\n"
      "v_exp_f32_e64 v144, v144\n"
      "v_mfma_f32_16x16x32_bf16 v[180:183], a[192:195], v[176:179], v[180:183]\n"
      "s_add_u32 m0, s68, 5120\n"
      "ds_read_b64_tr_b16 a[98:99], v232 offset:36864\n"
      "v_add_f32 v221, v221, v166\n"
      "v_add_f32 v221, v221, v165\n"
      "v_add_f32 v221, v221, v164\n"
      "v_add_f32 v221, v221, v163\n"
      "buffer_load_dwordx4 v235, s[56:59], s64 offen lds\n"
      "ds_read_b64_tr_b16 a[100:101], v232 offset:32776\n"
      "v_exp_f32_e64 v145, v145\n"
      "v_mfma_f32_16x16x32_bf16 v[184:187], a[196:199], v[176:179], v[184:187]\n"
      "ds_read_b64_tr_b16 a[102:103], v232 offset:36872\n"
      "v_add_f32 v222, v222, v145\n"
      "v_add_f32 v222, v222, v144\n"
      "v_add_f32_e64 v221, v221, v167\n"
      "v_cvt_pk_bf16_f32 v168, v144, v145\n"
      "ds_read_b64_tr_b16 a[104:105], v233 offset:32768\n"
      "v_exp_f32_e64 v146, v146\n"
      "v_mfma_f32_16x16x32_bf16 v[188:191], a[200:203], v[176:179], v[188:191]\n"
      "s_add_u32 m0, s68, 6144\n"
      "ds_read_b64_tr_b16 a[106:107], v233 offset:36864\n"
      "v_add_f32 v222, v222, v146\n"
      "s_add_u32 s64, s86, 24576\n"
      "ds_read_b64_tr_b16 a[108:109], v233 offset:32776\n"
      "v_exp_f32 v147, v147\n"
      "v_mfma_f32_16x16x32_bf16 v[192:195], a[204:207], v[176:179], v[192:195]\n"
      "buffer_load_dwordx4 v234, s[56:59], s64 offen lds\n"
      "ds_read_b64_tr_b16 a[110:111], v233 offset:36872\n"
      "v_add_f32_e64 v222, v222, v147\n"
      "v_cvt_pk_bf16_f32 v169, v146, v147\n"
      "ds_read_b64_tr_b16 a[112:113], v232 offset:33792\n"
      "v_exp_f32_e64 v148, v148\n"
      "v_mfma_f32_16x16x32_bf16 a[144:147], a[208:211], v[172:175], a[144:147]\n"
      "ds_read_b64_tr_b16 a[114:115], v232 offset:37888\n"
      "v_add_f32 v222, v222, v148\n"
      "ds_read_b64_tr_b16 a[116:117], v232 offset:33800\n"
      "v_exp_f32 v149, v149\n"
      "v_mfma_f32_16x16x32_bf16 a[148:151], a[212:215], v[172:175], a[148:151]\n"
      "s_add_u32 m0, s68, 7168\n"
      "ds_read_b64_tr_b16 a[118:119], v232 offset:37896\n"
      "v_add_f32_e64 v222, v222, v149\n"
      "v_cvt_pk_bf16_f32 v170, v148, v149\n"
      "buffer_load_dwordx4 v235, s[56:59], s64 offen lds\n"
      "ds_read_b64_tr_b16 a[120:121], v233 offset:33792\n"
      "v_exp_f32_e64 v150, v150\n"
      "v_mfma_f32_16x16x32_bf16 a[152:155], a[216:219], v[172:175], a[152:155]\n"
      "s_add_u32 s86, s86, 0x20000\n"
      "ds_read_b64_tr_b16 a[122:123], v233 offset:37888\n"
      "v_add_f32_e64 v222, v222, v150\n"
      "s_and_b32 s86, s86, 0xfffff\n"
      "ds_read_b64_tr_b16 a[124:125], v233 offset:33800\n"
      "v_exp_f32 v151, v151\n"
      "v_mfma_f32_16x16x32_bf16 a[156:159], a[220:223], v[172:175], a[156:159]\n"
      "ds_read_b64_tr_b16 a[126:127], v233 offset:37896\n"
      "v_add_f32 v222, v222, v151\n"
      "v_cvt_pk_bf16_f32 v171, v150, v151\n"
      "s_waitcnt lgkmcnt(15)\n"
      "v_mov_b32 v223, v220\n"
      "s_nop 1\n"
      "v_permlane16_swap_b32 v220, v223\n"
      "v_add_f32 v220, v220, v223\n"
      "v_mov_b32 v223, v220\n"
      "s_nop 1\n"
      "v_permlane32_swap_b32 v220, v223\n"
      "v_add_f32 v220, v220, v223\n"
      "v_subrev_u32 v223, 0x21800000, v220\n"
      "v_cmp_le_u32 vcc, 0x3c000000, v223\n"
      "s_nop 0\n"
      "s_cbranch_vccnz SLOW4_%=\n"
      "RET4_%=:\n"
      "v_rcp_f32 v252, v220\n"
      "v_accvgpr_read_b32 v236, a128\n"
      "v_accvgpr_read_b32 v237, a129\n"
      "v_accvgpr_read_b32 v238, a130\n"
      "ds_write_b128 v227, v[0:3] offset:16384\n"
      "v_accvgpr_read_b32 v239, a131\n"
      "v_accvgpr_read_b32 v240, a132\n"
      "v_accvgpr_read_b32 v241, a133\n"
      "v_accvgpr_read_b32 v242, a134\n"
      "v_accvgpr_read_b32 v243, a135\n"
      "s_add_u32 s71, s76, 0\n"
      "s_nop 0\n"
      "buffer_load_dwordx4 a[64:67], v230, s[60:63], s71 offen offset:0 sc1\n"
      "v_pk_mul_f32 v[236:237], v[236:237], v[252:253] op_sel_hi:[1,0]\n"
      "v_pk_mul_f32 v[238:239], v[238:239], v[252:253] op_sel_hi:[1,0]\n"
      "v_pk_mul_f32 v[240:241], v[240:241], v[252:253] op_sel_hi:[1,0]\n"
      "v_pk_mul_f32 v[242:243], v[242:243], v[252:253] op_sel_hi:[1,0]\n"
      "v_cvt_pk_bf16_f32 v64, v236, v237\n"
      "ds_write_b128 v227, v[4:7] offset:17408\n"
      "v_cvt_pk_bf16_f32 v65, v238, v239\n"
      "v_cvt_pk_bf16_f32 v66, v240, v241\n"
      "v_cvt_pk_bf16_f32 v67, v242, v243\n"
      "v_accvgpr_read_b32 v244, a136\n"
      "v_accvgpr_read_b32 v245, a137\n"
      "buffer_load_dwordx4 a[68:71], v230, s[60:63], s71 offen offset:1024 sc1\n"
      "v_accvgpr_read_b32 v246, a138\n"
      "v_accvgpr_read_b32 v247, a139\n"
      "v_accvgpr_read_b32 v248, a140\n"
      "v_accvgpr_read_b32 v249, a141\n"
      "v_accvgpr_read_b32 v250, a142\n"
      "ds_write_b128 v227, v[8:11] offset:18432\n"
      "v_accvgpr_read_b32 v251, a143\n"
      "v_pk_mul_f32 v[244:245], v[244:245], v[252:253] op_sel_hi:[1,0]\n"
      "v_pk_mul_f32 v[246:247], v[246:247], v[252:253] op_sel_hi:[1,0]\n"
      "v_pk_mul_f32 v[248:249], v[248:249], v[252:253] op_sel_hi:[1,0]\n"
      "v_pk_mul_f32 v[250:251], v[250:251], v[252:253] op_sel_hi:[1,0]\n"
      "s_add_u32 s71, s77, 0\n"
      "s_nop 0\n"
      "buffer_load_dwordx4 a[72:75], v230, s[60:63], s71 offen offset:0 sc1\n"
      "v_cvt_pk_bf16_f32 v68, v244, v245\n"
      "v_cvt_pk_bf16_f32 v69, v246, v247\n"
      "v_cvt_pk_bf16_f32 v70, v248, v249\n"
      "v_cvt_pk_bf16_f32 v71, v250, v251\n"
      "ds_write_b128 v227, v[12:15] offset:19456\n"
      "v_accvgpr_read_b32 v236, a144\n"
      "v_accvgpr_read_b32 v237, a145\n"
      "v_accvgpr_read_b32 v238, a146\n"
      "v_accvgpr_read_b32 v239, a147\n"
      "v_accvgpr_read_b32 v240, a148\n"
      "buffer_load_dwordx4 a[76:79], v230, s[60:63], s71 offen offset:1024 sc1\n"
      "v_accvgpr_read_b32 v241, a149\n"
      "v_accvgpr_read_b32 v242, a150\n"
      "v_accvgpr_read_b32 v243, a151\n"
      "v_pk_mul_f32 v[236:237], v[236:237], v[252:253] op_sel_hi:[1,0]\n"
      "v_pk_mul_f32 v[238:239], v[238:239], v[252:253] op_sel_hi:[1,0]\n"
      "ds_write_b128 v227, v[16:19] offset:20480\n"
      "v_pk_mul_f32 v[240:241], v[240:241], v[252:253] op_sel_hi:[1,0]\n"
      "v_pk_mul_f32 v[242:243], v[242:243], v[252:253] op_sel_hi:[1,0]\n"
      "v_cvt_pk_bf16_f32 v72, v236, v237\n"
      "v_cvt_pk_bf16_f32 v73, v238, v239\n"
      "v_cvt_pk_bf16_f32 v74, v240, v241\n"
      "s_add_u32 s71, s78, 0\n"
      "s_nop 0\n"
      "buffer_load_dwordx4 a[80:83], v230, s[60:63], s71 offen offset:0 sc1\n"
      "v_cvt_pk_bf16_f32 v75, v242, v243\n"
      "v_accvgpr_read_b32 v244, a152\n"
      "v_accvgpr_read_b32 v245, a153\n"
      "v_accvgpr_read_b32 v246, a154\n"
      "v_accvgpr_read_b32 v247, a155\n"
      "ds_write_b128 v227, v[20:23] offset:21504\n"
      "v_accvgpr_read_b32 v248, a156\n"
      "v_accvgpr_read_b32 v249, a157\n"
      "v_accvgpr_read_b32 v250, a158\n"
      "v_accvgpr_read_b32 v251, a159\n"
      "v_pk_mul_f32 v[244:245], v[244:245], v[252:253] op_sel_hi:[1,0]\n"
      "ds_write_b128 v227, v[24:27] offset:22528\n"
      "v_pk_mul_f32 v[246:247], v[246:247], v[252:253] op_sel_hi:[1,0]\n"
      "v_pk_mul_f32 v[248:249], v[248:249], v[252:253] op_sel_hi:[1,0]\n"
      "v_pk_mul_f32 v[250:251], v[250:251], v[252:253] op_sel_hi:[1,0]\n"
      "v_cvt_pk_bf16_f32 v76, v244, v245\n"
      "v_cvt_pk_bf16_f32 v77, v246, v247\n"
      "ds_write_b128 v227, v[28:31] offset:23552\n"
      "v_cvt_pk_bf16_f32 v78, v248, v249\n"
      "v_cvt_pk_bf16_f32 v79, v250, v251\n"
      "s_waitcnt vmcnt(27)\n"
      "s_nop 0\n"
      "ds_read_b64_tr_b16 a[128:129], v232 offset:0\n"
      "v_mfma_f32_16x16x32_bf16 v[112:115], a[192:195], v[168:171], v[112:115]\n"
      "ds_read_b64_tr_b16 a[130:131], v232 offset:4096\n"
      "buffer_load_dwordx4 a[84:87], v230, s[60:63], s71 offen offset:1024 sc1\n"
      "ds_read_b64_tr_b16 a[132:133], v232 offset:8\n"
      "v_mfma_f32_16x16x32_bf16 v[116:119], a[196:199], v[168:171], v[116:119]\n"
      "ds_read_b64_tr_b16 a[134:135], v232 offset:4104\n"
      "ds_read_b64_tr_b16 a[136:137], v233 offset:0\n"
      "v_mfma_f32_16x16x32_bf16 v[120:123], a[200:203], v[168:171], v[120:123]\n"
      "ds_read_b64_tr_b16 a[138:139], v233 offset:4096\n"
      "s_add_u32 s71, s76, 32768\n"
      "ds_read_b64_tr_b16 a[140:141], v233 offset:8\n"
      "v_mfma_f32_16x16x32_bf16 v[124:127], a[204:207], v[168:171], v[124:127]\n"
      "buffer_load_dwordx4 a[88:91], v230, s[60:63], s71 offen offset:0 sc1\n"
      "ds_read_b64_tr_b16 a[142:143], v233 offset:4104\n"
      "ds_read_b64_tr_b16 a[144:145], v232 offset:1024\n"
      "v_mfma_f32_16x16x32_bf16 v[196:199], a[208:211], v[176:179], v[196:199]\n"
      "ds_read_b64_tr_b16 a[146:147], v232 offset:5120\n"
      "ds_read_b64_tr_b16 a[148:149], v232 offset:1032\n"
      "v_mfma_f32_16x16x32_bf16 v[200:203], a[212:215], v[176:179], v[200:203]\n"
      "ds_read_b64_tr_b16 a[150:151], v232 offset:5128\n"
      "buffer_load_dwordx4 a[92:95], v230, s[60:63], s71 offen offset:1024 sc1\n"
      "ds_read_b64_tr_b16 a[152:153], v233 offset:1024\n"
      "v_mfma_f32_16x16x32_bf16 v[204:207], a[216:219], v[176:179], v[204:207]\n"
      "ds_read_b64_tr_b16 a[154:155], v233 offset:5120\n"
      "ds_read_b64_tr_b16 a[156:157], v233 offset:1032\n"
      "v_mfma_f32_16x16x32_bf16 v[208:211], a[220:223], v[176:179], v[208:211]\n"
      "ds_read_b64_tr_b16 a[158:159], v233 offset:5128\n"
      "s_waitcnt lgkmcnt(0)\n"
      "s_barrier\n"
      "v_mov_b32 v223, v221\n"
      "s_nop 1\n"
      "v_permlane16_swap_b32 v221, v223\n"
      "v_add_f32 v221, v221, v223\n"
      "v_mov_b32 v223, v221\n"
      "s_nop 1\n"
      "v_permlane32_swap_b32 v221, v223\n"
      "v_add_f32 v221, v221, v223\n"
      "v_subrev_u32 v223, 0x21800000, v221\n"
      "v_cmp_le_u32 vcc, 0x3c000000, v223\n"
      "s_nop 0\n"
      "s_cbranch_vccnz SLOW5_%=\n"
      "RET5_%=:\n"
      "v_rcp_f32 v252, v221\n"
      "s_nop 0\n"
      "v_pk_mul_f32 v[180:181], v[180:181], v[252:253] op_sel_hi:[1,0]\n"
      "v_pk_mul_f32 v[182:183], v[182:183], v[252:253] op_sel_hi:[1,0]\n"
      "v_pk_mul_f32 v[184:185], v[184:185], v[252:253] op_sel_hi:[1,0]\n"
      "v_pk_mul_f32 v[186:187], v[186:187], v[252:253] op_sel_hi:[1,0]\n"
      "s_add_u32 s71, s77, 32768\n"
      "buffer_load_dwordx4 v[0:3], v230, s[60:63], s71 offen offset:0 sc1\n"
      "v_cvt_pk_bf16_f32 v80, v180, v181\n"
      "v_cvt_pk_bf16_f32 v81, v182, v183\n"
      "v_cvt_pk_bf16_f32 v82, v184, v185\n"
      "v_cvt_pk_bf16_f32 v83, v186, v187\n"
      "v_pk_mul_f32 v[188:189], v[188:189], v[252:253] op_sel_hi:[1,0]\n"
      "v_pk_mul_f32 v[190:191], v[190:191], v[252:253] op_sel_hi:[1,0]\n"
      "buffer_load_dwordx4 v[4:7], v230, s[60:63], s71 offen offset:1024 sc1\n"
      "v_pk_mul_f32 v[192:193], v[192:193], v[252:253] op_sel_hi:[1,0]\n"
      "v_pk_mul_f32 v[194:195], v[194:195], v[252:253] op_sel_hi:[1,0]\n"
      "v_cvt_pk_bf16_f32 v84, v188, v189\n"
      "v_cvt_pk_bf16_f32 v85, v190, v191\n"
      "v_cvt_pk_bf16_f32 v86, v192, v193\n"
      "v_cvt_pk_bf16_f32 v87, v194, v195\n"
      "s_add_u32 s71, s78, 32768\n"
      "buffer_load_dwordx4 v[8:11], v230, s[60:63], s71 offen offset:0 sc1\n"
      "v_pk_mul_f32 v[196:197], v[196:197], v[252:253] op_sel_hi:[1,0]\n"
      "v_pk_mul_f32 v[198:199], v[198:199], v[252:253] op_sel_hi:[1,0]\n"
      "v_pk_mul_f32 v[200:201], v[200:201], v[252:253] op_sel_hi:[1,0]\n"
      "v_pk_mul_f32 v[202:203], v[202:203], v[252:253] op_sel_hi:[1,0]\n"
      "v_cvt_pk_bf16_f32 v88, v196, v197\n"
      "v_cvt_pk_bf16_f32 v89, v198, v199\n"
      "v_cvt_pk_bf16_f32 v90, v200, v201\n"
      "buffer_load_dwordx4 v[12:15], v230, s[60:63], s71 offen offset:1024 sc1\n"
      "v_cvt_pk_bf16_f32 v91, v202, v203\n"
      "v_pk_mul_f32 v[204:205], v[204:205], v[252:253] op_sel_hi:[1,0]\n"
      "v_pk_mul_f32 v[206:207], v[206:207], v[252:253] op_sel_hi:[1,0]\n"
      "v_pk_mul_f32 v[208:209], v[208:209], v[252:253] op_sel_hi:[1,0]\n"
      "v_pk_mul_f32 v[210:211], v[210:211], v[252:253] op_sel_hi:[1,0]\n"
      "v_cvt_pk_bf16_f32 v92, v204, v205\n"
      "s_add_u32 s70, s87, 196608\n"
      "buffer_store_dwordx4 v[48:51], v230, s[60:63], s70 offen offset:0 sc1\n"
      "v_cvt_pk_bf16_f32 v93, v206, v207\n"
      "v_cvt_pk_bf16_f32 v94, v208, v209\n"
      "v_cvt_pk_bf16_f32 v95, v210, v211\n"
      "ds_read_b128 v[16:19], v216 offset:0\n"
      "ds_read_b128 v[20:23], v216 offset:49152\n"
      "buffer_load_dword v254, v226, s[60:63], s92 offen sc1\n"
      "buffer_store_dwordx4 v[52:55], v230, s[60:63], s70 offen offset:2048 sc1\n"
      "v_mfma_f32_16x16x32_bf16 v[128:131], a[208:211], v[168:171], v[128:131]\n"
      "v_mfma_f32_16x16x32_bf16 v[132:135], a[212:215], v[168:171], v[132:135]\n"
      "s_add_u32 s70, s87, 200704\n"
      "buffer_store_dwordx4 v[56:59], v230, s[60:63], s70 offen offset:0 sc1\n"
      "v_mfma_f32_16x16x32_bf16 v[136:139], a[216:219], v[168:171], v[136:139]\n"
      "v_mfma_f32_16x16x32_bf16 v[140:143], a[220:223], v[168:171], v[140:143]\n"
      "s_nop 7\n"
      "v_mov_b32 v223, v222\n"
      "s_nop 1\n"
      "v_permlane16_swap_b32 v222, v223\n"
      "v_add_f32 v222, v222, v223\n"
      "v_mov_b32 v223, v222\n"
      "s_nop 1\n"
      "v_permlane32_swap_b32 v222, v223\n"
      "v_add_f32 v222, v222, v223\n"
      "v_subrev_u32 v223, 0x21800000, v222\n"
      "v_cmp_le_u32 vcc, 0x3c000000, v223\n"
      "s_nop 0\n"
      "s_cbranch_vccnz SLOW6_%=\n"
      "RET6_%=:\n"
      "v_rcp_f32_e64 v252, v222\n"
      "s_nop 0\n"
      "buffer_store_dwordx4 v[60:63], v230, s[60:63], s70 offen offset:2048 sc1\n"
      "s_add_u32 s70, s87, 262144\n"
      "buffer_store_dwordx4 v[64:67], v230, s[60:63], s70 offen offset:0 sc1\n"
      "buffer_store_dwordx4 v[68:71], v230, s[60:63], s70 offen offset:2048 sc1\n"
      "s_add_u32 s70, s87, 266240\n"
      "buffer_store_dwordx4 v[72:75], v230, s[60:63], s70 offen offset:0 sc1\n"
      "buffer_store_dwordx4 v[76:79], v230, s[60:63], s70 offen offset:2048 sc1\n"
      "v_lshrrev_b32 v220, 2, v230\n"
      "s_mov_b32 m0, s68\n"
      "v_add_u32_e64 v220, s68, v220\n"
      "v_xor_b32 v218, 0x2000, v220\n"
      "v_xor_b32 v219, 0xc000, v220\n"
      "v_xor_b32 v220, 0xe000, v220\n"
      "s_waitcnt vmcnt(7)\n"
      "s_add_u32 s97, s82, 3\n"
      "v_subrev_u32 v254, s97, v254\n"
      "v_cmp_lt_u32 vcc, 4, v254\n"
      "s_nop 0\n"
      "s_cbranch_vccnz PB_%=\n"
      "PBR_%=:\n"
      "s_waitcnt lgkmcnt(0)\n"
      "v_mfma_f32_16x16x32_bf16 v[144:147], a[160:163], v[16:19], 0\n"
      "s_waitcnt vmcnt(35)\n"
      "v_mul_f32 v112, v252, v112\n"
      "v_mul_f32 v113, v252, v113\n"
      "v_mfma_f32_16x16x32_bf16 v[148:151], a[164:167], v[16:19], 0\n"
      "ds_read_b64_tr_b16 v[180:181], v224 offset:32768\n"
      "s_add_u32 s71, s76, 65536\n"
      "v_mfma_f32_16x16x32_bf16 v[152:155], a[168:171], v[16:19], 0\n"
      "ds_read_b64_tr_b16 v[182:183], v224 offset:36864\n"
      "buffer_load_dwordx4 v[24:27], v230, s[60:63], s71 offen offset:0 sc1\n"
      "v_mfma_f32_16x16x32_bf16 v[156:159], a[172:175], v[16:19], 0\n"
      "ds_read_b64_tr_b16 v[184:185], v224 offset:32776\n"
      "v_cvt_pk_bf16_f32 v96, v112, v113\n"
      "v_mfma_f32_16x16x32_bf16 v[160:163], a[176:179], v[16:19], 0\n"
      "ds_read_b64_tr_b16 v[186:187], v224 offset:36872\n"
      "v_mul_f32_e64 v114, v252, v114\n"
      "v_mfma_f32_16x16x32_bf16 v[164:167], a[180:183], v[16:19], 0\n"
      "ds_read_b64_tr_b16 v[188:189], v225 offset:32768\n"
      "v_mul_f32_e64 v115, v252, v115\n"
      "v_mfma_f32_16x16x32_bf16 v[168:171], a[184:187], v[16:19], 0\n"
      "ds_read_b64_tr_b16 v[190:191], v225 offset:36864\n"
      "v_cvt_pk_bf16_f32 v97, v114, v115\n"
      "v_mfma_f32_16x16x32_bf16 v[172:175], a[188:191], v[16:19], 0\n"
      "ds_read_b64_tr_b16 v[192:193], v225 offset:32776\n"
      "v_mul_f32_e64 v116, v252, v116\n"
      "v_mfma_f32_16x16x32_bf16 v[144:147], a[0:3], v[20:23], v[144:147]\n"
      "ds_read_b64_tr_b16 v[194:195], v225 offset:36872\n"
      "v_mul_f32_e64 v117, v252, v117\n"
      "v_mfma_f32_16x16x32_bf16 v[148:151], a[4:7], v[20:23], v[148:151]\n"
      "ds_read_b64_tr_b16 v[196:197], v224 offset:33792\n"
      "v_cvt_pk_bf16_f32 v98, v116, v117\n"
      "v_mfma_f32_16x16x32_bf16 v[152:155], a[8:11], v[20:23], v[152:155]\n"
      "ds_read_b64_tr_b16 v[198:199], v224 offset:37888\n"
      "v_mul_f32_e64 v118, v252, v118\n"
      "v_mfma_f32_16x16x32_bf16 v[156:159], a[12:15], v[20:23], v[156:159]\n"
      "ds_read_b64_tr_b16 v[200:201], v224 offset:33800\n"
      "v_mul_f32_e64 v119, v252, v119\n"
      "v_mfma_f32_16x16x32_bf16 v[160:163], a[16:19], v[20:23], v[160:163]\n"
      "ds_read_b64_tr_b16 v[202:203], v224 offset:37896\n"
      "buffer_load_dwordx4 v[28:31], v230, s[60:63], s71 offen offset:1024 sc1\n"
      "v_mfma_f32_16x16x32_bf16 v[164:167], a[20:23], v[20:23], v[164:167]\n"
      "ds_read_b64_tr_b16 v[204:205], v225 offset:33792\n"
      "v_cvt_pk_bf16_f32 v99, v118, v119\n"
      "v_mfma_f32_16x16x32_bf16 v[168:171], a[24:27], v[20:23], v[168:171]\n"
      "ds_read_b64_tr_b16 v[206:207], v225 offset:37888\n"
      "v_mul_f32 v120, v252, v120\n"
      "v_mfma_f32_16x16x32_bf16 v[172:175], a[28:31], v[20:23], v[172:175]\n"
      "ds_read_b64_tr_b16 v[208:209], v225 offset:33800\n"
      "s_waitcnt vmcnt(22)\n"
      "v_mfma_f32_16x16x32_bf16 v[144:147], a[32:35], a[64:67], v[144:147]\n"
      "ds_read_b64_tr_b16 v[210:211], v225 offset:37896\n"
      "v_mul_f32_e64 v121, v252, v121\n"
      "v_mfma_f32_16x16x32_bf16 v[148:151], a[36:39], a[64:67], v[148:151]\n"
      "v_cvt_pk_bf16_f32 v100, v120, v121\n"
      "v_mul_f32_e64 v122, v252, v122\n"
      "v_mfma_f32_16x16x32_bf16 v[152:155], a[40:43], a[64:67], v[152:155]\n"
      "ds_write_addtid_b32 v40 offset:38912\n"
      "v_mul_f32_e64 v123, v252, v123\n"
      "v_mfma_f32_16x16x32_bf16 v[156:159], a[44:47], a[64:67], v[156:159]\n"
      "ds_write_addtid_b32 v41 offset:39168\n"
      "v_cvt_pk_bf16_f32 v101, v122, v123\n"
      "v_mfma_f32_16x16x32_bf16 v[160:163], a[48:51], a[64:67], v[160:163]\n"
      "ds_write_addtid_b32 v42 offset:39424\n"
      "v_mul_f32_e64 v124, v252, v124\n"
      "v_mfma_f32_16x16x32_bf16 v[164:167], a[52:55], a[64:67], v[164:167]\n"
      "ds_write_addtid_b32 v43 offset:39680\n"
      "s_add_u32 s71, s77, 65536\n"
      "v_mfma_f32_16x16x32_bf16 v[168:171], a[56:59], a[64:67], v[168:171]\n"
      "buffer_load_dwordx4 v[16:19], v230, s[60:63], s71 offen offset:0 sc1\n"
      "v_mul_f32 v125, v252, v125\n"
      "v_mfma_f32_16x16x32_bf16 v[172:175], a[60:63], a[64:67], v[172:175]\n"
      "ds_write_addtid_b32 v44 offset:39936\n"
      "v_cvt_pk_bf16_f32 v102, v124, v125\n"
      "s_waitcnt vmcnt(22)\n"
      "v_mfma_f32_16x16x32_bf16 v[144:147], a[224:227], a[68:71], v[144:147]\n"
      "ds_write_addtid_b32 v45 offset:40192\n"
      "v_mul_f32_e64 v126, v252, v126\n"
      "v_mfma_f32_16x16x32_bf16 v[148:151], a[228:231], a[68:71], v[148:151]\n"
      "ds_write_addtid_b32 v46 offset:40448\n"
      "v_mul_f32_e64 v127, v252, v127\n"
      "v_mfma_f32_16x16x32_bf16 v[152:155], a[232:235], a[68:71], v[152:155]\n"
      "ds_write_addtid_b32 v47 offset:40704\n"
      "v_cvt_pk_bf16_f32 v103, v126, v127\n"
      "v_mfma_f32_16x16x32_bf16 v[156:159], a[236:239], a[68:71], v[156:159]\n"
      "v_mul_f32 v128, v252, v128\n"
      "v_mul_f32 v129, v252, v129\n"
      "v_mfma_f32_16x16x32_bf16 v[160:163], a[240:243], a[68:71], v[160:163]\n"
      "s_waitcnt vmcnt(24)\n"
      "v_cvt_pk_bf16_f32 v104, v128, v129\n"
      "v_mul_f32 v130, v252, v130\n"
      "v_mfma_f32_16x16x32_bf16 v[164:167], a[244:247], a[68:71], v[164:167]\n"
      "ds_read_b64_tr_b16 a[192:193], v224 offset:0\n"
      "v_mul_f32 v131, v252, v131\n"
      "v_mfma_f32_16x16x32_bf16 v[168:171], a[248:251], a[68:71], v[168:171]\n"
      "ds_read_b64_tr_b16 a[194:195], v224 offset:4096\n"
      "v_cvt_pk_bf16_f32 v105, v130, v131\n"
      "v_mfma_f32_16x16x32_bf16 v[172:175], a[252:255], a[68:71], v[172:175]\n"
      "ds_read_b64_tr_b16 a[196:197], v224 offset:8\n"
      "s_waitcnt vmcnt(21)\n"
      "v_mfma_f32_16x16x32_bf16 v[144:147], a[96:99], a[72:75], v[144:147]\n"
      "ds_read_b64_tr_b16 a[198:199], v224 offset:4104\n"
      "buffer_load_dwordx4 a[64:67], v230, s[60:63], s71 offen offset:1024 sc1\n"
      "v_mfma_f32_16x16x32_bf16 v[148:151], a[100:103], a[72:75], v[148:151]\n"
      "ds_read_b64_tr_b16 a[200:201], v225 offset:0\n"
      "v_mul_f32_e64 v132, v252, v132\n"
      "v_mfma_f32_16x16x32_bf16 v[152:155], a[104:107], a[72:75], v[152:155]\n"
      "ds_read_b64_tr_b16 a[202:203], v225 offset:4096\n"
      "v_mul_f32_e64 v133, v252, v133\n"
      "v_mfma_f32_16x16x32_bf16 v[156:159], a[108:111], a[72:75], v[156:159]\n"
      "ds_read_b64_tr_b16 a[204:205], v225 offset:8\n"
      "v_cvt_pk_bf16_f32 v106, v132, v133\n"
      "v_mfma_f32_16x16x32_bf16 v[160:163], a[112:115], a[72:75], v[160:163]\n"
      "ds_read_b64_tr_b16 a[206:207], v225 offset:4104\n"
      "v_mul_f32 v134, v252, v134\n"
      "v_mfma_f32_16x16x32_bf16 v[164:167], a[116:119], a[72:75], v[164:167]\n"
      "ds_read_b64_tr_b16 a[208:209], v224 offset:1024\n"
      "v_mul_f32 v135, v252, v135\n"
      "v_mfma_f32_16x16x32_bf16 v[168:171], a[120:123], a[72:75], v[168:171]\n"
      "ds_read_b64_tr_b16 a[210:211], v224 offset:5120\n"
      "v_cvt_pk_bf16_f32 v107, v134, v135\n"
      "v_mfma_f32_16x16x32_bf16 v[172:175], a[124:127], a[72:75], v[172:175]\n"
      "ds_read_b64_tr_b16 a[212:213], v224 offset:1032\n"
      "v_mul_f32 v136, v252, v136\n"
      "s_waitcnt vmcnt(21)\n"
      "v_mfma_f32_16x16x32_bf16 v[144:147], a[128:131], a[76:79], v[144:147]\n"
      "ds_read_b64_tr_b16 a[214:215], v224 offset:5128\n"
      "v_mul_f32_e64 v137, v252, v137\n"
      "v_mfma_f32_16x16x32_bf16 v[148:151], a[132:135], a[76:79], v[148:151]\n"
      "ds_read_b64_tr_b16 a[216:217], v225 offset:1024\n"
      "s_add_u32 s71, s78, 65536\n"
      "v_mfma_f32_16x16x32_bf16 v[152:155], a[136:139], a[76:79], v[152:155]\n"
      "ds_read_b64_tr_b16 a[218:219], v225 offset:5120\n"
      "buffer_load_dwordx4 a[68:71], v230, s[60:63], s71 offen offset:0 sc1\n"
      "v_mfma_f32_16x16x32_bf16 v[156:159], a[140:143], a[76:79], v[156:159]\n"
      "ds_read_b64_tr_b16 a[220:221], v225 offset:1032\n"
      "v_cvt_pk_bf16_f32 v108, v136, v137\n"
      "v_mfma_f32_16x16x32_bf16 v[160:163], a[144:147], a[76:79], v[160:163]\n"
      "ds_read_b64_tr_b16 a[222:223], v225 offset:5128\n"
      "v_mul_f32_e64 v138, v252, v138\n"
      "v_mfma_f32_16x16x32_bf16 v[164:167], a[148:151], a[76:79], v[164:167]\n"
      "v_mul_f32 v139, v252, v139\n"
      "v_cvt_pk_bf16_f32 v109, v138, v139\n"
      "v_mfma_f32_16x16x32_bf16 v[168:171], a[152:155], a[76:79], v[168:171]\n"
      "s_lshr_b32 s65, %[wid], 1\n"
      "v_mul_f32 v140, v252, v140\n"
      "v_mul_f32 v141, v252, v141\n"
      "v_mfma_f32_16x16x32_bf16 v[172:175], a[156:159], a[76:79], v[172:175]\n"
      "s_mul_i32 s65, s65, 30720\n"
      "v_cvt_pk_bf16_f32 v110, v140, v141\n"
      "s_waitcnt vmcnt(21) lgkmcnt(15)\n"
      "v_mfma_f32_16x16x32_bf16 v[144:147], v[180:183], a[80:83], v[144:147]\n"
      "s_sub_u32 s65, s65, 10240\n"
      "v_mul_f32 v142, v252, v142\n"
      "v_mul_f32_e64 v143, v252, v143\n"
      "v_mfma_f32_16x16x32_bf16 v[148:151], v[184:187], a[80:83], v[148:151]\n"
      "v_cvt_pk_bf16_f32 v111, v142, v143\n"
      "v_mfma_f32_16x16x32_bf16 v[152:155], v[188:191], a[80:83], v[152:155]\n"
      "v_lshrrev_b32_e64 v227, 2, v230\n"
      "v_sub_u32_e64 v228, v216, v230\n"
      "v_mfma_f32_16x16x32_bf16 v[156:159], v[192:195], a[80:83], v[156:159]\n"
      "ds_read_b128 a[72:75], v217 offset:0\n"
      "v_add_u32_e64 v228, v228, v227\n"
      "v_mfma_f32_16x16x32_bf16 v[160:163], v[196:199], a[80:83], v[160:163]\n"
      "buffer_load_dwordx4 a[76:79], v230, s[60:63], s71 offen offset:1024 sc1\n"
      "v_xor_b32 v227, 0x2000, v228\n"
      "v_mfma_f32_16x16x32_bf16 v[164:167], v[200:203], a[80:83], v[164:167]\n"
      "ds_read_b128 v[20:23], v217 offset:49152\n"
      "v_add_u32_e64 v221, s65, v228\n"
      "v_mfma_f32_16x16x32_bf16 v[168:171], v[204:207], a[80:83], v[168:171]\n"
      "v_xor_b32 v222, 0x2000, v221\n"
      "v_and_b32 v229, 0xf0, v230\n"
      "v_mfma_f32_16x16x32_bf16 v[172:175], v[208:211], a[80:83], v[172:175]\n"
      "v_lshrrev_b32_e64 v236, 4, v230\n"
      "v_and_b32_e64 v236, 48, v236\n"
      "s_waitcnt vmcnt(21) lgkmcnt(2)\n"
      "v_mfma_f32_16x16x32_bf16 v[144:147], a[192:195], a[84:87], v[144:147]\n"
      "v_lshl_add_u32 v229, v229, 6, v236\n"
      "v_mfma_f32_16x16x32_bf16 v[148:151], a[196:199], a[84:87], v[148:151]\n"
      "v_mfma_f32_16x16x32_bf16 v[152:155], a[200:203], a[84:87], v[152:155]\n"
      "v_mfma_f32_16x16x32_bf16 v[156:159], a[204:207], a[84:87], v[156:159]\n"
      "v_mfma_f32_16x16x32_bf16 v[160:163], a[208:211], a[84:87], v[160:163]\n"
      "v_mfma_f32_16x16x32_bf16 v[164:167], a[212:215], a[84:87], v[164:167]\n"
      "s_add_u32 s70, s87, 327680\n"
      "v_mfma_f32_16x16x32_bf16 v[168:171], a[216:219], a[84:87], v[168:171]\n"
      "buffer_store_dwordx4 v[80:83], v230, s[60:63], s70 offen offset:0 sc1\n"
      "v_mfma_f32_16x16x32_bf16 v[172:175], a[220:223], a[84:87], v[172:175]\n"
      "s_waitcnt lgkmcnt(0)\n"
      "v_mfma_f32_16x16x32_bf16 v[112:115], a[160:163], a[72:75], 0\n"
      "ds_write_addtid_b32 v32 offset:6144\n"
      "v_mfma_f32_16x16x32_bf16 v[116:119], a[164:167], a[72:75], 0\n"
      "ds_write_addtid_b32 v33 offset:6400\n"
      "v_mfma_f32_16x16x32_bf16 v[120:123], a[168:171], a[72:75], 0\n"
      "buffer_store_dwordx4 v[84:87], v230, s[60:63], s70 offen offset:2048 sc1\n"
      "v_mfma_f32_16x16x32_bf16 v[124:127], a[172:175], a[72:75], 0\n"
      "ds_write_addtid_b32 v152 offset:32768\n"
      "v_mfma_f32_16x16x32_bf16 v[128:131], a[176:179], a[72:75], 0\n"
      "ds_write_addtid_b32 v153 offset:33024\n"
      "v_mfma_f32_16x16x32_bf16 v[132:135], a[180:183], a[72:75], 0\n"
      "ds_write_addtid_b32 v154 offset:33280\n"
      "v_mfma_f32_16x16x32_bf16 v[136:139], a[184:187], a[72:75], 0\n"
      "ds_write_addtid_b32 v155 offset:33536\n"
      "v_mfma_f32_16x16x32_bf16 v[140:143], a[188:191], a[72:75], 0\n"
      "ds_write_addtid_b32 v156 offset:33792\n"
      "v_mfma_f32_16x16x32_bf16 v[112:115], a[0:3], v[20:23], v[112:115]\n"
      "s_add_u32 s70, s87, 331776\n"
      "ds_write_addtid_b32 v157 offset:34048\n"
      "v_mfma_f32_16x16x32_bf16 v[116:119], a[4:7], v[20:23], v[116:119]\n"
      "buffer_store_dwordx4 v[88:91], v230, s[60:63], s70 offen offset:0 sc1\n"
      "v_mfma_f32_16x16x32_bf16 v[120:123], a[8:11], v[20:23], v[120:123]\n"
      "ds_write_addtid_b32 v158 offset:34304\n"
      "v_mfma_f32_16x16x32_bf16 v[124:127], a[12:15], v[20:23], v[124:127]\n"
      "ds_write_addtid_b32 v159 offset:34560\n"
      "v_mfma_f32_16x16x32_bf16 v[128:131], a[16:19], v[20:23], v[128:131]\n"
      "ds_write_addtid_b32 v160 offset:34816\n"
      "v_mfma_f32_16x16x32_bf16 v[132:135], a[20:23], v[20:23], v[132:135]\n"
      "ds_write_addtid_b32 v161 offset:35072\n"
      "v_mfma_f32_16x16x32_bf16 v[136:139], a[24:27], v[20:23], v[136:139]\n"
      "ds_write_addtid_b32 v162 offset:35328\n"
      "v_mfma_f32_16x16x32_bf16 v[140:143], a[28:31], v[20:23], v[140:143]\n"
      "ds_write_addtid_b32 v163 offset:35584\n"
      "s_waitcnt vmcnt(22)\n"
      "v_mfma_f32_16x16x32_bf16 v[112:115], a[32:35], a[88:91], v[112:115]\n"
      "ds_write_addtid_b32 v164 offset:35840\n"
      "s_add_u32 s71, s76, 98304\n"
      "v_mfma_f32_16x16x32_bf16 v[116:119], a[36:39], a[88:91], v[116:119]\n"
      "buffer_load_dwordx4 a[80:83], v230, s[60:63], s71 offen offset:0 sc1\n"
      "v_mfma_f32_16x16x32_bf16 v[120:123], a[40:43], a[88:91], v[120:123]\n"
      "ds_write_addtid_b32 v165 offset:36096\n"
      "v_mfma_f32_16x16x32_bf16 v[124:127], a[44:47], a[88:91], v[124:127]\n"
      "ds_write_addtid_b32 v166 offset:36352\n"
      "v_mfma_f32_16x16x32_bf16 v[128:131], a[48:51], a[88:91], v[128:131]\n"
      "ds_write_addtid_b32 v167 offset:36608\n"
      "v_mfma_f32_16x16x32_bf16 v[132:135], a[52:55], a[88:91], v[132:135]\n"
      "ds_write_addtid_b32 v168 offset:36864\n"
      "v_mfma_f32_16x16x32_bf16 v[136:139], a[56:59], a[88:91], v[136:139]\n"
      "ds_write_addtid_b32 v169 offset:37120\n"
      "v_mfma_f32_16x16x32_bf16 v[140:143], a[60:63], a[88:91], v[140:143]\n"
      "s_add_u32 s72, s82, 5\n"
      "v_mov_b32 v223, s72\n"
      "s_waitcnt vmcnt(10)\n"
      "v_mfma_f32_16x16x32_bf16 v[112:115], a[224:227], a[92:95], v[112:115]\n"
      "buffer_store_dword v223, v231, s[60:63], s91 offen sc1\n"
      "v_mfma_f32_16x16x32_bf16 v[116:119], a[228:231], a[92:95], v[116:119]\n"
      "ds_write_addtid_b32 v170 offset:37376\n"
      "v_mfma_f32_16x16x32_bf16 v[120:123], a[232:235], a[92:95], v[120:123]\n"
      "ds_write_addtid_b32 v171 offset:37632\n"
      "v_mfma_f32_16x16x32_bf16 v[124:127], a[236:239], a[92:95], v[124:127]\n"
      "ds_write_addtid_b32 v172 offset:37888\n"
      "v_mfma_f32_16x16x32_bf16 v[128:131], a[240:243], a[92:95], v[128:131]\n"
      "ds_write_addtid_b32 v173 offset:38144\n"
      "v_mfma_f32_16x16x32_bf16 v[132:135], a[244:247], a[92:95], v[132:135]\n"
      "ds_write_addtid_b32 v174 offset:38400\n"
      "v_mfma_f32_16x16x32_bf16 v[136:139], a[248:251], a[92:95], v[136:139]\n"
      "ds_write_addtid_b32 v175 offset:38656\n"
      "v_mfma_f32_16x16x32_bf16 v[140:143], a[252:255], a[92:95], v[140:143]\n"
      "s_waitcnt vmcnt(22)\n"
      "v_mfma_f32_16x16x32_bf16 v[112:115], a[96:99], v[0:3], v[112:115]\n"
      "s_waitcnt lgkmcnt(0)\n"
      "buffer_load_dwordx4 a[84:87], v230, s[60:63], s71 offen offset:1024 sc1\n"
      "v_mfma_f32_16x16x32_bf16 v[116:119], a[100:103], v[0:3], v[116:119]\n"
      "s_barrier\n"
      "v_mfma_f32_16x16x32_bf16 v[120:123], a[104:107], v[0:3], v[120:123]\n"
      "ds_read2st64_b32 v[236:237], v218 offset0:128 offset1:129\n"
      "v_mfma_f32_16x16x32_bf16 v[124:127], a[108:111], v[0:3], v[124:127]\n"
      "ds_read2st64_b32 v[238:239], v218 offset0:130 offset1:131\n"
      "v_mfma_f32_16x16x32_bf16 v[128:131], a[112:115], v[0:3], v[128:131]\n"
      "ds_read2st64_b32 v[240:241], v218 offset0:132 offset1:133\n"
      "v_mfma_f32_16x16x32_bf16 v[132:135], a[116:119], v[0:3], v[132:135]\n"
      "ds_read2st64_b32 v[242:243], v218 offset0:134 offset1:135\n"
      "v_mfma_f32_16x16x32_bf16 v[136:139], a[120:123], v[0:3], v[136:139]\n"
      "ds_read2st64_b32 v[244:245], v219 offset0:136 offset1:137\n"
      "s_add_u32 s71, s77, 98304\n"
      "v_mfma_f32_16x16x32_bf16 v[140:143], a[124:127], v[0:3], v[140:143]\n"
      "buffer_load_dwordx4 a[72:75], v230, s[60:63], s71 offen offset:0 sc1\n"
      "v_mfma_f32_16x16x32_bf16 v[112:115], a[128:131], v[4:7], v[112:115]\n"
      "ds_read2st64_b32 v[246:247], v219 offset0:138 offset1:139\n"
      "v_mfma_f32_16x16x32_bf16 v[116:119], a[132:135], v[4:7], v[116:119]\n"
      "ds_read2st64_b32 v[248:249], v219 offset0:140 offset1:141\n"
      "s_waitcnt lgkmcnt(3)\n"
      "v_mfma_f32_16x16x32_bf16 v[120:123], a[136:139], v[4:7], v[120:123]\n"
      "ds_read2st64_b32 v[250:251], v219 offset0:142 offset1:143\n"
      "v_add_f32_e64 v236, v236, v144\n"
      "v_mfma_f32_16x16x32_bf16 v[124:127], a[140:143], v[4:7], v[124:127]\n"
      "ds_read2st64_b32 v[176:177], v220 offset0:144 offset1:145\n"
      "v_add_f32 v237, v237, v145\n"
      "v_mfma_f32_16x16x32_bf16 v[128:131], a[144:147], v[4:7], v[128:131]\n"
      "ds_read2st64_b32 v[178:179], v220 offset0:146 offset1:147\n"
      "v_add_f32 v238, v238, v146\n"
      "v_mfma_f32_16x16x32_bf16 v[132:135], a[148:151], v[4:7], v[132:135]\n"
      "ds_read2st64_b32 v[212:213], v220 offset0:148 offset1:149\n"
      "v_add_f32_e64 v239, v239, v147\n"
      "v_mfma_f32_16x16x32_bf16 v[136:139], a[152:155], v[4:7], v[136:139]\n"
      "ds_read2st64_b32 v[214:215], v220 offset0:150 offset1:151\n"
      "v_add_f32 v240, v240, v148\n"
      "v_mfma_f32_16x16x32_bf16 v[140:143], a[156:159], v[4:7], v[140:143]\n"
      "buffer_load_dwordx4 a[88:91], v230, s[60:63], s71 offen offset:1024 sc1\n"
      "s_waitcnt vmcnt(23)\n"
      "v_mfma_f32_16x16x32_bf16 v[112:115], v[180:183], v[8:11], v[112:115]\n"
      "ds_read_b128 a[92:95], v216 offset:4096\n"
      "v_add_f32_e64 v241, v241, v149\n"
      "v_mfma_f32_16x16x32_bf16 v[116:119], v[184:187], v[8:11], v[116:119]\n"
      "ds_read_b128 v[40:43], v216 offset:53248\n"
      "v_add_f32 v242, v242, v150\n"
      "v_mfma_f32_16x16x32_bf16 v[120:123], v[188:191], v[8:11], v[120:123]\n"
      "ds_write_addtid_b32 v34 offset:6656\n"
      "v_add_f32 v243, v243, v151\n"
      "s_waitcnt lgkmcnt(3)\n"
      "v_mfma_f32_16x16x32_bf16 v[124:127], v[192:195], v[8:11], v[124:127]\n"
      "ds_write_addtid_b32 v35 offset:6912\n"
      "v_add_f32 v236, v236, v244\n"
      "v_mfma_f32_16x16x32_bf16 v[128:131], v[196:199], v[8:11], v[128:131]\n"
      "ds_write_addtid_b32 v36 offset:7168\n"
      "v_add_f32_e64 v237, v237, v245\n"
      "v_mfma_f32_16x16x32_bf16 v[132:135], v[200:203], v[8:11], v[132:135]\n"
      "ds_write_addtid_b32 v37 offset:7424\n"
      "s_add_u32 s71, s78, 98304\n"
      "v_mfma_f32_16x16x32_bf16 v[136:139], v[204:207], v[8:11], v[136:139]\n"
      "buffer_load_dwordx4 v[44:47], v230, s[60:63], s71 offen offset:0 sc1\n"
      "v_add_f32_e64 v238, v238, v246\n"
      "v_mfma_f32_16x16x32_bf16 v[140:143], v[208:211], v[8:11], v[140:143]\n"
      "ds_write_addtid_b32 v38 offset:7680\n"
      "v_add_f32_e64 v239, v239, v247\n"
      "v_mfma_f32_16x16x32_bf16 v[112:115], a[192:195], v[12:15], v[112:115]\n"
      "ds_write_addtid_b32 v39 offset:7936\n"
      "v_add_f32 v240, v240, v248\n"
      "v_mfma_f32_16x16x32_bf16 v[116:119], a[196:199], v[12:15], v[116:119]\n"
      "v_add_f32 v241, v241, v249\n"
      "v_add_f32_e64 v242, v242, v250\n"
      "v_mfma_f32_16x16x32_bf16 v[120:123], a[200:203], v[12:15], v[120:123]\n"
      "s_cmp_lg_u32 s79, 0\n"
      "s_cbranch_scc1 DS1_%=\n"
      "DS1R_%=:\n"
      "v_add_f32 v243, v243, v251\n"
      "v_mfma_f32_16x16x32_bf16 v[124:127], a[204:207], v[12:15], v[124:127]\n"
      "v_add_f32 v236, v236, v176\n"
      "v_add_f32 v237, v237, v177\n"
      "v_mfma_f32_16x16x32_bf16 v[128:131], a[208:211], v[12:15], v[128:131]\n"
      "v_add_f32 v238, v238, v178\n"
      "v_add_f32 v239, v239, v179\n"
      "v_mfma_f32_16x16x32_bf16 v[132:135], a[212:215], v[12:15], v[132:135]\n"
      "v_add_f32 v240, v240, v212\n"
      "v_add_f32 v241, v241, v213\n"
      "v_mfma_f32_16x16x32_bf16 v[136:139], a[216:219], v[12:15], v[136:139]\n"
      "buffer_load_dwordx4 v[20:23], v230, s[60:63], s71 offen offset:1024 sc1\n"
      "v_add_f32 v242, v242, v214\n"
      "v_mfma_f32_16x16x32_bf16 v[140:143], a[220:223], v[12:15], v[140:143]\n"
      "v_add_f32 v243, v243, v215\n"
      "s_waitcnt lgkmcnt(6)\n"
      "v_mfma_f32_16x16x32_bf16 v[144:147], a[160:163], a[92:95], 0\n"
      "ds_write_addtid_b32 v48 offset:16384\n"
      "v_cvt_pk_bf16_f32 v0, v236, v237\n"
      "v_mfma_f32_16x16x32_bf16 v[148:151], a[164:167], a[92:95], 0\n"
      "ds_write_addtid_b32 v49 offset:16640\n"
      "v_cvt_pk_bf16_f32 v1, v238, v239\n"
      "v_mfma_f32_16x16x32_bf16 v[152:155], a[168:171], a[92:95], 0\n"
      "ds_write_addtid_b32 v120 offset:0\n"
      "v_cvt_pk_bf16_f32 v2, v240, v241\n"
      "v_mfma_f32_16x16x32_bf16 v[156:159], a[172:175], a[92:95], 0\n"
      "ds_write_addtid_b32 v121 offset:256\n"
      "v_cvt_pk_bf16_f32 v3, v242, v243\n"
      "v_mfma_f32_16x16x32_bf16 v[160:163], a[176:179], a[92:95], 0\n"
      "ds_write_addtid_b32 v122 offset:512\n"
      "v_mfma_f32_16x16x32_bf16 v[164:167], a[180:183], a[92:95], 0\n"
      "ds_write_addtid_b32 v123 offset:768\n"
      "v_mfma_f32_16x16x32_bf16 v[168:171], a[184:187], a[92:95], 0\n"
      "ds_write_addtid_b32 v124 offset:1024\n"
      "v_mfma_f32_16x16x32_bf16 v[172:175], a[188:191], a[92:95], 0\n"
      "ds_write_addtid_b32 v125 offset:1280\n"
      "v_mfma_f32_16x16x32_bf16 v[144:147], a[0:3], v[40:43], v[144:147]\n"
      "buffer_store_dwordx4 v[92:95], v230, s[60:63], s70 offen offset:2048 sc1\n"
      "v_mfma_f32_16x16x32_bf16 v[148:151], a[4:7], v[40:43], v[148:151]\n"
      "ds_write_addtid_b32 v126 offset:1536\n"
      "v_mfma_f32_16x16x32_bf16 v[152:155], a[8:11], v[40:43], v[152:155]\n"
      "ds_write_addtid_b32 v127 offset:1792\n"
      "v_mfma_f32_16x16x32_bf16 v[156:159], a[12:15], v[40:43], v[156:159]\n"
      "s_bfe_u64 s[66:67], %[ttab], 262144\n"
      "ds_write_addtid_b32 v128 offset:2048\n"
      "v_mfma_f32_16x16x32_bf16 v[160:163], a[16:19], v[40:43], v[160:163]\n"
      "s_lshl_b32 s64, s66, 14\n"
      "ds_write_addtid_b32 v129 offset:2304\n"
      "v_mfma_f32_16x16x32_bf16 v[164:167], a[20:23], v[40:43], v[164:167]\n"
      "s_add_u32 s64, s64, s89\n"
      "ds_write_addtid_b32 v130 offset:2560\n"
      "v_mfma_f32_16x16x32_bf16 v[168:171], a[24:27], v[40:43], v[168:171]\n"
      "buffer_store_dwordx4 v[0:3], v230, s[60:63], s64 offen sc1\n"
      "v_mfma_f32_16x16x32_bf16 v[172:175], a[28:31], v[40:43], v[172:175]\n"
      "ds_write_addtid_b32 v131 offset:2816\n"
      "s_waitcnt vmcnt(16)\n"
      "v_mfma_f32_16x16x32_bf16 v[144:147], a[32:35], v[24:27], v[144:147]\n"
      "ds_write_addtid_b32 v132 offset:3072\n"
      "v_mfma_f32_16x16x32_bf16 v[148:151], a[36:39], v[24:27], v[148:151]\n"
      "ds_write_addtid_b32 v133 offset:3328\n"
      "v_mfma_f32_16x16x32_bf16 v[152:155], a[40:43], v[24:27], v[152:155]\n"
      "ds_write_addtid_b32 v134 offset:3584\n"
      "v_mfma_f32_16x16x32_bf16 v[156:159], a[44:47], v[24:27], v[156:159]\n"
      "ds_write_addtid_b32 v135 offset:3840\n"
      "v_mfma_f32_16x16x32_bf16 v[160:163], a[48:51], v[24:27], v[160:163]\n"
      "s_add_u32 s70, s87, 393216\n"
      "ds_write_addtid_b32 v136 offset:4096\n"
      "v_mfma_f32_16x16x32_bf16 v[164:167], a[52:55], v[24:27], v[164:167]\n"
      "buffer_store_dwordx4 v[96:99], v230, s[60:63], s70 offen offset:0 sc1\n"
      "v_mfma_f32_16x16x32_bf16 v[168:171], a[56:59], v[24:27], v[168:171]\n"
      "ds_write_addtid_b32 v137 offset:4352\n"
      "v_mfma_f32_16x16x32_bf16 v[172:175], a[60:63], v[24:27], v[172:175]\n"
      "ds_write_addtid_b32 v138 offset:4608\n"
      "v_mfma_f32_16x16x32_bf16 v[144:147], a[224:227], v[28:31], v[144:147]\n"
      "ds_write_addtid_b32 v139 offset:4864\n"
      "v_mfma_f32_16x16x32_bf16 v[148:151], a[228:231], v[28:31], v[148:151]\n"
      "ds_write_addtid_b32 v140 offset:5120\n"
      "v_mfma_f32_16x16x32_bf16 v[152:155], a[232:235], v[28:31], v[152:155]\n"
      "ds_write_addtid_b32 v141 offset:5376\n"
      "v_mfma_f32_16x16x32_bf16 v[156:159], a[236:239], v[28:31], v[156:159]\n"
      "ds_write_addtid_b32 v142 offset:5632\n"
      "s_add_u32 s71, s76, 131072\n"
      "v_mfma_f32_16x16x32_bf16 v[160:163], a[240:243], v[28:31], v[160:163]\n"
      "buffer_load_dwordx4 a[92:95], v230, s[60:63], s71 offen offset:0 sc1\n"
      "v_mfma_f32_16x16x32_bf16 v[164:167], a[244:247], v[28:31], v[164:167]\n"
      "ds_write_addtid_b32 v143 offset:5888\n"
      "v_mfma_f32_16x16x32_bf16 v[168:171], a[248:251], v[28:31], v[168:171]\n"
      "v_mfma_f32_16x16x32_bf16 v[172:175], a[252:255], v[28:31], v[172:175]\n"
      "s_waitcnt lgkmcnt(0)\n"
      "s_waitcnt vmcnt(16)\n"
      "v_mfma_f32_16x16x32_bf16 v[144:147], a[96:99], v[16:19], v[144:147]\n"
      "s_barrier\n"
      "v_mfma_f32_16x16x32_bf16 v[148:151], a[100:103], v[16:19], v[148:151]\n"
      "ds_read2st64_b32 v[236:237], v218 offset0:0 offset1:1\n"
      "v_mfma_f32_16x16x32_bf16 v[152:155], a[104:107], v[16:19], v[152:155]\n"
      "ds_read2st64_b32 v[238:239], v218 offset0:2 offset1:3\n"
      "v_mfma_f32_16x16x32_bf16 v[156:159], a[108:111], v[16:19], v[156:159]\n"
      "buffer_load_dwordx4 v[4:7], v230, s[60:63], s71 offen offset:1024 sc1\n"
      "v_mfma_f32_16x16x32_bf16 v[160:163], a[112:115], v[16:19], v[160:163]\n"
      "ds_read2st64_b32 v[240:241], v218 offset0:4 offset1:5\n"
      "v_mfma_f32_16x16x32_bf16 v[164:167], a[116:119], v[16:19], v[164:167]\n"
      "ds_read2st64_b32 v[242:243], v218 offset0:6 offset1:7\n"
      "v_mfma_f32_16x16x32_bf16 v[168:171], a[120:123], v[16:19], v[168:171]\n"
      "ds_read2st64_b32 v[244:245], v219 offset0:8 offset1:9\n"
      "v_mfma_f32_16x16x32_bf16 v[172:175], a[124:127], v[16:19], v[172:175]\n"
      "ds_read2st64_b32 v[246:247], v219 offset0:10 offset1:11\n"
      "v_mfma_f32_16x16x32_bf16 v[144:147], a[128:131], a[64:67], v[144:147]\n"
      "ds_read2st64_b32 v[248:249], v219 offset0:12 offset1:13\n"
      "s_add_u32 s71, s77, 131072\n"
      "v_mfma_f32_16x16x32_bf16 v[148:151], a[132:135], a[64:67], v[148:151]\n"
      "buffer_load_dwordx4 v[8:11], v230, s[60:63], s71 offen offset:0 sc1\n"
      "s_waitcnt lgkmcnt(3)\n"
      "v_mfma_f32_16x16x32_bf16 v[152:155], a[136:139], a[64:67], v[152:155]\n"
      "ds_read2st64_b32 v[250:251], v219 offset0:14 offset1:15\n"
      "v_add_f32 v236, v236, v112\n"
      "v_mfma_f32_16x16x32_bf16 v[156:159], a[140:143], a[64:67], v[156:159]\n"
      "ds_read2st64_b32 v[176:177], v220 offset0:16 offset1:17\n"
      "v_add_f32_e64 v237, v237, v113\n"
      "v_mfma_f32_16x16x32_bf16 v[160:163], a[144:147], a[64:67], v[160:163]\n"
      "ds_read2st64_b32 v[178:179], v220 offset0:18 offset1:19\n"
      "v_add_f32 v238, v238, v114\n"
      "v_mfma_f32_16x16x32_bf16 v[164:167], a[148:151], a[64:67], v[164:167]\n"
      "ds_read2st64_b32 v[212:213], v220 offset0:20 offset1:21\n"
      "v_add_f32 v239, v239, v115\n"
      "v_mfma_f32_16x16x32_bf16 v[168:171], a[152:155], a[64:67], v[168:171]\n"
      "ds_read2st64_b32 v[214:215], v220 offset0:22 offset1:23\n"
      "v_add_f32_e64 v240, v240, v116\n"
      "v_mfma_f32_16x16x32_bf16 v[172:175], a[156:159], a[64:67], v[172:175]\n"
      "ds_read_b128 v[32:35], v217 offset:4096\n"
      "s_waitcnt vmcnt(16)\n"
      "v_mfma_f32_16x16x32_bf16 v[144:147], v[180:183], a[68:71], v[144:147]\n"
      "buffer_load_dwordx4 v[12:15], v230, s[60:63], s71 offen offset:1024 sc1\n"
      "v_add_f32 v241, v241, v117\n"
      "v_mfma_f32_16x16x32_bf16 v[148:151], v[184:187], a[68:71], v[148:151]\n"
      "ds_read_b128 v[36:39], v217 offset:53248\n"
      "v_add_f32_e64 v242, v242, v118\n"
      "v_mfma_f32_16x16x32_bf16 v[152:155], v[188:191], a[68:71], v[152:155]\n"
      "ds_write_addtid_b32 v50 offset:16896\n"
      "v_add_f32 v243, v243, v119\n"
      "s_waitcnt lgkmcnt(3)\n"
      "v_mfma_f32_16x16x32_bf16 v[156:159], v[192:195], a[68:71], v[156:159]\n"
      "ds_write_addtid_b32 v51 offset:17152\n"
      "v_add_f32 v236, v236, v244\n"
      "v_mfma_f32_16x16x32_bf16 v[160:163], v[196:199], a[68:71], v[160:163]\n"
      "ds_write_addtid_b32 v52 offset:17408\n"
      "v_add_f32 v237, v237, v245\n"
      "v_mfma_f32_16x16x32_bf16 v[164:167], v[200:203], a[68:71], v[164:167]\n"
      "ds_write_addtid_b32 v53 offset:17664\n"
      "v_add_f32_e64 v238, v238, v246\n"
      "v_mfma_f32_16x16x32_bf16 v[168:171], v[204:207], a[68:71], v[168:171]\n"
      "ds_write_addtid_b32 v54 offset:17920\n"
      "s_add_u32 s71, s78, 131072\n"
      "v_mfma_f32_16x16x32_bf16 v[172:175], v[208:211], a[68:71], v[172:175]\n"
      "buffer_load_dwordx4 a[64:67], v230, s[60:63], s71 offen offset:0 sc1\n"
      "v_add_f32_e64 v239, v239, v247\n"
      "v_mfma_f32_16x16x32_bf16 v[144:147], a[192:195], a[76:79], v[144:147]\n"
      "ds_write_addtid_b32 v55 offset:18176\n"
      "v_add_f32 v240, v240, v248\n"
      "v_mfma_f32_16x16x32_bf16 v[148:151], a[196:199], a[76:79], v[148:151]\n"
      "v_add_f32 v241, v241, v249\n"
      "v_add_f32 v242, v242, v250\n"
      "v_mfma_f32_16x16x32_bf16 v[152:155], a[200:203], a[76:79], v[152:155]\n"
      "v_add_f32 v243, v243, v251\n"
      "v_add_f32 v236, v236, v176\n"
      "v_mfma_f32_16x16x32_bf16 v[156:159], a[204:207], a[76:79], v[156:159]\n"
      "v_add_f32 v237, v237, v177\n"
      "v_add_f32 v238, v238, v178\n"
      "v_mfma_f32_16x16x32_bf16 v[160:163], a[208:211], a[76:79], v[160:163]\n"
      "v_add_f32 v239, v239, v179\n"
      "v_add_f32_e64 v240, v240, v212\n"
      "v_mfma_f32_16x16x32_bf16 v[164:167], a[212:215], a[76:79], v[164:167]\n"
      "v_add_f32_e64 v241, v241, v213\n"
      "v_add_f32_e64 v242, v242, v214\n"
      "v_mfma_f32_16x16x32_bf16 v[168:171], a[216:219], a[76:79], v[168:171]\n"
      "buffer_load_dwordx4 a[68:71], v230, s[60:63], s71 offen offset:1024 sc1\n"
      "v_add_f32 v243, v243, v215\n"
      "v_mfma_f32_16x16x32_bf16 v[172:175], a[220:223], a[76:79], v[172:175]\n"
      "s_waitcnt lgkmcnt(6)\n"
      "v_mfma_f32_16x16x32_bf16 v[112:115], a[160:163], v[32:35], 0\n"
      "ds_write_addtid_b32 v56 offset:18432\n"
      "v_cvt_pk_bf16_f32 v40, v236, v237\n"
      "v_mfma_f32_16x16x32_bf16 v[116:119], a[164:167], v[32:35], 0\n"
      "ds_write_addtid_b32 v57 offset:18688\n"
      "v_cvt_pk_bf16_f32 v41, v238, v239\n"
      "v_mfma_f32_16x16x32_bf16 v[120:123], a[168:171], v[32:35], 0\n"
      "ds_write_addtid_b32 v152 offset:32768\n"
      "v_cvt_pk_bf16_f32 v42, v240, v241\n"
      "v_mfma_f32_16x16x32_bf16 v[124:127], a[172:175], v[32:35], 0\n"
      "ds_write_addtid_b32 v153 offset:33024\n"
      "v_cvt_pk_bf16_f32 v43, v242, v243\n"
      "v_mfma_f32_16x16x32_bf16 v[128:131], a[176:179], v[32:35], 0\n"
      "ds_write_addtid_b32 v154 offset:33280\n"
      "v_mfma_f32_16x16x32_bf16 v[132:135], a[180:183], v[32:35], 0\n"
      "ds_write_addtid_b32 v155 offset:33536\n"
      "v_mfma_f32_16x16x32_bf16 v[136:139], a[184:187], v[32:35], 0\n"
      "ds_write_addtid_b32 v156 offset:33792\n"
      "v_mfma_f32_16x16x32_bf16 v[140:143], a[188:191], v[32:35], 0\n"
      "ds_write_addtid_b32 v157 offset:34048\n"
      "v_mfma_f32_16x16x32_bf16 v[112:115], a[0:3], v[36:39], v[112:115]\n"
      "buffer_load_dword v254, v226, s[60:63], s92 offen sc1\n"
      "v_mfma_f32_16x16x32_bf16 v[116:119], a[4:7], v[36:39], v[116:119]\n"
      "ds_write_addtid_b32 v158 offset:34304\n"
      "v_mfma_f32_16x16x32_bf16 v[120:123], a[8:11], v[36:39], v[120:123]\n"
      "ds_write_addtid_b32 v159 offset:34560\n"
      "v_mfma_f32_16x16x32_bf16 v[124:127], a[12:15], v[36:39], v[124:127]\n"
      "s_bfe_u64 s[66:67], %[ttab], 262148\n"
      "ds_write_addtid_b32 v160 offset:34816\n"
      "v_mfma_f32_16x16x32_bf16 v[128:131], a[16:19], v[36:39], v[128:131]\n"
      "s_lshl_b32 s64, s66, 14\n"
      "ds_write_addtid_b32 v161 offset:35072\n"
      "v_mfma_f32_16x16x32_bf16 v[132:135], a[20:23], v[36:39], v[132:135]\n"
      "s_add_u32 s64, s64, s89\n"
      "ds_write_addtid_b32 v162 offset:35328\n"
      "v_mfma_f32_16x16x32_bf16 v[136:139], a[24:27], v[36:39], v[136:139]\n"
      "buffer_store_dwordx4 v[40:43], v230, s[60:63], s64 offen sc1\n"
      "v_mfma_f32_16x16x32_bf16 v[140:143], a[28:31], v[36:39], v[140:143]\n"
      "ds_write_addtid_b32 v163 offset:35584\n"
      "s_waitcnt vmcnt(15)\n"
      "v_mfma_f32_16x16x32_bf16 v[112:115], a[32:35], a[80:83], v[112:115]\n"
      "ds_write_addtid_b32 v164 offset:35840\n"
      "v_mfma_f32_16x16x32_bf16 v[116:119], a[36:39], a[80:83], v[116:119]\n"
      "ds_write_addtid_b32 v165 offset:36096\n"
      "v_mfma_f32_16x16x32_bf16 v[120:123], a[40:43], a[80:83], v[120:123]\n"
      "ds_write_addtid_b32 v166 offset:36352\n"
      "v_mfma_f32_16x16x32_bf16 v[124:127], a[44:47], a[80:83], v[124:127]\n"
      "ds_write_addtid_b32 v167 offset:36608\n"
      "v_mfma_f32_16x16x32_bf16 v[128:131], a[48:51], a[80:83], v[128:131]\n"
      "buffer_store_dwordx4 v[100:103], v230, s[60:63], s70 offen offset:2048 sc1\n"
      "v_mfma_f32_16x16x32_bf16 v[132:135], a[52:55], a[80:83], v[132:135]\n"
      "ds_write_addtid_b32 v168 offset:36864\n"
      "v_mfma_f32_16x16x32_bf16 v[136:139], a[56:59], a[80:83], v[136:139]\n"
      "ds_write_addtid_b32 v169 offset:37120\n"
      "v_mfma_f32_16x16x32_bf16 v[140:143], a[60:63], a[80:83], v[140:143]\n"
      "ds_write_addtid_b32 v170 offset:37376\n"
      "v_mfma_f32_16x16x32_bf16 v[112:115], a[224:227], a[84:87], v[112:115]\n"
      "ds_write_addtid_b32 v171 offset:37632\n"
      "v_mfma_f32_16x16x32_bf16 v[116:119], a[228:231], a[84:87], v[116:119]\n"
      "s_add_u32 s70, s87, 397312\n"
      "ds_write_addtid_b32 v172 offset:37888\n"
      "v_mfma_f32_16x16x32_bf16 v[120:123], a[232:235], a[84:87], v[120:123]\n"
      "buffer_store_dwordx4 v[104:107], v230, s[60:63], s70 offen offset:0 sc1\n"
      "v_mfma_f32_16x16x32_bf16 v[124:127], a[236:239], a[84:87], v[124:127]\n"
      "ds_write_addtid_b32 v173 offset:38144\n"
      "v_mfma_f32_16x16x32_bf16 v[128:131], a[240:243], a[84:87], v[128:131]\n"
      "ds_write_addtid_b32 v174 offset:38400\n"
      "v_mfma_f32_16x16x32_bf16 v[132:135], a[244:247], a[84:87], v[132:135]\n"
      "ds_write_addtid_b32 v175 offset:38656\n"
      "v_mfma_f32_16x16x32_bf16 v[136:139], a[248:251], a[84:87], v[136:139]\n"
      "v_mfma_f32_16x16x32_bf16 v[140:143], a[252:255], a[84:87], v[140:143]\n"
      "s_add_u32 s71, s76, 163840\n"
      "s_waitcnt vmcnt(15)\n"
      "v_mfma_f32_16x16x32_bf16 v[112:115], a[96:99], a[72:75], v[112:115]\n"
      "s_waitcnt lgkmcnt(0)\n"
      "buffer_load_dwordx4 a[76:79], v230, s[60:63], s71 offen offset:0 sc1\n"
      "v_mfma_f32_16x16x32_bf16 v[116:119], a[100:103], a[72:75], v[116:119]\n"
      "s_barrier\n"
      "v_mfma_f32_16x16x32_bf16 v[120:123], a[104:107], a[72:75], v[120:123]\n"
      "ds_read2st64_b32 v[236:237], v218 offset0:128 offset1:129\n"
      "v_mfma_f32_16x16x32_bf16 v[124:127], a[108:111], a[72:75], v[124:127]\n"
      "ds_read2st64_b32 v[238:239], v218 offset0:130 offset1:131\n"
      "v_mfma_f32_16x16x32_bf16 v[128:131], a[112:115], a[72:75], v[128:131]\n"
      "ds_read2st64_b32 v[240:241], v218 offset0:132 offset1:133\n"
      "v_mfma_f32_16x16x32_bf16 v[132:135], a[116:119], a[72:75], v[132:135]\n"
      "ds_read2st64_b32 v[242:243], v218 offset0:134 offset1:135\n"
      "v_mfma_f32_16x16x32_bf16 v[136:139], a[120:123], a[72:75], v[136:139]\n"
      "buffer_load_dwordx4 a[80:83], v230, s[60:63], s71 offen offset:1024 sc1\n"
      "v_mfma_f32_16x16x32_bf16 v[140:143], a[124:127], a[72:75], v[140:143]\n"
      "ds_read2st64_b32 v[244:245], v219 offset0:136 offset1:137\n"
      "v_mfma_f32_16x16x32_bf16 v[112:115], a[128:131], a[88:91], v[112:115]\n"
      "ds_read2st64_b32 v[246:247], v219 offset0:138 offset1:139\n"
      "v_mfma_f32_16x16x32_bf16 v[116:119], a[132:135], a[88:91], v[116:119]\n"
      "ds_read2st64_b32 v[248:249], v219 offset0:140 offset1:141\n"
      "s_waitcnt lgkmcnt(3)\n"
      "v_mfma_f32_16x16x32_bf16 v[120:123], a[136:139], a[88:91], v[120:123]\n"
      "ds_read2st64_b32 v[250:251], v219 offset0:142 offset1:143\n"
      "v_add_f32 v236, v236, v144\n"
      "v_mfma_f32_16x16x32_bf16 v[124:127], a[140:143], a[88:91], v[124:127]\n"
      "ds_read2st64_b32 v[176:177], v220 offset0:144 offset1:145\n"
      "s_add_u32 s71, s77, 163840\n"
      "v_mfma_f32_16x16x32_bf16 v[128:131], a[144:147], a[88:91], v[128:131]\n"
      "buffer_load_dwordx4 a[84:87], v230, s[60:63], s71 offen offset:0 sc1\n"
      "v_add_f32 v237, v237, v145\n"
      "v_mfma_f32_16x16x32_bf16 v[132:135], a[148:151], a[88:91], v[132:135]\n"
      "ds_read2st64_b32 v[178:179], v220 offset0:146 offset1:147\n"
      "v_add_f32 v238, v238, v146\n"
      "v_mfma_f32_16x16x32_bf16 v[136:139], a[152:155], a[88:91], v[136:139]\n"
      "ds_read2st64_b32 v[212:213], v220 offset0:148 offset1:149\n"
      "v_add_f32_e64 v239, v239, v147\n"
      "v_mfma_f32_16x16x32_bf16 v[140:143], a[156:159], a[88:91], v[140:143]\n"
      "ds_read2st64_b32 v[214:215], v220 offset0:150 offset1:151\n"
      "s_waitcnt vmcnt(16)\n"
      "v_mfma_f32_16x16x32_bf16 v[112:115], v[180:183], v[44:47], v[112:115]\n"
      "ds_read2st64_b32 a[72:73], v221 offset0:0 offset1:1\n"
      "v_add_f32 v240, v240, v148\n"
      "v_mfma_f32_16x16x32_bf16 v[116:119], v[184:187], v[44:47], v[116:119]\n"
      "ds_read2st64_b32 a[74:75], v221 offset0:2 offset1:3\n"
      "v_add_f32_e64 v241, v241, v149\n"
      "v_mfma_f32_16x16x32_bf16 v[120:123], v[188:191], v[44:47], v[120:123]\n"
      "buffer_load_dwordx4 a[88:91], v230, s[60:63], s71 offen offset:1024 sc1\n"
      "v_add_f32 v242, v242, v150\n"
      "v_mfma_f32_16x16x32_bf16 v[124:127], v[192:195], v[44:47], v[124:127]\n"
      "ds_read2st64_b32 v[24:25], v221 offset0:192 offset1:193\n"
      "v_add_f32 v243, v243, v151\n"
      "s_waitcnt lgkmcnt(3)\n"
      "v_mfma_f32_16x16x32_bf16 v[128:131], v[196:199], v[44:47], v[128:131]\n"
      "ds_read2st64_b32 v[26:27], v221 offset0:194 offset1:195\n"
      "v_add_f32 v236, v236, v244\n"
      "v_mfma_f32_16x16x32_bf16 v[132:135], v[200:203], v[44:47], v[132:135]\n"
      "ds_write_addtid_b32 v58 offset:18944\n"
      "v_add_f32 v237, v237, v245\n"
      "v_mfma_f32_16x16x32_bf16 v[136:139], v[204:207], v[44:47], v[136:139]\n"
      "ds_write_addtid_b32 v59 offset:19200\n"
      "v_add_f32 v238, v238, v246\n"
      "v_mfma_f32_16x16x32_bf16 v[140:143], v[208:211], v[44:47], v[140:143]\n"
      "ds_write_addtid_b32 v60 offset:19456\n"
      "s_add_u32 s71, s78, 163840\n"
      "v_mfma_f32_16x16x32_bf16 v[112:115], a[192:195], v[20:23], v[112:115]\n"
      "buffer_load_dwordx4 v[28:31], v230, s[60:63], s71 offen offset:0 sc1\n"
      "v_add_f32 v239, v239, v247\n"
      "v_mfma_f32_16x16x32_bf16 v[116:119], a[196:199], v[20:23], v[116:119]\n"
      "ds_write_addtid_b32 v61 offset:19712\n"
      "v_add_f32 v240, v240, v248\n"
      "v_mfma_f32_16x16x32_bf16 v[120:123], a[200:203], v[20:23], v[120:123]\n"
      "s_cmp_lg_u32 s79, 0\n"
      "s_cbranch_scc1 DS3_%=\n"
      "DS3R_%=:\n"
      "v_add_f32_e64 v241, v241, v249\n"
      "v_mfma_f32_16x16x32_bf16 v[124:127], a[204:207], v[20:23], v[124:127]\n"
      "ds_write_addtid_b32 v62 offset:19968\n"
      "v_add_f32 v242, v242, v250\n"
      "v_mfma_f32_16x16x32_bf16 v[128:131], a[208:211], v[20:23], v[128:131]\n"
      "s_waitcnt vmcnt(8)\n"
      "s_add_u32 s97, s82, 5\n"
      "v_add_f32 v243, v243, v251\n"
      "v_mfma_f32_16x16x32_bf16 v[132:135], a[212:215], v[20:23], v[132:135]\n"
      "v_subrev_u32 v254, s97, v254\n"
      "v_cmp_lt_u32 vcc, 2, v254\n"
      "v_add_f32_e64 v236, v236, v176\n"
      "v_mfma_f32_16x16x32_bf16 v[136:139], a[216:219], v[20:23], v[136:139]\n"
      "s_cbranch_vccnz PD4_%=\n"
      "PD4R_%=:\n"
      "buffer_load_dwordx4 v[16:19], v230, s[60:63], s71 offen offset:1024 sc1\n"
      "v_mfma_f32_16x16x32_bf16 v[140:143], a[220:223], v[20:23], v[140:143]\n"
      "ds_write_addtid_b32 v63 offset:20224\n"
      "s_waitcnt lgkmcnt(6)\n"
      "v_mfma_f32_16x16x32_bf16 v[144:147], a[160:163], a[72:75], 0\n"
      "ds_write_addtid_b32 v64 offset:20480\n"
      "v_add_f32_e64 v237, v237, v177\n"
      "v_mfma_f32_16x16x32_bf16 v[148:151], a[164:167], a[72:75], 0\n"
      "ds_write_addtid_b32 v65 offset:20736\n"
      "v_add_f32 v238, v238, v178\n"
      "v_mfma_f32_16x16x32_bf16 v[152:155], a[168:171], a[72:75], 0\n"
      "ds_write_addtid_b32 v120 offset:0\n"
      "v_add_f32 v239, v239, v179\n"
      "v_mfma_f32_16x16x32_bf16 v[156:159], a[172:175], a[72:75], 0\n"
      "ds_write_addtid_b32 v121 offset:256\n"
      "v_add_f32_e64 v240, v240, v212\n"
      "v_mfma_f32_16x16x32_bf16 v[160:163], a[176:179], a[72:75], 0\n"
      "ds_write_addtid_b32 v122 offset:512\n"
      "v_add_f32_e64 v241, v241, v213\n"
      "v_mfma_f32_16x16x32_bf16 v[164:167], a[180:183], a[72:75], 0\n"
      "ds_write_addtid_b32 v123 offset:768\n"
      "v_add_f32_e64 v242, v242, v214\n"
      "v_mfma_f32_16x16x32_bf16 v[168:171], a[184:187], a[72:75], 0\n"
      "ds_write_addtid_b32 v124 offset:1024\n"
      "v_add_f32_e64 v243, v243, v215\n"
      "v_mfma_f32_16x16x32_bf16 v[172:175], a[188:191], a[72:75], 0\n"
      "ds_write_addtid_b32 v125 offset:1280\n"
      "v_cvt_pk_bf16_f32 v0, v236, v237\n"
      "v_mfma_f32_16x16x32_bf16 v[144:147], a[0:3], v[24:27], v[144:147]\n"
      "buffer_store_dwordx4 v[108:111], v230, s[60:63], s70 offen offset:2048 sc1\n"
      "v_cvt_pk_bf16_f32 v1, v238, v239\n"
      "v_mfma_f32_16x16x32_bf16 v[148:151], a[4:7], v[24:27], v[148:151]\n"
      "ds_write_addtid_b32 v126 offset:1536\n"
      "v_cvt_pk_bf16_f32 v2, v240, v241\n"
      "v_mfma_f32_16x16x32_bf16 v[152:155], a[8:11], v[24:27], v[152:155]\n"
      "ds_write_addtid_b32 v127 offset:1792\n"
      "v_cvt_pk_bf16_f32 v3, v242, v243\n"
      "v_mfma_f32_16x16x32_bf16 v[156:159], a[12:15], v[24:27], v[156:159]\n"
      "ds_write_addtid_b32 v128 offset:2048\n"
      "v_mfma_f32_16x16x32_bf16 v[160:163], a[16:19], v[24:27], v[160:163]\n"
      "s_bfe_u64 s[66:67], %[ttab], 262152\n"
      "ds_write_addtid_b32 v129 offset:2304\n"
      "v_mfma_f32_16x16x32_bf16 v[164:167], a[20:23], v[24:27], v[164:167]\n"
      "s_lshl_b32 s64, s66, 14\n"
      "ds_write_addtid_b32 v130 offset:2560\n"
      "v_mfma_f32_16x16x32_bf16 v[168:171], a[24:27], v[24:27], v[168:171]\n"
      "s_add_u32 s64, s64, s89\n"
      "ds_write_addtid_b32 v131 offset:2816\n"
      "v_mfma_f32_16x16x32_bf16 v[172:175], a[28:31], v[24:27], v[172:175]\n"
      "buffer_store_dwordx4 v[0:3], v230, s[60:63], s64 offen sc1\n"
      "s_waitcnt vmcnt(16)\n"
      "v_mfma_f32_16x16x32_bf16 v[144:147], a[32:35], a[92:95], v[144:147]\n"
      "ds_write_addtid_b32 v132 offset:3072\n"
      "v_mfma_f32_16x16x32_bf16 v[148:151], a[36:39], a[92:95], v[148:151]\n"
      "ds_write_addtid_b32 v133 offset:3328\n"
      "v_mfma_f32_16x16x32_bf16 v[152:155], a[40:43], a[92:95], v[152:155]\n"
      "ds_write_addtid_b32 v134 offset:3584\n"
      "v_mfma_f32_16x16x32_bf16 v[156:159], a[44:47], a[92:95], v[156:159]\n"
      "ds_write_addtid_b32 v135 offset:3840\n"
      "v_mfma_f32_16x16x32_bf16 v[160:163], a[48:51], a[92:95], v[160:163]\n"
      "ds_write_addtid_b32 v136 offset:4096\n"
      "v_mfma_f32_16x16x32_bf16 v[164:167], a[52:55], a[92:95], v[164:167]\n"
      "ds_write_addtid_b32 v137 offset:4352\n"
      "v_mfma_f32_16x16x32_bf16 v[168:171], a[56:59], a[92:95], v[168:171]\n"
      "ds_write_addtid_b32 v138 offset:4608\n"
      "s_add_u32 s71, s76, 196608\n"
      "v_mfma_f32_16x16x32_bf16 v[172:175], a[60:63], a[92:95], v[172:175]\n"
      "buffer_load_dwordx4 a[72:75], v230, s[60:63], s71 offen offset:0 sc1\n"
      "v_mfma_f32_16x16x32_bf16 v[144:147], a[224:227], v[4:7], v[144:147]\n"
      "ds_write_addtid_b32 v139 offset:4864\n"
      "v_mfma_f32_16x16x32_bf16 v[148:151], a[228:231], v[4:7], v[148:151]\n"
      "ds_write_addtid_b32 v140 offset:5120\n"
      "v_mfma_f32_16x16x32_bf16 v[152:155], a[232:235], v[4:7], v[152:155]\n"
      "ds_write_addtid_b32 v141 offset:5376\n"
      "v_mfma_f32_16x16x32_bf16 v[156:159], a[236:239], v[4:7], v[156:159]\n"
      "ds_write_addtid_b32 v142 offset:5632\n"
      "v_mfma_f32_16x16x32_bf16 v[160:163], a[240:243], v[4:7], v[160:163]\n"
      "ds_write_addtid_b32 v143 offset:5888\n"
      "v_mfma_f32_16x16x32_bf16 v[164:167], a[244:247], v[4:7], v[164:167]\n"
      "v_mfma_f32_16x16x32_bf16 v[168:171], a[248:251], v[4:7], v[168:171]\n"
      "v_mfma_f32_16x16x32_bf16 v[172:175], a[252:255], v[4:7], v[172:175]\n"
      "buffer_load_dwordx4 a[92:95], v230, s[60:63], s71 offen offset:1024 sc1\n"
      "s_waitcnt vmcnt(16)\n"
      "v_mfma_f32_16x16x32_bf16 v[144:147], a[96:99], v[8:11], v[144:147]\n"
      "s_waitcnt lgkmcnt(0)\n"
      "v_mfma_f32_16x16x32_bf16 v[148:151], a[100:103], v[8:11], v[148:151]\n"
      "s_barrier\n"
      "v_mfma_f32_16x16x32_bf16 v[152:155], a[104:107], v[8:11], v[152:155]\n"
      "ds_read2st64_b32 v[236:237], v218 offset0:0 offset1:1\n"
      "v_mfma_f32_16x16x32_bf16 v[156:159], a[108:111], v[8:11], v[156:159]\n"
      "ds_read2st64_b32 v[238:239], v218 offset0:2 offset1:3\n"
      "v_mfma_f32_16x16x32_bf16 v[160:163], a[112:115], v[8:11], v[160:163]\n"
      "ds_read2st64_b32 v[240:241], v218 offset0:4 offset1:5\n"
      "v_mfma_f32_16x16x32_bf16 v[164:167], a[116:119], v[8:11], v[164:167]\n"
      "ds_read2st64_b32 v[242:243], v218 offset0:6 offset1:7\n"
      "s_add_u32 s71, s77, 196608\n"
      "v_mfma_f32_16x16x32_bf16 v[168:171], a[120:123], v[8:11], v[168:171]\n"
      "buffer_load_dwordx4 v[48:51], v230, s[60:63], s71 offen offset:0 sc1\n"
      "v_mfma_f32_16x16x32_bf16 v[172:175], a[124:127], v[8:11], v[172:175]\n"
      "ds_read2st64_b32 v[244:245], v219 offset0:8 offset1:9\n"
      "v_mfma_f32_16x16x32_bf16 v[144:147], a[128:131], v[12:15], v[144:147]\n"
      "ds_read2st64_b32 v[246:247], v219 offset0:10 offset1:11\n"
      "v_mfma_f32_16x16x32_bf16 v[148:151], a[132:135], v[12:15], v[148:151]\n"
      "ds_read2st64_b32 v[248:249], v219 offset0:12 offset1:13\n"
      "s_waitcnt lgkmcnt(3)\n"
      "v_mfma_f32_16x16x32_bf16 v[152:155], a[136:139], v[12:15], v[152:155]\n"
      "ds_read2st64_b32 v[250:251], v219 offset0:14 offset1:15\n"
      "v_add_f32 v236, v236, v112\n"
      "v_mfma_f32_16x16x32_bf16 v[156:159], a[140:143], v[12:15], v[156:159]\n"
      "ds_read2st64_b32 v[176:177], v220 offset0:16 offset1:17\n"
      "v_add_f32 v237, v237, v113\n"
      "v_mfma_f32_16x16x32_bf16 v[160:163], a[144:147], v[12:15], v[160:163]\n"
      "ds_read2st64_b32 v[178:179], v220 offset0:18 offset1:19\n"
      "v_add_f32 v238, v238, v114\n"
      "v_mfma_f32_16x16x32_bf16 v[164:167], a[148:151], v[12:15], v[164:167]\n"
      "ds_read2st64_b32 v[212:213], v220 offset0:20 offset1:21\n"
      "v_add_f32_e64 v239, v239, v115\n"
      "v_mfma_f32_16x16x32_bf16 v[168:171], a[152:155], v[12:15], v[168:171]\n"
      "buffer_load_dwordx4 v[52:55], v230, s[60:63], s71 offen offset:1024 sc1\n"
      "v_add_f32 v240, v240, v116\n"
      "v_mfma_f32_16x16x32_bf16 v[172:175], a[156:159], v[12:15], v[172:175]\n"
      "ds_read2st64_b32 v[214:215], v220 offset0:22 offset1:23\n"
      "s_waitcnt vmcnt(16)\n"
      "v_mfma_f32_16x16x32_bf16 v[144:147], v[180:183], a[64:67], v[144:147]\n"
      "ds_read2st64_b32 v[32:33], v222 offset0:0 offset1:1\n"
      "v_add_f32_e64 v241, v241, v117\n"
      "v_mfma_f32_16x16x32_bf16 v[148:151], v[184:187], a[64:67], v[148:151]\n"
      "ds_read2st64_b32 v[34:35], v222 offset0:2 offset1:3\n"
      "v_add_f32_e64 v242, v242, v118\n"
      "v_mfma_f32_16x16x32_bf16 v[152:155], v[188:191], a[64:67], v[152:155]\n"
      "ds_read2st64_b32 v[36:37], v222 offset0:192 offset1:193\n"
      "v_add_f32 v243, v243, v119\n"
      "v_mfma_f32_16x16x32_bf16 v[156:159], v[192:195], a[64:67], v[156:159]\n"
      "ds_read2st64_b32 v[38:39], v222 offset0:194 offset1:195\n"
      "s_waitcnt lgkmcnt(4)\n"
      "v_mfma_f32_16x16x32_bf16 v[160:163], v[196:199], a[64:67], v[160:163]\n"
      "ds_write_addtid_b32 v66 offset:20992\n"
      "v_add_f32_e64 v236, v236, v244\n"
      "v_mfma_f32_16x16x32_bf16 v[164:167], v[200:203], a[64:67], v[164:167]\n"
      "ds_write_addtid_b32 v67 offset:21248\n"
      "s_add_u32 s71, s78, 196608\n"
      "v_mfma_f32_16x16x32_bf16 v[168:171], v[204:207], a[64:67], v[168:171]\n"
      "buffer_load_dwordx4 v[44:47], v230, s[60:63], s71 offen offset:0 sc1\n"
      "v_add_f32_e64 v237, v237, v245\n"
      "v_mfma_f32_16x16x32_bf16 v[172:175], v[208:211], a[64:67], v[172:175]\n"
      "ds_write_addtid_b32 v68 offset:21504\n"
      "v_add_f32_e64 v238, v238, v246\n"
      "v_mfma_f32_16x16x32_bf16 v[144:147], a[192:195], a[68:71], v[144:147]\n"
      "ds_write_addtid_b32 v69 offset:21760\n"
      "v_add_f32 v239, v239, v247\n"
      "v_mfma_f32_16x16x32_bf16 v[148:151], a[196:199], a[68:71], v[148:151]\n"
      "ds_write_addtid_b32 v70 offset:22016\n"
      "v_add_f32 v240, v240, v248\n"
      "v_mfma_f32_16x16x32_bf16 v[152:155], a[200:203], a[68:71], v[152:155]\n"
      "ds_write_addtid_b32 v71 offset:22272\n"
      "v_add_f32 v241, v241, v249\n"
      "v_mfma_f32_16x16x32_bf16 v[156:159], a[204:207], a[68:71], v[156:159]\n"
      "v_add_f32 v242, v242, v250\n"
      "v_add_f32 v243, v243, v251\n"
      "v_mfma_f32_16x16x32_bf16 v[160:163], a[208:211], a[68:71], v[160:163]\n"
      "v_add_f32 v236, v236, v176\n"
      "v_add_f32 v237, v237, v177\n"
      "v_mfma_f32_16x16x32_bf16 v[164:167], a[212:215], a[68:71], v[164:167]\n"
      "v_add_f32 v238, v238, v178\n"
      "v_add_f32 v239, v239, v179\n"
      "v_mfma_f32_16x16x32_bf16 v[168:171], a[216:219], a[68:71], v[168:171]\n"
      "buffer_load_dwordx4 a[64:67], v230, s[60:63], s71 offen offset:1024 sc1\n"
      "v_add_f32 v240, v240, v212\n"
      "v_mfma_f32_16x16x32_bf16 v[172:175], a[220:223], a[68:71], v[172:175]\n"
      "v_add_f32 v241, v241, v213\n"
      "v_add_f32_e64 v242, v242, v214\n"
      "s_waitcnt lgkmcnt(6)\n"
      "v_mfma_f32_16x16x32_bf16 v[112:115], a[160:163], v[32:35], 0\n"
      "ds_write_addtid_b32 v72 offset:22528\n"
      "v_add_f32_e64 v243, v243, v215\n"
      "v_mfma_f32_16x16x32_bf16 v[116:119], a[164:167], v[32:35], 0\n"
      "ds_write_addtid_b32 v73 offset:22784\n"
      "v_cvt_pk_bf16_f32 v20, v236, v237\n"
      "v_mfma_f32_16x16x32_bf16 v[120:123], a[168:171], v[32:35], 0\n"
      "ds_write_addtid_b32 v152 offset:32768\n"
      "v_cvt_pk_bf16_f32 v21, v238, v239\n"
      "v_mfma_f32_16x16x32_bf16 v[124:127], a[172:175], v[32:35], 0\n"
      "ds_write_addtid_b32 v153 offset:33024\n"
      "v_cvt_pk_bf16_f32 v22, v240, v241\n"
      "v_mfma_f32_16x16x32_bf16 v[128:131], a[176:179], v[32:35], 0\n"
      "ds_write_addtid_b32 v154 offset:33280\n"
      "v_cvt_pk_bf16_f32 v23, v242, v243\n"
      "v_mfma_f32_16x16x32_bf16 v[132:135], a[180:183], v[32:35], 0\n"
      "ds_write_addtid_b32 v155 offset:33536\n"
      "v_mfma_f32_16x16x32_bf16 v[136:139], a[184:187], v[32:35], 0\n"
      "ds_write_addtid_b32 v156 offset:33792\n"
      "v_mfma_f32_16x16x32_bf16 v[140:143], a[188:191], v[32:35], 0\n"
      "ds_write_addtid_b32 v157 offset:34048\n"
      "s_add_u32 s71, s76, 229376\n"
      "v_mfma_f32_16x16x32_bf16 v[112:115], a[0:3], v[36:39], v[112:115]\n"
      "buffer_load_dwordx4 a[68:71], v230, s[60:63], s71 offen offset:0 sc1\n"
      "v_mfma_f32_16x16x32_bf16 v[116:119], a[4:7], v[36:39], v[116:119]\n"
      "ds_write_addtid_b32 v158 offset:34304\n"
      "v_mfma_f32_16x16x32_bf16 v[120:123], a[8:11], v[36:39], v[120:123]\n"
      "ds_write_addtid_b32 v159 offset:34560\n"
      "v_mfma_f32_16x16x32_bf16 v[124:127], a[12:15], v[36:39], v[124:127]\n"
      "ds_write_addtid_b32 v160 offset:34816\n"
      "v_mfma_f32_16x16x32_bf16 v[128:131], a[16:19], v[36:39], v[128:131]\n"
      "s_bfe_u64 s[66:67], %[ttab], 262156\n"
      "ds_write_addtid_b32 v161 offset:35072\n"
      "v_mfma_f32_16x16x32_bf16 v[132:135], a[20:23], v[36:39], v[132:135]\n"
      "s_lshl_b32 s64, s66, 14\n"
      "ds_write_addtid_b32 v162 offset:35328\n"
      "v_mfma_f32_16x16x32_bf16 v[136:139], a[24:27], v[36:39], v[136:139]\n"
      "s_add_u32 s64, s64, s89\n"
      "ds_write_addtid_b32 v163 offset:35584\n"
      "v_mfma_f32_16x16x32_bf16 v[140:143], a[28:31], v[36:39], v[140:143]\n"
      "buffer_store_dwordx4 v[20:23], v230, s[60:63], s64 offen sc1\n"
      "s_waitcnt vmcnt(14)\n"
      "s_nop 0\n"
      "v_mfma_f32_16x16x32_bf16 v[112:115], a[32:35], a[76:79], v[112:115]\n"
      "ds_write_addtid_b32 v164 offset:35840\n"
      "v_mfma_f32_16x16x32_bf16 v[116:119], a[36:39], a[76:79], v[116:119]\n"
      "ds_write_addtid_b32 v165 offset:36096\n"
      "v_mfma_f32_16x16x32_bf16 v[120:123], a[40:43], a[76:79], v[120:123]\n"
      "ds_write_addtid_b32 v166 offset:36352\n"
      "v_mfma_f32_16x16x32_bf16 v[124:127], a[44:47], a[76:79], v[124:127]\n"
      "ds_write_addtid_b32 v167 offset:36608\n"
      "v_mfma_f32_16x16x32_bf16 v[128:131], a[48:51], a[76:79], v[128:131]\n"
      "ds_write_addtid_b32 v168 offset:36864\n"
      "v_mfma_f32_16x16x32_bf16 v[132:135], a[52:55], a[76:79], v[132:135]\n"
      "ds_write_addtid_b32 v169 offset:37120\n"
      "v_mfma_f32_16x16x32_bf16 v[136:139], a[56:59], a[76:79], v[136:139]\n"
      "ds_write_addtid_b32 v170 offset:37376\n"
      "v_mfma_f32_16x16x32_bf16 v[140:143], a[60:63], a[76:79], v[140:143]\n"
      "buffer_load_dwordx4 v[40:43], v230, s[60:63], s71 offen offset:1024 sc1\n"
      "v_mfma_f32_16x16x32_bf16 v[112:115], a[224:227], a[80:83], v[112:115]\n"
      "ds_write_addtid_b32 v171 offset:37632\n"
      "v_mfma_f32_16x16x32_bf16 v[116:119], a[228:231], a[80:83], v[116:119]\n"
      "ds_write_addtid_b32 v172 offset:37888\n"
      "v_mfma_f32_16x16x32_bf16 v[120:123], a[232:235], a[80:83], v[120:123]\n"
      "ds_write_addtid_b32 v173 offset:38144\n"
      "v_mfma_f32_16x16x32_bf16 v[124:127], a[236:239], a[80:83], v[124:127]\n"
      "ds_write_addtid_b32 v174 offset:38400\n"
      "v_mfma_f32_16x16x32_bf16 v[128:131], a[240:243], a[80:83], v[128:131]\n"
      "ds_write_addtid_b32 v175 offset:38656\n"
      "v_mfma_f32_16x16x32_bf16 v[132:135], a[244:247], a[80:83], v[132:135]\n"
      "v_mfma_f32_16x16x32_bf16 v[136:139], a[248:251], a[80:83], v[136:139]\n"
      "s_add_u32 s71, s77, 229376\n"
      "v_mfma_f32_16x16x32_bf16 v[140:143], a[252:255], a[80:83], v[140:143]\n"
      "buffer_load_dwordx4 a[76:79], v230, s[60:63], s71 offen offset:0 sc1\n"
      "s_waitcnt vmcnt(14)\n"
      "v_mfma_f32_16x16x32_bf16 v[112:115], a[96:99], a[84:87], v[112:115]\n"
      "s_waitcnt lgkmcnt(0)\n"
      "v_mfma_f32_16x16x32_bf16 v[116:119], a[100:103], a[84:87], v[116:119]\n"
      "s_barrier\n"
      "v_mfma_f32_16x16x32_bf16 v[120:123], a[104:107], a[84:87], v[120:123]\n"
      "ds_read2st64_b32 v[236:237], v218 offset0:128 offset1:129\n"
      "v_mfma_f32_16x16x32_bf16 v[124:127], a[108:111], a[84:87], v[124:127]\n"
      "ds_read2st64_b32 v[238:239], v218 offset0:130 offset1:131\n"
      "v_mfma_f32_16x16x32_bf16 v[128:131], a[112:115], a[84:87], v[128:131]\n"
      "ds_read2st64_b32 v[240:241], v218 offset0:132 offset1:133\n"
      "v_mfma_f32_16x16x32_bf16 v[132:135], a[116:119], a[84:87], v[132:135]\n"
      "ds_read2st64_b32 v[242:243], v218 offset0:134 offset1:135\n"
      "v_mfma_f32_16x16x32_bf16 v[136:139], a[120:123], a[84:87], v[136:139]\n"
      "buffer_load_dwordx4 a[80:83], v230, s[60:63], s71 offen offset:1024 sc1\n"
      "v_mfma_f32_16x16x32_bf16 v[140:143], a[124:127], a[84:87], v[140:143]\n"
      "ds_read2st64_b32 v[244:245], v219 offset0:136 offset1:137\n"
      "v_mfma_f32_16x16x32_bf16 v[112:115], a[128:131], a[88:91], v[112:115]\n"
      "ds_read2st64_b32 v[246:247], v219 offset0:138 offset1:139\n"
      "v_mfma_f32_16x16x32_bf16 v[116:119], a[132:135], a[88:91], v[116:119]\n"
      "ds_read2st64_b32 v[248:249], v219 offset0:140 offset1:141\n"
      "s_waitcnt lgkmcnt(3)\n"
      "v_mfma_f32_16x16x32_bf16 v[120:123], a[136:139], a[88:91], v[120:123]\n"
      "ds_read2st64_b32 v[250:251], v219 offset0:142 offset1:143\n"
      "v_add_f32 v236, v236, v144\n"
      "v_mfma_f32_16x16x32_bf16 v[124:127], a[140:143], a[88:91], v[124:127]\n"
      "s_add_u32 s72, s82, 7\n"
      "ds_read2st64_b32 v[176:177], v220 offset0:144 offset1:145\n"
      "v_mfma_f32_16x16x32_bf16 v[128:131], a[144:147], a[88:91], v[128:131]\n"
      "v_mov_b32 v223, s72\n"
      "ds_read2st64_b32 v[178:179], v220 offset0:146 offset1:147\n"
      "v_mfma_f32_16x16x32_bf16 v[132:135], a[148:151], a[88:91], v[132:135]\n"
      "s_waitcnt vmcnt(12)\n"
      "ds_read2st64_b32 v[212:213], v220 offset0:148 offset1:149\n"
      "v_mfma_f32_16x16x32_bf16 v[136:139], a[152:155], a[88:91], v[136:139]\n"
      "buffer_store_dword v223, v231, s[60:63], s91 offen sc1\n"
      "v_add_f32_e64 v237, v237, v145\n"
      "v_mfma_f32_16x16x32_bf16 v[140:143], a[156:159], a[88:91], v[140:143]\n"
      "ds_read2st64_b32 v[214:215], v220 offset0:150 offset1:151\n"
      "s_waitcnt vmcnt(14)\n"
      "v_mfma_f32_16x16x32_bf16 v[112:115], v[180:183], v[28:31], v[112:115]\n"
      "ds_read2st64_b32 a[84:85], v228 offset0:0 offset1:1\n"
      "v_add_f32 v238, v238, v146\n"
      "v_mfma_f32_16x16x32_bf16 v[116:119], v[184:187], v[28:31], v[116:119]\n"
      "ds_read2st64_b32 a[86:87], v228 offset0:2 offset1:3\n"
      "v_add_f32_e64 v239, v239, v147\n"
      "v_mfma_f32_16x16x32_bf16 v[120:123], v[188:191], v[28:31], v[120:123]\n"
      "ds_read2st64_b32 v[56:57], v228 offset0:192 offset1:193\n"
      "v_add_f32 v240, v240, v148\n"
      "v_mfma_f32_16x16x32_bf16 v[124:127], v[192:195], v[28:31], v[124:127]\n"
      "ds_read2st64_b32 v[58:59], v228 offset0:194 offset1:195\n"
      "v_add_f32 v241, v241, v149\n"
      "v_mfma_f32_16x16x32_bf16 v[128:131], v[196:199], v[28:31], v[128:131]\n"
      "ds_write_addtid_b32 v74 offset:23040\n"
      "v_add_f32_e64 v242, v242, v150\n"
      "v_mfma_f32_16x16x32_bf16 v[132:135], v[200:203], v[28:31], v[132:135]\n"
      "ds_write_addtid_b32 v75 offset:23296\n"
      "s_add_u32 s71, s78, 229376\n"
      "v_mfma_f32_16x16x32_bf16 v[136:139], v[204:207], v[28:31], v[136:139]\n"
      "buffer_load_dwordx4 a[88:91], v230, s[60:63], s71 offen offset:0 sc1\n"
      "v_add_f32 v243, v243, v151\n"
      "s_waitcnt lgkmcnt(6)\n"
      "v_mfma_f32_16x16x32_bf16 v[140:143], v[208:211], v[28:31], v[140:143]\n"
      "ds_write_addtid_b32 v76 offset:23552\n"
      "v_add_f32_e64 v236, v236, v244\n"
      "v_mfma_f32_16x16x32_bf16 v[112:115], a[192:195], v[16:19], v[112:115]\n"
      "ds_write_addtid_b32 v77 offset:23808\n"
      "v_add_f32_e64 v237, v237, v245\n"
      "v_mfma_f32_16x16x32_bf16 v[116:119], a[196:199], v[16:19], v[116:119]\n"
      "ds_write_addtid_b32 v78 offset:24064\n"
      "v_add_f32 v238, v238, v246\n"
      "v_mfma_f32_16x16x32_bf16 v[120:123], a[200:203], v[16:19], v[120:123]\n"
      "ds_write_addtid_b32 v79 offset:24320\n"
      "v_add_f32 v239, v239, v247\n"
      "v_mfma_f32_16x16x32_bf16 v[124:127], a[204:207], v[16:19], v[124:127]\n"
      "v_add_f32 v240, v240, v248\n"
      "v_add_f32 v241, v241, v249\n"
      "v_mfma_f32_16x16x32_bf16 v[128:131], a[208:211], v[16:19], v[128:131]\n"
      "v_add_f32 v242, v242, v250\n"
      "v_add_f32 v243, v243, v251\n"
      "v_mfma_f32_16x16x32_bf16 v[132:135], a[212:215], v[16:19], v[132:135]\n"
      "v_add_f32 v236, v236, v176\n"
      "v_add_f32 v237, v237, v177\n"
      "v_mfma_f32_16x16x32_bf16 v[136:139], a[216:219], v[16:19], v[136:139]\n"
      "buffer_load_dwordx4 v[60:63], v230, s[60:63], s71 offen offset:1024 sc1\n"
      "v_add_f32 v238, v238, v178\n"
      "v_mfma_f32_16x16x32_bf16 v[140:143], a[220:223], v[16:19], v[140:143]\n"
      "v_add_f32 v239, v239, v179\n"
      "v_add_f32 v240, v240, v212\n"
      "s_waitcnt lgkmcnt(6)\n"
      "v_mfma_f32_16x16x32_bf16 v[144:147], a[160:163], a[84:87], 0\n"
      "ds_write_addtid_b32 v80 offset:6144\n"
      "v_add_f32 v241, v241, v213\n"
      "v_mfma_f32_16x16x32_bf16 v[148:151], a[164:167], a[84:87], 0\n"
      "ds_write_addtid_b32 v81 offset:6400\n"
      "v_add_f32 v242, v242, v214\n"
      "v_mfma_f32_16x16x32_bf16 v[152:155], a[168:171], a[84:87], 0\n"
      "ds_write_addtid_b32 v120 offset:0\n"
      "v_add_f32_e64 v243, v243, v215\n"
      "v_mfma_f32_16x16x32_bf16 v[156:159], a[172:175], a[84:87], 0\n"
      "ds_write_addtid_b32 v121 offset:256\n"
      "v_cvt_pk_bf16_f32 v24, v236, v237\n"
      "v_mfma_f32_16x16x32_bf16 v[160:163], a[176:179], a[84:87], 0\n"
      "ds_write_addtid_b32 v122 offset:512\n"
      "v_cvt_pk_bf16_f32 v25, v238, v239\n"
      "v_mfma_f32_16x16x32_bf16 v[164:167], a[180:183], a[84:87], 0\n"
      "ds_write_addtid_b32 v123 offset:768\n"
      "v_cvt_pk_bf16_f32 v26, v240, v241\n"
      "v_mfma_f32_16x16x32_bf16 v[168:171], a[184:187], a[84:87], 0\n"
      "ds_write_addtid_b32 v124 offset:1024\n"
      "v_cvt_pk_bf16_f32 v27, v242, v243\n"
      "v_mfma_f32_16x16x32_bf16 v[172:175], a[188:191], a[84:87], 0\n"
      "ds_write_addtid_b32 v125 offset:1280\n"
      "s_add_u32 s71, s76, 262144\n"
      "v_mfma_f32_16x16x32_bf16 v[144:147], a[0:3], v[56:59], v[144:147]\n"
      "buffer_load_dwordx4 v[4:7], v230, s[60:63], s71 offen offset:0 sc1\n"
      "v_min_u32 v24, -2, v24\n"
      "v_mfma_f32_16x16x32_bf16 v[148:151], a[4:7], v[56:59], v[148:151]\n"
      "ds_write_addtid_b32 v126 offset:1536\n"
      "v_min_u32 v25, -2, v25\n"
      "v_mfma_f32_16x16x32_bf16 v[152:155], a[8:11], v[56:59], v[152:155]\n"
      "ds_write_addtid_b32 v127 offset:1792\n"
      "v_min_u32 v26, -2, v26\n"
      "v_mfma_f32_16x16x32_bf16 v[156:159], a[12:15], v[56:59], v[156:159]\n"
      "ds_write_addtid_b32 v128 offset:2048\n"
      "v_min_u32 v27, -2, v27\n"
      "v_mfma_f32_16x16x32_bf16 v[160:163], a[16:19], v[56:59], v[160:163]\n"
      "ds_write_addtid_b32 v129 offset:2304\n"
      "v_mfma_f32_16x16x32_bf16 v[164:167], a[20:23], v[56:59], v[164:167]\n"
      "ds_write_addtid_b32 v130 offset:2560\n"
      "v_mfma_f32_16x16x32_bf16 v[168:171], a[24:27], v[56:59], v[168:171]\n"
      "s_bfe_u64 s[66:67], %[ttab], 262160\n"
      "ds_write_addtid_b32 v131 offset:2816\n"
      "v_mfma_f32_16x16x32_bf16 v[172:175], a[28:31], v[56:59], v[172:175]\n"
      "s_lshl_b32 s64, s66, 14\n"
      "s_waitcnt vmcnt(13)\n"
      "v_mfma_f32_16x16x32_bf16 v[144:147], a[32:35], a[72:75], v[144:147]\n"
      "s_add_u32 s64, s64, s89\n"
      "ds_write_addtid_b32 v132 offset:3072\n"
      "v_mfma_f32_16x16x32_bf16 v[148:151], a[36:39], a[72:75], v[148:151]\n"
      "buffer_store_dwordx4 v[24:27], v230, s[60:63], s64 offen sc1\n"
      "v_mfma_f32_16x16x32_bf16 v[152:155], a[40:43], a[72:75], v[152:155]\n"
      "ds_write_addtid_b32 v133 offset:3328\n"
      "v_mfma_f32_16x16x32_bf16 v[156:159], a[44:47], a[72:75], v[156:159]\n"
      "ds_write_addtid_b32 v134 offset:3584\n"
      "v_mfma_f32_16x16x32_bf16 v[160:163], a[48:51], a[72:75], v[160:163]\n"
      "ds_write_addtid_b32 v135 offset:3840\n"
      "v_mfma_f32_16x16x32_bf16 v[164:167], a[52:55], a[72:75], v[164:167]\n"
      "ds_write_addtid_b32 v136 offset:4096\n"
      "v_mfma_f32_16x16x32_bf16 v[168:171], a[56:59], a[72:75], v[168:171]\n"
      "ds_write_addtid_b32 v137 offset:4352\n"
      "v_mfma_f32_16x16x32_bf16 v[172:175], a[60:63], a[72:75], v[172:175]\n"
      "ds_write_addtid_b32 v138 offset:4608\n"
      "v_mfma_f32_16x16x32_bf16 v[144:147], a[224:227], a[92:95], v[144:147]\n"
      "ds_write_addtid_b32 v139 offset:4864\n"
      "v_mfma_f32_16x16x32_bf16 v[148:151], a[228:231], a[92:95], v[148:151]\n"
      "ds_write_addtid_b32 v140 offset:5120\n"
      "v_mfma_f32_16x16x32_bf16 v[152:155], a[232:235], a[92:95], v[152:155]\n"
      "buffer_load_dwordx4 a[84:87], v230, s[60:63], s71 offen offset:1024 sc1\n"
      "v_mfma_f32_16x16x32_bf16 v[156:159], a[236:239], a[92:95], v[156:159]\n"
      "ds_write_addtid_b32 v141 offset:5376\n"
      "v_mfma_f32_16x16x32_bf16 v[160:163], a[240:243], a[92:95], v[160:163]\n"
      "ds_write_addtid_b32 v142 offset:5632\n"
      "v_mfma_f32_16x16x32_bf16 v[164:167], a[244:247], a[92:95], v[164:167]\n"
      "ds_write_addtid_b32 v143 offset:5888\n"
      "v_mfma_f32_16x16x32_bf16 v[168:171], a[248:251], a[92:95], v[168:171]\n"
      "v_mfma_f32_16x16x32_bf16 v[172:175], a[252:255], a[92:95], v[172:175]\n"
      "s_waitcnt lgkmcnt(0)\n"
      "s_waitcnt vmcnt(13)\n"
      "v_mfma_f32_16x16x32_bf16 v[144:147], a[96:99], v[48:51], v[144:147]\n"
      "s_barrier\n"
      "v_mfma_f32_16x16x32_bf16 v[148:151], a[100:103], v[48:51], v[148:151]\n"
      "ds_read2st64_b32 v[236:237], v218 offset0:0 offset1:1\n"
      "v_mfma_f32_16x16x32_bf16 v[152:155], a[104:107], v[48:51], v[152:155]\n"
      "ds_read2st64_b32 v[238:239], v218 offset0:2 offset1:3\n"
      "s_add_u32 s71, s77, 262144\n"
      "v_mfma_f32_16x16x32_bf16 v[156:159], a[108:111], v[48:51], v[156:159]\n"
      "buffer_load_dwordx4 a[72:75], v230, s[60:63], s71 offen offset:0 sc1\n"
      "v_mfma_f32_16x16x32_bf16 v[160:163], a[112:115], v[48:51], v[160:163]\n"
      "ds_read2st64_b32 v[240:241], v218 offset0:4 offset1:5\n"
      "v_mfma_f32_16x16x32_bf16 v[164:167], a[116:119], v[48:51], v[164:167]\n"
      "ds_read2st64_b32 v[242:243], v218 offset0:6 offset1:7\n"
      "v_mfma_f32_16x16x32_bf16 v[168:171], a[120:123], v[48:51], v[168:171]\n"
      "ds_read2st64_b32 v[244:245], v219 offset0:8 offset1:9\n"
      "v_mfma_f32_16x16x32_bf16 v[172:175], a[124:127], v[48:51], v[172:175]\n"
      "ds_read2st64_b32 v[246:247], v219 offset0:10 offset1:11\n"
      "v_mfma_f32_16x16x32_bf16 v[144:147], a[128:131], v[52:55], v[144:147]\n"
      "ds_read2st64_b32 v[248:249], v219 offset0:12 offset1:13\n"
      "v_mfma_f32_16x16x32_bf16 v[148:151], a[132:135], v[52:55], v[148:151]\n"
      "ds_read2st64_b32 v[250:251], v219 offset0:14 offset1:15\n"
      "s_waitcnt lgkmcnt(4)\n"
      "v_mfma_f32_16x16x32_bf16 v[152:155], a[136:139], v[52:55], v[152:155]\n"
      "ds_read2st64_b32 v[176:177], v220 offset0:16 offset1:17\n"
      "v_add_f32 v236, v236, v112\n"
      "v_mfma_f32_16x16x32_bf16 v[156:159], a[140:143], v[52:55], v[156:159]\n"
      "ds_read2st64_b32 v[178:179], v220 offset0:18 offset1:19\n"
      "v_add_f32_e64 v237, v237, v113\n"
      "v_mfma_f32_16x16x32_bf16 v[160:163], a[144:147], v[52:55], v[160:163]\n"
      "buffer_load_dwordx4 a[92:95], v230, s[60:63], s71 offen offset:1024 sc1\n"
      "v_add_f32 v238, v238, v114\n"
      "v_mfma_f32_16x16x32_bf16 v[164:167], a[148:151], v[52:55], v[164:167]\n"
      "ds_read2st64_b32 v[212:213], v220 offset0:20 offset1:21\n"
      "v_add_f32 v239, v239, v115\n"
      "v_mfma_f32_16x16x32_bf16 v[168:171], a[152:155], v[52:55], v[168:171]\n"
      "ds_read2st64_b32 v[214:215], v220 offset0:22 offset1:23\n"
      "v_add_f32_e64 v240, v240, v116\n"
      "v_mfma_f32_16x16x32_bf16 v[172:175], a[156:159], v[52:55], v[172:175]\n"
      "ds_read2st64_b32 v[8:9], v227 offset0:0 offset1:1\n"
      "s_waitcnt vmcnt(13)\n"
      "v_mfma_f32_16x16x32_bf16 v[144:147], v[180:183], v[44:47], v[144:147]\n"
      "ds_read2st64_b32 v[10:11], v227 offset0:2 offset1:3\n"
      "v_add_f32 v241, v241, v117\n"
      "v_mfma_f32_16x16x32_bf16 v[148:151], v[184:187], v[44:47], v[148:151]\n"
      "ds_read2st64_b32 v[12:13], v227 offset0:192 offset1:193\n"
      "v_add_f32_e64 v242, v242, v118\n"
      "v_mfma_f32_16x16x32_bf16 v[152:155], v[188:191], v[44:47], v[152:155]\n"
      "ds_read2st64_b32 v[14:15], v227 offset0:194 offset1:195\n"
      "v_add_f32 v243, v243, v119\n"
      "s_waitcnt lgkmcnt(4)\n"
      "v_mfma_f32_16x16x32_bf16 v[156:159], v[192:195], v[44:47], v[156:159]\n"
      "ds_write_addtid_b32 v82 offset:6656\n"
      "v_add_f32_e64 v236, v236, v244\n"
      "v_mfma_f32_16x16x32_bf16 v[160:163], v[196:199], v[44:47], v[160:163]\n"
      "ds_write_addtid_b32 v83 offset:6912\n"
      "s_add_u32 s71, s78, 262144\n"
      "v_mfma_f32_16x16x32_bf16 v[164:167], v[200:203], v[44:47], v[164:167]\n"
      "buffer_load_dwordx4 v[0:3], v230, s[60:63], s71 offen offset:0 sc1\n"
      "v_add_f32_e64 v237, v237, v245\n"
      "v_mfma_f32_16x16x32_bf16 v[168:171], v[204:207], v[44:47], v[168:171]\n"
      "ds_write_addtid_b32 v84 offset:7168\n"
      "v_add_f32 v238, v238, v246\n"
      "v_mfma_f32_16x16x32_bf16 v[172:175], v[208:211], v[44:47], v[172:175]\n"
      "ds_write_addtid_b32 v85 offset:7424\n"
      "v_add_f32 v239, v239, v247\n"
      "v_mfma_f32_16x16x32_bf16 v[144:147], a[192:195], a[64:67], v[144:147]\n"
      "ds_write_addtid_b32 v86 offset:7680\n"
      "v_add_f32_e64 v240, v240, v248\n"
      "v_mfma_f32_16x16x32_bf16 v[148:151], a[196:199], a[64:67], v[148:151]\n"
      "ds_write_addtid_b32 v87 offset:7936\n"
      "v_add_f32 v241, v241, v249\n"
      "v_mfma_f32_16x16x32_bf16 v[152:155], a[200:203], a[64:67], v[152:155]\n"
      "v_add_f32 v242, v242, v250\n"
      "v_add_f32_e64 v243, v243, v251\n"
      "v_mfma_f32_16x16x32_bf16 v[156:159], a[204:207], a[64:67], v[156:159]\n"
      "v_add_f32 v236, v236, v176\n"
      "v_add_f32 v237, v237, v177\n"
      "v_mfma_f32_16x16x32_bf16 v[160:163], a[208:211], a[64:67], v[160:163]\n"
      "v_add_f32 v238, v238, v178\n"
      "v_add_f32 v239, v239, v179\n"
      "v_mfma_f32_16x16x32_bf16 v[164:167], a[212:215], a[64:67], v[164:167]\n"
      "v_add_f32 v240, v240, v212\n"
      "v_add_f32 v241, v241, v213\n"
      "v_mfma_f32_16x16x32_bf16 v[168:171], a[216:219], a[64:67], v[168:171]\n"
      "buffer_load_dwordx4 v[64:67], v230, s[60:63], s71 offen offset:1024 sc1\n"
      "v_add_f32 v242, v242, v214\n"
      "v_mfma_f32_16x16x32_bf16 v[172:175], a[220:223], a[64:67], v[172:175]\n"
      "v_add_f32_e64 v243, v243, v215\n"
      "s_waitcnt lgkmcnt(6)\n"
      "v_mfma_f32_16x16x32_bf16 v[112:115], a[160:163], v[8:11], 0\n"
      "ds_write_addtid_b32 v88 offset:38912\n"
      "v_cvt_pk_bf16_f32 v68, v236, v237\n"
      "v_mfma_f32_16x16x32_bf16 v[116:119], a[164:167], v[8:11], 0\n"
      "ds_write_addtid_b32 v89 offset:39168\n"
      "v_cvt_pk_bf16_f32 v69, v238, v239\n"
      "v_mfma_f32_16x16x32_bf16 v[120:123], a[168:171], v[8:11], 0\n"
      "ds_write_addtid_b32 v152 offset:32768\n"
      "v_cvt_pk_bf16_f32 v70, v240, v241\n"
      "v_mfma_f32_16x16x32_bf16 v[124:127], a[172:175], v[8:11], 0\n"
      "ds_write_addtid_b32 v153 offset:33024\n"
      "v_cvt_pk_bf16_f32 v71, v242, v243\n"
      "v_mfma_f32_16x16x32_bf16 v[128:131], a[176:179], v[8:11], 0\n"
      "ds_write_addtid_b32 v154 offset:33280\n"
      "v_min_u32 v68, -2, v68\n"
      "v_mfma_f32_16x16x32_bf16 v[132:135], a[180:183], v[8:11], 0\n"
      "ds_write_addtid_b32 v155 offset:33536\n"
      "v_min_u32 v69, -2, v69\n"
      "v_mfma_f32_16x16x32_bf16 v[136:139], a[184:187], v[8:11], 0\n"
      "ds_write_addtid_b32 v156 offset:33792\n"
      "v_min_u32_e64 v70, -2, v70\n"
      "v_mfma_f32_16x16x32_bf16 v[140:143], a[188:191], v[8:11], 0\n"
      "ds_write_addtid_b32 v157 offset:34048\n"
      "v_min_u32 v71, -2, v71\n"
      "v_mfma_f32_16x16x32_bf16 v[112:115], a[0:3], v[12:15], v[112:115]\n"
      "buffer_load_dword v254, v226, s[60:63], s92 offen sc1\n"
      "v_mfma_f32_16x16x32_bf16 v[116:119], a[4:7], v[12:15], v[116:119]\n"
      "ds_write_addtid_b32 v158 offset:34304\n"
      "v_mfma_f32_16x16x32_bf16 v[120:123], a[8:11], v[12:15], v[120:123]\n"
      "ds_write_addtid_b32 v159 offset:34560\n"
      "v_mfma_f32_16x16x32_bf16 v[124:127], a[12:15], v[12:15], v[124:127]\n"
      "ds_write_addtid_b32 v160 offset:34816\n"
      "v_mfma_f32_16x16x32_bf16 v[128:131], a[16:19], v[12:15], v[128:131]\n"
      "s_bfe_u64 s[66:67], %[ttab], 262164\n"
      "ds_write_addtid_b32 v161 offset:35072\n"
      "v_mfma_f32_16x16x32_bf16 v[132:135], a[20:23], v[12:15], v[132:135]\n"
      "s_lshl_b32 s64, s66, 14\n"
      "ds_write_addtid_b32 v162 offset:35328\n"
      "v_mfma_f32_16x16x32_bf16 v[136:139], a[24:27], v[12:15], v[136:139]\n"
      "s_add_u32 s64, s64, s89\n"
      "ds_write_addtid_b32 v163 offset:35584\n"
      "v_mfma_f32_16x16x32_bf16 v[140:143], a[28:31], v[12:15], v[140:143]\n"
      "buffer_store_dwordx4 v[68:71], v230, s[60:63], s64 offen sc1\n"
      "s_waitcnt vmcnt(14)\n"
      "v_mfma_f32_16x16x32_bf16 v[112:115], a[32:35], a[68:71], v[112:115]\n"
      "ds_write_addtid_b32 v164 offset:35840\n"
      "v_mfma_f32_16x16x32_bf16 v[116:119], a[36:39], a[68:71], v[116:119]\n"
      "ds_write_addtid_b32 v165 offset:36096\n"
      "v_mfma_f32_16x16x32_bf16 v[120:123], a[40:43], a[68:71], v[120:123]\n"
      "ds_write_addtid_b32 v166 offset:36352\n"
      "v_mfma_f32_16x16x32_bf16 v[124:127], a[44:47], a[68:71], v[124:127]\n"
      "ds_write_addtid_b32 v167 offset:36608\n"
      "v_mfma_f32_16x16x32_bf16 v[128:131], a[48:51], a[68:71], v[128:131]\n"
      "ds_write_addtid_b32 v168 offset:36864\n"
      "v_mfma_f32_16x16x32_bf16 v[132:135], a[52:55], a[68:71], v[132:135]\n"
      "ds_write_addtid_b32 v169 offset:37120\n"
      "v_mfma_f32_16x16x32_bf16 v[136:139], a[56:59], a[68:71], v[136:139]\n"
      "ds_write_addtid_b32 v170 offset:37376\n"
      "s_add_u32 s71, s76, 294912\n"
      "v_mfma_f32_16x16x32_bf16 v[140:143], a[60:63], a[68:71], v[140:143]\n"
      "buffer_load_dwordx4 a[64:67], v230, s[60:63], s71 offen offset:0 sc1\n"
      "v_mfma_f32_16x16x32_bf16 v[112:115], a[224:227], v[40:43], v[112:115]\n"
      "ds_write_addtid_b32 v171 offset:37632\n"
      "v_mfma_f32_16x16x32_bf16 v[116:119], a[228:231], v[40:43], v[116:119]\n"
      "ds_write_addtid_b32 v172 offset:37888\n"
      "v_mfma_f32_16x16x32_bf16 v[120:123], a[232:235], v[40:43], v[120:123]\n"
      "ds_write_addtid_b32 v173 offset:38144\n"
      "v_mfma_f32_16x16x32_bf16 v[124:127], a[236:239], v[40:43], v[124:127]\n"
      "ds_write_addtid_b32 v174 offset:38400\n"
      "v_mfma_f32_16x16x32_bf16 v[128:131], a[240:243], v[40:43], v[128:131]\n"
      "ds_write_addtid_b32 v175 offset:38656\n"
      "v_mfma_f32_16x16x32_bf16 v[132:135], a[244:247], v[40:43], v[132:135]\n"
      "v_mfma_f32_16x16x32_bf16 v[136:139], a[248:251], v[40:43], v[136:139]\n"
      "v_mfma_f32_16x16x32_bf16 v[140:143], a[252:255], v[40:43], v[140:143]\n"
      "buffer_load_dwordx4 a[68:71], v230, s[60:63], s71 offen offset:1024 sc1\n"
      "s_waitcnt vmcnt(14)\n"
      "v_mfma_f32_16x16x32_bf16 v[112:115], a[96:99], a[76:79], v[112:115]\n"
      "s_waitcnt lgkmcnt(0)\n"
      "v_mfma_f32_16x16x32_bf16 v[116:119], a[100:103], a[76:79], v[116:119]\n"
      "s_barrier\n"
      "v_mfma_f32_16x16x32_bf16 v[120:123], a[104:107], a[76:79], v[120:123]\n"
      "ds_read2st64_b32 v[236:237], v218 offset0:128 offset1:129\n"
      "v_mfma_f32_16x16x32_bf16 v[124:127], a[108:111], a[76:79], v[124:127]\n"
      "ds_read2st64_b32 v[238:239], v218 offset0:130 offset1:131\n"
      "v_mfma_f32_16x16x32_bf16 v[128:131], a[112:115], a[76:79], v[128:131]\n"
      "ds_read2st64_b32 v[240:241], v218 offset0:132 offset1:133\n"
      "v_mfma_f32_16x16x32_bf16 v[132:135], a[116:119], a[76:79], v[132:135]\n"
      "ds_read2st64_b32 v[242:243], v218 offset0:134 offset1:135\n"
      "s_add_u32 s71, s77, 294912\n"
      "v_mfma_f32_16x16x32_bf16 v[136:139], a[120:123], a[76:79], v[136:139]\n"
      "buffer_load_dwordx4 v[32:35], v230, s[60:63], s71 offen offset:0 sc1\n"
      "v_mfma_f32_16x16x32_bf16 v[140:143], a[124:127], a[76:79], v[140:143]\n"
      "ds_read2st64_b32 v[244:245], v219 offset0:136 offset1:137\n"
      "v_mfma_f32_16x16x32_bf16 v[112:115], a[128:131], a[80:83], v[112:115]\n"
      "ds_read2st64_b32 v[246:247], v219 offset0:138 offset1:139\n"
      "v_mfma_f32_16x16x32_bf16 v[116:119], a[132:135], a[80:83], v[116:119]\n"
      "ds_read2st64_b32 v[248:249], v219 offset0:140 offset1:141\n"
      "s_waitcnt lgkmcnt(3)\n"
      "v_mfma_f32_16x16x32_bf16 v[120:123], a[136:139], a[80:83], v[120:123]\n"
      "ds_read2st64_b32 v[250:251], v219 offset0:142 offset1:143\n"
      "v_add_f32_e64 v236, v236, v144\n"
      "v_mfma_f32_16x16x32_bf16 v[124:127], a[140:143], a[80:83], v[124:127]\n"
      "ds_read2st64_b32 v[176:177], v220 offset0:144 offset1:145\n"
      "v_add_f32 v237, v237, v145\n"
      "v_mfma_f32_16x16x32_bf16 v[128:131], a[144:147], a[80:83], v[128:131]\n"
      "ds_read2st64_b32 v[178:179], v220 offset0:146 offset1:147\n"
      "v_add_f32 v238, v238, v146\n"
      "v_mfma_f32_16x16x32_bf16 v[132:135], a[148:151], a[80:83], v[132:135]\n"
      "ds_read2st64_b32 v[212:213], v220 offset0:148 offset1:149\n"
      "v_add_f32_e64 v239, v239, v147\n"
      "v_mfma_f32_16x16x32_bf16 v[136:139], a[152:155], a[80:83], v[136:139]\n"
      "buffer_load_dwordx4 a[76:79], v230, s[60:63], s71 offen offset:1024 sc1\n"
      "v_add_f32 v240, v240, v148\n"
      "v_mfma_f32_16x16x32_bf16 v[140:143], a[156:159], a[80:83], v[140:143]\n"
      "ds_read2st64_b32 v[214:215], v220 offset0:150 offset1:151\n"
      "s_waitcnt vmcnt(13)\n"
      "v_mfma_f32_16x16x32_bf16 v[112:115], v[180:183], a[88:91], v[112:115]\n"
      "ds_read2st64_b32 v[36:37], v228 offset0:16 offset1:17\n"
      "v_add_f32_e64 v241, v241, v149\n"
      "v_mfma_f32_16x16x32_bf16 v[116:119], v[184:187], a[88:91], v[116:119]\n"
      "ds_read2st64_b32 v[38:39], v228 offset0:18 offset1:19\n"
      "v_add_f32_e64 v242, v242, v150\n"
      "v_mfma_f32_16x16x32_bf16 v[120:123], v[188:191], a[88:91], v[120:123]\n"
      "ds_read2st64_b32 v[28:29], v228 offset0:208 offset1:209\n"
      "v_add_f32 v243, v243, v151\n"
      "v_mfma_f32_16x16x32_bf16 v[124:127], v[192:195], a[88:91], v[124:127]\n"
      "ds_read2st64_b32 v[30:31], v228 offset0:210 offset1:211\n"
      "s_waitcnt lgkmcnt(4)\n"
      "v_mfma_f32_16x16x32_bf16 v[128:131], v[196:199], a[88:91], v[128:131]\n"
      "ds_write_addtid_b32 v90 offset:39424\n"
      "v_add_f32_e64 v236, v236, v244\n"
      "v_mfma_f32_16x16x32_bf16 v[132:135], v[200:203], a[88:91], v[132:135]\n"
      "ds_write_addtid_b32 v91 offset:39680\n"
      "s_add_u32 s71, s78, 294912\n"
      "v_mfma_f32_16x16x32_bf16 v[136:139], v[204:207], a[88:91], v[136:139]\n"
      "buffer_load_dwordx4 a[80:83], v230, s[60:63], s71 offen offset:0 sc1\n"
      "v_add_f32_e64 v237, v237, v245\n"
      "v_mfma_f32_16x16x32_bf16 v[140:143], v[208:211], a[88:91], v[140:143]\n"
      "ds_write_addtid_b32 v92 offset:39936\n"
      "v_add_f32_e64 v238, v238, v246\n"
      "v_mfma_f32_16x16x32_bf16 v[112:115], a[192:195], v[60:63], v[112:115]\n"
      "ds_write_addtid_b32 v93 offset:40192\n"
      "v_add_f32 v239, v239, v247\n"
      "v_mfma_f32_16x16x32_bf16 v[116:119], a[196:199], v[60:63], v[116:119]\n"
      "ds_write_addtid_b32 v94 offset:40448\n"
      "v_add_f32 v240, v240, v248\n"
      "v_mfma_f32_16x16x32_bf16 v[120:123], a[200:203], v[60:63], v[120:123]\n"
      "ds_write_addtid_b32 v95 offset:40704\n"
      "v_add_f32 v241, v241, v249\n"
      "v_mfma_f32_16x16x32_bf16 v[124:127], a[204:207], v[60:63], v[124:127]\n"
      "v_add_f32 v242, v242, v250\n"
      "v_add_f32 v243, v243, v251\n"
      "v_mfma_f32_16x16x32_bf16 v[128:131], a[208:211], v[60:63], v[128:131]\n"
      "s_waitcnt vmcnt(6)\n"
      "s_add_u32 s97, s82, 7\n"
      "v_add_f32 v236, v236, v176\n"
      "v_mfma_f32_16x16x32_bf16 v[132:135], a[212:215], v[60:63], v[132:135]\n"
      "v_subrev_u32 v254, s97, v254\n"
      "v_cmp_lt_u32_e64 vcc, 0, v254\n"
      "v_add_f32_e64 v237, v237, v177\n"
      "v_mfma_f32_16x16x32_bf16 v[136:139], a[216:219], v[60:63], v[136:139]\n"
      "s_cbranch_vccnz PD6_%=\n"
      "PD6R_%=:\n"
      "buffer_load_dwordx4 a[88:91], v230, s[60:63], s71 offen offset:1024 sc1\n"
      "v_mfma_f32_16x16x32_bf16 v[140:143], a[220:223], v[60:63], v[140:143]\n"
      "v_add_f32 v238, v238, v178\n"
      "v_add_f32_e64 v239, v239, v179\n"
      "s_waitcnt lgkmcnt(6)\n"
      "v_mfma_f32_16x16x32_bf16 v[144:147], a[160:163], v[36:39], 0\n"
      "ds_write_addtid_b32 v96 offset:16384\n"
      "v_add_f32_e64 v240, v240, v212\n"
      "v_mfma_f32_16x16x32_bf16 v[148:151], a[164:167], v[36:39], 0\n"
      "ds_write_addtid_b32 v97 offset:16640\n"
      "v_add_f32_e64 v241, v241, v213\n"
      "v_mfma_f32_16x16x32_bf16 v[152:155], a[168:171], v[36:39], 0\n"
      "ds_write_addtid_b32 v120 offset:0\n"
      "v_add_f32_e64 v242, v242, v214\n"
      "v_mfma_f32_16x16x32_bf16 v[156:159], a[172:175], v[36:39], 0\n"
      "ds_write_addtid_b32 v121 offset:256\n"
      "v_add_f32_e64 v243, v243, v215\n"
      "v_mfma_f32_16x16x32_bf16 v[160:163], a[176:179], v[36:39], 0\n"
      "ds_write_addtid_b32 v122 offset:512\n"
      "v_cvt_pk_bf16_f32 v16, v236, v237\n"
      "v_mfma_f32_16x16x32_bf16 v[164:167], a[180:183], v[36:39], 0\n"
      "ds_write_addtid_b32 v123 offset:768\n"
      "v_cvt_pk_bf16_f32 v17, v238, v239\n"
      "v_mfma_f32_16x16x32_bf16 v[168:171], a[184:187], v[36:39], 0\n"
      "ds_write_addtid_b32 v124 offset:1024\n"
      "v_cvt_pk_bf16_f32 v18, v240, v241\n"
      "v_mfma_f32_16x16x32_bf16 v[172:175], a[188:191], v[36:39], 0\n"
      "ds_write_addtid_b32 v125 offset:1280\n"
      "s_add_u32 s71, s76, 327680\n"
      "v_mfma_f32_16x16x32_bf16 v[144:147], a[0:3], v[28:31], v[144:147]\n"
      "buffer_load_dwordx4 v[20:23], v230, s[60:63], s71 offen offset:0 sc1\n"
      "v_cvt_pk_bf16_f32 v19, v242, v243\n"
      "v_mfma_f32_16x16x32_bf16 v[148:151], a[4:7], v[28:31], v[148:151]\n"
      "ds_write_addtid_b32 v126 offset:1536\n"
      "v_min_u32_e64 v16, -2, v16\n"
      "v_mfma_f32_16x16x32_bf16 v[152:155], a[8:11], v[28:31], v[152:155]\n"
      "ds_write_addtid_b32 v127 offset:1792\n"
      "v_min_u32_e64 v17, -2, v17\n"
      "v_mfma_f32_16x16x32_bf16 v[156:159], a[12:15], v[28:31], v[156:159]\n"
      "ds_write_addtid_b32 v128 offset:2048\n"
      "v_min_u32_e64 v18, -2, v18\n"
      "v_mfma_f32_16x16x32_bf16 v[160:163], a[16:19], v[28:31], v[160:163]\n"
      "s_bfe_u64 s[66:67], %[ttab], 262168\n"
      "ds_write_addtid_b32 v129 offset:2304\n"
      "v_mfma_f32_16x16x32_bf16 v[164:167], a[20:23], v[28:31], v[164:167]\n"
      "s_lshl_b32 s64, s66, 14\n"
      "ds_write_addtid_b32 v130 offset:2560\n"
      "v_mfma_f32_16x16x32_bf16 v[168:171], a[24:27], v[28:31], v[168:171]\n"
      "s_add_u32 s64, s64, s89\n"
      "ds_write_addtid_b32 v131 offset:2816\n"
      "v_mfma_f32_16x16x32_bf16 v[172:175], a[28:31], v[28:31], v[172:175]\n"
      "buffer_store_dwordx4 v[16:19], v230, s[60:63], s64 offen sc1\n"
      "s_waitcnt vmcnt(14)\n"
      "v_mfma_f32_16x16x32_bf16 v[144:147], a[32:35], v[4:7], v[144:147]\n"
      "ds_write_addtid_b32 v132 offset:3072\n"
      "v_min_u32_e64 v19, -2, v19\n"
      "v_mfma_f32_16x16x32_bf16 v[148:151], a[36:39], v[4:7], v[148:151]\n"
      "ds_write_addtid_b32 v133 offset:3328\n"
      "s_bfe_u64 s[84:85], %[ttab], 262184\n"
      "v_mfma_f32_16x16x32_bf16 v[152:155], a[40:43], v[4:7], v[152:155]\n"
      "ds_write_addtid_b32 v134 offset:3584\n"
      "s_lshl_b32 s79, s84, 14\n"
      "v_mfma_f32_16x16x32_bf16 v[156:159], a[44:47], v[4:7], v[156:159]\n"
      "ds_write_addtid_b32 v135 offset:3840\n"
      "v_mfma_f32_16x16x32_bf16 v[160:163], a[48:51], v[4:7], v[160:163]\n"
      "ds_write_addtid_b32 v136 offset:4096\n"
      "v_mfma_f32_16x16x32_bf16 v[164:167], a[52:55], v[4:7], v[164:167]\n"
      "ds_write_addtid_b32 v137 offset:4352\n"
      "v_mfma_f32_16x16x32_bf16 v[168:171], a[56:59], v[4:7], v[168:171]\n"
      "ds_write_addtid_b32 v138 offset:4608\n"
      "v_mfma_f32_16x16x32_bf16 v[172:175], a[60:63], v[4:7], v[172:175]\n"
      "buffer_load_dwordx4 v[72:75], v230, s[60:63], s71 offen offset:1024 sc1\n"
      "v_mfma_f32_16x16x32_bf16 v[144:147], a[224:227], a[84:87], v[144:147]\n"
      "ds_write_addtid_b32 v139 offset:4864\n"
      "v_mfma_f32_16x16x32_bf16 v[148:151], a[228:231], a[84:87], v[148:151]\n"
      "ds_write_addtid_b32 v140 offset:5120\n"
      "v_mfma_f32_16x16x32_bf16 v[152:155], a[232:235], a[84:87], v[152:155]\n"
      "ds_write_addtid_b32 v141 offset:5376\n"
      "v_mfma_f32_16x16x32_bf16 v[156:159], a[236:239], a[84:87], v[156:159]\n"
      "ds_write_addtid_b32 v142 offset:5632\n"
      "v_mfma_f32_16x16x32_bf16 v[160:163], a[240:243], a[84:87], v[160:163]\n"
      "ds_write_addtid_b32 v143 offset:5888\n"
      "v_mfma_f32_16x16x32_bf16 v[164:167], a[244:247], a[84:87], v[164:167]\n"
      "s_cmp_lt_u32 %[g], 2\n"
      "s_cselect_b32 s79, s79, 1879048192\n"
      "v_mfma_f32_16x16x32_bf16 v[168:171], a[248:251], a[84:87], v[168:171]\n"
      "s_bfe_u64 s[84:85], %[ttab], 262188\n"
      "s_add_u32 s71, s77, 327680\n"
      "v_mfma_f32_16x16x32_bf16 v[172:175], a[252:255], a[84:87], v[172:175]\n"
      "buffer_load_dwordx4 v[76:79], v230, s[60:63], s71 offen offset:0 sc1\n"
      "s_waitcnt vmcnt(14)\n"
      "v_mfma_f32_16x16x32_bf16 v[144:147], a[96:99], a[72:75], v[144:147]\n"
      "s_waitcnt lgkmcnt(0)\n"
      "s_lshl_b32 s80, s84, 14\n"
      "v_mfma_f32_16x16x32_bf16 v[148:151], a[100:103], a[72:75], v[148:151]\n"
      "s_barrier\n"
      "v_mfma_f32_16x16x32_bf16 v[152:155], a[104:107], a[72:75], v[152:155]\n"
      "ds_read2st64_b32 v[236:237], v218 offset0:0 offset1:1\n"
      "s_bfe_u64 s[84:85], %[ttab], 262192\n"
      "v_mfma_f32_16x16x32_bf16 v[156:159], a[108:111], a[72:75], v[156:159]\n"
      "ds_read2st64_b32 v[238:239], v218 offset0:2 offset1:3\n"
      "s_lshl_b32 s81, s84, 14\n"
      "v_mfma_f32_16x16x32_bf16 v[160:163], a[112:115], a[72:75], v[160:163]\n"
      "ds_read2st64_b32 v[240:241], v218 offset0:4 offset1:5\n"
      "s_bfe_u64 s[84:85], %[ttab], 262196\n"
      "v_mfma_f32_16x16x32_bf16 v[164:167], a[116:119], a[72:75], v[164:167]\n"
      "ds_read2st64_b32 v[242:243], v218 offset0:6 offset1:7\n"
      "s_lshl_b32 s83, s84, 14\n"
      "v_mfma_f32_16x16x32_bf16 v[168:171], a[120:123], a[72:75], v[168:171]\n"
      "buffer_load_dwordx4 v[56:59], v230, s[60:63], s71 offen offset:1024 sc1\n"
      "v_mfma_f32_16x16x32_bf16 v[172:175], a[124:127], a[72:75], v[172:175]\n"
      "ds_read2st64_b32 v[244:245], v219 offset0:8 offset1:9\n"
      "v_mfma_f32_16x16x32_bf16 v[144:147], a[128:131], a[92:95], v[144:147]\n"
      "ds_read2st64_b32 v[246:247], v219 offset0:10 offset1:11\n"
      "v_mfma_f32_16x16x32_bf16 v[148:151], a[132:135], a[92:95], v[148:151]\n"
      "ds_read2st64_b32 v[248:249], v219 offset0:12 offset1:13\n"
      "s_waitcnt lgkmcnt(3)\n"
      "v_mfma_f32_16x16x32_bf16 v[152:155], a[136:139], a[92:95], v[152:155]\n"
      "ds_read2st64_b32 v[250:251], v219 offset0:14 offset1:15\n"
      "v_add_f32_e64 v236, v236, v112\n"
      "v_mfma_f32_16x16x32_bf16 v[156:159], a[140:143], a[92:95], v[156:159]\n"
      "ds_read2st64_b32 v[176:177], v220 offset0:16 offset1:17\n"
      "v_add_f32_e64 v237, v237, v113\n"
      "v_mfma_f32_16x16x32_bf16 v[160:163], a[144:147], a[92:95], v[160:163]\n"
      "ds_read2st64_b32 v[178:179], v220 offset0:18 offset1:19\n"
      "v_add_f32_e64 v238, v238, v114\n"
      "v_mfma_f32_16x16x32_bf16 v[164:167], a[148:151], a[92:95], v[164:167]\n"
      "ds_read2st64_b32 v[212:213], v220 offset0:20 offset1:21\n"
      "s_add_u32 s71, s78, 327680\n"
      "v_mfma_f32_16x16x32_bf16 v[168:171], a[152:155], a[92:95], v[168:171]\n"
      "buffer_load_dwordx4 v[48:51], v230, s[60:63], s71 offen offset:0 sc1\n"
      "v_add_f32 v239, v239, v115\n"
      "v_mfma_f32_16x16x32_bf16 v[172:175], a[156:159], a[92:95], v[172:175]\n"
      "ds_read2st64_b32 v[214:215], v220 offset0:22 offset1:23\n"
      "s_waitcnt vmcnt(14)\n"
      "v_mfma_f32_16x16x32_bf16 v[144:147], v[180:183], v[0:3], v[144:147]\n"
      "ds_read2st64_b32 a[84:85], v227 offset0:16 offset1:17\n"
      "v_add_f32 v240, v240, v116\n"
      "v_mfma_f32_16x16x32_bf16 v[148:151], v[184:187], v[0:3], v[148:151]\n"
      "ds_read2st64_b32 a[86:87], v227 offset0:18 offset1:19\n"
      "v_add_f32 v241, v241, v117\n"
      "v_mfma_f32_16x16x32_bf16 v[152:155], v[188:191], v[0:3], v[152:155]\n"
      "ds_read2st64_b32 a[72:73], v227 offset0:208 offset1:209\n"
      "v_add_f32_e64 v242, v242, v118\n"
      "v_mfma_f32_16x16x32_bf16 v[156:159], v[192:195], v[0:3], v[156:159]\n"
      "ds_read2st64_b32 a[74:75], v227 offset0:210 offset1:211\n"
      "v_add_f32 v243, v243, v119\n"
      "s_waitcnt lgkmcnt(4)\n"
      "v_mfma_f32_16x16x32_bf16 v[160:163], v[196:199], v[0:3], v[160:163]\n"
      "ds_write_addtid_b32 v98 offset:16896\n"
      "v_add_f32 v236, v236, v244\n"
      "v_mfma_f32_16x16x32_bf16 v[164:167], v[200:203], v[0:3], v[164:167]\n"
      "ds_write_addtid_b32 v99 offset:17152\n"
      "v_add_f32 v237, v237, v245\n"
      "v_mfma_f32_16x16x32_bf16 v[168:171], v[204:207], v[0:3], v[168:171]\n"
      "buffer_load_dwordx4 v[52:55], v230, s[60:63], s71 offen offset:1024 sc1\n"
      "v_add_f32 v238, v238, v246\n"
      "v_mfma_f32_16x16x32_bf16 v[172:175], v[208:211], v[0:3], v[172:175]\n"
      "ds_write_addtid_b32 v100 offset:17408\n"
      "v_add_f32 v239, v239, v247\n"
      "v_mfma_f32_16x16x32_bf16 v[144:147], a[192:195], v[64:67], v[144:147]\n"
      "ds_write_addtid_b32 v101 offset:17664\n"
      "v_add_f32_e64 v240, v240, v248\n"
      "v_mfma_f32_16x16x32_bf16 v[148:151], a[196:199], v[64:67], v[148:151]\n"
      "ds_write_addtid_b32 v102 offset:17920\n"
      "v_add_f32 v241, v241, v249\n"
      "v_mfma_f32_16x16x32_bf16 v[152:155], a[200:203], v[64:67], v[152:155]\n"
      "ds_write_addtid_b32 v103 offset:18176\n"
      "v_add_f32 v242, v242, v250\n"
      "v_mfma_f32_16x16x32_bf16 v[156:159], a[204:207], v[64:67], v[156:159]\n"
      "s_add_u32 s72, s82, 5\n"
      "v_add_f32 v243, v243, v251\n"
      "v_add_f32 v236, v236, v176\n"
      "v_mfma_f32_16x16x32_bf16 v[160:163], a[208:211], v[64:67], v[160:163]\n"
      "v_mov_b32 v223, s72\n"
      "v_add_f32 v237, v237, v177\n"
      "v_add_f32_e64 v238, v238, v178\n"
      "v_mfma_f32_16x16x32_bf16 v[164:167], a[212:215], v[64:67], v[164:167]\n"
      "s_waitcnt vmcnt(13)\n"
      "v_add_f32_e64 v239, v239, v179\n"
      "v_add_f32_e64 v240, v240, v212\n"
      "v_mfma_f32_16x16x32_bf16 v[168:171], a[216:219], v[64:67], v[168:171]\n"
      "buffer_store_dword v223, v231, s[60:63], s93 offen sc1\n"
      "v_add_f32_e64 v241, v241, v213\n"
      "v_mfma_f32_16x16x32_bf16 v[172:175], a[220:223], v[64:67], v[172:175]\n"
      "v_add_f32 v242, v242, v214\n"
      "v_add_f32_e64 v243, v243, v215\n"
      "s_waitcnt lgkmcnt(6)\n"
      "v_mfma_f32_16x16x32_bf16 v[112:115], a[160:163], a[84:87], 0\n"
      "ds_write_addtid_b32 v104 offset:18432\n"
      "v_cvt_pk_bf16_f32 v44, v236, v237\n"
      "v_mfma_f32_16x16x32_bf16 v[116:119], a[164:167], a[84:87], 0\n"
      "ds_write_addtid_b32 v105 offset:18688\n"
      "v_cvt_pk_bf16_f32 v45, v238, v239\n"
      "v_mfma_f32_16x16x32_bf16 v[120:123], a[168:171], a[84:87], 0\n"
      "ds_write_addtid_b32 v152 offset:32768\n"
      "v_cvt_pk_bf16_f32 v46, v240, v241\n"
      "v_mfma_f32_16x16x32_bf16 v[124:127], a[172:175], a[84:87], 0\n"
      "ds_write_addtid_b32 v153 offset:33024\n"
      "v_cvt_pk_bf16_f32 v47, v242, v243\n"
      "v_mfma_f32_16x16x32_bf16 v[128:131], a[176:179], a[84:87], 0\n"
      "ds_write_addtid_b32 v154 offset:33280\n"
      "v_min_u32_e64 v44, -2, v44\n"
      "v_mfma_f32_16x16x32_bf16 v[132:135], a[180:183], a[84:87], 0\n"
      "ds_write_addtid_b32 v155 offset:33536\n"
      "v_min_u32_e64 v45, -2, v45\n"
      "v_mfma_f32_16x16x32_bf16 v[136:139], a[184:187], a[84:87], 0\n"
      "ds_write_addtid_b32 v156 offset:33792\n"
      "v_min_u32_e64 v46, -2, v46\n"
      "v_mfma_f32_16x16x32_bf16 v[140:143], a[188:191], a[84:87], 0\n"
      "ds_write_addtid_b32 v157 offset:34048\n"
      "s_add_u32 s71, s76, 360448\n"
      "v_mfma_f32_16x16x32_bf16 v[112:115], a[0:3], a[72:75], v[112:115]\n"
      "buffer_load_dwordx4 v[24:27], v230, s[60:63], s71 offen offset:0 sc1\n"
      "v_min_u32_e64 v47, -2, v47\n"
      "v_mfma_f32_16x16x32_bf16 v[116:119], a[4:7], a[72:75], v[116:119]\n"
      "ds_write_addtid_b32 v158 offset:34304\n"
      "v_mfma_f32_16x16x32_bf16 v[120:123], a[8:11], a[72:75], v[120:123]\n"
      "ds_write_addtid_b32 v159 offset:34560\n"
      "v_mfma_f32_16x16x32_bf16 v[124:127], a[12:15], a[72:75], v[124:127]\n"
      "ds_write_addtid_b32 v160 offset:34816\n"
      "v_mfma_f32_16x16x32_bf16 v[128:131], a[16:19], a[72:75], v[128:131]\n"
      "ds_write_addtid_b32 v161 offset:35072\n"
      "v_mfma_f32_16x16x32_bf16 v[132:135], a[20:23], a[72:75], v[132:135]\n"
      "ds_write_addtid_b32 v162 offset:35328\n"
      "v_mfma_f32_16x16x32_bf16 v[136:139], a[24:27], a[72:75], v[136:139]\n"
      "s_bfe_u64 s[66:67], %[ttab], 262172\n"
      "ds_write_addtid_b32 v163 offset:35584\n"
      "v_mfma_f32_16x16x32_bf16 v[140:143], a[28:31], a[72:75], v[140:143]\n"
      "s_lshl_b32 s64, s66, 14\n"
      "s_waitcnt vmcnt(13)\n"
      "v_mfma_f32_16x16x32_bf16 v[112:115], a[32:35], a[64:67], v[112:115]\n"
      "s_add_u32 s64, s64, s89\n"
      "ds_write_addtid_b32 v164 offset:35840\n"
      "v_mfma_f32_16x16x32_bf16 v[116:119], a[36:39], a[64:67], v[116:119]\n"
      "buffer_store_dwordx4 v[44:47], v230, s[60:63], s64 offen sc1\n"
      "v_mfma_f32_16x16x32_bf16 v[120:123], a[40:43], a[64:67], v[120:123]\n"
      "ds_write_addtid_b32 v165 offset:36096\n"
      "v_mfma_f32_16x16x32_bf16 v[124:127], a[44:47], a[64:67], v[124:127]\n"
      "ds_write_addtid_b32 v166 offset:36352\n"
      "v_mfma_f32_16x16x32_bf16 v[128:131], a[48:51], a[64:67], v[128:131]\n"
      "ds_write_addtid_b32 v167 offset:36608\n"
      "v_mfma_f32_16x16x32_bf16 v[132:135], a[52:55], a[64:67], v[132:135]\n"
      "ds_write_addtid_b32 v168 offset:36864\n"
      "v_mfma_f32_16x16x32_bf16 v[136:139], a[56:59], a[64:67], v[136:139]\n"
      "ds_write_addtid_b32 v169 offset:37120\n"
      "v_mfma_f32_16x16x32_bf16 v[140:143], a[60:63], a[64:67], v[140:143]\n"
      "ds_write_addtid_b32 v170 offset:37376\n"
      "v_mfma_f32_16x16x32_bf16 v[112:115], a[224:227], a[68:71], v[112:115]\n"
      "ds_write_addtid_b32 v171 offset:37632\n"
      "v_mfma_f32_16x16x32_bf16 v[116:119], a[228:231], a[68:71], v[116:119]\n"
      "ds_write_addtid_b32 v172 offset:37888\n"
      "v_mfma_f32_16x16x32_bf16 v[120:123], a[232:235], a[68:71], v[120:123]\n"
      "buffer_load_dwordx4 v[80:83], v230, s[60:63], s71 offen offset:1024 sc1\n"
      "v_mfma_f32_16x16x32_bf16 v[124:127], a[236:239], a[68:71], v[124:127]\n"
      "ds_write_addtid_b32 v173 offset:38144\n"
      "v_mfma_f32_16x16x32_bf16 v[128:131], a[240:243], a[68:71], v[128:131]\n"
      "ds_write_addtid_b32 v174 offset:38400\n"
      "v_mfma_f32_16x16x32_bf16 v[132:135], a[244:247], a[68:71], v[132:135]\n"
      "ds_write_addtid_b32 v175 offset:38656\n"
      "v_mfma_f32_16x16x32_bf16 v[136:139], a[248:251], a[68:71], v[136:139]\n"
      "v_mfma_f32_16x16x32_bf16 v[140:143], a[252:255], a[68:71], v[140:143]\n"
      "s_waitcnt lgkmcnt(0)\n"
      "s_waitcnt vmcnt(13)\n"
      "v_mfma_f32_16x16x32_bf16 v[112:115], a[96:99], v[32:35], v[112:115]\n"
      "s_barrier\n"
      "v_mfma_f32_16x16x32_bf16 v[116:119], a[100:103], v[32:35], v[116:119]\n"
      "ds_read2st64_b32 v[236:237], v218 offset0:128 offset1:129\n"
      "v_mfma_f32_16x16x32_bf16 v[120:123], a[104:107], v[32:35], v[120:123]\n"
      "ds_read2st64_b32 v[238:239], v218 offset0:130 offset1:131\n"
      "s_add_u32 s71, s77, 360448\n"
      "v_mfma_f32_16x16x32_bf16 v[124:127], a[108:111], v[32:35], v[124:127]\n"
      "buffer_load_dwordx4 v[84:87], v230, s[60:63], s71 offen offset:0 sc1\n"
      "v_mfma_f32_16x16x32_bf16 v[128:131], a[112:115], v[32:35], v[128:131]\n"
      "ds_read2st64_b32 v[240:241], v218 offset0:132 offset1:133\n"
      "v_mfma_f32_16x16x32_bf16 v[132:135], a[116:119], v[32:35], v[132:135]\n"
      "ds_read2st64_b32 v[242:243], v218 offset0:134 offset1:135\n"
      "v_mfma_f32_16x16x32_bf16 v[136:139], a[120:123], v[32:35], v[136:139]\n"
      "ds_read2st64_b32 v[244:245], v219 offset0:136 offset1:137\n"
      "v_mfma_f32_16x16x32_bf16 v[140:143], a[124:127], v[32:35], v[140:143]\n"
      "ds_read2st64_b32 v[246:247], v219 offset0:138 offset1:139\n"
      "v_mfma_f32_16x16x32_bf16 v[112:115], a[128:131], a[76:79], v[112:115]\n"
      "ds_read2st64_b32 v[248:249], v219 offset0:140 offset1:141\n"
      "v_mfma_f32_16x16x32_bf16 v[116:119], a[132:135], a[76:79], v[116:119]\n"
      "ds_read2st64_b32 v[250:251], v219 offset0:142 offset1:143\n"
      "s_waitcnt lgkmcnt(4)\n"
      "v_mfma_f32_16x16x32_bf16 v[120:123], a[136:139], a[76:79], v[120:123]\n"
      "ds_read2st64_b32 v[176:177], v220 offset0:144 offset1:145\n"
      "v_add_f32 v236, v236, v144\n"
      "v_mfma_f32_16x16x32_bf16 v[124:127], a[140:143], a[76:79], v[124:127]\n"
      "ds_read2st64_b32 v[178:179], v220 offset0:146 offset1:147\n"
      "v_add_f32_e64 v237, v237, v145\n"
      "v_mfma_f32_16x16x32_bf16 v[128:131], a[144:147], a[76:79], v[128:131]\n"
      "buffer_load_dwordx4 v[8:11], v230, s[60:63], s71 offen offset:1024 sc1\n"
      "v_add_f32 v238, v238, v146\n"
      "v_mfma_f32_16x16x32_bf16 v[132:135], a[148:151], a[76:79], v[132:135]\n"
      "ds_read2st64_b32 v[212:213], v220 offset0:148 offset1:149\n"
      "v_add_f32 v239, v239, v147\n"
      "v_mfma_f32_16x16x32_bf16 v[136:139], a[152:155], a[76:79], v[136:139]\n"
      "ds_read2st64_b32 v[214:215], v220 offset0:150 offset1:151\n"
      "v_add_f32_e64 v240, v240, v148\n"
      "v_mfma_f32_16x16x32_bf16 v[140:143], a[156:159], a[76:79], v[140:143]\n"
      "ds_read2st64_b32 v[12:13], v221 offset0:0 offset1:1\n"
      "s_waitcnt vmcnt(13)\n"
      "v_mfma_f32_16x16x32_bf16 v[112:115], v[180:183], a[80:83], v[112:115]\n"
      "ds_read2st64_b32 v[14:15], v221 offset0:2 offset1:3\n"
      "v_add_f32 v241, v241, v149\n"
      "v_mfma_f32_16x16x32_bf16 v[116:119], v[184:187], a[80:83], v[116:119]\n"
      "ds_read2st64_b32 v[40:41], v221 offset0:192 offset1:193\n"
      "v_add_f32_e64 v242, v242, v150\n"
      "v_mfma_f32_16x16x32_bf16 v[120:123], v[188:191], a[80:83], v[120:123]\n"
      "ds_read2st64_b32 v[42:43], v221 offset0:194 offset1:195\n"
      "v_add_f32 v243, v243, v151\n"
      "s_waitcnt lgkmcnt(4)\n"
      "v_mfma_f32_16x16x32_bf16 v[124:127], v[192:195], a[80:83], v[124:127]\n"
      "ds_write_addtid_b32 v106 offset:18944\n"
      "v_add_f32_e64 v236, v236, v244\n"
      "v_mfma_f32_16x16x32_bf16 v[128:131], v[196:199], a[80:83], v[128:131]\n"
      "ds_write_addtid_b32 v107 offset:19200\n"
      "s_add_u32 s71, s78, 360448\n"
      "v_mfma_f32_16x16x32_bf16 v[132:135], v[200:203], a[80:83], v[132:135]\n"
      "buffer_load_dwordx4 v[60:63], v230, s[60:63], s71 offen offset:0 sc1\n"
      "v_add_f32_e64 v237, v237, v245\n"
      "v_mfma_f32_16x16x32_bf16 v[136:139], v[204:207], a[80:83], v[136:139]\n"
      "ds_write_addtid_b32 v108 offset:19456\n"
      "v_add_f32 v238, v238, v246\n"
      "v_mfma_f32_16x16x32_bf16 v[140:143], v[208:211], a[80:83], v[140:143]\n"
      "ds_write_addtid_b32 v109 offset:19712\n"
      "v_add_f32 v239, v239, v247\n"
      "v_mfma_f32_16x16x32_bf16 v[112:115], a[192:195], a[88:91], v[112:115]\n"
      "ds_write_addtid_b32 v110 offset:19968\n"
      "v_add_f32_e64 v240, v240, v248\n"
      "v_mfma_f32_16x16x32_bf16 v[116:119], a[196:199], a[88:91], v[116:119]\n"
      "ds_write_addtid_b32 v111 offset:20224\n"
      "v_add_f32 v241, v241, v249\n"
      "v_mfma_f32_16x16x32_bf16 v[120:123], a[200:203], a[88:91], v[120:123]\n"
      "v_add_f32 v242, v242, v250\n"
      "v_add_f32_e64 v243, v243, v251\n"
      "v_mfma_f32_16x16x32_bf16 v[124:127], a[204:207], a[88:91], v[124:127]\n"
      "v_add_f32 v236, v236, v176\n"
      "v_add_f32 v237, v237, v177\n"
      "v_mfma_f32_16x16x32_bf16 v[128:131], a[208:211], a[88:91], v[128:131]\n"
      "v_add_f32_e64 v238, v238, v178\n"
      "v_add_f32_e64 v239, v239, v179\n"
      "v_mfma_f32_16x16x32_bf16 v[132:135], a[212:215], a[88:91], v[132:135]\n"
      "v_add_f32_e64 v240, v240, v212\n"
      "v_add_f32_e64 v241, v241, v213\n"
      "v_mfma_f32_16x16x32_bf16 v[136:139], a[216:219], a[88:91], v[136:139]\n"
      "buffer_load_dwordx4 v[68:71], v230, s[60:63], s71 offen offset:1024 sc1\n"
      "v_add_f32_e64 v242, v242, v214\n"
      "v_mfma_f32_16x16x32_bf16 v[140:143], a[220:223], a[88:91], v[140:143]\n"
      "v_add_f32 v243, v243, v215\n"
      "s_waitcnt lgkmcnt(6)\n"
      "v_mfma_f32_16x16x32_bf16 v[144:147], a[160:163], v[12:15], 0\n"
      "v_cvt_pk_bf16_f32 v88, v236, v237\n"
      "v_cvt_pk_bf16_f32 v89, v238, v239\n"
      "v_mfma_f32_16x16x32_bf16 v[148:151], a[164:167], v[12:15], 0\n"
      "v_cvt_pk_bf16_f32 v90, v240, v241\n"
      "v_cvt_pk_bf16_f32 v91, v242, v243\n"
      "v_mfma_f32_16x16x32_bf16 v[152:155], a[168:171], v[12:15], 0\n"
      "ds_write_addtid_b32 v120 offset:0\n"
      "v_min_u32_e64 v88, -2, v88\n"
      "v_mfma_f32_16x16x32_bf16 v[156:159], a[172:175], v[12:15], 0\n"
      "ds_write_addtid_b32 v121 offset:256\n"
      "v_min_u32_e64 v89, -2, v89\n"
      "v_mfma_f32_16x16x32_bf16 v[160:163], a[176:179], v[12:15], 0\n"
      "ds_write_addtid_b32 v122 offset:512\n"
      "v_min_u32_e64 v90, -2, v90\n"
      "v_mfma_f32_16x16x32_bf16 v[164:167], a[180:183], v[12:15], 0\n"
      "ds_write_addtid_b32 v123 offset:768\n"
      "v_min_u32_e64 v91, -2, v91\n"
      "v_mfma_f32_16x16x32_bf16 v[168:171], a[184:187], v[12:15], 0\n"
      "ds_write_addtid_b32 v124 offset:1024\n"
      "v_mfma_f32_16x16x32_bf16 v[172:175], a[188:191], v[12:15], 0\n"
      "ds_write_addtid_b32 v125 offset:1280\n"
      "s_add_u32 s71, s76, 393216\n"
      "v_mfma_f32_16x16x32_bf16 v[144:147], a[0:3], v[40:43], v[144:147]\n"
      "buffer_load_dwordx4 v[92:95], v230, s[60:63], s71 offen offset:0 sc1\n"
      "v_mfma_f32_16x16x32_bf16 v[148:151], a[4:7], v[40:43], v[148:151]\n"
      "ds_write_addtid_b32 v126 offset:1536\n"
      "v_mfma_f32_16x16x32_bf16 v[152:155], a[8:11], v[40:43], v[152:155]\n"
      "ds_write_addtid_b32 v127 offset:1792\n"
      "v_mfma_f32_16x16x32_bf16 v[156:159], a[12:15], v[40:43], v[156:159]\n"
      "ds_write_addtid_b32 v128 offset:2048\n"
      "v_mfma_f32_16x16x32_bf16 v[160:163], a[16:19], v[40:43], v[160:163]\n"
      "ds_write_addtid_b32 v129 offset:2304\n"
      "v_mfma_f32_16x16x32_bf16 v[164:167], a[20:23], v[40:43], v[164:167]\n"
      "ds_write_addtid_b32 v130 offset:2560\n"
      "v_mfma_f32_16x16x32_bf16 v[168:171], a[24:27], v[40:43], v[168:171]\n"
      "s_bfe_u64 s[66:67], %[ttab], 262176\n"
      "ds_write_addtid_b32 v131 offset:2816\n"
      "v_mfma_f32_16x16x32_bf16 v[172:175], a[28:31], v[40:43], v[172:175]\n"
      "s_lshl_b32 s64, s66, 14\n"
      "s_waitcnt vmcnt(13)\n"
      "v_mfma_f32_16x16x32_bf16 v[144:147], a[32:35], v[20:23], v[144:147]\n"
      "s_add_u32 s64, s64, s89\n"
      "ds_write_addtid_b32 v132 offset:3072\n"
      "v_mfma_f32_16x16x32_bf16 v[148:151], a[36:39], v[20:23], v[148:151]\n"
      "buffer_store_dwordx4 v[88:91], v230, s[60:63], s64 offen sc1\n"
      "v_mfma_f32_16x16x32_bf16 v[152:155], a[40:43], v[20:23], v[152:155]\n"
      "ds_write_addtid_b32 v133 offset:3328\n"
      "v_mfma_f32_16x16x32_bf16 v[156:159], a[44:47], v[20:23], v[156:159]\n"
      "ds_write_addtid_b32 v134 offset:3584\n"
      "v_mfma_f32_16x16x32_bf16 v[160:163], a[48:51], v[20:23], v[160:163]\n"
      "ds_write_addtid_b32 v135 offset:3840\n"
      "v_mfma_f32_16x16x32_bf16 v[164:167], a[52:55], v[20:23], v[164:167]\n"
      "ds_write_addtid_b32 v136 offset:4096\n"
      "v_mfma_f32_16x16x32_bf16 v[168:171], a[56:59], v[20:23], v[168:171]\n"
      "ds_write_addtid_b32 v137 offset:4352\n"
      "v_mfma_f32_16x16x32_bf16 v[172:175], a[60:63], v[20:23], v[172:175]\n"
      "ds_write_addtid_b32 v138 offset:4608\n"
      "v_mfma_f32_16x16x32_bf16 v[144:147], a[224:227], v[72:75], v[144:147]\n"
      "ds_write_addtid_b32 v139 offset:4864\n"
      "v_mfma_f32_16x16x32_bf16 v[148:151], a[228:231], v[72:75], v[148:151]\n"
      "ds_write_addtid_b32 v140 offset:5120\n"
      "v_mfma_f32_16x16x32_bf16 v[152:155], a[232:235], v[72:75], v[152:155]\n"
      "buffer_load_dwordx4 v[36:39], v230, s[60:63], s71 offen offset:1024 sc1\n"
      "v_mfma_f32_16x16x32_bf16 v[156:159], a[236:239], v[72:75], v[156:159]\n"
      "ds_write_addtid_b32 v141 offset:5376\n"
      "v_mfma_f32_16x16x32_bf16 v[160:163], a[240:243], v[72:75], v[160:163]\n"
      "ds_write_addtid_b32 v142 offset:5632\n"
      "v_mfma_f32_16x16x32_bf16 v[164:167], a[244:247], v[72:75], v[164:167]\n"
      "ds_write_addtid_b32 v143 offset:5888\n"
      "v_mfma_f32_16x16x32_bf16 v[168:171], a[248:251], v[72:75], v[168:171]\n"
      "v_mfma_f32_16x16x32_bf16 v[172:175], a[252:255], v[72:75], v[172:175]\n"
      "s_waitcnt lgkmcnt(0)\n"
      "s_waitcnt vmcnt(13)\n"
      "v_mfma_f32_16x16x32_bf16 v[144:147], a[96:99], v[76:79], v[144:147]\n"
      "s_barrier\n"
      "v_mfma_f32_16x16x32_bf16 v[148:151], a[100:103], v[76:79], v[148:151]\n"
      "ds_read2st64_b32 v[236:237], v218 offset0:0 offset1:1\n"
      "v_mfma_f32_16x16x32_bf16 v[152:155], a[104:107], v[76:79], v[152:155]\n"
      "ds_read2st64_b32 v[238:239], v218 offset0:2 offset1:3\n"
      "s_add_u32 s71, s77, 393216\n"
      "v_mfma_f32_16x16x32_bf16 v[156:159], a[108:111], v[76:79], v[156:159]\n"
      "buffer_load_dwordx4 v[28:31], v230, s[60:63], s71 offen offset:0 sc1\n"
      "v_mfma_f32_16x16x32_bf16 v[160:163], a[112:115], v[76:79], v[160:163]\n"
      "ds_read2st64_b32 v[240:241], v218 offset0:4 offset1:5\n"
      "v_mfma_f32_16x16x32_bf16 v[164:167], a[116:119], v[76:79], v[164:167]\n"
      "ds_read2st64_b32 v[242:243], v218 offset0:6 offset1:7\n"
      "v_mfma_f32_16x16x32_bf16 v[168:171], a[120:123], v[76:79], v[168:171]\n"
      "ds_read2st64_b32 v[244:245], v219 offset0:8 offset1:9\n"
      "v_mfma_f32_16x16x32_bf16 v[172:175], a[124:127], v[76:79], v[172:175]\n"
      "ds_read2st64_b32 v[246:247], v219 offset0:10 offset1:11\n"
      "v_mfma_f32_16x16x32_bf16 v[144:147], a[128:131], v[56:59], v[144:147]\n"
      "ds_read2st64_b32 v[248:249], v219 offset0:12 offset1:13\n"
      "v_mfma_f32_16x16x32_bf16 v[148:151], a[132:135], v[56:59], v[148:151]\n"
      "ds_read2st64_b32 v[250:251], v219 offset0:14 offset1:15\n"
      "s_waitcnt lgkmcnt(4)\n"
      "v_mfma_f32_16x16x32_bf16 v[152:155], a[136:139], v[56:59], v[152:155]\n"
      "ds_read2st64_b32 v[176:177], v220 offset0:16 offset1:17\n"
      "v_add_f32 v236, v236, v112\n"
      "v_mfma_f32_16x16x32_bf16 v[156:159], a[140:143], v[56:59], v[156:159]\n"
      "ds_read2st64_b32 v[178:179], v220 offset0:18 offset1:19\n"
      "v_add_f32_e64 v237, v237, v113\n"
      "v_mfma_f32_16x16x32_bf16 v[160:163], a[144:147], v[56:59], v[160:163]\n"
      "buffer_load_dwordx4 v[4:7], v230, s[60:63], s71 offen offset:1024 sc1\n"
      "v_add_f32 v238, v238, v114\n"
      "v_mfma_f32_16x16x32_bf16 v[164:167], a[148:151], v[56:59], v[164:167]\n"
      "ds_read2st64_b32 v[212:213], v220 offset0:20 offset1:21\n"
      "v_add_f32 v239, v239, v115\n"
      "v_mfma_f32_16x16x32_bf16 v[168:171], a[152:155], v[56:59], v[168:171]\n"
      "ds_read2st64_b32 v[214:215], v220 offset0:22 offset1:23\n"
      "v_add_f32_e64 v240, v240, v116\n"
      "v_mfma_f32_16x16x32_bf16 v[172:175], a[156:159], v[56:59], v[172:175]\n"
      "ds_read2st64_b32 v[0:1], v222 offset0:0 offset1:1\n"
      "s_waitcnt vmcnt(13)\n"
      "v_mfma_f32_16x16x32_bf16 v[144:147], v[180:183], v[48:51], v[144:147]\n"
      "ds_read2st64_b32 v[2:3], v222 offset0:2 offset1:3\n"
      "v_add_f32 v241, v241, v117\n"
      "v_mfma_f32_16x16x32_bf16 v[148:151], v[184:187], v[48:51], v[148:151]\n"
      "ds_read2st64_b32 v[64:65], v222 offset0:192 offset1:193\n"
      "v_add_f32_e64 v242, v242, v118\n"
      "v_mfma_f32_16x16x32_bf16 v[152:155], v[188:191], v[48:51], v[152:155]\n"
      "ds_read2st64_b32 v[66:67], v222 offset0:194 offset1:195\n"
      "v_add_f32 v243, v243, v119\n"
      "s_waitcnt lgkmcnt(4)\n"
      "v_mfma_f32_16x16x32_bf16 v[156:159], v[192:195], v[48:51], v[156:159]\n"
      "v_add_f32 v236, v236, v244\n"
      "v_add_f32 v237, v237, v245\n"
      "v_mfma_f32_16x16x32_bf16 v[160:163], v[196:199], v[48:51], v[160:163]\n"
      "s_add_u32 s71, s78, 393216\n"
      "v_add_f32 v238, v238, v246\n"
      "v_add_f32 v239, v239, v247\n"
      "v_mfma_f32_16x16x32_bf16 v[164:167], v[200:203], v[48:51], v[164:167]\n"
      "buffer_load_dwordx4 v[16:19], v230, s[60:63], s71 offen offset:0 sc1\n"
      "v_add_f32 v240, v240, v248\n"
      "v_mfma_f32_16x16x32_bf16 v[168:171], v[204:207], v[48:51], v[168:171]\n"
      "v_add_f32 v241, v241, v249\n"
      "v_add_f32_e64 v242, v242, v250\n"
      "v_mfma_f32_16x16x32_bf16 v[172:175], v[208:211], v[48:51], v[172:175]\n"
      "v_add_f32 v243, v243, v251\n"
      "v_add_f32 v236, v236, v176\n"
      "v_mfma_f32_16x16x32_bf16 v[144:147], a[192:195], v[52:55], v[144:147]\n"
      "v_add_f32_e64 v237, v237, v177\n"
      "v_add_f32_e64 v238, v238, v178\n"
      "v_mfma_f32_16x16x32_bf16 v[148:151], a[196:199], v[52:55], v[148:151]\n"
      "v_add_f32_e64 v239, v239, v179\n"
      "v_add_f32_e64 v240, v240, v212\n"
      "v_mfma_f32_16x16x32_bf16 v[152:155], a[200:203], v[52:55], v[152:155]\n"
      "v_add_f32_e64 v241, v241, v213\n"
      "v_add_f32_e64 v242, v242, v214\n"
      "v_mfma_f32_16x16x32_bf16 v[156:159], a[204:207], v[52:55], v[156:159]\n"
      "v_add_f32_e64 v243, v243, v215\n"
      "v_mfma_f32_16x16x32_bf16 v[160:163], a[208:211], v[52:55], v[160:163]\n"
      "v_mfma_f32_16x16x32_bf16 v[164:167], a[212:215], v[52:55], v[164:167]\n"
      "v_mfma_f32_16x16x32_bf16 v[168:171], a[216:219], v[52:55], v[168:171]\n"
      "buffer_load_dwordx4 v[96:99], v230, s[60:63], s71 offen offset:1024 sc1\n"
      "v_mfma_f32_16x16x32_bf16 v[172:175], a[220:223], v[52:55], v[172:175]\n"
      "s_waitcnt lgkmcnt(0)\n"
      "v_mfma_f32_16x16x32_bf16 v[112:115], a[160:163], v[0:3], 0\n"
      "v_and_b32 v226, 12, v226\n"
      "v_cvt_pk_bf16_f32 v100, v236, v237\n"
      "v_mfma_f32_16x16x32_bf16 v[116:119], a[164:167], v[0:3], 0\n"
      "v_cvt_pk_bf16_f32 v101, v238, v239\n"
      "v_cvt_pk_bf16_f32 v102, v240, v241\n"
      "v_mfma_f32_16x16x32_bf16 v[120:123], a[168:171], v[0:3], 0\n"
      "ds_write_addtid_b32 v152 offset:32768\n"
      "ds_write_addtid_b32 v153 offset:33024\n"
      "v_mfma_f32_16x16x32_bf16 v[124:127], a[172:175], v[0:3], 0\n"
      "ds_write_addtid_b32 v154 offset:33280\n"
      "ds_write_addtid_b32 v155 offset:33536\n"
      "v_mfma_f32_16x16x32_bf16 v[128:131], a[176:179], v[0:3], 0\n"
      "ds_write_addtid_b32 v156 offset:33792\n"
      "ds_write_addtid_b32 v157 offset:34048\n"
      "v_mfma_f32_16x16x32_bf16 v[132:135], a[180:183], v[0:3], 0\n"
      "ds_write_addtid_b32 v158 offset:34304\n"
      "ds_write_addtid_b32 v159 offset:34560\n"
      "v_mfma_f32_16x16x32_bf16 v[136:139], a[184:187], v[0:3], 0\n"
      "ds_write_addtid_b32 v160 offset:34816\n"
      "ds_write_addtid_b32 v161 offset:35072\n"
      "v_mfma_f32_16x16x32_bf16 v[140:143], a[188:191], v[0:3], 0\n"
      "ds_write_addtid_b32 v162 offset:35328\n"
      "ds_write_addtid_b32 v163 offset:35584\n"
      "v_mfma_f32_16x16x32_bf16 v[112:115], a[0:3], v[64:67], v[112:115]\n"
      "buffer_load_dword v254, v226, s[60:63], s94 offen sc1\n"
      "ds_write_addtid_b32 v164 offset:35840\n"
      "v_mfma_f32_16x16x32_bf16 v[116:119], a[4:7], v[64:67], v[116:119]\n"
      "ds_write_addtid_b32 v165 offset:36096\n"
      "ds_write_addtid_b32 v166 offset:36352\n"
      "v_mfma_f32_16x16x32_bf16 v[120:123], a[8:11], v[64:67], v[120:123]\n"
      "ds_write_addtid_b32 v167 offset:36608\n"
      "ds_write_addtid_b32 v168 offset:36864\n"
      "v_mfma_f32_16x16x32_bf16 v[124:127], a[12:15], v[64:67], v[124:127]\n"
      "ds_write_addtid_b32 v169 offset:37120\n"
      "ds_write_addtid_b32 v170 offset:37376\n"
      "v_mfma_f32_16x16x32_bf16 v[128:131], a[16:19], v[64:67], v[128:131]\n"
      "ds_write_addtid_b32 v171 offset:37632\n"
      "s_add_u32 s71, s76, 425984\n"
      "v_mfma_f32_16x16x32_bf16 v[132:135], a[20:23], v[64:67], v[132:135]\n"
      "ds_write_addtid_b32 v172 offset:37888\n"
      "buffer_load_dwordx4 a[92:95], v230, s[60:63], s71 offen offset:0 sc1\n"
      "v_mfma_f32_16x16x32_bf16 v[136:139], a[24:27], v[64:67], v[136:139]\n"
      "ds_write_addtid_b32 v173 offset:38144\n"
      "ds_write_addtid_b32 v174 offset:38400\n"
      "v_mfma_f32_16x16x32_bf16 v[140:143], a[28:31], v[64:67], v[140:143]\n"
      "ds_write_addtid_b32 v175 offset:38656\n"
      "s_waitcnt vmcnt(13)\n"
      "v_mfma_f32_16x16x32_bf16 v[112:115], a[32:35], v[24:27], v[112:115]\n"
      "v_cvt_pk_bf16_f32 v103, v242, v243\n"
      "v_min_u32 v100, -2, v100\n"
      "v_mfma_f32_16x16x32_bf16 v[116:119], a[36:39], v[24:27], v[116:119]\n"
      "v_min_u32_e64 v101, -2, v101\n"
      "v_min_u32_e64 v102, -2, v102\n"
      "v_mfma_f32_16x16x32_bf16 v[120:123], a[40:43], v[24:27], v[120:123]\n"
      "s_waitcnt lgkmcnt(0)\n"
      "buffer_load_dwordx4 a[84:87], v230, s[60:63], s71 offen offset:1024 sc1\n"
      "v_mfma_f32_16x16x32_bf16 v[124:127], a[44:47], v[24:27], v[124:127]\n"
      "s_barrier\n"
      "v_mfma_f32_16x16x32_bf16 v[128:131], a[48:51], v[24:27], v[128:131]\n"
      "ds_read2st64_b32 v[236:237], v218 offset0:128 offset1:129\n"
      "ds_read2st64_b32 v[238:239], v218 offset0:130 offset1:131\n"
      "v_mfma_f32_16x16x32_bf16 v[132:135], a[52:55], v[24:27], v[132:135]\n"
      "ds_read2st64_b32 v[240:241], v218 offset0:132 offset1:133\n"
      "ds_read2st64_b32 v[242:243], v218 offset0:134 offset1:135\n"
      "v_mfma_f32_16x16x32_bf16 v[136:139], a[56:59], v[24:27], v[136:139]\n"
      "s_bfe_u64 s[66:67], %[ttab], 262180\n"
      "s_lshl_b32 s64, s66, 14\n"
      "s_add_u32 s64, s64, s89\n"
      "v_mfma_f32_16x16x32_bf16 v[140:143], a[60:63], v[24:27], v[140:143]\n"
      "buffer_store_dwordx4 v[100:103], v230, s[60:63], s64 offen sc1\n"
      "ds_read2st64_b32 v[244:245], v219 offset0:136 offset1:137\n"
      "v_mfma_f32_16x16x32_bf16 v[112:115], a[224:227], v[80:83], v[112:115]\n"
      "ds_read2st64_b32 v[246:247], v219 offset0:138 offset1:139\n"
      "ds_read2st64_b32 v[248:249], v219 offset0:140 offset1:141\n"
      "v_mfma_f32_16x16x32_bf16 v[116:119], a[228:231], v[80:83], v[116:119]\n"
      "ds_read2st64_b32 v[250:251], v219 offset0:142 offset1:143\n"
      "ds_read2st64_b32 v[176:177], v220 offset0:144 offset1:145\n"
      "s_waitcnt lgkmcnt(5)\n"
      "v_mfma_f32_16x16x32_bf16 v[120:123], a[232:235], v[80:83], v[120:123]\n"
      "ds_read2st64_b32 v[178:179], v220 offset0:146 offset1:147\n"
      "ds_read2st64_b32 v[212:213], v220 offset0:148 offset1:149\n"
      "v_mfma_f32_16x16x32_bf16 v[124:127], a[236:239], v[80:83], v[124:127]\n"
      "ds_read2st64_b32 v[214:215], v220 offset0:150 offset1:151\n"
      "s_add_u32 s71, s77, 425984\n"
      "v_mfma_f32_16x16x32_bf16 v[128:131], a[240:243], v[80:83], v[128:131]\n"
      "buffer_load_dwordx4 a[72:75], v230, s[60:63], s71 offen offset:0 sc1\n"
      "v_min_u32 v103, -2, v103\n"
      "v_mfma_f32_16x16x32_bf16 v[132:135], a[244:247], v[80:83], v[132:135]\n"
      "v_add_f32 v236, v236, v144\n"
      "v_add_f32 v237, v237, v145\n"
      "v_mfma_f32_16x16x32_bf16 v[136:139], a[248:251], v[80:83], v[136:139]\n"
      "v_add_f32 v238, v238, v146\n"
      "v_add_f32 v239, v239, v147\n"
      "v_mfma_f32_16x16x32_bf16 v[140:143], a[252:255], v[80:83], v[140:143]\n"
      "v_add_f32 v240, v240, v148\n"
      "v_add_f32 v241, v241, v149\n"
      "s_waitcnt vmcnt(14)\n"
      "v_mfma_f32_16x16x32_bf16 v[112:115], a[96:99], v[84:87], v[112:115]\n"
      "v_add_f32 v242, v242, v150\n"
      "v_add_f32 v243, v243, v151\n"
      "s_waitcnt lgkmcnt(0)\n"
      "v_mfma_f32_16x16x32_bf16 v[116:119], a[100:103], v[84:87], v[116:119]\n"
      "buffer_load_dwordx4 a[64:67], v230, s[60:63], s71 offen offset:1024 sc1\n"
      "v_add_f32 v236, v236, v244\n"
      "v_mfma_f32_16x16x32_bf16 v[120:123], a[104:107], v[84:87], v[120:123]\n"
      "v_add_f32 v237, v237, v245\n"
      "v_add_f32_e64 v238, v238, v246\n"
      "v_mfma_f32_16x16x32_bf16 v[124:127], a[108:111], v[84:87], v[124:127]\n"
      "v_add_f32 v239, v239, v247\n"
      "v_add_f32 v240, v240, v248\n"
      "v_mfma_f32_16x16x32_bf16 v[128:131], a[112:115], v[84:87], v[128:131]\n"
      "v_add_f32 v241, v241, v249\n"
      "v_add_f32 v242, v242, v250\n"
      "v_mfma_f32_16x16x32_bf16 v[132:135], a[116:119], v[84:87], v[132:135]\n"
      "s_add_u32 s71, s78, 425984\n"
      "v_add_f32 v243, v243, v251\n"
      "v_add_f32 v32, v236, v176\n"
      "v_mfma_f32_16x16x32_bf16 v[136:139], a[120:123], v[84:87], v[136:139]\n"
      "buffer_load_dwordx4 a[68:71], v230, s[60:63], s71 offen offset:0 sc1\n"
      "v_add_f32 v33, v237, v177\n"
      "v_mfma_f32_16x16x32_bf16 v[140:143], a[124:127], v[84:87], v[140:143]\n"
      "v_add_f32 v34, v238, v178\n"
      "v_add_f32_e64 v35, v239, v179\n"
      "v_mfma_f32_16x16x32_bf16 v[112:115], a[128:131], v[8:11], v[112:115]\n"
      "ds_read2st64_b32 v[108:109], v228 offset0:0 offset1:1\n"
      "v_add_f32_e64 v44, v240, v212\n"
      "v_mfma_f32_16x16x32_bf16 v[116:119], a[132:135], v[8:11], v[116:119]\n"
      "ds_read2st64_b32 v[110:111], v228 offset0:2 offset1:3\n"
      "v_add_f32 v45, v241, v213\n"
      "v_mfma_f32_16x16x32_bf16 v[120:123], a[136:139], v[8:11], v[120:123]\n"
      "ds_read2st64_b32 v[12:13], v228 offset0:192 offset1:193\n"
      "v_add_f32 v46, v242, v214\n"
      "v_mfma_f32_16x16x32_bf16 v[124:127], a[140:143], v[8:11], v[124:127]\n"
      "ds_read2st64_b32 v[14:15], v228 offset0:194 offset1:195\n"
      "v_add_f32_e64 v47, v243, v215\n"
      "v_mfma_f32_16x16x32_bf16 v[128:131], a[144:147], v[8:11], v[128:131]\n"
      "buffer_load_dwordx4 a[76:79], v230, s[60:63], s71 offen offset:1024 sc1\n"
      "v_cvt_pk_bf16_f32 v104, v32, v33\n"
      "v_mfma_f32_16x16x32_bf16 v[132:135], a[148:151], v[8:11], v[132:135]\n"
      "v_cvt_pk_bf16_f32 v105, v34, v35\n"
      "v_cvt_pk_bf16_f32 v106, v44, v45\n"
      "v_mfma_f32_16x16x32_bf16 v[136:139], a[152:155], v[8:11], v[136:139]\n"
      "v_cvt_pk_bf16_f32 v107, v46, v47\n"
      "v_min_u32_e64 v104, -2, v104\n"
      "v_mfma_f32_16x16x32_bf16 v[140:143], a[156:159], v[8:11], v[140:143]\n"
      "v_min_u32 v105, -2, v105\n"
      "v_min_u32_e64 v106, -2, v106\n"
      "s_waitcnt vmcnt(15)\n"
      "v_mfma_f32_16x16x32_bf16 v[112:115], v[180:183], v[60:63], v[112:115]\n"
      "v_min_u32 v107, -2, v107\n"
      "s_bfe_u64 s[66:67], %[ttab], 262184\n"
      "s_lshl_b32 s64, s66, 14\n"
      "s_add_u32 s64, s64, s89\n"
      "s_cmp_lt_u32 %[g], 2\n"
      "s_cselect_b32 s64, 1879048192, s64\n"
      "buffer_store_dwordx4 v[104:107], v230, s[60:63], s64 offen sc1\n"
      "v_mfma_f32_16x16x32_bf16 v[116:119], v[184:187], v[60:63], v[116:119]\n"
      "v_mfma_f32_16x16x32_bf16 v[120:123], v[188:191], v[60:63], v[120:123]\n"
      "v_mfma_f32_16x16x32_bf16 v[124:127], v[192:195], v[60:63], v[124:127]\n"
      "v_mfma_f32_16x16x32_bf16 v[128:131], v[196:199], v[60:63], v[128:131]\n"
      "v_mfma_f32_16x16x32_bf16 v[132:135], v[200:203], v[60:63], v[132:135]\n"
      "v_mfma_f32_16x16x32_bf16 v[136:139], v[204:207], v[60:63], v[136:139]\n"
      "v_mfma_f32_16x16x32_bf16 v[140:143], v[208:211], v[60:63], v[140:143]\n"
      "v_mfma_f32_16x16x32_bf16 v[112:115], a[192:195], v[68:71], v[112:115]\n"
      "v_mfma_f32_16x16x32_bf16 v[116:119], a[196:199], v[68:71], v[116:119]\n"
      "v_mfma_f32_16x16x32_bf16 v[120:123], a[200:203], v[68:71], v[120:123]\n"
      "v_mfma_f32_16x16x32_bf16 v[124:127], a[204:207], v[68:71], v[124:127]\n"
      "v_mfma_f32_16x16x32_bf16 v[128:131], a[208:211], v[68:71], v[128:131]\n"
      "v_mfma_f32_16x16x32_bf16 v[132:135], a[212:215], v[68:71], v[132:135]\n"
      "v_mfma_f32_16x16x32_bf16 v[136:139], a[216:219], v[68:71], v[136:139]\n"
      "v_mfma_f32_16x16x32_bf16 v[140:143], a[220:223], v[68:71], v[140:143]\n"
      "s_waitcnt lgkmcnt(0)\n"
      "v_mfma_f32_16x16x32_bf16 v[144:147], a[160:163], v[108:111], 0\n"
      "v_mfma_f32_16x16x32_bf16 v[148:151], a[164:167], v[108:111], 0\n"
      "v_mfma_f32_16x16x32_bf16 v[152:155], a[168:171], v[108:111], 0\n"
      "ds_write_addtid_b32 v120 offset:0\n"
      "v_mfma_f32_16x16x32_bf16 v[156:159], a[172:175], v[108:111], 0\n"
      "ds_write_addtid_b32 v121 offset:256\n"
      "v_mfma_f32_16x16x32_bf16 v[160:163], a[176:179], v[108:111], 0\n"
      "s_waitcnt vmcnt(8)\n"
      "s_add_u32 s97, s82, 5\n"
      "v_mfma_f32_16x16x32_bf16 v[164:167], a[180:183], v[108:111], 0\n"
      "v_subrev_u32 v254, s97, v254\n"
      "v_cmp_lt_u32_e64 vcc, 2, v254\n"
      "v_mfma_f32_16x16x32_bf16 v[168:171], a[184:187], v[108:111], 0\n"
      "ds_write_addtid_b32 v122 offset:512\n"
      "s_cbranch_vccnz PE5_%=\n"
      "PE5R_%=:\n"
      "v_mfma_f32_16x16x32_bf16 v[172:175], a[188:191], v[108:111], 0\n"
      "ds_write_addtid_b32 v123 offset:768\n"
      "v_mfma_f32_16x16x32_bf16 v[144:147], a[0:3], v[12:15], v[144:147]\n"
      "ds_write_addtid_b32 v124 offset:1024\n"
      "v_mfma_f32_16x16x32_bf16 v[148:151], a[4:7], v[12:15], v[148:151]\n"
      "ds_write_addtid_b32 v125 offset:1280\n"
      "s_add_u32 s65, s81, s75\n"
      "v_mfma_f32_16x16x32_bf16 v[152:155], a[8:11], v[12:15], v[152:155]\n"
      "buffer_load_dwordx4 v[72:75], v230, s[60:63], s65 offen sc1\n"
      "v_mfma_f32_16x16x32_bf16 v[156:159], a[12:15], v[12:15], v[156:159]\n"
      "ds_write_addtid_b32 v126 offset:1536\n"
      "v_mfma_f32_16x16x32_bf16 v[160:163], a[16:19], v[12:15], v[160:163]\n"
      "ds_write_addtid_b32 v127 offset:1792\n"
      "v_mfma_f32_16x16x32_bf16 v[164:167], a[20:23], v[12:15], v[164:167]\n"
      "ds_write_addtid_b32 v128 offset:2048\n"
      "v_mfma_f32_16x16x32_bf16 v[168:171], a[24:27], v[12:15], v[168:171]\n"
      "ds_write_addtid_b32 v129 offset:2304\n"
      "v_mfma_f32_16x16x32_bf16 v[172:175], a[28:31], v[12:15], v[172:175]\n"
      "s_add_u32 s65, s83, s75\n"
      "s_waitcnt vmcnt(14)\n"
      "v_mfma_f32_16x16x32_bf16 v[144:147], a[32:35], v[92:95], v[144:147]\n"
      "buffer_load_dwordx4 v[76:79], v230, s[60:63], s65 offen sc1\n"
      "v_mfma_f32_16x16x32_bf16 v[148:151], a[36:39], v[92:95], v[148:151]\n"
      "ds_write_addtid_b32 v130 offset:2560\n"
      "v_mfma_f32_16x16x32_bf16 v[152:155], a[40:43], v[92:95], v[152:155]\n"
      "ds_write_addtid_b32 v131 offset:2816\n"
      "v_mfma_f32_16x16x32_bf16 v[156:159], a[44:47], v[92:95], v[156:159]\n"
      "ds_write_addtid_b32 v132 offset:3072\n"
      "v_mfma_f32_16x16x32_bf16 v[160:163], a[48:51], v[92:95], v[160:163]\n"
      "ds_write_addtid_b32 v133 offset:3328\n"
      "v_mfma_f32_16x16x32_bf16 v[164:167], a[52:55], v[92:95], v[164:167]\n"
      "ds_write_addtid_b32 v134 offset:3584\n"
      "s_add_u32 s65, s79, s75\n"
      "v_mfma_f32_16x16x32_bf16 v[168:171], a[56:59], v[92:95], v[168:171]\n"
      "buffer_load_dwordx4 v[56:59], v230, s[60:63], s65 offen sc1\n"
      "v_mfma_f32_16x16x32_bf16 v[172:175], a[60:63], v[92:95], v[172:175]\n"
      "ds_write_addtid_b32 v135 offset:3840\n"
      "v_mfma_f32_16x16x32_bf16 v[144:147], a[224:227], v[36:39], v[144:147]\n"
      "ds_write_addtid_b32 v136 offset:4096\n"
      "v_mfma_f32_16x16x32_bf16 v[148:151], a[228:231], v[36:39], v[148:151]\n"
      "ds_write_addtid_b32 v137 offset:4352\n"
      "v_mfma_f32_16x16x32_bf16 v[152:155], a[232:235], v[36:39], v[152:155]\n"
      "ds_write_addtid_b32 v138 offset:4608\n"
      "s_add_u32 s65, s80, s75\n"
      "v_mfma_f32_16x16x32_bf16 v[156:159], a[236:239], v[36:39], v[156:159]\n"
      "buffer_load_dwordx4 v[48:51], v230, s[60:63], s65 offen sc1\n"
      "v_mfma_f32_16x16x32_bf16 v[160:163], a[240:243], v[36:39], v[160:163]\n"
      "ds_write_addtid_b32 v139 offset:4864\n"
      "v_mfma_f32_16x16x32_bf16 v[164:167], a[244:247], v[36:39], v[164:167]\n"
      "ds_write_addtid_b32 v140 offset:5120\n"
      "v_mfma_f32_16x16x32_bf16 v[168:171], a[248:251], v[36:39], v[168:171]\n"
      "ds_write_addtid_b32 v141 offset:5376\n"
      "v_mfma_f32_16x16x32_bf16 v[172:175], a[252:255], v[36:39], v[172:175]\n"
      "ds_write_addtid_b32 v142 offset:5632\n"
      "s_waitcnt vmcnt(15)\n"
      "v_mfma_f32_16x16x32_bf16 v[144:147], a[96:99], v[28:31], v[144:147]\n"
      "ds_write_addtid_b32 v143 offset:5888\n"
      "s_add_u32 s65, s79, s74\n"
      "v_mfma_f32_16x16x32_bf16 v[148:151], a[100:103], v[28:31], v[148:151]\n"
      "buffer_load_dwordx4 v[52:55], v230, s[60:63], s65 offen sc1\n"
      "v_mfma_f32_16x16x32_bf16 v[152:155], a[104:107], v[28:31], v[152:155]\n"
      "s_waitcnt lgkmcnt(0)\n"
      "v_mfma_f32_16x16x32_bf16 v[156:159], a[108:111], v[28:31], v[156:159]\n"
      "s_barrier\n"
      "v_mfma_f32_16x16x32_bf16 v[160:163], a[112:115], v[28:31], v[160:163]\n"
      "ds_read2st64_b32 v[236:237], v218 offset0:0 offset1:1\n"
      "v_mfma_f32_16x16x32_bf16 v[164:167], a[116:119], v[28:31], v[164:167]\n"
      "ds_read2st64_b32 v[238:239], v218 offset0:2 offset1:3\n"
      "v_mfma_f32_16x16x32_bf16 v[168:171], a[120:123], v[28:31], v[168:171]\n"
      "ds_read2st64_b32 v[240:241], v218 offset0:4 offset1:5\n"
      "s_add_u32 s65, s80, s74\n"
      "v_mfma_f32_16x16x32_bf16 v[172:175], a[124:127], v[28:31], v[172:175]\n"
      "buffer_load_dwordx4 v[88:91], v230, s[60:63], s65 offen sc1\n"
      "v_mfma_f32_16x16x32_bf16 v[144:147], a[128:131], v[4:7], v[144:147]\n"
      "ds_read2st64_b32 v[242:243], v218 offset0:6 offset1:7\n"
      "v_mfma_f32_16x16x32_bf16 v[148:151], a[132:135], v[4:7], v[148:151]\n"
      "ds_read2st64_b32 v[244:245], v219 offset0:8 offset1:9\n"
      "v_mfma_f32_16x16x32_bf16 v[152:155], a[136:139], v[4:7], v[152:155]\n"
      "ds_read2st64_b32 v[246:247], v219 offset0:10 offset1:11\n"
      "v_mfma_f32_16x16x32_bf16 v[156:159], a[140:143], v[4:7], v[156:159]\n"
      "ds_read2st64_b32 v[248:249], v219 offset0:12 offset1:13\n"
      "s_add_u32 s65, s80, s73\n"
      "v_mfma_f32_16x16x32_bf16 v[160:163], a[144:147], v[4:7], v[160:163]\n"
      "buffer_load_dwordx4 v[0:3], v230, s[60:63], s65 offen sc1\n"
      "s_waitcnt lgkmcnt(3)\n"
      "v_add_f32 v236, v236, v112\n"
      "v_add_f32 v237, v237, v113\n"
      "v_mfma_f32_16x16x32_bf16 v[164:167], a[148:151], v[4:7], v[164:167]\n"
      "ds_read2st64_b32 v[250:251], v219 offset0:14 offset1:15\n"
      "v_add_f32 v238, v238, v114\n"
      "v_add_f32 v239, v239, v115\n"
      "v_mfma_f32_16x16x32_bf16 v[168:171], a[152:155], v[4:7], v[168:171]\n"
      "ds_read2st64_b32 v[176:177], v220 offset0:16 offset1:17\n"
      "v_add_f32 v240, v240, v116\n"
      "v_add_f32_e64 v241, v241, v117\n"
      "v_mfma_f32_16x16x32_bf16 v[172:175], a[156:159], v[4:7], v[172:175]\n"
      "ds_read2st64_b32 v[178:179], v220 offset0:18 offset1:19\n"
      "v_add_f32 v242, v242, v118\n"
      "v_add_f32 v243, v243, v119\n"
      "s_waitcnt vmcnt(16)\n"
      "v_mfma_f32_16x16x32_bf16 v[144:147], v[180:183], v[16:19], v[144:147]\n"
      "ds_read2st64_b32 v[212:213], v220 offset0:20 offset1:21\n"
      "v_mfma_f32_16x16x32_bf16 v[148:151], v[184:187], v[16:19], v[148:151]\n"
      "ds_read2st64_b32 v[214:215], v220 offset0:22 offset1:23\n"
      "s_add_u32 s65, s79, s73\n"
      "v_mfma_f32_16x16x32_bf16 v[152:155], v[188:191], v[16:19], v[152:155]\n"
      "buffer_load_dwordx4 v[64:67], v230, s[60:63], s65 offen sc1\n"
      "v_mfma_f32_16x16x32_bf16 v[156:159], v[192:195], v[16:19], v[156:159]\n"
      "ds_read2st64_b32 a[80:81], v227 offset0:0 offset1:1\n"
      "v_mfma_f32_16x16x32_bf16 v[160:163], v[196:199], v[16:19], v[160:163]\n"
      "ds_read2st64_b32 a[82:83], v227 offset0:2 offset1:3\n"
      "v_mfma_f32_16x16x32_bf16 v[164:167], v[200:203], v[16:19], v[164:167]\n"
      "ds_read2st64_b32 a[88:89], v227 offset0:192 offset1:193\n"
      "s_waitcnt lgkmcnt(3)\n"
      "v_add_f32 v236, v236, v244\n"
      "v_add_f32 v237, v237, v245\n"
      "v_mfma_f32_16x16x32_bf16 v[168:171], v[204:207], v[16:19], v[168:171]\n"
      "ds_read2st64_b32 a[90:91], v227 offset0:194 offset1:195\n"
      "v_add_f32 v238, v238, v246\n"
      "v_add_f32 v239, v239, v247\n"
      "v_mfma_f32_16x16x32_bf16 v[172:175], v[208:211], v[16:19], v[172:175]\n"
      "s_add_u32 s65, s81, s74\n"
      "v_add_f32 v240, v240, v248\n"
      "v_add_f32 v241, v241, v249\n"
      "v_mfma_f32_16x16x32_bf16 v[144:147], a[192:195], v[96:99], v[144:147]\n"
      "buffer_load_dwordx4 v[24:27], v230, s[60:63], s65 offen sc1\n"
      "v_add_f32 v242, v242, v250\n"
      "v_add_f32 v243, v243, v251\n"
      "v_mfma_f32_16x16x32_bf16 v[148:151], a[196:199], v[96:99], v[148:151]\n"
      "v_add_f32 v40, v236, v176\n"
      "v_add_f32 v41, v237, v177\n"
      "v_mfma_f32_16x16x32_bf16 v[152:155], a[200:203], v[96:99], v[152:155]\n"
      "v_add_f32_e64 v42, v238, v178\n"
      "v_add_f32_e64 v43, v239, v179\n"
      "v_mfma_f32_16x16x32_bf16 v[156:159], a[204:207], v[96:99], v[156:159]\n"
      "v_add_f32_e64 v20, v240, v212\n"
      "v_add_f32_e64 v21, v241, v213\n"
      "v_mfma_f32_16x16x32_bf16 v[160:163], a[208:211], v[96:99], v[160:163]\n"
      "v_add_f32_e64 v22, v242, v214\n"
      "v_add_f32 v23, v243, v215\n"
      "v_mfma_f32_16x16x32_bf16 v[164:167], a[212:215], v[96:99], v[164:167]\n"
      "s_add_u32 s65, s83, s74\n"
      "v_mfma_f32_16x16x32_bf16 v[168:171], a[216:219], v[96:99], v[168:171]\n"
      "buffer_load_dwordx4 v[80:83], v230, s[60:63], s65 offen sc1\n"
      "v_mfma_f32_16x16x32_bf16 v[172:175], a[220:223], v[96:99], v[172:175]\n"
      "s_waitcnt vmcnt(11) lgkmcnt(0)\n"
      "v_mfma_f32_16x16x32_bf16 v[120:123], a[168:171], a[80:83], 0\n"
      "v_mfma_f32_16x16x32_bf16 v[124:127], a[172:175], a[80:83], 0\n"
      "v_mfma_f32_16x16x32_bf16 v[120:123], a[8:11], a[88:91], v[120:123]\n"
      "ds_write_addtid_b32 v152 offset:32768\n"
      "ds_write_addtid_b32 v153 offset:33024\n"
      "v_mfma_f32_16x16x32_bf16 v[124:127], a[12:15], a[88:91], v[124:127]\n"
      "ds_write_addtid_b32 v154 offset:33280\n"
      "ds_write_addtid_b32 v155 offset:33536\n"
      "v_mfma_f32_16x16x32_bf16 v[120:123], a[40:43], a[92:95], v[120:123]\n"
      "ds_write_addtid_b32 v156 offset:33792\n"
      "s_add_u32 s65, s81, s73\n"
      "v_mfma_f32_16x16x32_bf16 v[124:127], a[44:47], a[92:95], v[124:127]\n"
      "ds_write_addtid_b32 v157 offset:34048\n"
      "buffer_load_dwordx4 v[108:111], v230, s[60:63], s65 offen sc1\n"
      "v_mfma_f32_16x16x32_bf16 v[120:123], a[232:235], a[84:87], v[120:123]\n"
      "ds_write_addtid_b32 v158 offset:34304\n"
      "ds_write_addtid_b32 v159 offset:34560\n"
      "v_mfma_f32_16x16x32_bf16 v[124:127], a[236:239], a[84:87], v[124:127]\n"
      "ds_write_addtid_b32 v160 offset:34816\n"
      "ds_write_addtid_b32 v161 offset:35072\n"
      "v_mfma_f32_16x16x32_bf16 v[120:123], a[104:107], a[72:75], v[120:123]\n"
      "ds_write_addtid_b32 v162 offset:35328\n"
      "ds_write_addtid_b32 v163 offset:35584\n"
      "v_mfma_f32_16x16x32_bf16 v[124:127], a[108:111], a[72:75], v[124:127]\n"
      "ds_write_addtid_b32 v164 offset:35840\n"
      "ds_write_addtid_b32 v165 offset:36096\n"
      "v_mfma_f32_16x16x32_bf16 v[120:123], a[136:139], a[64:67], v[120:123]\n"
      "ds_write_addtid_b32 v166 offset:36352\n"
      "ds_write_addtid_b32 v167 offset:36608\n"
      "v_mfma_f32_16x16x32_bf16 v[124:127], a[140:143], a[64:67], v[124:127]\n"
      "ds_write_addtid_b32 v168 offset:36864\n"
      "ds_write_addtid_b32 v169 offset:37120\n"
      "v_mfma_f32_16x16x32_bf16 v[120:123], v[188:191], a[68:71], v[120:123]\n"
      "ds_write_addtid_b32 v170 offset:37376\n"
      "ds_write_addtid_b32 v171 offset:37632\n"
      "v_mfma_f32_16x16x32_bf16 v[124:127], v[192:195], a[68:71], v[124:127]\n"
      "ds_write_addtid_b32 v172 offset:37888\n"
      "ds_write_addtid_b32 v173 offset:38144\n"
      "v_mfma_f32_16x16x32_bf16 v[120:123], a[200:203], a[76:79], v[120:123]\n"
      "ds_write_addtid_b32 v174 offset:38400\n"
      "ds_write_addtid_b32 v175 offset:38656\n"
      "v_mfma_f32_16x16x32_bf16 v[124:127], a[204:207], a[76:79], v[124:127]\n"
      "v_mfma_f32_16x16x32_bf16 v[128:131], a[176:179], a[80:83], 0\n"
      "s_waitcnt lgkmcnt(0)\n"
      "v_mfma_f32_16x16x32_bf16 v[132:135], a[180:183], a[80:83], 0\n"
      "s_barrier\n"
      "v_mfma_f32_16x16x32_bf16 v[128:131], a[16:19], a[88:91], v[128:131]\n"
      "ds_read2st64_b32 v[236:237], v218 offset0:128 offset1:129\n"
      "ds_read2st64_b32 v[238:239], v218 offset0:130 offset1:131\n"
      "v_mfma_f32_16x16x32_bf16 v[132:135], a[20:23], a[88:91], v[132:135]\n"
      "ds_read2st64_b32 v[240:241], v218 offset0:132 offset1:133\n"
      "ds_read2st64_b32 v[242:243], v218 offset0:134 offset1:135\n"
      "v_mfma_f32_16x16x32_bf16 v[128:131], a[48:51], a[92:95], v[128:131]\n"
      "ds_read2st64_b32 v[244:245], v219 offset0:136 offset1:137\n"
      "ds_read2st64_b32 v[246:247], v219 offset0:138 offset1:139\n"
      "v_mfma_f32_16x16x32_bf16 v[132:135], a[52:55], a[92:95], v[132:135]\n"
      "ds_read2st64_b32 v[248:249], v219 offset0:140 offset1:141\n"
      "ds_read2st64_b32 v[250:251], v219 offset0:142 offset1:143\n"
      "v_mfma_f32_16x16x32_bf16 v[128:131], a[240:243], a[84:87], v[128:131]\n"
      "ds_read2st64_b32 v[176:177], v220 offset0:144 offset1:145\n"
      "ds_read2st64_b32 v[178:179], v220 offset0:146 offset1:147\n"
      "v_mfma_f32_16x16x32_bf16 v[132:135], a[244:247], a[84:87], v[132:135]\n"
      "ds_read2st64_b32 v[212:213], v220 offset0:148 offset1:149\n"
      "ds_read2st64_b32 v[214:215], v220 offset0:150 offset1:151\n"
      "s_waitcnt lgkmcnt(8)\n"
      "v_add_f32 v236, v236, v144\n"
      "v_add_f32 v237, v237, v145\n"
      "v_mfma_f32_16x16x32_bf16 v[128:131], a[112:115], a[72:75], v[128:131]\n"
      "ds_write_addtid_b32 v120 offset:0\n"
      "s_waitcnt vmcnt(7)\n"
      "v_lshlrev_b32 v60, 16, v72\n"
      "v_and_b32 v61, 0xffff0000, v72\n"
      "v_mfma_f32_16x16x32_bf16 v[132:135], a[116:119], a[72:75], v[132:135]\n"
      "ds_write_addtid_b32 v121 offset:256\n"
      "v_lshlrev_b32 v62, 16, v73\n"
      "v_and_b32 v63, 0xffff0000, v73\n"
      "v_mfma_f32_16x16x32_bf16 v[128:131], a[144:147], a[64:67], v[128:131]\n"
      "ds_write_addtid_b32 v122 offset:512\n"
      "v_lshlrev_b32_e64 v68, 16, v74\n"
      "v_and_b32 v69, 0xffff0000, v74\n"
      "v_mfma_f32_16x16x32_bf16 v[132:135], a[148:151], a[64:67], v[132:135]\n"
      "ds_write_addtid_b32 v123 offset:768\n"
      "v_lshlrev_b32_e64 v70, 16, v75\n"
      "v_and_b32 v71, 0xffff0000, v75\n"
      "v_mfma_f32_16x16x32_bf16 v[128:131], v[196:199], a[68:71], v[128:131]\n"
      "ds_write_addtid_b32 v124 offset:1024\n"
      "v_lshlrev_b32_e64 v100, 16, v76\n"
      "v_and_b32 v101, 0xffff0000, v76\n"
      "v_mfma_f32_16x16x32_bf16 v[132:135], v[200:203], a[68:71], v[132:135]\n"
      "ds_write_addtid_b32 v125 offset:1280\n"
      "v_lshlrev_b32_e64 v102, 16, v77\n"
      "v_and_b32 v103, 0xffff0000, v77\n"
      "v_mfma_f32_16x16x32_bf16 v[128:131], a[208:211], a[76:79], v[128:131]\n"
      "ds_write_addtid_b32 v126 offset:1536\n"
      "v_lshlrev_b32_e64 v104, 16, v78\n"
      "v_and_b32 v105, 0xffff0000, v78\n"
      "v_mfma_f32_16x16x32_bf16 v[132:135], a[212:215], a[76:79], v[132:135]\n"
      "ds_write_addtid_b32 v127 offset:1792\n"
      "v_lshlrev_b32_e64 v106, 16, v79\n"
      "v_and_b32 v107, 0xffff0000, v79\n"
      "v_mfma_f32_16x16x32_bf16 v[136:139], a[184:187], a[80:83], 0\n"
      "v_lshlrev_b32_e64 v84, 16, v56\n"
      "v_and_b32 v85, 0xffff0000, v56\n"
      "v_mfma_f32_16x16x32_bf16 v[140:143], a[188:191], a[80:83], 0\n"
      "v_lshlrev_b32 v86, 16, v57\n"
      "v_and_b32 v87, 0xffff0000, v57\n"
      "v_mfma_f32_16x16x32_bf16 v[136:139], a[24:27], a[88:91], v[136:139]\n"
      "v_lshlrev_b32 v8, 16, v58\n"
      "v_and_b32 v9, 0xffff0000, v58\n"
      "v_mfma_f32_16x16x32_bf16 v[140:143], a[28:31], a[88:91], v[140:143]\n"
      "ds_write_addtid_b32 v128 offset:2048\n"
      "v_lshlrev_b32_e64 v10, 16, v59\n"
      "v_and_b32 v11, 0xffff0000, v59\n"
      "v_mfma_f32_16x16x32_bf16 v[136:139], a[56:59], a[92:95], v[136:139]\n"
      "ds_write_addtid_b32 v129 offset:2304\n"
      "v_add_f32 v32, v32, v84\n"
      "v_add_f32 v33, v33, v85\n"
      "v_mfma_f32_16x16x32_bf16 v[140:143], a[60:63], a[92:95], v[140:143]\n"
      "ds_write_addtid_b32 v130 offset:2560\n"
      "v_add_f32 v34, v34, v86\n"
      "v_add_f32 v35, v35, v87\n"
      "v_mfma_f32_16x16x32_bf16 v[136:139], a[248:251], a[84:87], v[136:139]\n"
      "ds_write_addtid_b32 v131 offset:2816\n"
      "v_add_f32 v44, v44, v8\n"
      "v_add_f32 v45, v45, v9\n"
      "v_mfma_f32_16x16x32_bf16 v[140:143], a[252:255], a[84:87], v[140:143]\n"
      "ds_write_addtid_b32 v132 offset:3072\n"
      "s_add_u32 s65, s83, s73\n"
      "v_add_f32 v46, v46, v10\n"
      "v_add_f32 v47, v47, v11\n"
      "v_mfma_f32_16x16x32_bf16 v[136:139], a[120:123], a[72:75], v[136:139]\n"
      "buffer_load_dwordx4 v[12:15], v230, s[60:63], s65 offen sc1\n"
      "v_lshlrev_b32 v84, 16, v48\n"
      "v_and_b32 v85, 0xffff0000, v48\n"
      "v_mfma_f32_16x16x32_bf16 v[140:143], a[124:127], a[72:75], v[140:143]\n"
      "ds_write_addtid_b32 v133 offset:3328\n"
      "v_lshlrev_b32_e64 v86, 16, v49\n"
      "v_and_b32 v87, 0xffff0000, v49\n"
      "v_mfma_f32_16x16x32_bf16 v[136:139], a[152:155], a[64:67], v[136:139]\n"
      "ds_write_addtid_b32 v134 offset:3584\n"
      "v_lshlrev_b32_e64 v8, 16, v50\n"
      "v_and_b32 v9, 0xffff0000, v50\n"
      "v_mfma_f32_16x16x32_bf16 v[140:143], a[156:159], a[64:67], v[140:143]\n"
      "ds_write_addtid_b32 v135 offset:3840\n"
      "v_lshlrev_b32_e64 v10, 16, v51\n"
      "v_and_b32 v11, 0xffff0000, v51\n"
      "v_mfma_f32_16x16x32_bf16 v[136:139], v[204:207], a[68:71], v[136:139]\n"
      "v_add_f32 v40, v40, v84\n"
      "v_add_f32 v41, v41, v85\n"
      "v_mfma_f32_16x16x32_bf16 v[140:143], v[208:211], a[68:71], v[140:143]\n"
      "v_add_f32 v42, v42, v86\n"
      "v_add_f32 v43, v43, v87\n"
      "v_mfma_f32_16x16x32_bf16 v[136:139], a[216:219], a[76:79], v[136:139]\n"
      "v_add_f32 v20, v20, v8\n"
      "v_add_f32 v21, v21, v9\n"
      "v_mfma_f32_16x16x32_bf16 v[140:143], a[220:223], a[76:79], v[140:143]\n"
      "v_add_f32 v22, v22, v10\n"
      "v_add_f32 v23, v23, v11\n"
      "v_mfma_f32_16x16x32_bf16 v[112:115], a[160:163], a[80:83], 0\n"
      "v_add_f32 v238, v238, v146\n"
      "v_add_f32 v239, v239, v147\n"
      "v_mfma_f32_16x16x32_bf16 v[116:119], a[164:167], a[80:83], 0\n"
      "v_add_f32 v240, v240, v148\n"
      "v_add_f32 v241, v241, v149\n"
      "v_mfma_f32_16x16x32_bf16 v[112:115], a[0:3], a[88:91], v[112:115]\n"
      "ds_write_addtid_b32 v136 offset:4096\n"
      "ds_write_addtid_b32 v137 offset:4352\n"
      "v_add_f32 v242, v242, v150\n"
      "v_add_f32_e64 v243, v243, v151\n"
      "s_waitcnt lgkmcnt(15)\n"
      "v_mfma_f32_16x16x32_bf16 v[116:119], a[4:7], a[88:91], v[116:119]\n"
      "ds_write_addtid_b32 v138 offset:4608\n"
      "ds_write_addtid_b32 v139 offset:4864\n"
      "v_add_f32 v236, v236, v244\n"
      "v_add_f32 v237, v237, v245\n"
      "v_mfma_f32_16x16x32_bf16 v[112:115], a[32:35], a[92:95], v[112:115]\n"
      "ds_write_addtid_b32 v140 offset:5120\n"
      "ds_write_addtid_b32 v141 offset:5376\n"
      "v_add_f32 v238, v238, v246\n"
      "v_add_f32 v239, v239, v247\n"
      "v_mfma_f32_16x16x32_bf16 v[116:119], a[36:39], a[92:95], v[116:119]\n"
      "ds_write_addtid_b32 v142 offset:5632\n"
      "ds_write_addtid_b32 v143 offset:5888\n"
      "v_add_f32 v240, v240, v248\n"
      "v_add_f32 v241, v241, v249\n"
      "v_mfma_f32_16x16x32_bf16 v[112:115], a[224:227], a[84:87], v[112:115]\n"
      "v_add_f32 v242, v242, v250\n"
      "v_add_f32 v243, v243, v251\n"
      "v_mfma_f32_16x16x32_bf16 v[116:119], a[228:231], a[84:87], v[116:119]\n"
      "v_add_f32 v236, v236, v176\n"
      "v_add_f32 v237, v237, v177\n"
      "v_mfma_f32_16x16x32_bf16 v[112:115], a[96:99], a[72:75], v[112:115]\n"
      "v_add_f32 v238, v238, v178\n"
      "v_add_f32 v239, v239, v179\n"
      "v_mfma_f32_16x16x32_bf16 v[116:119], a[100:103], a[72:75], v[116:119]\n"
      "s_waitcnt lgkmcnt(0)\n"
      "s_barrier\n"
      "v_add_f32 v240, v240, v212\n"
      "v_add_f32 v241, v241, v213\n"
      "v_mfma_f32_16x16x32_bf16 v[112:115], a[128:131], a[64:67], v[112:115]\n"
      "ds_read2st64_b32 v[152:153], v218 offset0:0 offset1:1\n"
      "ds_read2st64_b32 v[154:155], v218 offset0:2 offset1:3\n"
      "v_add_f32 v242, v242, v214\n"
      "v_add_f32 v243, v243, v215\n"
      "v_mfma_f32_16x16x32_bf16 v[116:119], a[132:135], a[64:67], v[116:119]\n"
      "ds_read2st64_b32 v[156:157], v218 offset0:4 offset1:5\n"
      "ds_read2st64_b32 v[158:159], v218 offset0:6 offset1:7\n"
      "v_add_f32 v60, v60, v236\n"
      "v_add_f32 v61, v61, v237\n"
      "v_mfma_f32_16x16x32_bf16 v[112:115], v[180:183], a[68:71], v[112:115]\n"
      "ds_read2st64_b32 v[160:161], v219 offset0:8 offset1:9\n"
      "ds_read2st64_b32 v[162:163], v219 offset0:10 offset1:11\n"
      "v_add_f32 v62, v62, v238\n"
      "v_add_f32 v63, v63, v239\n"
      "v_mfma_f32_16x16x32_bf16 v[116:119], v[184:187], a[68:71], v[116:119]\n"
      "ds_read2st64_b32 v[164:165], v219 offset0:12 offset1:13\n"
      "ds_read2st64_b32 v[166:167], v219 offset0:14 offset1:15\n"
      "v_add_f32 v68, v68, v240\n"
      "v_add_f32 v69, v69, v241\n"
      "v_mfma_f32_16x16x32_bf16 v[112:115], a[192:195], a[76:79], v[112:115]\n"
      "ds_read2st64_b32 v[168:169], v220 offset0:16 offset1:17\n"
      "ds_read2st64_b32 v[170:171], v220 offset0:18 offset1:19\n"
      "v_add_f32 v70, v70, v242\n"
      "v_add_f32 v71, v71, v243\n"
      "v_mfma_f32_16x16x32_bf16 v[116:119], a[196:199], a[76:79], v[116:119]\n"
      "ds_read2st64_b32 v[172:173], v220 offset0:20 offset1:21\n"
      "ds_read2st64_b32 v[174:175], v220 offset0:22 offset1:23\n"
      "s_waitcnt vmcnt(5)\n"
      "v_max_u32 v254, v0, v1\n"
      "v_max3_u32 v254, v254, v2, v3\n"
      "v_cmp_eq_u32 vcc, -1, v254\n"
      "s_nop 0\n"
      "s_cbranch_vccnz PV1_%=\n"
      "PV1R_%=:\n"
      "v_lshlrev_b32 v84, 16, v52\n"
      "v_and_b32 v85, 0xffff0000, v52\n"
      "v_lshlrev_b32_e64 v86, 16, v53\n"
      "v_and_b32 v87, 0xffff0000, v53\n"
      "v_lshlrev_b32 v8, 16, v54\n"
      "v_and_b32 v9, 0xffff0000, v54\n"
      "v_lshlrev_b32 v10, 16, v55\n"
      "v_and_b32 v11, 0xffff0000, v55\n"
      "v_pk_add_f32 v[32:33], v[32:33], v[84:85]\n"
      "v_pk_add_f32 v[34:35], v[34:35], v[86:87]\n"
      "v_pk_add_f32 v[44:45], v[44:45], v[8:9]\n"
      "v_pk_add_f32 v[46:47], v[46:47], v[10:11]\n"
      "v_lshlrev_b32 v84, 16, v88\n"
      "v_and_b32 v85, 0xffff0000, v88\n"
      "v_lshlrev_b32 v86, 16, v89\n"
      "v_and_b32 v87, 0xffff0000, v89\n"
      "v_lshlrev_b32_e64 v8, 16, v90\n"
      "v_and_b32 v9, 0xffff0000, v90\n"
      "v_lshlrev_b32_e64 v10, 16, v91\n"
      "v_and_b32 v11, 0xffff0000, v91\n"
      "v_pk_add_f32 v[40:41], v[40:41], v[84:85]\n"
      "v_pk_add_f32 v[42:43], v[42:43], v[86:87]\n"
      "v_pk_add_f32 v[20:21], v[20:21], v[8:9]\n"
      "v_pk_add_f32 v[22:23], v[22:23], v[10:11]\n"
      "v_lshlrev_b32_e64 v84, 16, v0\n"
      "v_and_b32 v85, 0xffff0000, v0\n"
      "v_lshlrev_b32 v86, 16, v1\n"
      "v_and_b32 v87, 0xffff0000, v1\n"
      "v_lshlrev_b32 v8, 16, v2\n"
      "v_and_b32 v9, 0xffff0000, v2\n"
      "v_lshlrev_b32_e64 v10, 16, v3\n"
      "v_and_b32 v11, 0xffff0000, v3\n"
      "v_pk_add_f32 v[40:41], v[40:41], v[84:85]\n"
      "v_pk_add_f32 v[42:43], v[42:43], v[86:87]\n"
      "v_pk_add_f32 v[20:21], v[20:21], v[8:9]\n"
      "v_pk_add_f32 v[22:23], v[22:23], v[10:11]\n"
      "v_cvt_pk_bf16_f32 v40, v40, v41\n"
      "v_cvt_pk_bf16_f32 v41, v42, v43\n"
      "v_cvt_pk_bf16_f32 v42, v20, v21\n"
      "v_cvt_pk_bf16_f32 v43, v22, v23\n"
      "s_add_u32 s66, s80, s90\n"
      "buffer_store_dwordx4 v[40:43], v229, s[44:47], s66 offen sc1 nt\n"
      "s_waitcnt vmcnt(3)\n"
      "v_max_u32_e64 v254, v64, v65\n"
      "v_max3_u32 v254, v254, v66, v67\n"
      "v_max3_u32 v254, v254, v24, v25\n"
      "v_max3_u32 v254, v254, v26, v27\n"
      "v_max3_u32 v254, v254, v80, v81\n"
      "v_max3_u32 v254, v254, v82, v83\n"
      "v_cmp_eq_u32 vcc, -1, v254\n"
      "s_nop 0\n"
      "s_cbranch_vccnz PV2_%=\n"
      "PV2R_%=:\n"
      "v_lshlrev_b32 v84, 16, v64\n"
      "v_and_b32 v85, 0xffff0000, v64\n"
      "v_lshlrev_b32 v86, 16, v65\n"
      "v_and_b32 v87, 0xffff0000, v65\n"
      "v_lshlrev_b32 v8, 16, v66\n"
      "v_and_b32 v9, 0xffff0000, v66\n"
      "v_lshlrev_b32_e64 v10, 16, v67\n"
      "v_and_b32 v11, 0xffff0000, v67\n"
      "v_pk_add_f32 v[32:33], v[32:33], v[84:85]\n"
      "v_pk_add_f32 v[34:35], v[34:35], v[86:87]\n"
      "v_pk_add_f32 v[44:45], v[44:45], v[8:9]\n"
      "v_pk_add_f32 v[46:47], v[46:47], v[10:11]\n"
      "v_cvt_pk_bf16_f32 v32, v32, v33\n"
      "v_cvt_pk_bf16_f32 v33, v34, v35\n"
      "v_cvt_pk_bf16_f32 v34, v44, v45\n"
      "v_cvt_pk_bf16_f32 v35, v46, v47\n"
      "s_add_u32 s66, s79, s90\n"
      "buffer_store_dwordx4 v[32:35], v229, s[44:47], s66 offen sc1 nt\n"
      "v_lshlrev_b32 v84, 16, v24\n"
      "v_and_b32 v85, 0xffff0000, v24\n"
      "v_lshlrev_b32 v86, 16, v25\n"
      "v_and_b32 v87, 0xffff0000, v25\n"
      "v_lshlrev_b32 v8, 16, v26\n"
      "v_and_b32 v9, 0xffff0000, v26\n"
      "v_lshlrev_b32_e64 v10, 16, v27\n"
      "v_and_b32 v11, 0xffff0000, v27\n"
      "v_pk_add_f32 v[60:61], v[60:61], v[84:85]\n"
      "v_pk_add_f32 v[62:63], v[62:63], v[86:87]\n"
      "v_pk_add_f32 v[68:69], v[68:69], v[8:9]\n"
      "v_pk_add_f32 v[70:71], v[70:71], v[10:11]\n"
      "v_lshlrev_b32_e64 v84, 16, v80\n"
      "v_and_b32 v85, 0xffff0000, v80\n"
      "v_lshlrev_b32_e64 v86, 16, v81\n"
      "v_and_b32 v87, 0xffff0000, v81\n"
      "v_lshlrev_b32_e64 v8, 16, v82\n"
      "v_and_b32 v9, 0xffff0000, v82\n"
      "v_lshlrev_b32 v10, 16, v83\n"
      "v_and_b32 v11, 0xffff0000, v83\n"
      "v_pk_add_f32 v[100:101], v[100:101], v[84:85]\n"
      "v_pk_add_f32 v[102:103], v[102:103], v[86:87]\n"
      "v_pk_add_f32 v[104:105], v[104:105], v[8:9]\n"
      "v_pk_add_f32 v[106:107], v[106:107], v[10:11]\n"
      "s_waitcnt lgkmcnt(0)\n"
      "v_pk_add_f32 v[152:153], v[152:153], v[112:113]\n"
      "v_pk_add_f32 v[154:155], v[154:155], v[114:115]\n"
      "v_pk_add_f32 v[156:157], v[156:157], v[116:117]\n"
      "v_pk_add_f32 v[158:159], v[158:159], v[118:119]\n"
      "v_pk_add_f32 v[160:161], v[160:161], v[168:169]\n"
      "v_pk_add_f32 v[162:163], v[162:163], v[170:171]\n"
      "v_pk_add_f32 v[164:165], v[164:165], v[172:173]\n"
      "v_pk_add_f32 v[166:167], v[166:167], v[174:175]\n"
      "v_pk_add_f32 v[152:153], v[152:153], v[160:161]\n"
      "v_pk_add_f32 v[154:155], v[154:155], v[162:163]\n"
      "v_pk_add_f32 v[156:157], v[156:157], v[164:165]\n"
      "v_pk_add_f32 v[158:159], v[158:159], v[166:167]\n"
      "v_pk_add_f32 v[100:101], v[100:101], v[152:153]\n"
      "v_pk_add_f32 v[102:103], v[102:103], v[154:155]\n"
      "v_pk_add_f32 v[104:105], v[104:105], v[156:157]\n"
      "v_pk_add_f32 v[106:107], v[106:107], v[158:159]\n"
      "s_waitcnt vmcnt(3)\n"
      "v_max_u32 v254, v108, v109\n"
      "v_max3_u32 v254, v254, v110, v111\n"
      "v_cmp_eq_u32 vcc, -1, v254\n"
      "s_nop 0\n"
      "s_cbranch_vccnz PV3_%=\n"
      "PV3R_%=:\n"
      "v_lshlrev_b32 v84, 16, v108\n"
      "v_and_b32 v85, 0xffff0000, v108\n"
      "v_lshlrev_b32_e64 v86, 16, v109\n"
      "v_and_b32 v87, 0xffff0000, v109\n"
      "v_lshlrev_b32 v8, 16, v110\n"
      "v_and_b32 v9, 0xffff0000, v110\n"
      "v_lshlrev_b32 v10, 16, v111\n"
      "v_and_b32 v11, 0xffff0000, v111\n"
      "v_pk_add_f32 v[60:61], v[60:61], v[84:85]\n"
      "v_pk_add_f32 v[62:63], v[62:63], v[86:87]\n"
      "v_pk_add_f32 v[68:69], v[68:69], v[8:9]\n"
      "v_pk_add_f32 v[70:71], v[70:71], v[10:11]\n"
      "v_cvt_pk_bf16_f32 v60, v60, v61\n"
      "v_cvt_pk_bf16_f32 v61, v62, v63\n"
      "v_cvt_pk_bf16_f32 v62, v68, v69\n"
      "v_cvt_pk_bf16_f32 v63, v70, v71\n"
      "s_add_u32 s66, s81, s90\n"
      "buffer_store_dwordx4 v[60:63], v229, s[44:47], s66 offen sc1 nt\n"
      "s_waitcnt vmcnt(3)\n"
      "v_max_u32 v254, v12, v13\n"
      "v_max3_u32 v254, v254, v14, v15\n"
      "v_cmp_eq_u32 vcc, -1, v254\n"
      "s_nop 0\n"
      "s_cbranch_vccnz PV4_%=\n"
      "PV4R_%=:\n"
      "v_lshlrev_b32 v84, 16, v12\n"
      "v_and_b32 v85, 0xffff0000, v12\n"
      "v_lshlrev_b32 v86, 16, v13\n"
      "v_and_b32 v87, 0xffff0000, v13\n"
      "v_lshlrev_b32_e64 v8, 16, v14\n"
      "v_and_b32 v9, 0xffff0000, v14\n"
      "v_lshlrev_b32_e64 v10, 16, v15\n"
      "v_and_b32 v11, 0xffff0000, v15\n"
      "v_pk_add_f32 v[100:101], v[100:101], v[84:85]\n"
      "v_pk_add_f32 v[102:103], v[102:103], v[86:87]\n"
      "v_pk_add_f32 v[104:105], v[104:105], v[8:9]\n"
      "v_pk_add_f32 v[106:107], v[106:107], v[10:11]\n"
      "v_cvt_pk_bf16_f32 v100, v100, v101\n"
      "v_cvt_pk_bf16_f32 v101, v102, v103\n"
      "v_cvt_pk_bf16_f32 v102, v104, v105\n"
      "v_cvt_pk_bf16_f32 v103, v106, v107\n"
      "s_add_u32 s66, s83, s90\n"
      "buffer_store_dwordx4 v[100:103], v229, s[44:47], s66 offen sc1 nt\n"
      "s_or_b32 s64, %[g], %[j]\n"
      "s_or_b32 s64, s64, %[wid]\n"
      "s_lshl_b32 s65, %[b], 7\n"
      "s_add_u32 s65, s65, 16384\n"
      "s_cmp_eq_u32 s64, 0\n"
      "s_cselect_b32 s65, s65, 1879048192\n"
      "v_mov_b32 v223, s82\n"
      "buffer_store_dword v223, v231, s[60:63], s65 offen sc1\n"
      "MAINEND_%=:\n"
      "s_endpgm\n"
      "PC1_%=:\n"
      "s_sleep 1\n"
      "s_memrealtime s[76:77]\n"
      "s_waitcnt lgkmcnt(0)\n"
      "s_sub_u32 s77, s76, s96\n"
      "s_cmp_lt_i32 s77, 0\n"
      "s_cbranch_scc1 PC1_%=\n"
      "s_mov_b32 s76, s96\n"
      "s_branch PC1R_%=\n"
      "PC2_%=:\n"
      "s_sleep 1\n"
      "s_memrealtime s[76:77]\n"
      "s_waitcnt lgkmcnt(0)\n"
      "s_sub_u32 s77, s76, s96\n"
      "s_cmp_lt_i32 s77, 0\n"
      "s_cbranch_scc1 PC2_%=\n"
      "s_mov_b32 s76, s96\n"
      "s_branch PC2R_%=\n"
      "PC0_%=:\n"
      "s_sleep 1\n"
      "s_memrealtime s[76:77]\n"
      "s_waitcnt lgkmcnt(0)\n"
      "s_sub_u32 s77, s76, s96\n"
      "s_cmp_lt_i32 s77, 0\n"
      "s_cbranch_scc1 PC0_%=\n"
      "s_mov_b32 s76, s96\n"
      "s_branch PC0R_%=\n"
      "PC33_%=:\n"
      "s_sleep 1\n"
      "s_memrealtime s[76:77]\n"
      "s_waitcnt lgkmcnt(0)\n"
      "s_sub_u32 s77, s76, s96\n"
      "s_cmp_lt_i32 s77, 0\n"
      "s_cbranch_scc1 PC33_%=\n"
      "s_mov_b32 s76, s96\n"
      "s_branch PC33R_%=\n"
      "PA_%=:\n"
      "buffer_load_dword v254, v214, s[60:63], s92 offen sc1\n"
      "s_waitcnt vmcnt(0)\n"
      "s_add_u32 s97, s82, 1\n"
      "v_subrev_u32 v254, s97, v254\n"
      "v_cmp_lt_u32 vcc, 6, v254\n"
      "s_nop 0\n"
      "s_cbranch_vccnz PA_%=\n"
      "s_branch PAR_%=\n"
      "PB_%=:\n"
      "buffer_load_dword v254, v226, s[60:63], s92 offen sc1\n"
      "s_waitcnt vmcnt(0)\n"
      "s_add_u32 s97, s82, 3\n"
      "v_subrev_u32 v254, s97, v254\n"
      "v_cmp_lt_u32_e64 vcc, 4, v254\n"
      "s_nop 0\n"
      "s_cbranch_vccnz PB_%=\n"
      "s_branch PBR_%=\n"
      "DS1_%=:\n"
      "s_sleep 9\n"
      "s_branch DS1R_%=\n"
      "PD4_%=:\n"
      "buffer_load_dword v254, v226, s[60:63], s92 offen sc1\n"
      "s_waitcnt vmcnt(0)\n"
      "s_add_u32 s97, s82, 5\n"
      "v_subrev_u32 v254, s97, v254\n"
      "v_cmp_lt_u32 vcc, 2, v254\n"
      "s_nop 0\n"
      "s_cbranch_vccnz PD4_%=\n"
      "s_branch PD4R_%=\n"
      "DS3_%=:\n"
      "s_sleep 9\n"
      "s_branch DS3R_%=\n"
      "PD6_%=:\n"
      "buffer_load_dword v254, v226, s[60:63], s92 offen sc1\n"
      "s_waitcnt vmcnt(0)\n"
      "s_add_u32 s97, s82, 7\n"
      "v_subrev_u32 v254, s97, v254\n"
      "v_cmp_lt_u32 vcc, 0, v254\n"
      "s_nop 0\n"
      "s_cbranch_vccnz PD6_%=\n"
      "s_branch PD6R_%=\n"
      "PE5_%=:\n"
      "buffer_load_dword v254, v226, s[60:63], s94 offen sc1\n"
      "s_waitcnt vmcnt(0)\n"
      "s_add_u32 s97, s82, 5\n"
      "v_subrev_u32 v254, s97, v254\n"
      "v_cmp_lt_u32 vcc, 2, v254\n"
      "s_nop 0\n"
      "s_cbranch_vccnz PE5_%=\n"
      "s_branch PE5R_%=\n"
      "PV1_%=:\n"
      "s_bfe_u64 s[42:43], %[ttab], 262188\n"
      "s_lshl_b32 s42, s42, 14\n"
      "s_add_u32 s42, s42, s73\n"
      "buffer_load_dwordx4 v[0:3], v230, s[60:63], s42 offen sc1\n"
      "s_waitcnt vmcnt(0)\n"
      "v_max_u32 v254, v0, v1\n"
      "v_max3_u32 v254, v254, v2, v3\n"
      "v_cmp_eq_u32 vcc, -1, v254\n"
      "s_nop 0\n"
      "s_cbranch_vccnz PV1_%=\n"
      "s_branch PV1R_%=\n"
      "PV2_%=:\n"
      "s_bfe_u64 s[42:43], %[ttab], 262184\n"
      "s_lshl_b32 s42, s42, 14\n"
      "s_add_u32 s42, s42, s73\n"
      "s_cmp_lt_u32 %[g], 2\n"
      "s_cselect_b32 s42, s42, 1879048192\n"
      "buffer_load_dwordx4 v[64:67], v230, s[60:63], s42 offen sc1\n"
      "s_bfe_u64 s[42:43], %[ttab], 262192\n"
      "s_lshl_b32 s42, s42, 14\n"
      "s_add_u32 s42, s42, s74\n"
      "buffer_load_dwordx4 v[24:27], v230, s[60:63], s42 offen sc1\n"
      "s_bfe_u64 s[42:43], %[ttab], 262196\n"
      "s_lshl_b32 s42, s42, 14\n"
      "s_add_u32 s42, s42, s74\n"
      "buffer_load_dwordx4 v[80:83], v230, s[60:63], s42 offen sc1\n"
      "s_waitcnt vmcnt(0)\n"
      "v_max_u32 v254, v64, v65\n"
      "v_max3_u32 v254, v254, v66, v67\n"
      "v_max3_u32 v254, v254, v24, v25\n"
      "v_max3_u32 v254, v254, v26, v27\n"
      "v_max3_u32 v254, v254, v80, v81\n"
      "v_max3_u32 v254, v254, v82, v83\n"
      "v_cmp_eq_u32 vcc, -1, v254\n"
      "s_nop 0\n"
      "s_cbranch_vccnz PV2_%=\n"
      "s_branch PV2R_%=\n"
      "PV3_%=:\n"
      "s_bfe_u64 s[42:43], %[ttab], 262192\n"
      "s_lshl_b32 s42, s42, 14\n"
      "s_add_u32 s42, s42, s73\n"
      "buffer_load_dwordx4 v[108:111], v230, s[60:63], s42 offen sc1\n"
      "s_waitcnt vmcnt(0)\n"
      "v_max_u32 v254, v108, v109\n"
      "v_max3_u32 v254, v254, v110, v111\n"
      "v_cmp_eq_u32 vcc, -1, v254\n"
      "s_nop 0\n"
      "s_cbranch_vccnz PV3_%=\n"
      "s_branch PV3R_%=\n"
      "PV4_%=:\n"
      "s_bfe_u64 s[42:43], %[ttab], 262196\n"
      "s_lshl_b32 s42, s42, 14\n"
      "s_add_u32 s42, s42, s73\n"
      "buffer_load_dwordx4 v[12:15], v230, s[60:63], s42 offen sc1\n"
      "s_waitcnt vmcnt(0)\n"
      "v_max_u32 v254, v12, v13\n"
      "v_max3_u32 v254, v254, v14, v15\n"
      "v_cmp_eq_u32 vcc, -1, v254\n"
      "s_nop 0\n"
      "s_cbranch_vccnz PV4_%=\n"
      "s_branch PV4R_%=\n"
      "OOLEND_%=:\n"
      "ATTB_%=:\n"
      "v_mfma_f32_16x16x32_bf16 v[152:155], a[224:227], v[48:51], 0\n"
      "v_exp_f32_e64 v144, v144\n"
      "v_mfma_f32_16x16x32_bf16 a[32:35], a[192:195], v[176:179], a[32:35]\n"
      "v_add_f32 v217, v217, v161\n"
      "v_add_f32 v217, v217, v160\n"
      "v_mfma_f32_16x16x32_bf16 v[156:159], a[228:231], v[48:51], 0\n"
      "v_exp_f32_e64 v145, v145\n"
      "v_mfma_f32_16x16x32_bf16 a[36:39], a[196:199], v[176:179], a[36:39]\n"
      "v_add_f32_e64 v217, v217, v162\n"
      "v_cvt_pk_bf16_f32 v168, v144, v145\n"
      "v_mfma_f32_16x16x32_bf16 v[152:155], a[232:235], v[52:55], v[152:155]\n"
      "s_memrealtime s[76:77]\n"
      "v_exp_f32 v146, v146\n"
      "v_mfma_f32_16x16x32_bf16 a[40:43], a[200:203], v[176:179], a[40:43]\n"
      "v_add_f32 v217, v217, v164\n"
      "v_add_f32_e64 v217, v217, v163\n"
      "v_mfma_f32_16x16x32_bf16 v[156:159], a[236:239], v[52:55], v[156:159]\n"
      "v_exp_f32 v147, v147\n"
      "v_mfma_f32_16x16x32_bf16 a[44:47], a[204:207], v[176:179], a[44:47]\n"
      "v_add_f32 v217, v217, v165\n"
      "v_cvt_pk_bf16_f32 v169, v146, v147\n"
      "v_mfma_f32_16x16x32_bf16 v[152:155], a[240:243], v[56:59], v[152:155]\n"
      "s_waitcnt vmcnt(8) lgkmcnt(0)\n"
      "v_exp_f32 v148, v148\n"
      "v_mfma_f32_16x16x32_bf16 a[16:19], a[208:211], v[172:175], a[16:19]\n"
      "s_sub_u32 s77, s76, s96\n"
      "s_cmp_lt_i32 s77, 0\n"
      "s_cbranch_scc1 PC1B_%=\n"
      "PC1RB_%=:\n"
      "v_add_f32 v217, v217, v167\n"
      "v_add_f32 v217, v217, v166\n"
      "v_mfma_f32_16x16x32_bf16 v[156:159], a[244:247], v[56:59], v[156:159]\n"
      "s_barrier\n"
      "v_exp_f32_e64 v149, v149\n"
      "v_mfma_f32_16x16x32_bf16 a[20:23], a[212:215], v[172:175], a[20:23]\n"
      "s_add_u32 s96, s76, s78\n"
      "v_add_f32 v218, v218, v144\n"
      "v_cvt_pk_bf16_f32 v170, v148, v149\n"
      "v_mfma_f32_16x16x32_bf16 v[152:155], a[248:251], v[60:63], v[152:155]\n"
      "v_exp_f32 v150, v150\n"
      "v_mfma_f32_16x16x32_bf16 a[24:27], a[216:219], v[172:175], a[24:27]\n"
      "s_add_u32 m0, s68, 16384\n"
      "v_add_f32 v218, v218, v146\n"
      "v_add_f32_e64 v218, v218, v145\n"
      "v_mfma_f32_16x16x32_bf16 v[156:159], a[252:255], v[60:63], v[156:159]\n"
      "buffer_load_dwordx4 v230, s[52:55], s80 offen offset:0 lds\n"
      "v_exp_f32 v151, v151\n"
      "v_mfma_f32_16x16x32_bf16 a[28:31], a[220:223], v[172:175], a[28:31]\n"
      "v_add_f32 v218, v218, v147\n"
      "v_cvt_pk_bf16_f32 v171, v150, v151\n"
      "v_mfma_f32_16x16x32_bf16 v[160:163], a[224:227], v[64:67], 0\n"
      "v_exp_f32 v152, v152\n"
      "v_mfma_f32_16x16x32_bf16 a[64:67], a[192:195], v[168:171], a[64:67]\n"
      "v_add_f32 v218, v218, v149\n"
      "v_add_f32 v218, v218, v148\n"
      "v_mfma_f32_16x16x32_bf16 v[164:167], a[228:231], v[64:67], 0\n"
      "v_exp_f32 v153, v153\n"
      "v_mfma_f32_16x16x32_bf16 a[68:71], a[196:199], v[168:171], a[68:71]\n"
      "v_add_f32_e64 v218, v218, v150\n"
      "v_cvt_pk_bf16_f32 v172, v152, v153\n"
      "v_mfma_f32_16x16x32_bf16 v[160:163], a[232:235], v[68:71], v[160:163]\n"
      "v_exp_f32 v154, v154\n"
      "v_mfma_f32_16x16x32_bf16 a[72:75], a[200:203], v[168:171], a[72:75]\n"
      "v_add_f32 v219, v219, v152\n"
      "v_add_f32 v218, v218, v151\n"
      "v_mfma_f32_16x16x32_bf16 v[164:167], a[236:239], v[68:71], v[164:167]\n"
      "v_exp_f32 v155, v155\n"
      "v_mfma_f32_16x16x32_bf16 a[76:79], a[204:207], v[168:171], a[76:79]\n"
      "v_add_f32_e64 v219, v219, v153\n"
      "v_cvt_pk_bf16_f32 v173, v154, v155\n"
      "v_mfma_f32_16x16x32_bf16 v[160:163], a[240:243], v[72:75], v[160:163]\n"
      "v_exp_f32 v156, v156\n"
      "v_mfma_f32_16x16x32_bf16 a[48:51], a[208:211], v[176:179], a[48:51]\n"
      "v_add_f32 v219, v219, v155\n"
      "v_add_f32 v219, v219, v154\n"
      "v_mfma_f32_16x16x32_bf16 v[164:167], a[244:247], v[72:75], v[164:167]\n"
      "buffer_load_dwordx4 v231, s[52:55], s80 offen offset:1024 lds\n"
      "v_exp_f32 v157, v157\n"
      "v_mfma_f32_16x16x32_bf16 a[52:55], a[212:215], v[176:179], a[52:55]\n"
      "v_add_f32 v219, v219, v156\n"
      "v_cvt_pk_bf16_f32 v174, v156, v157\n"
      "v_mfma_f32_16x16x32_bf16 v[160:163], a[248:251], v[76:79], v[160:163]\n"
      "v_exp_f32 v158, v158\n"
      "v_mfma_f32_16x16x32_bf16 a[56:59], a[216:219], v[176:179], a[56:59]\n"
      "v_add_f32 v219, v219, v158\n"
      "v_add_f32 v219, v219, v157\n"
      "v_mfma_f32_16x16x32_bf16 v[164:167], a[252:255], v[76:79], v[164:167]\n"
      "v_exp_f32 v159, v159\n"
      "v_mfma_f32_16x16x32_bf16 a[60:63], a[220:223], v[176:179], a[60:63]\n"
      "v_add_f32 v219, v219, v159\n"
      "v_cvt_pk_bf16_f32 v175, v158, v159\n"
      "v_mfma_f32_16x16x32_bf16 v[144:147], a[224:227], v[80:83], 0\n"
      "v_exp_f32 v160, v160\n"
      "v_mfma_f32_16x16x32_bf16 a[96:99], a[192:195], v[172:175], a[96:99]\n"
      "v_add_f32 v220, v220, v160\n"
      "v_mfma_f32_16x16x32_bf16 v[148:151], a[228:231], v[80:83], 0\n"
      "v_exp_f32 v161, v161\n"
      "v_mfma_f32_16x16x32_bf16 a[100:103], a[196:199], v[172:175], a[100:103]\n"
      "v_add_f32 v220, v220, v161\n"
      "v_cvt_pk_bf16_f32 v176, v160, v161\n"
      "v_mfma_f32_16x16x32_bf16 v[144:147], a[232:235], v[84:87], v[144:147]\n"
      "v_exp_f32 v162, v162\n"
      "v_mfma_f32_16x16x32_bf16 a[104:107], a[200:203], v[172:175], a[104:107]\n"
      "v_add_f32 v220, v220, v162\n"
      "v_mfma_f32_16x16x32_bf16 v[148:151], a[236:239], v[84:87], v[148:151]\n"
      "buffer_load_dwordx4 v230, s[52:55], s80 offen offset:2048 lds\n"
      "v_exp_f32 v163, v163\n"
      "v_mfma_f32_16x16x32_bf16 a[108:111], a[204:207], v[172:175], a[108:111]\n"
      "v_add_f32 v220, v220, v163\n"
      "v_cvt_pk_bf16_f32 v177, v162, v163\n"
      "v_mfma_f32_16x16x32_bf16 v[144:147], a[240:243], v[88:91], v[144:147]\n"
      "v_exp_f32 v164, v164\n"
      "v_mfma_f32_16x16x32_bf16 a[80:83], a[208:211], v[168:171], a[80:83]\n"
      "v_add_f32 v220, v220, v164\n"
      "v_mfma_f32_16x16x32_bf16 v[148:151], a[244:247], v[88:91], v[148:151]\n"
      "v_exp_f32 v165, v165\n"
      "v_mfma_f32_16x16x32_bf16 a[84:87], a[212:215], v[168:171], a[84:87]\n"
      "v_add_f32 v220, v220, v165\n"
      "v_cvt_pk_bf16_f32 v178, v164, v165\n"
      "v_mfma_f32_16x16x32_bf16 v[144:147], a[248:251], v[92:95], v[144:147]\n"
      "v_exp_f32 v166, v166\n"
      "v_mfma_f32_16x16x32_bf16 a[88:91], a[216:219], v[168:171], a[88:91]\n"
      "v_add_f32 v220, v220, v166\n"
      "v_mfma_f32_16x16x32_bf16 v[148:151], a[252:255], v[92:95], v[148:151]\n"
      "v_exp_f32_e64 v167, v167\n"
      "v_mfma_f32_16x16x32_bf16 a[92:95], a[220:223], v[168:171], a[92:95]\n"
      "v_add_f32 v220, v220, v167\n"
      "v_cvt_pk_bf16_f32 v179, v166, v167\n"
      "v_mfma_f32_16x16x32_bf16 v[152:155], a[224:227], v[96:99], 0\n"
      "v_exp_f32 v144, v144\n"
      "v_mfma_f32_16x16x32_bf16 a[128:131], a[192:195], v[176:179], a[128:131]\n"
      "ds_read_b128 a[224:227], v224 offset:32768\n"
      "v_add_f32_e64 v221, v221, v144\n"
      "v_mfma_f32_16x16x32_bf16 v[156:159], a[228:231], v[96:99], 0\n"
      "buffer_load_dwordx4 v231, s[52:55], s80 offen offset:3072 lds\n"
      "v_exp_f32_e64 v145, v145\n"
      "v_mfma_f32_16x16x32_bf16 a[132:135], a[196:199], v[176:179], a[132:135]\n"
      "ds_read_b128 a[228:231], v224 offset:36864\n"
      "v_cvt_pk_bf16_f32 v168, v144, v145\n"
      "v_mfma_f32_16x16x32_bf16 v[152:155], a[232:235], v[100:103], v[152:155]\n"
      "v_exp_f32_e64 v146, v146\n"
      "v_mfma_f32_16x16x32_bf16 a[136:139], a[200:203], v[176:179], a[136:139]\n"
      "ds_read_b128 a[232:235], v225 offset:32768\n"
      "v_add_f32 v221, v221, v145\n"
      "v_mfma_f32_16x16x32_bf16 v[156:159], a[236:239], v[100:103], v[156:159]\n"
      "v_exp_f32 v147, v147\n"
      "v_mfma_f32_16x16x32_bf16 a[140:143], a[204:207], v[176:179], a[140:143]\n"
      "ds_read_b128 a[236:239], v225 offset:36864\n"
      "v_cvt_pk_bf16_f32 v169, v146, v147\n"
      "v_mfma_f32_16x16x32_bf16 v[152:155], a[240:243], v[104:107], v[152:155]\n"
      "v_exp_f32_e64 v148, v148\n"
      "v_mfma_f32_16x16x32_bf16 a[112:115], a[208:211], v[172:175], a[112:115]\n"
      "ds_read_b128 a[240:243], v224 offset:33792\n"
      "v_add_f32_e64 v221, v221, v146\n"
      "v_mfma_f32_16x16x32_bf16 v[156:159], a[244:247], v[104:107], v[156:159]\n"
      "s_add_u32 m0, s68, 20480\n"
      "v_exp_f32_e64 v149, v149\n"
      "v_mfma_f32_16x16x32_bf16 a[116:119], a[212:215], v[172:175], a[116:119]\n"
      "ds_read_b128 a[244:247], v224 offset:37888\n"
      "s_add_u32 s64, s80, 4096\n"
      "v_cvt_pk_bf16_f32 v170, v148, v149\n"
      "v_mfma_f32_16x16x32_bf16 v[152:155], a[248:251], v[108:111], v[152:155]\n"
      "buffer_load_dwordx4 v230, s[52:55], s64 offen offset:0 lds\n"
      "v_exp_f32 v150, v150\n"
      "v_mfma_f32_16x16x32_bf16 a[120:123], a[216:219], v[172:175], a[120:123]\n"
      "ds_read_b128 a[248:251], v225 offset:33792\n"
      "v_add_f32 v221, v221, v147\n"
      "v_mfma_f32_16x16x32_bf16 v[156:159], a[252:255], v[108:111], v[156:159]\n"
      "s_waitcnt lgkmcnt(3)\n"
      "v_exp_f32 v151, v151\n"
      "v_mfma_f32_16x16x32_bf16 a[124:127], a[220:223], v[172:175], a[124:127]\n"
      "ds_read_b128 a[252:255], v225 offset:37888\n"
      "v_cvt_pk_bf16_f32 v171, v150, v151\n"
      "v_mfma_f32_16x16x32_bf16 v[160:163], a[224:227], v[0:3], 0\n"
      "v_exp_f32 v152, v152\n"
      "v_mfma_f32_16x16x32_bf16 v[180:183], a[192:195], v[168:171], v[180:183]\n"
      "v_add_f32 v221, v221, v149\n"
      "v_add_f32_e64 v221, v221, v148\n"
      "v_mfma_f32_16x16x32_bf16 v[164:167], a[228:231], v[0:3], 0\n"
      "v_exp_f32 v153, v153\n"
      "v_mfma_f32_16x16x32_bf16 v[184:187], a[196:199], v[168:171], v[184:187]\n"
      "v_add_f32 v221, v221, v150\n"
      "v_cvt_pk_bf16_f32 v172, v152, v153\n"
      "v_mfma_f32_16x16x32_bf16 v[160:163], a[232:235], v[4:7], v[160:163]\n"
      "v_exp_f32 v154, v154\n"
      "v_mfma_f32_16x16x32_bf16 v[188:191], a[200:203], v[168:171], v[188:191]\n"
      "v_add_f32 v222, v222, v152\n"
      "v_add_f32_e64 v221, v221, v151\n"
      "v_mfma_f32_16x16x32_bf16 v[164:167], a[236:239], v[4:7], v[164:167]\n"
      "s_waitcnt lgkmcnt(2)\n"
      "v_exp_f32 v155, v155\n"
      "v_mfma_f32_16x16x32_bf16 v[192:195], a[204:207], v[168:171], v[192:195]\n"
      "v_add_f32_e64 v222, v222, v153\n"
      "v_cvt_pk_bf16_f32 v173, v154, v155\n"
      "v_mfma_f32_16x16x32_bf16 v[160:163], a[240:243], v[8:11], v[160:163]\n"
      "v_exp_f32 v156, v156\n"
      "v_mfma_f32_16x16x32_bf16 a[144:147], a[208:211], v[176:179], a[144:147]\n"
      "v_add_f32 v222, v222, v155\n"
      "v_add_f32 v222, v222, v154\n"
      "v_mfma_f32_16x16x32_bf16 v[164:167], a[244:247], v[8:11], v[164:167]\n"
      "s_waitcnt lgkmcnt(0)\n"
      "v_exp_f32 v157, v157\n"
      "v_mfma_f32_16x16x32_bf16 a[148:151], a[212:215], v[176:179], a[148:151]\n"
      "v_add_f32 v222, v222, v156\n"
      "v_cvt_pk_bf16_f32 v174, v156, v157\n"
      "v_mfma_f32_16x16x32_bf16 v[160:163], a[248:251], v[12:15], v[160:163]\n"
      "v_exp_f32_e64 v158, v158\n"
      "v_mfma_f32_16x16x32_bf16 a[152:155], a[216:219], v[176:179], a[152:155]\n"
      "s_add_u32 m0, s68, 20480\n"
      "v_add_f32 v222, v222, v158\n"
      "v_add_f32 v222, v222, v157\n"
      "v_mfma_f32_16x16x32_bf16 v[164:167], a[252:255], v[12:15], v[164:167]\n"
      "buffer_load_dwordx4 v231, s[52:55], s64 offen offset:1024 lds\n"
      "v_exp_f32 v159, v159\n"
      "v_mfma_f32_16x16x32_bf16 a[156:159], a[220:223], v[176:179], a[156:159]\n"
      "v_add_f32 v222, v222, v159\n"
      "v_cvt_pk_bf16_f32 v175, v158, v159\n"
      "v_mfma_f32_16x16x32_bf16 v[144:147], a[224:227], v[16:19], 0\n"
      "v_exp_f32 v160, v160\n"
      "v_mfma_f32_16x16x32_bf16 v[112:115], a[192:195], v[172:175], v[112:115]\n"
      "ds_read_b64_tr_b16 a[192:193], v226 offset:32768\n"
      "v_add_f32 v216, v216, v160\n"
      "v_mfma_f32_16x16x32_bf16 v[148:151], a[228:231], v[16:19], 0\n"
      "v_exp_f32_e64 v161, v161\n"
      "v_mfma_f32_16x16x32_bf16 v[116:119], a[196:199], v[172:175], v[116:119]\n"
      "ds_read_b64_tr_b16 a[194:195], v226 offset:36864\n"
      "v_cvt_pk_bf16_f32 v176, v160, v161\n"
      "v_mfma_f32_16x16x32_bf16 v[144:147], a[232:235], v[20:23], v[144:147]\n"
      "v_exp_f32 v162, v162\n"
      "v_mfma_f32_16x16x32_bf16 v[120:123], a[200:203], v[172:175], v[120:123]\n"
      "ds_read_b64_tr_b16 a[196:197], v227 offset:32768\n"
      "v_add_f32 v216, v216, v161\n"
      "v_mfma_f32_16x16x32_bf16 v[148:151], a[236:239], v[20:23], v[148:151]\n"
      "v_exp_f32_e64 v163, v163\n"
      "v_mfma_f32_16x16x32_bf16 v[124:127], a[204:207], v[172:175], v[124:127]\n"
      "ds_read_b64_tr_b16 a[198:199], v227 offset:36864\n"
      "v_cvt_pk_bf16_f32 v177, v162, v163\n"
      "v_mfma_f32_16x16x32_bf16 v[144:147], a[240:243], v[24:27], v[144:147]\n"
      "v_exp_f32_e64 v164, v164\n"
      "v_mfma_f32_16x16x32_bf16 v[196:199], a[208:211], v[168:171], v[196:199]\n"
      "ds_read_b64_tr_b16 a[200:201], v228 offset:32768\n"
      "s_add_u32 m0, s68, 20480\n"
      "v_add_f32_e64 v216, v216, v162\n"
      "v_mfma_f32_16x16x32_bf16 v[148:151], a[244:247], v[24:27], v[148:151]\n"
      "buffer_load_dwordx4 v230, s[52:55], s64 offen offset:2048 lds\n"
      "v_exp_f32_e64 v165, v165\n"
      "v_mfma_f32_16x16x32_bf16 v[200:203], a[212:215], v[168:171], v[200:203]\n"
      "ds_read_b64_tr_b16 a[202:203], v228 offset:36864\n"
      "v_cvt_pk_bf16_f32 v178, v164, v165\n"
      "v_mfma_f32_16x16x32_bf16 v[144:147], a[248:251], v[28:31], v[144:147]\n"
      "v_exp_f32_e64 v166, v166\n"
      "v_mfma_f32_16x16x32_bf16 v[204:207], a[216:219], v[168:171], v[204:207]\n"
      "ds_read_b64_tr_b16 a[204:205], v229 offset:32768\n"
      "v_add_f32 v216, v216, v163\n"
      "v_mfma_f32_16x16x32_bf16 v[148:151], a[252:255], v[28:31], v[148:151]\n"
      "v_exp_f32 v167, v167\n"
      "v_mfma_f32_16x16x32_bf16 v[208:211], a[220:223], v[168:171], v[208:211]\n"
      "ds_read_b64_tr_b16 a[206:207], v229 offset:36864\n"
      "v_cvt_pk_bf16_f32 v179, v166, v167\n"
      "v_mfma_f32_16x16x32_bf16 v[152:155], a[224:227], v[32:35], 0\n"
      "v_exp_f32_e64 v144, v144\n"
      "v_mfma_f32_16x16x32_bf16 v[128:131], a[208:211], v[172:175], v[128:131]\n"
      "ds_read_b64_tr_b16 a[208:209], v226 offset:33792\n"
      "v_add_f32 v216, v216, v164\n"
      "v_mfma_f32_16x16x32_bf16 v[156:159], a[228:231], v[32:35], 0\n"
      "v_exp_f32 v145, v145\n"
      "v_mfma_f32_16x16x32_bf16 v[132:135], a[212:215], v[172:175], v[132:135]\n"
      "ds_read_b64_tr_b16 a[210:211], v226 offset:37888\n"
      "v_cvt_pk_bf16_f32 v168, v144, v145\n"
      "v_mfma_f32_16x16x32_bf16 v[152:155], a[232:235], v[36:39], v[152:155]\n"
      "v_exp_f32 v146, v146\n"
      "v_mfma_f32_16x16x32_bf16 v[136:139], a[216:219], v[172:175], v[136:139]\n"
      "ds_read_b64_tr_b16 a[212:213], v227 offset:33792\n"
      "v_add_f32 v216, v216, v165\n"
      "v_mfma_f32_16x16x32_bf16 v[156:159], a[236:239], v[36:39], v[156:159]\n"
      "v_exp_f32_e64 v147, v147\n"
      "v_mfma_f32_16x16x32_bf16 v[140:143], a[220:223], v[172:175], v[140:143]\n"
      "ds_read_b64_tr_b16 a[214:215], v227 offset:37888\n"
      "v_cvt_pk_bf16_f32 v169, v146, v147\n"
      "v_mfma_f32_16x16x32_bf16 v[152:155], a[240:243], v[40:43], v[152:155]\n"
      "s_waitcnt lgkmcnt(4)\n"
      "v_exp_f32 v148, v148\n"
      "v_mfma_f32_16x16x32_bf16 a[0:3], a[192:195], v[176:179], a[0:3]\n"
      "ds_read_b64_tr_b16 a[216:217], v228 offset:33792\n"
      "v_add_f32 v216, v216, v166\n"
      "v_mfma_f32_16x16x32_bf16 v[156:159], a[244:247], v[40:43], v[156:159]\n"
      "v_exp_f32 v149, v149\n"
      "v_mfma_f32_16x16x32_bf16 a[4:7], a[196:199], v[176:179], a[4:7]\n"
      "ds_read_b64_tr_b16 a[218:219], v228 offset:37888\n"
      "s_add_u32 m0, s68, 20480\n"
      "v_cvt_pk_bf16_f32 v170, v148, v149\n"
      "v_mfma_f32_16x16x32_bf16 v[152:155], a[248:251], v[44:47], v[152:155]\n"
      "buffer_load_dwordx4 v231, s[52:55], s64 offen offset:3072 lds\n"
      "v_exp_f32 v150, v150\n"
      "v_mfma_f32_16x16x32_bf16 a[8:11], a[200:203], v[176:179], a[8:11]\n"
      "ds_read_b64_tr_b16 a[220:221], v229 offset:33792\n"
      "s_add_u32 s80, s80, 8192\n"
      "v_add_f32 v216, v216, v167\n"
      "v_mfma_f32_16x16x32_bf16 v[156:159], a[252:255], v[44:47], v[156:159]\n"
      "s_and_b32 s80, s80, 122880\n"
      "v_exp_f32_e64 v151, v151\n"
      "v_mfma_f32_16x16x32_bf16 a[12:15], a[204:207], v[176:179], a[12:15]\n"
      "ds_read_b64_tr_b16 a[222:223], v229 offset:37888\n"
      "v_cvt_pk_bf16_f32 v171, v150, v151\n"
      "v_mfma_f32_16x16x32_bf16 v[160:163], a[224:227], v[48:51], 0\n"
      "v_exp_f32_e64 v152, v152\n"
      "v_mfma_f32_16x16x32_bf16 a[32:35], a[192:195], v[168:171], a[32:35]\n"
      "v_add_f32 v217, v217, v145\n"
      "v_add_f32 v217, v217, v144\n"
      "v_mfma_f32_16x16x32_bf16 v[164:167], a[228:231], v[48:51], 0\n"
      "v_exp_f32_e64 v153, v153\n"
      "v_mfma_f32_16x16x32_bf16 a[36:39], a[196:199], v[168:171], a[36:39]\n"
      "v_add_f32_e64 v217, v217, v146\n"
      "v_cvt_pk_bf16_f32 v172, v152, v153\n"
      "v_mfma_f32_16x16x32_bf16 v[160:163], a[232:235], v[52:55], v[160:163]\n"
      "s_memrealtime s[76:77]\n"
      "v_exp_f32 v154, v154\n"
      "v_mfma_f32_16x16x32_bf16 a[40:43], a[200:203], v[168:171], a[40:43]\n"
      "v_add_f32 v217, v217, v148\n"
      "v_add_f32_e64 v217, v217, v147\n"
      "v_mfma_f32_16x16x32_bf16 v[164:167], a[236:239], v[52:55], v[164:167]\n"
      "v_exp_f32 v155, v155\n"
      "v_mfma_f32_16x16x32_bf16 a[44:47], a[204:207], v[168:171], a[44:47]\n"
      "v_add_f32 v217, v217, v149\n"
      "v_cvt_pk_bf16_f32 v173, v154, v155\n"
      "v_mfma_f32_16x16x32_bf16 v[160:163], a[240:243], v[56:59], v[160:163]\n"
      "s_waitcnt vmcnt(8) lgkmcnt(0)\n"
      "v_exp_f32 v156, v156\n"
      "v_mfma_f32_16x16x32_bf16 a[16:19], a[208:211], v[176:179], a[16:19]\n"
      "s_sub_u32 s77, s76, s96\n"
      "s_cmp_lt_i32 s77, 0\n"
      "s_cbranch_scc1 PC2B_%=\n"
      "PC2RB_%=:\n"
      "v_add_f32 v217, v217, v151\n"
      "v_add_f32 v217, v217, v150\n"
      "v_mfma_f32_16x16x32_bf16 v[164:167], a[244:247], v[56:59], v[164:167]\n"
      "s_barrier\n"
      "v_exp_f32_e64 v157, v157\n"
      "v_mfma_f32_16x16x32_bf16 a[20:23], a[212:215], v[176:179], a[20:23]\n"
      "s_add_u32 s96, s76, s78\n"
      "v_add_f32 v218, v218, v152\n"
      "v_cvt_pk_bf16_f32 v174, v156, v157\n"
      "v_mfma_f32_16x16x32_bf16 v[160:163], a[248:251], v[60:63], v[160:163]\n"
      "SYRB_%=:\n"
      "v_exp_f32 v158, v158\n"
      "v_mfma_f32_16x16x32_bf16 a[24:27], a[216:219], v[176:179], a[24:27]\n"
      "s_add_u32 m0, s68, 32768\n"
      "v_add_f32 v218, v218, v154\n"
      "v_add_f32_e64 v218, v218, v153\n"
      "v_mfma_f32_16x16x32_bf16 v[164:167], a[252:255], v[60:63], v[164:167]\n"
      "buffer_load_dwordx4 v230, s[52:55], s80 offen offset:0 lds\n"
      "v_exp_f32 v159, v159\n"
      "v_mfma_f32_16x16x32_bf16 a[28:31], a[220:223], v[176:179], a[28:31]\n"
      "v_add_f32 v218, v218, v155\n"
      "v_cvt_pk_bf16_f32 v175, v158, v159\n"
      "v_mfma_f32_16x16x32_bf16 v[144:147], a[224:227], v[64:67], 0\n"
      "v_exp_f32 v160, v160\n"
      "v_mfma_f32_16x16x32_bf16 a[64:67], a[192:195], v[172:175], a[64:67]\n"
      "v_add_f32 v218, v218, v157\n"
      "v_add_f32 v218, v218, v156\n"
      "v_mfma_f32_16x16x32_bf16 v[148:151], a[228:231], v[64:67], 0\n"
      "v_exp_f32 v161, v161\n"
      "v_mfma_f32_16x16x32_bf16 a[68:71], a[196:199], v[172:175], a[68:71]\n"
      "v_add_f32_e64 v218, v218, v158\n"
      "v_cvt_pk_bf16_f32 v176, v160, v161\n"
      "v_mfma_f32_16x16x32_bf16 v[144:147], a[232:235], v[68:71], v[144:147]\n"
      "v_exp_f32 v162, v162\n"
      "v_mfma_f32_16x16x32_bf16 a[72:75], a[200:203], v[172:175], a[72:75]\n"
      "v_add_f32 v219, v219, v160\n"
      "v_add_f32 v218, v218, v159\n"
      "v_mfma_f32_16x16x32_bf16 v[148:151], a[236:239], v[68:71], v[148:151]\n"
      "v_exp_f32 v163, v163\n"
      "v_mfma_f32_16x16x32_bf16 a[76:79], a[204:207], v[172:175], a[76:79]\n"
      "v_add_f32_e64 v219, v219, v161\n"
      "v_cvt_pk_bf16_f32 v177, v162, v163\n"
      "v_mfma_f32_16x16x32_bf16 v[144:147], a[240:243], v[72:75], v[144:147]\n"
      "v_exp_f32 v164, v164\n"
      "v_mfma_f32_16x16x32_bf16 a[48:51], a[208:211], v[168:171], a[48:51]\n"
      "v_add_f32 v219, v219, v163\n"
      "v_add_f32 v219, v219, v162\n"
      "v_mfma_f32_16x16x32_bf16 v[148:151], a[244:247], v[72:75], v[148:151]\n"
      "buffer_load_dwordx4 v231, s[52:55], s80 offen offset:1024 lds\n"
      "v_exp_f32 v165, v165\n"
      "v_mfma_f32_16x16x32_bf16 a[52:55], a[212:215], v[168:171], a[52:55]\n"
      "v_add_f32 v219, v219, v164\n"
      "v_cvt_pk_bf16_f32 v178, v164, v165\n"
      "v_mfma_f32_16x16x32_bf16 v[144:147], a[248:251], v[76:79], v[144:147]\n"
      "v_exp_f32 v166, v166\n"
      "v_mfma_f32_16x16x32_bf16 a[56:59], a[216:219], v[168:171], a[56:59]\n"
      "v_add_f32 v219, v219, v166\n"
      "v_add_f32 v219, v219, v165\n"
      "v_mfma_f32_16x16x32_bf16 v[148:151], a[252:255], v[76:79], v[148:151]\n"
      "v_exp_f32 v167, v167\n"
      "v_mfma_f32_16x16x32_bf16 a[60:63], a[220:223], v[168:171], a[60:63]\n"
      "v_add_f32 v219, v219, v167\n"
      "v_cvt_pk_bf16_f32 v179, v166, v167\n"
      "v_mfma_f32_16x16x32_bf16 v[152:155], a[224:227], v[80:83], 0\n"
      "v_exp_f32 v144, v144\n"
      "v_mfma_f32_16x16x32_bf16 a[96:99], a[192:195], v[176:179], a[96:99]\n"
      "v_add_f32 v220, v220, v144\n"
      "v_mfma_f32_16x16x32_bf16 v[156:159], a[228:231], v[80:83], 0\n"
      "v_exp_f32 v145, v145\n"
      "v_mfma_f32_16x16x32_bf16 a[100:103], a[196:199], v[176:179], a[100:103]\n"
      "v_add_f32 v220, v220, v145\n"
      "v_cvt_pk_bf16_f32 v168, v144, v145\n"
      "v_mfma_f32_16x16x32_bf16 v[152:155], a[232:235], v[84:87], v[152:155]\n"
      "v_exp_f32 v146, v146\n"
      "v_mfma_f32_16x16x32_bf16 a[104:107], a[200:203], v[176:179], a[104:107]\n"
      "v_add_f32 v220, v220, v146\n"
      "v_mfma_f32_16x16x32_bf16 v[156:159], a[236:239], v[84:87], v[156:159]\n"
      "buffer_load_dwordx4 v230, s[52:55], s80 offen offset:2048 lds\n"
      "v_exp_f32 v147, v147\n"
      "v_mfma_f32_16x16x32_bf16 a[108:111], a[204:207], v[176:179], a[108:111]\n"
      "v_add_f32 v220, v220, v147\n"
      "v_cvt_pk_bf16_f32 v169, v146, v147\n"
      "v_mfma_f32_16x16x32_bf16 v[152:155], a[240:243], v[88:91], v[152:155]\n"
      "v_exp_f32 v148, v148\n"
      "v_mfma_f32_16x16x32_bf16 a[80:83], a[208:211], v[172:175], a[80:83]\n"
      "v_add_f32 v220, v220, v148\n"
      "v_mfma_f32_16x16x32_bf16 v[156:159], a[244:247], v[88:91], v[156:159]\n"
      "v_exp_f32 v149, v149\n"
      "v_mfma_f32_16x16x32_bf16 a[84:87], a[212:215], v[172:175], a[84:87]\n"
      "v_add_f32 v220, v220, v149\n"
      "v_cvt_pk_bf16_f32 v170, v148, v149\n"
      "v_mfma_f32_16x16x32_bf16 v[152:155], a[248:251], v[92:95], v[152:155]\n"
      "v_exp_f32 v150, v150\n"
      "v_mfma_f32_16x16x32_bf16 a[88:91], a[216:219], v[172:175], a[88:91]\n"
      "v_add_f32 v220, v220, v150\n"
      "v_mfma_f32_16x16x32_bf16 v[156:159], a[252:255], v[92:95], v[156:159]\n"
      "v_exp_f32_e64 v151, v151\n"
      "v_mfma_f32_16x16x32_bf16 a[92:95], a[220:223], v[172:175], a[92:95]\n"
      "v_add_f32 v220, v220, v151\n"
      "v_cvt_pk_bf16_f32 v171, v150, v151\n"
      "v_mfma_f32_16x16x32_bf16 v[160:163], a[224:227], v[96:99], 0\n"
      "v_exp_f32 v152, v152\n"
      "v_mfma_f32_16x16x32_bf16 a[128:131], a[192:195], v[168:171], a[128:131]\n"
      "ds_read_b128 a[224:227], v224 offset:0\n"
      "v_add_f32_e64 v221, v221, v152\n"
      "v_mfma_f32_16x16x32_bf16 v[164:167], a[228:231], v[96:99], 0\n"
      "buffer_load_dwordx4 v231, s[52:55], s80 offen offset:3072 lds\n"
      "v_exp_f32_e64 v153, v153\n"
      "v_mfma_f32_16x16x32_bf16 a[132:135], a[196:199], v[168:171], a[132:135]\n"
      "ds_read_b128 a[228:231], v224 offset:4096\n"
      "v_cvt_pk_bf16_f32 v172, v152, v153\n"
      "v_mfma_f32_16x16x32_bf16 v[160:163], a[232:235], v[100:103], v[160:163]\n"
      "v_exp_f32_e64 v154, v154\n"
      "v_mfma_f32_16x16x32_bf16 a[136:139], a[200:203], v[168:171], a[136:139]\n"
      "ds_read_b128 a[232:235], v225 offset:0\n"
      "v_add_f32 v221, v221, v153\n"
      "v_mfma_f32_16x16x32_bf16 v[164:167], a[236:239], v[100:103], v[164:167]\n"
      "v_exp_f32 v155, v155\n"
      "v_mfma_f32_16x16x32_bf16 a[140:143], a[204:207], v[168:171], a[140:143]\n"
      "ds_read_b128 a[236:239], v225 offset:4096\n"
      "v_cvt_pk_bf16_f32 v173, v154, v155\n"
      "v_mfma_f32_16x16x32_bf16 v[160:163], a[240:243], v[104:107], v[160:163]\n"
      "v_exp_f32_e64 v156, v156\n"
      "v_mfma_f32_16x16x32_bf16 a[112:115], a[208:211], v[176:179], a[112:115]\n"
      "ds_read_b128 a[240:243], v224 offset:1024\n"
      "v_add_f32_e64 v221, v221, v154\n"
      "v_mfma_f32_16x16x32_bf16 v[164:167], a[244:247], v[104:107], v[164:167]\n"
      "s_add_u32 m0, s68, 36864\n"
      "v_exp_f32_e64 v157, v157\n"
      "v_mfma_f32_16x16x32_bf16 a[116:119], a[212:215], v[176:179], a[116:119]\n"
      "ds_read_b128 a[244:247], v224 offset:5120\n"
      "s_add_u32 s64, s80, 4096\n"
      "v_cvt_pk_bf16_f32 v174, v156, v157\n"
      "v_mfma_f32_16x16x32_bf16 v[160:163], a[248:251], v[108:111], v[160:163]\n"
      "buffer_load_dwordx4 v230, s[52:55], s64 offen offset:0 lds\n"
      "v_exp_f32 v158, v158\n"
      "v_mfma_f32_16x16x32_bf16 a[120:123], a[216:219], v[176:179], a[120:123]\n"
      "ds_read_b128 a[248:251], v225 offset:1024\n"
      "v_add_f32 v221, v221, v155\n"
      "v_mfma_f32_16x16x32_bf16 v[164:167], a[252:255], v[108:111], v[164:167]\n"
      "s_waitcnt lgkmcnt(3)\n"
      "v_exp_f32 v159, v159\n"
      "v_mfma_f32_16x16x32_bf16 a[124:127], a[220:223], v[176:179], a[124:127]\n"
      "ds_read_b128 a[252:255], v225 offset:5120\n"
      "v_cvt_pk_bf16_f32 v175, v158, v159\n"
      "v_mfma_f32_16x16x32_bf16 v[144:147], a[224:227], v[0:3], 0\n"
      "v_exp_f32 v160, v160\n"
      "v_mfma_f32_16x16x32_bf16 v[180:183], a[192:195], v[172:175], v[180:183]\n"
      "s_add_u32 m0, s73, 0\n"
      "v_add_f32 v221, v221, v157\n"
      "v_add_f32 v221, v221, v156\n"
      "v_mfma_f32_16x16x32_bf16 v[148:151], a[228:231], v[0:3], 0\n"
      "buffer_load_dwordx4 v234, s[56:59], s86 offen lds\n"
      "v_exp_f32 v161, v161\n"
      "v_mfma_f32_16x16x32_bf16 v[184:187], a[196:199], v[172:175], v[184:187]\n"
      "v_add_f32 v221, v221, v158\n"
      "v_cvt_pk_bf16_f32 v176, v160, v161\n"
      "v_mfma_f32_16x16x32_bf16 v[144:147], a[232:235], v[4:7], v[144:147]\n"
      "v_exp_f32 v162, v162\n"
      "v_mfma_f32_16x16x32_bf16 v[188:191], a[200:203], v[172:175], v[188:191]\n"
      "v_add_f32 v222, v222, v160\n"
      "v_add_f32 v221, v221, v159\n"
      "v_mfma_f32_16x16x32_bf16 v[148:151], a[236:239], v[4:7], v[148:151]\n"
      "s_waitcnt lgkmcnt(2)\n"
      "v_exp_f32_e64 v163, v163\n"
      "v_mfma_f32_16x16x32_bf16 v[192:195], a[204:207], v[172:175], v[192:195]\n"
      "s_add_u32 m0, s73, 1024\n"
      "v_add_f32_e64 v222, v222, v161\n"
      "v_cvt_pk_bf16_f32 v177, v162, v163\n"
      "v_mfma_f32_16x16x32_bf16 v[144:147], a[240:243], v[8:11], v[144:147]\n"
      "buffer_load_dwordx4 v235, s[56:59], s86 offen lds\n"
      "v_exp_f32 v164, v164\n"
      "v_mfma_f32_16x16x32_bf16 a[144:147], a[208:211], v[168:171], a[144:147]\n"
      "v_add_f32 v222, v222, v163\n"
      "v_add_f32_e64 v222, v222, v162\n"
      "v_mfma_f32_16x16x32_bf16 v[148:151], a[244:247], v[8:11], v[148:151]\n"
      "s_waitcnt lgkmcnt(0)\n"
      "v_exp_f32 v165, v165\n"
      "v_mfma_f32_16x16x32_bf16 a[148:151], a[212:215], v[168:171], a[148:151]\n"
      "v_add_f32_e64 v222, v222, v164\n"
      "v_cvt_pk_bf16_f32 v178, v164, v165\n"
      "v_mfma_f32_16x16x32_bf16 v[144:147], a[248:251], v[12:15], v[144:147]\n"
      "v_exp_f32_e64 v166, v166\n"
      "v_mfma_f32_16x16x32_bf16 a[152:155], a[216:219], v[168:171], a[152:155]\n"
      "s_add_u32 m0, s68, 36864\n"
      "v_add_f32 v222, v222, v166\n"
      "v_add_f32 v222, v222, v165\n"
      "v_mfma_f32_16x16x32_bf16 v[148:151], a[252:255], v[12:15], v[148:151]\n"
      "buffer_load_dwordx4 v231, s[52:55], s64 offen offset:1024 lds\n"
      "v_exp_f32 v167, v167\n"
      "v_mfma_f32_16x16x32_bf16 a[156:159], a[220:223], v[168:171], a[156:159]\n"
      "v_add_f32 v222, v222, v167\n"
      "v_cvt_pk_bf16_f32 v179, v166, v167\n"
      "v_mfma_f32_16x16x32_bf16 v[152:155], a[224:227], v[16:19], 0\n"
      "v_exp_f32 v144, v144\n"
      "v_mfma_f32_16x16x32_bf16 v[112:115], a[192:195], v[176:179], v[112:115]\n"
      "ds_read_b64_tr_b16 a[192:193], v226 offset:0\n"
      "v_add_f32 v216, v216, v144\n"
      "v_mfma_f32_16x16x32_bf16 v[156:159], a[228:231], v[16:19], 0\n"
      "s_add_u32 m0, s73, 2048\n"
      "v_exp_f32_e64 v145, v145\n"
      "v_mfma_f32_16x16x32_bf16 v[116:119], a[196:199], v[176:179], v[116:119]\n"
      "ds_read_b64_tr_b16 a[194:195], v226 offset:4096\n"
      "s_add_u32 s65, s86, 8192\n"
      "v_cvt_pk_bf16_f32 v168, v144, v145\n"
      "v_mfma_f32_16x16x32_bf16 v[152:155], a[232:235], v[20:23], v[152:155]\n"
      "buffer_load_dwordx4 v234, s[56:59], s65 offen lds\n"
      "v_exp_f32 v146, v146\n"
      "v_mfma_f32_16x16x32_bf16 v[120:123], a[200:203], v[176:179], v[120:123]\n"
      "ds_read_b64_tr_b16 a[196:197], v227 offset:0\n"
      "v_add_f32 v216, v216, v145\n"
      "v_mfma_f32_16x16x32_bf16 v[156:159], a[236:239], v[20:23], v[156:159]\n"
      "v_exp_f32_e64 v147, v147\n"
      "v_mfma_f32_16x16x32_bf16 v[124:127], a[204:207], v[176:179], v[124:127]\n"
      "ds_read_b64_tr_b16 a[198:199], v227 offset:4096\n"
      "v_cvt_pk_bf16_f32 v169, v146, v147\n"
      "v_mfma_f32_16x16x32_bf16 v[152:155], a[240:243], v[24:27], v[152:155]\n"
      "v_exp_f32_e64 v148, v148\n"
      "v_mfma_f32_16x16x32_bf16 v[196:199], a[208:211], v[172:175], v[196:199]\n"
      "ds_read_b64_tr_b16 a[200:201], v228 offset:0\n"
      "s_add_u32 m0, s68, 36864\n"
      "v_add_f32 v216, v216, v146\n"
      "v_mfma_f32_16x16x32_bf16 v[156:159], a[244:247], v[24:27], v[156:159]\n"
      "buffer_load_dwordx4 v230, s[52:55], s64 offen offset:2048 lds\n"
      "v_exp_f32 v149, v149\n"
      "v_mfma_f32_16x16x32_bf16 v[200:203], a[212:215], v[172:175], v[200:203]\n"
      "ds_read_b64_tr_b16 a[202:203], v228 offset:4096\n"
      "v_cvt_pk_bf16_f32 v170, v148, v149\n"
      "v_mfma_f32_16x16x32_bf16 v[152:155], a[248:251], v[28:31], v[152:155]\n"
      "v_exp_f32 v150, v150\n"
      "v_mfma_f32_16x16x32_bf16 v[204:207], a[216:219], v[172:175], v[204:207]\n"
      "ds_read_b64_tr_b16 a[204:205], v229 offset:0\n"
      "v_add_f32 v216, v216, v147\n"
      "v_mfma_f32_16x16x32_bf16 v[156:159], a[252:255], v[28:31], v[156:159]\n"
      "v_exp_f32_e64 v151, v151\n"
      "v_mfma_f32_16x16x32_bf16 v[208:211], a[220:223], v[172:175], v[208:211]\n"
      "ds_read_b64_tr_b16 a[206:207], v229 offset:4096\n"
      "v_cvt_pk_bf16_f32 v171, v150, v151\n"
      "v_mfma_f32_16x16x32_bf16 v[160:163], a[224:227], v[32:35], 0\n"
      "v_exp_f32_e64 v152, v152\n"
      "v_mfma_f32_16x16x32_bf16 v[128:131], a[208:211], v[176:179], v[128:131]\n"
      "ds_read_b64_tr_b16 a[208:209], v226 offset:1024\n"
      "s_add_u32 m0, s73, 3072\n"
      "v_add_f32_e64 v216, v216, v148\n"
      "v_mfma_f32_16x16x32_bf16 v[164:167], a[228:231], v[32:35], 0\n"
      "buffer_load_dwordx4 v235, s[56:59], s65 offen lds\n"
      "v_exp_f32 v153, v153\n"
      "v_mfma_f32_16x16x32_bf16 v[132:135], a[212:215], v[176:179], v[132:135]\n"
      "ds_read_b64_tr_b16 a[210:211], v226 offset:5120\n"
      "s_add_u32 s86, s86, s75\n"
      "v_cvt_pk_bf16_f32 v172, v152, v153\n"
      "v_mfma_f32_16x16x32_bf16 v[160:163], a[232:235], v[36:39], v[160:163]\n"
      "s_and_b32 s86, s86, 0xfffff\n"
      "v_exp_f32_e64 v154, v154\n"
      "v_mfma_f32_16x16x32_bf16 v[136:139], a[216:219], v[176:179], v[136:139]\n"
      "ds_read_b64_tr_b16 a[212:213], v227 offset:1024\n"
      "s_xor_b32 s75, s75, 98304\n"
      "v_add_f32 v216, v216, v149\n"
      "v_mfma_f32_16x16x32_bf16 v[164:167], a[236:239], v[36:39], v[164:167]\n"
      "s_add_u32 s73, s73, s74\n"
      "v_exp_f32_e64 v155, v155\n"
      "v_mfma_f32_16x16x32_bf16 v[140:143], a[220:223], v[176:179], v[140:143]\n"
      "ds_read_b64_tr_b16 a[214:215], v227 offset:5120\n"
      "s_xor_b32 s74, s74, 24576\n"
      "v_cvt_pk_bf16_f32 v173, v154, v155\n"
      "v_mfma_f32_16x16x32_bf16 v[160:163], a[240:243], v[40:43], v[160:163]\n"
      "s_waitcnt lgkmcnt(4)\n"
      "v_exp_f32 v156, v156\n"
      "v_mfma_f32_16x16x32_bf16 a[0:3], a[192:195], v[168:171], a[0:3]\n"
      "ds_read_b64_tr_b16 a[216:217], v228 offset:1024\n"
      "v_add_f32 v216, v216, v150\n"
      "v_mfma_f32_16x16x32_bf16 v[164:167], a[244:247], v[40:43], v[164:167]\n"
      "v_exp_f32 v157, v157\n"
      "v_mfma_f32_16x16x32_bf16 a[4:7], a[196:199], v[168:171], a[4:7]\n"
      "ds_read_b64_tr_b16 a[218:219], v228 offset:5120\n"
      "s_add_u32 m0, s68, 36864\n"
      "v_cvt_pk_bf16_f32 v174, v156, v157\n"
      "v_mfma_f32_16x16x32_bf16 v[160:163], a[248:251], v[44:47], v[160:163]\n"
      "buffer_load_dwordx4 v231, s[52:55], s64 offen offset:3072 lds\n"
      "v_exp_f32_e64 v158, v158\n"
      "v_mfma_f32_16x16x32_bf16 a[8:11], a[200:203], v[168:171], a[8:11]\n"
      "ds_read_b64_tr_b16 a[220:221], v229 offset:1024\n"
      "s_add_u32 s80, s80, 8192\n"
      "v_add_f32 v216, v216, v151\n"
      "v_mfma_f32_16x16x32_bf16 v[164:167], a[252:255], v[44:47], v[164:167]\n"
      "s_and_b32 s80, s80, 122880\n"
      "v_exp_f32 v159, v159\n"
      "v_mfma_f32_16x16x32_bf16 a[12:15], a[204:207], v[168:171], a[12:15]\n"
      "ds_read_b64_tr_b16 a[222:223], v229 offset:5120\n"
      "v_cvt_pk_bf16_f32 v175, v158, v159\n"
      "v_mfma_f32_16x16x32_bf16 v[144:147], a[224:227], v[48:51], 0\n"
      "v_exp_f32 v160, v160\n"
      "v_mfma_f32_16x16x32_bf16 a[32:35], a[192:195], v[172:175], a[32:35]\n"
      "v_add_f32 v217, v217, v153\n"
      "v_add_f32 v217, v217, v152\n"
      "v_mfma_f32_16x16x32_bf16 v[148:151], a[228:231], v[48:51], 0\n"
      "v_exp_f32 v161, v161\n"
      "v_mfma_f32_16x16x32_bf16 a[36:39], a[196:199], v[172:175], a[36:39]\n"
      "v_add_f32_e64 v217, v217, v154\n"
      "v_cvt_pk_bf16_f32 v176, v160, v161\n"
      "v_mfma_f32_16x16x32_bf16 v[144:147], a[232:235], v[52:55], v[144:147]\n"
      "s_memrealtime s[76:77]\n"
      "v_exp_f32 v162, v162\n"
      "v_mfma_f32_16x16x32_bf16 a[40:43], a[200:203], v[172:175], a[40:43]\n"
      "v_add_f32 v217, v217, v156\n"
      "v_add_f32_e64 v217, v217, v155\n"
      "v_mfma_f32_16x16x32_bf16 v[148:151], a[236:239], v[52:55], v[148:151]\n"
      "v_exp_f32 v163, v163\n"
      "v_mfma_f32_16x16x32_bf16 a[44:47], a[204:207], v[172:175], a[44:47]\n"
      "v_add_f32 v217, v217, v157\n"
      "v_cvt_pk_bf16_f32 v177, v162, v163\n"
      "v_mfma_f32_16x16x32_bf16 v[144:147], a[240:243], v[56:59], v[144:147]\n"
      "s_waitcnt vmcnt(12) lgkmcnt(0)\n"
      "v_exp_f32 v164, v164\n"
      "v_mfma_f32_16x16x32_bf16 a[16:19], a[208:211], v[168:171], a[16:19]\n"
      "s_sub_u32 s77, s76, s96\n"
      "s_cmp_lt_i32 s77, 0\n"
      "s_cbranch_scc1 PC0B_%=\n"
      "PC0RB_%=:\n"
      "v_add_f32 v217, v217, v159\n"
      "v_add_f32 v217, v217, v158\n"
      "v_mfma_f32_16x16x32_bf16 v[148:151], a[244:247], v[56:59], v[148:151]\n"
      "s_barrier\n"
      "v_exp_f32_e64 v165, v165\n"
      "v_mfma_f32_16x16x32_bf16 a[20:23], a[212:215], v[168:171], a[20:23]\n"
      "s_add_u32 s96, s76, s78\n"
      "v_add_f32 v218, v218, v160\n"
      "v_cvt_pk_bf16_f32 v178, v164, v165\n"
      "v_mfma_f32_16x16x32_bf16 v[144:147], a[248:251], v[60:63], v[144:147]\n"
      "v_exp_f32 v166, v166\n"
      "v_mfma_f32_16x16x32_bf16 a[24:27], a[216:219], v[168:171], a[24:27]\n"
      "s_add_u32 m0, s68, 0\n"
      "v_add_f32 v218, v218, v162\n"
      "v_add_f32 v218, v218, v161\n"
      "v_mfma_f32_16x16x32_bf16 v[148:151], a[252:255], v[60:63], v[148:151]\n"
      "buffer_load_dwordx4 v230, s[52:55], s80 offen offset:0 lds\n"
      "v_exp_f32 v167, v167\n"
      "v_mfma_f32_16x16x32_bf16 a[28:31], a[220:223], v[168:171], a[28:31]\n"
      "v_add_f32 v218, v218, v163\n"
      "v_cvt_pk_bf16_f32 v179, v166, v167\n"
      "v_mfma_f32_16x16x32_bf16 v[152:155], a[224:227], v[64:67], 0\n"
      "v_exp_f32 v144, v144\n"
      "v_mfma_f32_16x16x32_bf16 a[64:67], a[192:195], v[176:179], a[64:67]\n"
      "v_add_f32 v218, v218, v165\n"
      "v_add_f32_e64 v218, v218, v164\n"
      "v_mfma_f32_16x16x32_bf16 v[156:159], a[228:231], v[64:67], 0\n"
      "v_exp_f32 v145, v145\n"
      "v_mfma_f32_16x16x32_bf16 a[68:71], a[196:199], v[176:179], a[68:71]\n"
      "v_add_f32 v218, v218, v166\n"
      "v_cvt_pk_bf16_f32 v168, v144, v145\n"
      "v_mfma_f32_16x16x32_bf16 v[152:155], a[232:235], v[68:71], v[152:155]\n"
      "v_exp_f32 v146, v146\n"
      "v_mfma_f32_16x16x32_bf16 a[72:75], a[200:203], v[176:179], a[72:75]\n"
      "v_add_f32 v219, v219, v144\n"
      "v_add_f32_e64 v218, v218, v167\n"
      "v_mfma_f32_16x16x32_bf16 v[156:159], a[236:239], v[68:71], v[156:159]\n"
      "v_exp_f32 v147, v147\n"
      "v_mfma_f32_16x16x32_bf16 a[76:79], a[204:207], v[176:179], a[76:79]\n"
      "v_add_f32 v219, v219, v145\n"
      "v_cvt_pk_bf16_f32 v169, v146, v147\n"
      "v_mfma_f32_16x16x32_bf16 v[152:155], a[240:243], v[72:75], v[152:155]\n"
      "v_exp_f32 v148, v148\n"
      "v_mfma_f32_16x16x32_bf16 a[48:51], a[208:211], v[172:175], a[48:51]\n"
      "v_add_f32 v219, v219, v147\n"
      "v_add_f32_e64 v219, v219, v146\n"
      "v_mfma_f32_16x16x32_bf16 v[156:159], a[244:247], v[72:75], v[156:159]\n"
      "buffer_load_dwordx4 v231, s[52:55], s80 offen offset:1024 lds\n"
      "v_exp_f32 v149, v149\n"
      "v_mfma_f32_16x16x32_bf16 a[52:55], a[212:215], v[172:175], a[52:55]\n"
      "v_add_f32 v219, v219, v148\n"
      "v_cvt_pk_bf16_f32 v170, v148, v149\n"
      "v_mfma_f32_16x16x32_bf16 v[152:155], a[248:251], v[76:79], v[152:155]\n"
      "v_exp_f32 v150, v150\n"
      "v_mfma_f32_16x16x32_bf16 a[56:59], a[216:219], v[172:175], a[56:59]\n"
      "v_add_f32 v219, v219, v150\n"
      "v_add_f32 v219, v219, v149\n"
      "v_mfma_f32_16x16x32_bf16 v[156:159], a[252:255], v[76:79], v[156:159]\n"
      "v_exp_f32 v151, v151\n"
      "v_mfma_f32_16x16x32_bf16 a[60:63], a[220:223], v[172:175], a[60:63]\n"
      "v_add_f32_e64 v219, v219, v151\n"
      "v_cvt_pk_bf16_f32 v171, v150, v151\n"
      "v_mfma_f32_16x16x32_bf16 v[160:163], a[224:227], v[80:83], 0\n"
      "v_exp_f32 v152, v152\n"
      "v_mfma_f32_16x16x32_bf16 a[96:99], a[192:195], v[168:171], a[96:99]\n"
      "v_add_f32 v220, v220, v152\n"
      "v_mfma_f32_16x16x32_bf16 v[164:167], a[228:231], v[80:83], 0\n"
      "v_exp_f32 v153, v153\n"
      "v_mfma_f32_16x16x32_bf16 a[100:103], a[196:199], v[168:171], a[100:103]\n"
      "v_add_f32 v220, v220, v153\n"
      "v_cvt_pk_bf16_f32 v172, v152, v153\n"
      "v_mfma_f32_16x16x32_bf16 v[160:163], a[232:235], v[84:87], v[160:163]\n"
      "v_exp_f32 v154, v154\n"
      "v_mfma_f32_16x16x32_bf16 a[104:107], a[200:203], v[168:171], a[104:107]\n"
      "v_add_f32 v220, v220, v154\n"
      "v_mfma_f32_16x16x32_bf16 v[164:167], a[236:239], v[84:87], v[164:167]\n"
      "buffer_load_dwordx4 v230, s[52:55], s80 offen offset:2048 lds\n"
      "v_exp_f32 v155, v155\n"
      "v_mfma_f32_16x16x32_bf16 a[108:111], a[204:207], v[168:171], a[108:111]\n"
      "v_add_f32 v220, v220, v155\n"
      "v_cvt_pk_bf16_f32 v173, v154, v155\n"
      "v_mfma_f32_16x16x32_bf16 v[160:163], a[240:243], v[88:91], v[160:163]\n"
      "v_exp_f32 v156, v156\n"
      "v_mfma_f32_16x16x32_bf16 a[80:83], a[208:211], v[176:179], a[80:83]\n"
      "v_add_f32 v220, v220, v156\n"
      "v_mfma_f32_16x16x32_bf16 v[164:167], a[244:247], v[88:91], v[164:167]\n"
      "v_exp_f32 v157, v157\n"
      "v_mfma_f32_16x16x32_bf16 a[84:87], a[212:215], v[176:179], a[84:87]\n"
      "v_add_f32 v220, v220, v157\n"
      "v_cvt_pk_bf16_f32 v174, v156, v157\n"
      "v_mfma_f32_16x16x32_bf16 v[160:163], a[248:251], v[92:95], v[160:163]\n"
      "v_exp_f32 v158, v158\n"
      "v_mfma_f32_16x16x32_bf16 a[88:91], a[216:219], v[176:179], a[88:91]\n"
      "v_add_f32 v220, v220, v158\n"
      "v_mfma_f32_16x16x32_bf16 v[164:167], a[252:255], v[92:95], v[164:167]\n"
      "v_exp_f32_e64 v159, v159\n"
      "v_mfma_f32_16x16x32_bf16 a[92:95], a[220:223], v[176:179], a[92:95]\n"
      "v_add_f32 v220, v220, v159\n"
      "v_cvt_pk_bf16_f32 v175, v158, v159\n"
      "v_mfma_f32_16x16x32_bf16 v[144:147], a[224:227], v[96:99], 0\n"
      "v_exp_f32 v160, v160\n"
      "v_mfma_f32_16x16x32_bf16 a[128:131], a[192:195], v[172:175], a[128:131]\n"
      "ds_read_b128 a[224:227], v224 offset:16384\n"
      "v_add_f32_e64 v221, v221, v160\n"
      "v_mfma_f32_16x16x32_bf16 v[148:151], a[228:231], v[96:99], 0\n"
      "buffer_load_dwordx4 v231, s[52:55], s80 offen offset:3072 lds\n"
      "v_exp_f32_e64 v161, v161\n"
      "v_mfma_f32_16x16x32_bf16 a[132:135], a[196:199], v[172:175], a[132:135]\n"
      "ds_read_b128 a[228:231], v224 offset:20480\n"
      "v_cvt_pk_bf16_f32 v176, v160, v161\n"
      "v_mfma_f32_16x16x32_bf16 v[144:147], a[232:235], v[100:103], v[144:147]\n"
      "v_exp_f32_e64 v162, v162\n"
      "v_mfma_f32_16x16x32_bf16 a[136:139], a[200:203], v[172:175], a[136:139]\n"
      "ds_read_b128 a[232:235], v225 offset:16384\n"
      "v_add_f32 v221, v221, v161\n"
      "v_mfma_f32_16x16x32_bf16 v[148:151], a[236:239], v[100:103], v[148:151]\n"
      "v_exp_f32 v163, v163\n"
      "v_mfma_f32_16x16x32_bf16 a[140:143], a[204:207], v[172:175], a[140:143]\n"
      "ds_read_b128 a[236:239], v225 offset:20480\n"
      "v_cvt_pk_bf16_f32 v177, v162, v163\n"
      "v_mfma_f32_16x16x32_bf16 v[144:147], a[240:243], v[104:107], v[144:147]\n"
      "v_exp_f32_e64 v164, v164\n"
      "v_mfma_f32_16x16x32_bf16 a[112:115], a[208:211], v[168:171], a[112:115]\n"
      "ds_read_b128 a[240:243], v224 offset:17408\n"
      "v_add_f32_e64 v221, v221, v162\n"
      "v_mfma_f32_16x16x32_bf16 v[148:151], a[244:247], v[104:107], v[148:151]\n"
      "s_add_u32 m0, s68, 4096\n"
      "v_exp_f32_e64 v165, v165\n"
      "v_mfma_f32_16x16x32_bf16 a[116:119], a[212:215], v[168:171], a[116:119]\n"
      "ds_read_b128 a[244:247], v224 offset:21504\n"
      "s_add_u32 s64, s80, 4096\n"
      "v_cvt_pk_bf16_f32 v178, v164, v165\n"
      "v_mfma_f32_16x16x32_bf16 v[144:147], a[248:251], v[108:111], v[144:147]\n"
      "buffer_load_dwordx4 v230, s[52:55], s64 offen offset:0 lds\n"
      "v_exp_f32 v166, v166\n"
      "v_mfma_f32_16x16x32_bf16 a[120:123], a[216:219], v[168:171], a[120:123]\n"
      "ds_read_b128 a[248:251], v225 offset:17408\n"
      "v_add_f32 v221, v221, v163\n"
      "v_mfma_f32_16x16x32_bf16 v[148:151], a[252:255], v[108:111], v[148:151]\n"
      "s_waitcnt lgkmcnt(3)\n"
      "v_exp_f32 v167, v167\n"
      "v_mfma_f32_16x16x32_bf16 a[124:127], a[220:223], v[168:171], a[124:127]\n"
      "ds_read_b128 a[252:255], v225 offset:21504\n"
      "v_cvt_pk_bf16_f32 v179, v166, v167\n"
      "v_mfma_f32_16x16x32_bf16 v[152:155], a[224:227], v[0:3], 0\n"
      "v_exp_f32 v144, v144\n"
      "v_mfma_f32_16x16x32_bf16 v[180:183], a[192:195], v[176:179], v[180:183]\n"
      "v_add_f32 v221, v221, v165\n"
      "v_add_f32_e64 v221, v221, v164\n"
      "v_mfma_f32_16x16x32_bf16 v[156:159], a[228:231], v[0:3], 0\n"
      "v_exp_f32 v145, v145\n"
      "v_mfma_f32_16x16x32_bf16 v[184:187], a[196:199], v[176:179], v[184:187]\n"
      "v_add_f32 v221, v221, v166\n"
      "v_cvt_pk_bf16_f32 v168, v144, v145\n"
      "v_mfma_f32_16x16x32_bf16 v[152:155], a[232:235], v[4:7], v[152:155]\n"
      "v_exp_f32 v146, v146\n"
      "v_mfma_f32_16x16x32_bf16 v[188:191], a[200:203], v[176:179], v[188:191]\n"
      "v_add_f32 v222, v222, v144\n"
      "v_add_f32_e64 v221, v221, v167\n"
      "v_mfma_f32_16x16x32_bf16 v[156:159], a[236:239], v[4:7], v[156:159]\n"
      "s_waitcnt lgkmcnt(2)\n"
      "v_exp_f32 v147, v147\n"
      "v_mfma_f32_16x16x32_bf16 v[192:195], a[204:207], v[176:179], v[192:195]\n"
      "v_add_f32_e64 v222, v222, v145\n"
      "v_cvt_pk_bf16_f32 v169, v146, v147\n"
      "v_mfma_f32_16x16x32_bf16 v[152:155], a[240:243], v[8:11], v[152:155]\n"
      "v_exp_f32 v148, v148\n"
      "v_mfma_f32_16x16x32_bf16 a[144:147], a[208:211], v[172:175], a[144:147]\n"
      "v_add_f32 v222, v222, v147\n"
      "v_add_f32 v222, v222, v146\n"
      "v_mfma_f32_16x16x32_bf16 v[156:159], a[244:247], v[8:11], v[156:159]\n"
      "s_waitcnt lgkmcnt(0)\n"
      "v_exp_f32 v149, v149\n"
      "v_mfma_f32_16x16x32_bf16 a[148:151], a[212:215], v[172:175], a[148:151]\n"
      "v_add_f32 v222, v222, v148\n"
      "v_cvt_pk_bf16_f32 v170, v148, v149\n"
      "v_mfma_f32_16x16x32_bf16 v[152:155], a[248:251], v[12:15], v[152:155]\n"
      "v_exp_f32_e64 v150, v150\n"
      "v_mfma_f32_16x16x32_bf16 a[152:155], a[216:219], v[172:175], a[152:155]\n"
      "s_add_u32 m0, s68, 4096\n"
      "v_add_f32 v222, v222, v150\n"
      "v_add_f32 v222, v222, v149\n"
      "v_mfma_f32_16x16x32_bf16 v[156:159], a[252:255], v[12:15], v[156:159]\n"
      "buffer_load_dwordx4 v231, s[52:55], s64 offen offset:1024 lds\n"
      "v_exp_f32 v151, v151\n"
      "v_mfma_f32_16x16x32_bf16 a[156:159], a[220:223], v[172:175], a[156:159]\n"
      "v_add_f32 v222, v222, v151\n"
      "v_cvt_pk_bf16_f32 v171, v150, v151\n"
      "v_mfma_f32_16x16x32_bf16 v[160:163], a[224:227], v[16:19], 0\n"
      "v_exp_f32 v152, v152\n"
      "v_mfma_f32_16x16x32_bf16 v[112:115], a[192:195], v[168:171], v[112:115]\n"
      "ds_read_b64_tr_b16 a[192:193], v226 offset:16384\n"
      "v_add_f32 v216, v216, v152\n"
      "v_mfma_f32_16x16x32_bf16 v[164:167], a[228:231], v[16:19], 0\n"
      "v_exp_f32_e64 v153, v153\n"
      "v_mfma_f32_16x16x32_bf16 v[116:119], a[196:199], v[168:171], v[116:119]\n"
      "ds_read_b64_tr_b16 a[194:195], v226 offset:20480\n"
      "v_cvt_pk_bf16_f32 v172, v152, v153\n"
      "v_mfma_f32_16x16x32_bf16 v[160:163], a[232:235], v[20:23], v[160:163]\n"
      "v_exp_f32 v154, v154\n"
      "v_mfma_f32_16x16x32_bf16 v[120:123], a[200:203], v[168:171], v[120:123]\n"
      "ds_read_b64_tr_b16 a[196:197], v227 offset:16384\n"
      "v_add_f32 v216, v216, v153\n"
      "v_mfma_f32_16x16x32_bf16 v[164:167], a[236:239], v[20:23], v[164:167]\n"
      "v_exp_f32_e64 v155, v155\n"
      "v_mfma_f32_16x16x32_bf16 v[124:127], a[204:207], v[168:171], v[124:127]\n"
      "ds_read_b64_tr_b16 a[198:199], v227 offset:20480\n"
      "v_cvt_pk_bf16_f32 v173, v154, v155\n"
      "v_mfma_f32_16x16x32_bf16 v[160:163], a[240:243], v[24:27], v[160:163]\n"
      "v_exp_f32_e64 v156, v156\n"
      "v_mfma_f32_16x16x32_bf16 v[196:199], a[208:211], v[176:179], v[196:199]\n"
      "ds_read_b64_tr_b16 a[200:201], v228 offset:16384\n"
      "s_add_u32 m0, s68, 4096\n"
      "v_add_f32_e64 v216, v216, v154\n"
      "v_mfma_f32_16x16x32_bf16 v[164:167], a[244:247], v[24:27], v[164:167]\n"
      "buffer_load_dwordx4 v230, s[52:55], s64 offen offset:2048 lds\n"
      "v_exp_f32_e64 v157, v157\n"
      "v_mfma_f32_16x16x32_bf16 v[200:203], a[212:215], v[176:179], v[200:203]\n"
      "ds_read_b64_tr_b16 a[202:203], v228 offset:20480\n"
      "v_cvt_pk_bf16_f32 v174, v156, v157\n"
      "v_mfma_f32_16x16x32_bf16 v[160:163], a[248:251], v[28:31], v[160:163]\n"
      "v_exp_f32_e64 v158, v158\n"
      "v_mfma_f32_16x16x32_bf16 v[204:207], a[216:219], v[176:179], v[204:207]\n"
      "ds_read_b64_tr_b16 a[204:205], v229 offset:16384\n"
      "v_add_f32 v216, v216, v155\n"
      "v_mfma_f32_16x16x32_bf16 v[164:167], a[252:255], v[28:31], v[164:167]\n"
      "v_exp_f32 v159, v159\n"
      "v_mfma_f32_16x16x32_bf16 v[208:211], a[220:223], v[176:179], v[208:211]\n"
      "ds_read_b64_tr_b16 a[206:207], v229 offset:20480\n"
      "v_cvt_pk_bf16_f32 v175, v158, v159\n"
      "v_mfma_f32_16x16x32_bf16 v[144:147], a[224:227], v[32:35], 0\n"
      "v_exp_f32_e64 v160, v160\n"
      "v_mfma_f32_16x16x32_bf16 v[128:131], a[208:211], v[168:171], v[128:131]\n"
      "ds_read_b64_tr_b16 a[208:209], v226 offset:17408\n"
      "v_add_f32 v216, v216, v156\n"
      "v_mfma_f32_16x16x32_bf16 v[148:151], a[228:231], v[32:35], 0\n"
      "v_exp_f32 v161, v161\n"
      "v_mfma_f32_16x16x32_bf16 v[132:135], a[212:215], v[168:171], v[132:135]\n"
      "ds_read_b64_tr_b16 a[210:211], v226 offset:21504\n"
      "v_cvt_pk_bf16_f32 v176, v160, v161\n"
      "v_mfma_f32_16x16x32_bf16 v[144:147], a[232:235], v[36:39], v[144:147]\n"
      "v_exp_f32 v162, v162\n"
      "v_mfma_f32_16x16x32_bf16 v[136:139], a[216:219], v[168:171], v[136:139]\n"
      "ds_read_b64_tr_b16 a[212:213], v227 offset:17408\n"
      "v_add_f32 v216, v216, v157\n"
      "v_mfma_f32_16x16x32_bf16 v[148:151], a[236:239], v[36:39], v[148:151]\n"
      "v_exp_f32_e64 v163, v163\n"
      "v_mfma_f32_16x16x32_bf16 v[140:143], a[220:223], v[168:171], v[140:143]\n"
      "ds_read_b64_tr_b16 a[214:215], v227 offset:21504\n"
      "v_cvt_pk_bf16_f32 v177, v162, v163\n"
      "v_mfma_f32_16x16x32_bf16 v[144:147], a[240:243], v[40:43], v[144:147]\n"
      "s_waitcnt lgkmcnt(4)\n"
      "v_exp_f32 v164, v164\n"
      "v_mfma_f32_16x16x32_bf16 a[0:3], a[192:195], v[172:175], a[0:3]\n"
      "ds_read_b64_tr_b16 a[216:217], v228 offset:17408\n"
      "v_add_f32 v216, v216, v158\n"
      "v_mfma_f32_16x16x32_bf16 v[148:151], a[244:247], v[40:43], v[148:151]\n"
      "v_exp_f32 v165, v165\n"
      "v_mfma_f32_16x16x32_bf16 a[4:7], a[196:199], v[172:175], a[4:7]\n"
      "ds_read_b64_tr_b16 a[218:219], v228 offset:21504\n"
      "s_add_u32 m0, s68, 4096\n"
      "v_cvt_pk_bf16_f32 v178, v164, v165\n"
      "v_mfma_f32_16x16x32_bf16 v[144:147], a[248:251], v[44:47], v[144:147]\n"
      "buffer_load_dwordx4 v231, s[52:55], s64 offen offset:3072 lds\n"
      "v_exp_f32 v166, v166\n"
      "v_mfma_f32_16x16x32_bf16 a[8:11], a[200:203], v[172:175], a[8:11]\n"
      "ds_read_b64_tr_b16 a[220:221], v229 offset:17408\n"
      "s_add_u32 s80, s80, 8192\n"
      "v_add_f32 v216, v216, v159\n"
      "v_mfma_f32_16x16x32_bf16 v[148:151], a[252:255], v[44:47], v[148:151]\n"
      "s_and_b32 s80, s80, 122880\n"
      "v_exp_f32_e64 v167, v167\n"
      "v_mfma_f32_16x16x32_bf16 a[12:15], a[204:207], v[172:175], a[12:15]\n"
      "ds_read_b64_tr_b16 a[222:223], v229 offset:21504\n"
      "v_cvt_pk_bf16_f32 v179, v166, v167\n"
      "s_and_b32 s78, s81, 255\n"
      "s_lshr_b32 s81, s81, 8\n"
      "s_sub_u32 s83, s83, 1\n"
      "s_cmp_lg_u32 s83, 0\n"
      "s_cbranch_scc1 ATTB_%=\n"
      "s_branch ATTX_%=\n"
      "PC1B_%=:\n"
      "s_sleep 1\n"
      "s_memrealtime s[76:77]\n"
      "s_waitcnt lgkmcnt(0)\n"
      "s_sub_u32 s77, s76, s96\n"
      "s_cmp_lt_i32 s77, 0\n"
      "s_cbranch_scc1 PC1B_%=\n"
      "s_mov_b32 s76, s96\n"
      "s_branch PC1RB_%=\n"
      "PC2B_%=:\n"
      "s_sleep 1\n"
      "s_memrealtime s[76:77]\n"
      "s_waitcnt lgkmcnt(0)\n"
      "s_sub_u32 s77, s76, s96\n"
      "s_cmp_lt_i32 s77, 0\n"
      "s_cbranch_scc1 PC2B_%=\n"
      "s_mov_b32 s76, s96\n"
      "s_branch PC2RB_%=\n"
      "PC0B_%=:\n"
      "s_sleep 1\n"
      "s_memrealtime s[76:77]\n"
      "s_waitcnt lgkmcnt(0)\n"
      "s_sub_u32 s77, s76, s96\n"
      "s_cmp_lt_i32 s77, 0\n"
      "s_cbranch_scc1 PC0B_%=\n"
      "s_mov_b32 s76, s96\n"
      "s_branch PC0RB_%=\n"
      "SLOW0_%=:\n"
      "v_mbcnt_lo_u32_b32 v253, -1, 0\n"
      "v_mbcnt_hi_u32_b32 v253, -1, v253\n"
      "v_and_b32 v212, 15, v253\n"
      "v_lshlrev_b32 v213, 1, v212\n"
      "v_lshlrev_b32 v212, 8, v212\n"
      "v_and_b32 v253, 48, v253\n"
      "v_add_u32_e64 v212, v212, v253\n"
      "v_lshl_add_u32 v213, v253, 6, v213\n"
      "v_mov_b32 v252, 0xff800000\n"
      "s_mov_b32 s96, 0\n"
      "SLA0_%=:\n"
      "s_mov_b64 s[48:49], %[pk]\n"
      "s_mov_b32 s50, 131072\n"
      "s_add_u32 s97, s96, 0\n"
      "buffer_load_dwordx4 v[236:239], v212, s[48:51], s97 offen offset:0\n"
      "s_waitcnt vmcnt(0)\n"
      "v_mfma_f32_16x16x32_bf16 v[240:243], v[236:239], v[0:3], 0\n"
      "s_nop 3\n"
      "buffer_load_dwordx4 v[236:239], v212, s[48:51], s97 offen offset:64\n"
      "s_waitcnt vmcnt(0)\n"
      "v_mfma_f32_16x16x32_bf16 v[240:243], v[236:239], v[4:7], v[240:243]\n"
      "s_nop 3\n"
      "buffer_load_dwordx4 v[236:239], v212, s[48:51], s97 offen offset:128\n"
      "s_waitcnt vmcnt(0)\n"
      "v_mfma_f32_16x16x32_bf16 v[240:243], v[236:239], v[8:11], v[240:243]\n"
      "s_nop 3\n"
      "buffer_load_dwordx4 v[236:239], v212, s[48:51], s97 offen offset:192\n"
      "s_waitcnt vmcnt(0)\n"
      "v_mfma_f32_16x16x32_bf16 v[240:243], v[236:239], v[12:15], v[240:243]\n"
      "s_nop 3\n"
      "s_add_u32 s97, s96, 4096\n"
      "buffer_load_dwordx4 v[236:239], v212, s[48:51], s97 offen offset:0\n"
      "s_waitcnt vmcnt(0)\n"
      "v_mfma_f32_16x16x32_bf16 v[244:247], v[236:239], v[0:3], 0\n"
      "s_nop 3\n"
      "buffer_load_dwordx4 v[236:239], v212, s[48:51], s97 offen offset:64\n"
      "s_waitcnt vmcnt(0)\n"
      "v_mfma_f32_16x16x32_bf16 v[244:247], v[236:239], v[4:7], v[244:247]\n"
      "s_nop 3\n"
      "buffer_load_dwordx4 v[236:239], v212, s[48:51], s97 offen offset:128\n"
      "s_waitcnt vmcnt(0)\n"
      "v_mfma_f32_16x16x32_bf16 v[244:247], v[236:239], v[8:11], v[244:247]\n"
      "s_nop 3\n"
      "buffer_load_dwordx4 v[236:239], v212, s[48:51], s97 offen offset:192\n"
      "s_waitcnt vmcnt(0)\n"
      "v_mfma_f32_16x16x32_bf16 v[244:247], v[236:239], v[12:15], v[244:247]\n"
      "s_nop 3\n"
      "s_nop 7\n"
      "v_max3_f32 v252, v252, v240, v241\n"
      "v_max3_f32 v252, v252, v242, v243\n"
      "v_max3_f32 v252, v252, v244, v245\n"
      "v_max3_f32 v252, v252, v246, v247\n"
      "s_add_u32 s96, s96, 8192\n"
      "s_cmp_lt_u32 s96, 0x20000\n"
      "s_cbranch_scc1 SLA0_%=\n"
      "v_mov_b32 v253, v252\n"
      "s_nop 1\n"
      "v_permlane16_swap_b32 v252, v253\n"
      "v_max_f32 v252, v252, v253\n"
      "v_mov_b32 v253, v252\n"
      "s_nop 1\n"
      "v_permlane32_swap_b32 v252, v253\n"
      "v_max_f32_e64 v252, v252, v253\n"
      "v_accvgpr_write_b32 a0, 0\n"
      "v_accvgpr_write_b32 a1, 0\n"
      "v_accvgpr_write_b32 a2, 0\n"
      "v_accvgpr_write_b32 a3, 0\n"
      "v_accvgpr_write_b32 a4, 0\n"
      "v_accvgpr_write_b32 a5, 0\n"
      "v_accvgpr_write_b32 a6, 0\n"
      "v_accvgpr_write_b32 a7, 0\n"
      "v_accvgpr_write_b32 a8, 0\n"
      "v_accvgpr_write_b32 a9, 0\n"
      "v_accvgpr_write_b32 a10, 0\n"
      "v_accvgpr_write_b32 a11, 0\n"
      "v_accvgpr_write_b32 a12, 0\n"
      "v_accvgpr_write_b32 a13, 0\n"
      "v_accvgpr_write_b32 a14, 0\n"
      "v_accvgpr_write_b32 a15, 0\n"
      "v_accvgpr_write_b32 a16, 0\n"
      "v_accvgpr_write_b32 a17, 0\n"
      "v_accvgpr_write_b32 a18, 0\n"
      "v_accvgpr_write_b32 a19, 0\n"
      "v_accvgpr_write_b32 a20, 0\n"
      "v_accvgpr_write_b32 a21, 0\n"
      "v_accvgpr_write_b32 a22, 0\n"
      "v_accvgpr_write_b32 a23, 0\n"
      "v_accvgpr_write_b32 a24, 0\n"
      "v_accvgpr_write_b32 a25, 0\n"
      "v_accvgpr_write_b32 a26, 0\n"
      "v_accvgpr_write_b32 a27, 0\n"
      "v_accvgpr_write_b32 a28, 0\n"
      "v_accvgpr_write_b32 a29, 0\n"
      "v_accvgpr_write_b32 a30, 0\n"
      "v_accvgpr_write_b32 a31, 0\n"
      "v_mov_b32 v216, 0\n"
      "s_mov_b32 s96, 0\n"
      "SLB0_%=:\n"
      "s_mov_b64 s[48:49], %[pk]\n"
      "s_mov_b32 s50, 131072\n"
      "s_add_u32 s97, s96, 0\n"
      "buffer_load_dwordx4 v[236:239], v212, s[48:51], s97 offen offset:0\n"
      "s_waitcnt vmcnt(0)\n"
      "v_mfma_f32_16x16x32_bf16 v[240:243], v[236:239], v[0:3], 0\n"
      "s_nop 3\n"
      "buffer_load_dwordx4 v[236:239], v212, s[48:51], s97 offen offset:64\n"
      "s_waitcnt vmcnt(0)\n"
      "v_mfma_f32_16x16x32_bf16 v[240:243], v[236:239], v[4:7], v[240:243]\n"
      "s_nop 3\n"
      "buffer_load_dwordx4 v[236:239], v212, s[48:51], s97 offen offset:128\n"
      "s_waitcnt vmcnt(0)\n"
      "v_mfma_f32_16x16x32_bf16 v[240:243], v[236:239], v[8:11], v[240:243]\n"
      "s_nop 3\n"
      "buffer_load_dwordx4 v[236:239], v212, s[48:51], s97 offen offset:192\n"
      "s_waitcnt vmcnt(0)\n"
      "v_mfma_f32_16x16x32_bf16 v[240:243], v[236:239], v[12:15], v[240:243]\n"
      "s_nop 3\n"
      "s_add_u32 s97, s96, 4096\n"
      "buffer_load_dwordx4 v[236:239], v212, s[48:51], s97 offen offset:0\n"
      "s_waitcnt vmcnt(0)\n"
      "v_mfma_f32_16x16x32_bf16 v[244:247], v[236:239], v[0:3], 0\n"
      "s_nop 3\n"
      "buffer_load_dwordx4 v[236:239], v212, s[48:51], s97 offen offset:64\n"
      "s_waitcnt vmcnt(0)\n"
      "v_mfma_f32_16x16x32_bf16 v[244:247], v[236:239], v[4:7], v[244:247]\n"
      "s_nop 3\n"
      "buffer_load_dwordx4 v[236:239], v212, s[48:51], s97 offen offset:128\n"
      "s_waitcnt vmcnt(0)\n"
      "v_mfma_f32_16x16x32_bf16 v[244:247], v[236:239], v[8:11], v[244:247]\n"
      "s_nop 3\n"
      "buffer_load_dwordx4 v[236:239], v212, s[48:51], s97 offen offset:192\n"
      "s_waitcnt vmcnt(0)\n"
      "v_mfma_f32_16x16x32_bf16 v[244:247], v[236:239], v[12:15], v[244:247]\n"
      "s_nop 3\n"
      "s_nop 7\n"
      "v_sub_f32 v240, v240, v252\n"
      "v_sub_f32 v241, v241, v252\n"
      "v_sub_f32 v242, v242, v252\n"
      "v_sub_f32 v243, v243, v252\n"
      "v_sub_f32 v244, v244, v252\n"
      "v_sub_f32 v245, v245, v252\n"
      "v_sub_f32 v246, v246, v252\n"
      "v_sub_f32 v247, v247, v252\n"
      "v_exp_f32 v240, v240\n"
      "v_exp_f32 v241, v241\n"
      "v_exp_f32 v242, v242\n"
      "v_exp_f32 v243, v243\n"
      "v_exp_f32 v244, v244\n"
      "v_exp_f32 v245, v245\n"
      "v_exp_f32 v246, v246\n"
      "v_exp_f32 v247, v247\n"
      "s_nop 0\n"
      "v_add_f32 v216, v216, v240\n"
      "v_add_f32 v216, v216, v241\n"
      "v_add_f32 v216, v216, v242\n"
      "v_add_f32 v216, v216, v243\n"
      "v_add_f32_e64 v216, v216, v244\n"
      "v_add_f32_e64 v216, v216, v245\n"
      "v_add_f32_e64 v216, v216, v246\n"
      "v_add_f32_e64 v216, v216, v247\n"
      "v_cvt_pk_bf16_f32 v248, v240, v241\n"
      "v_cvt_pk_bf16_f32 v249, v242, v243\n"
      "v_cvt_pk_bf16_f32 v250, v244, v245\n"
      "v_cvt_pk_bf16_f32 v251, v246, v247\n"
      "s_mov_b64 s[48:49], %[pv]\n"
      "buffer_load_ushort v240, v213, s[48:51], s96 offen offset:0\n"
      "buffer_load_ushort v241, v213, s[48:51], s96 offen offset:256\n"
      "buffer_load_ushort v242, v213, s[48:51], s96 offen offset:512\n"
      "buffer_load_ushort v243, v213, s[48:51], s96 offen offset:768\n"
      "buffer_load_ushort v244, v213, s[48:51], s97 offen offset:0\n"
      "buffer_load_ushort v245, v213, s[48:51], s97 offen offset:256\n"
      "buffer_load_ushort v246, v213, s[48:51], s97 offen offset:512\n"
      "buffer_load_ushort v247, v213, s[48:51], s97 offen offset:768\n"
      "s_waitcnt vmcnt(0)\n"
      "v_lshl_or_b32 v236, v241, 16, v240\n"
      "v_lshl_or_b32 v237, v243, 16, v242\n"
      "v_lshl_or_b32 v238, v245, 16, v244\n"
      "v_lshl_or_b32 v239, v247, 16, v246\n"
      "s_nop 1\n"
      "v_mfma_f32_16x16x32_bf16 a[0:3], v[236:239], v[248:251], a[0:3]\n"
      "s_nop 3\n"
      "buffer_load_ushort v240, v213, s[48:51], s96 offen offset:32\n"
      "buffer_load_ushort v241, v213, s[48:51], s96 offen offset:288\n"
      "buffer_load_ushort v242, v213, s[48:51], s96 offen offset:544\n"
      "buffer_load_ushort v243, v213, s[48:51], s96 offen offset:800\n"
      "buffer_load_ushort v244, v213, s[48:51], s97 offen offset:32\n"
      "buffer_load_ushort v245, v213, s[48:51], s97 offen offset:288\n"
      "buffer_load_ushort v246, v213, s[48:51], s97 offen offset:544\n"
      "buffer_load_ushort v247, v213, s[48:51], s97 offen offset:800\n"
      "s_waitcnt vmcnt(0)\n"
      "v_lshl_or_b32 v236, v241, 16, v240\n"
      "v_lshl_or_b32 v237, v243, 16, v242\n"
      "v_lshl_or_b32 v238, v245, 16, v244\n"
      "v_lshl_or_b32 v239, v247, 16, v246\n"
      "s_nop 1\n"
      "v_mfma_f32_16x16x32_bf16 a[4:7], v[236:239], v[248:251], a[4:7]\n"
      "s_nop 3\n"
      "buffer_load_ushort v240, v213, s[48:51], s96 offen offset:64\n"
      "buffer_load_ushort v241, v213, s[48:51], s96 offen offset:320\n"
      "buffer_load_ushort v242, v213, s[48:51], s96 offen offset:576\n"
      "buffer_load_ushort v243, v213, s[48:51], s96 offen offset:832\n"
      "buffer_load_ushort v244, v213, s[48:51], s97 offen offset:64\n"
      "buffer_load_ushort v245, v213, s[48:51], s97 offen offset:320\n"
      "buffer_load_ushort v246, v213, s[48:51], s97 offen offset:576\n"
      "buffer_load_ushort v247, v213, s[48:51], s97 offen offset:832\n"
      "s_waitcnt vmcnt(0)\n"
      "v_lshl_or_b32 v236, v241, 16, v240\n"
      "v_lshl_or_b32 v237, v243, 16, v242\n"
      "v_lshl_or_b32 v238, v245, 16, v244\n"
      "v_lshl_or_b32 v239, v247, 16, v246\n"
      "s_nop 1\n"
      "v_mfma_f32_16x16x32_bf16 a[8:11], v[236:239], v[248:251], a[8:11]\n"
      "s_nop 3\n"
      "buffer_load_ushort v240, v213, s[48:51], s96 offen offset:96\n"
      "buffer_load_ushort v241, v213, s[48:51], s96 offen offset:352\n"
      "buffer_load_ushort v242, v213, s[48:51], s96 offen offset:608\n"
      "buffer_load_ushort v243, v213, s[48:51], s96 offen offset:864\n"
      "buffer_load_ushort v244, v213, s[48:51], s97 offen offset:96\n"
      "buffer_load_ushort v245, v213, s[48:51], s97 offen offset:352\n"
      "buffer_load_ushort v246, v213, s[48:51], s97 offen offset:608\n"
      "buffer_load_ushort v247, v213, s[48:51], s97 offen offset:864\n"
      "s_waitcnt vmcnt(0)\n"
      "v_lshl_or_b32 v236, v241, 16, v240\n"
      "v_lshl_or_b32 v237, v243, 16, v242\n"
      "v_lshl_or_b32 v238, v245, 16, v244\n"
      "v_lshl_or_b32 v239, v247, 16, v246\n"
      "s_nop 1\n"
      "v_mfma_f32_16x16x32_bf16 a[12:15], v[236:239], v[248:251], a[12:15]\n"
      "s_nop 3\n"
      "buffer_load_ushort v240, v213, s[48:51], s96 offen offset:128\n"
      "buffer_load_ushort v241, v213, s[48:51], s96 offen offset:384\n"
      "buffer_load_ushort v242, v213, s[48:51], s96 offen offset:640\n"
      "buffer_load_ushort v243, v213, s[48:51], s96 offen offset:896\n"
      "buffer_load_ushort v244, v213, s[48:51], s97 offen offset:128\n"
      "buffer_load_ushort v245, v213, s[48:51], s97 offen offset:384\n"
      "buffer_load_ushort v246, v213, s[48:51], s97 offen offset:640\n"
      "buffer_load_ushort v247, v213, s[48:51], s97 offen offset:896\n"
      "s_waitcnt vmcnt(0)\n"
      "v_lshl_or_b32 v236, v241, 16, v240\n"
      "v_lshl_or_b32 v237, v243, 16, v242\n"
      "v_lshl_or_b32 v238, v245, 16, v244\n"
      "v_lshl_or_b32 v239, v247, 16, v246\n"
      "s_nop 1\n"
      "v_mfma_f32_16x16x32_bf16 a[16:19], v[236:239], v[248:251], a[16:19]\n"
      "s_nop 3\n"
      "buffer_load_ushort v240, v213, s[48:51], s96 offen offset:160\n"
      "buffer_load_ushort v241, v213, s[48:51], s96 offen offset:416\n"
      "buffer_load_ushort v242, v213, s[48:51], s96 offen offset:672\n"
      "buffer_load_ushort v243, v213, s[48:51], s96 offen offset:928\n"
      "buffer_load_ushort v244, v213, s[48:51], s97 offen offset:160\n"
      "buffer_load_ushort v245, v213, s[48:51], s97 offen offset:416\n"
      "buffer_load_ushort v246, v213, s[48:51], s97 offen offset:672\n"
      "buffer_load_ushort v247, v213, s[48:51], s97 offen offset:928\n"
      "s_waitcnt vmcnt(0)\n"
      "v_lshl_or_b32 v236, v241, 16, v240\n"
      "v_lshl_or_b32 v237, v243, 16, v242\n"
      "v_lshl_or_b32 v238, v245, 16, v244\n"
      "v_lshl_or_b32 v239, v247, 16, v246\n"
      "s_nop 1\n"
      "v_mfma_f32_16x16x32_bf16 a[20:23], v[236:239], v[248:251], a[20:23]\n"
      "s_nop 3\n"
      "buffer_load_ushort v240, v213, s[48:51], s96 offen offset:192\n"
      "buffer_load_ushort v241, v213, s[48:51], s96 offen offset:448\n"
      "buffer_load_ushort v242, v213, s[48:51], s96 offen offset:704\n"
      "buffer_load_ushort v243, v213, s[48:51], s96 offen offset:960\n"
      "buffer_load_ushort v244, v213, s[48:51], s97 offen offset:192\n"
      "buffer_load_ushort v245, v213, s[48:51], s97 offen offset:448\n"
      "buffer_load_ushort v246, v213, s[48:51], s97 offen offset:704\n"
      "buffer_load_ushort v247, v213, s[48:51], s97 offen offset:960\n"
      "s_waitcnt vmcnt(0)\n"
      "v_lshl_or_b32 v236, v241, 16, v240\n"
      "v_lshl_or_b32 v237, v243, 16, v242\n"
      "v_lshl_or_b32 v238, v245, 16, v244\n"
      "v_lshl_or_b32 v239, v247, 16, v246\n"
      "s_nop 1\n"
      "v_mfma_f32_16x16x32_bf16 a[24:27], v[236:239], v[248:251], a[24:27]\n"
      "s_nop 3\n"
      "buffer_load_ushort v240, v213, s[48:51], s96 offen offset:224\n"
      "buffer_load_ushort v241, v213, s[48:51], s96 offen offset:480\n"
      "buffer_load_ushort v242, v213, s[48:51], s96 offen offset:736\n"
      "buffer_load_ushort v243, v213, s[48:51], s96 offen offset:992\n"
      "buffer_load_ushort v244, v213, s[48:51], s97 offen offset:224\n"
      "buffer_load_ushort v245, v213, s[48:51], s97 offen offset:480\n"
      "buffer_load_ushort v246, v213, s[48:51], s97 offen offset:736\n"
      "buffer_load_ushort v247, v213, s[48:51], s97 offen offset:992\n"
      "s_waitcnt vmcnt(0)\n"
      "v_lshl_or_b32 v236, v241, 16, v240\n"
      "v_lshl_or_b32 v237, v243, 16, v242\n"
      "v_lshl_or_b32 v238, v245, 16, v244\n"
      "v_lshl_or_b32 v239, v247, 16, v246\n"
      "s_nop 1\n"
      "v_mfma_f32_16x16x32_bf16 a[28:31], v[236:239], v[248:251], a[28:31]\n"
      "s_nop 3\n"
      "s_add_u32 s96, s96, 8192\n"
      "s_cmp_lt_u32 s96, 0x20000\n"
      "s_cbranch_scc1 SLB0_%=\n"
      "v_mov_b32 v253, v216\n"
      "s_nop 1\n"
      "v_permlane16_swap_b32 v216, v253\n"
      "v_add_f32 v216, v216, v253\n"
      "v_mov_b32 v253, v216\n"
      "s_nop 1\n"
      "v_permlane32_swap_b32 v216, v253\n"
      "v_add_f32 v216, v216, v253\n"
      "s_nop 3\n"
      "s_branch RET0_%=\n"
      "SLOW1_%=:\n"
      "v_mbcnt_lo_u32_b32 v253, -1, 0\n"
      "v_mbcnt_hi_u32_b32 v253, -1, v253\n"
      "v_and_b32 v212, 15, v253\n"
      "v_lshlrev_b32 v213, 1, v212\n"
      "v_lshlrev_b32 v212, 8, v212\n"
      "v_and_b32 v253, 48, v253\n"
      "v_add_u32_e64 v212, v212, v253\n"
      "v_lshl_add_u32 v213, v253, 6, v213\n"
      "v_mov_b32 v252, 0xff800000\n"
      "s_mov_b32 s96, 0\n"
      "SLA1_%=:\n"
      "s_mov_b64 s[48:49], %[pk]\n"
      "s_mov_b32 s50, 131072\n"
      "s_add_u32 s97, s96, 0\n"
      "buffer_load_dwordx4 v[236:239], v212, s[48:51], s97 offen offset:0\n"
      "s_waitcnt vmcnt(0)\n"
      "v_mfma_f32_16x16x32_bf16 v[240:243], v[236:239], v[16:19], 0\n"
      "s_nop 3\n"
      "buffer_load_dwordx4 v[236:239], v212, s[48:51], s97 offen offset:64\n"
      "s_waitcnt vmcnt(0)\n"
      "v_mfma_f32_16x16x32_bf16 v[240:243], v[236:239], v[20:23], v[240:243]\n"
      "s_nop 3\n"
      "buffer_load_dwordx4 v[236:239], v212, s[48:51], s97 offen offset:128\n"
      "s_waitcnt vmcnt(0)\n"
      "v_mfma_f32_16x16x32_bf16 v[240:243], v[236:239], v[24:27], v[240:243]\n"
      "s_nop 3\n"
      "buffer_load_dwordx4 v[236:239], v212, s[48:51], s97 offen offset:192\n"
      "s_waitcnt vmcnt(0)\n"
      "v_mfma_f32_16x16x32_bf16 v[240:243], v[236:239], v[28:31], v[240:243]\n"
      "s_nop 3\n"
      "s_add_u32 s97, s96, 4096\n"
      "buffer_load_dwordx4 v[236:239], v212, s[48:51], s97 offen offset:0\n"
      "s_waitcnt vmcnt(0)\n"
      "v_mfma_f32_16x16x32_bf16 v[244:247], v[236:239], v[16:19], 0\n"
      "s_nop 3\n"
      "buffer_load_dwordx4 v[236:239], v212, s[48:51], s97 offen offset:64\n"
      "s_waitcnt vmcnt(0)\n"
      "v_mfma_f32_16x16x32_bf16 v[244:247], v[236:239], v[20:23], v[244:247]\n"
      "s_nop 3\n"
      "buffer_load_dwordx4 v[236:239], v212, s[48:51], s97 offen offset:128\n"
      "s_waitcnt vmcnt(0)\n"
      "v_mfma_f32_16x16x32_bf16 v[244:247], v[236:239], v[24:27], v[244:247]\n"
      "s_nop 3\n"
      "buffer_load_dwordx4 v[236:239], v212, s[48:51], s97 offen offset:192\n"
      "s_waitcnt vmcnt(0)\n"
      "v_mfma_f32_16x16x32_bf16 v[244:247], v[236:239], v[28:31], v[244:247]\n"
      "s_nop 3\n"
      "s_nop 7\n"
      "v_max3_f32 v252, v252, v240, v241\n"
      "v_max3_f32 v252, v252, v242, v243\n"
      "v_max3_f32 v252, v252, v244, v245\n"
      "v_max3_f32 v252, v252, v246, v247\n"
      "s_add_u32 s96, s96, 8192\n"
      "s_cmp_lt_u32 s96, 0x20000\n"
      "s_cbranch_scc1 SLA1_%=\n"
      "v_mov_b32 v253, v252\n"
      "s_nop 1\n"
      "v_permlane16_swap_b32 v252, v253\n"
      "v_max_f32 v252, v252, v253\n"
      "v_mov_b32 v253, v252\n"
      "s_nop 1\n"
      "v_permlane32_swap_b32 v252, v253\n"
      "v_max_f32_e64 v252, v252, v253\n"
      "v_accvgpr_write_b32 a32, 0\n"
      "v_accvgpr_write_b32 a33, 0\n"
      "v_accvgpr_write_b32 a34, 0\n"
      "v_accvgpr_write_b32 a35, 0\n"
      "v_accvgpr_write_b32 a36, 0\n"
      "v_accvgpr_write_b32 a37, 0\n"
      "v_accvgpr_write_b32 a38, 0\n"
      "v_accvgpr_write_b32 a39, 0\n"
      "v_accvgpr_write_b32 a40, 0\n"
      "v_accvgpr_write_b32 a41, 0\n"
      "v_accvgpr_write_b32 a42, 0\n"
      "v_accvgpr_write_b32 a43, 0\n"
      "v_accvgpr_write_b32 a44, 0\n"
      "v_accvgpr_write_b32 a45, 0\n"
      "v_accvgpr_write_b32 a46, 0\n"
      "v_accvgpr_write_b32 a47, 0\n"
      "v_accvgpr_write_b32 a48, 0\n"
      "v_accvgpr_write_b32 a49, 0\n"
      "v_accvgpr_write_b32 a50, 0\n"
      "v_accvgpr_write_b32 a51, 0\n"
      "v_accvgpr_write_b32 a52, 0\n"
      "v_accvgpr_write_b32 a53, 0\n"
      "v_accvgpr_write_b32 a54, 0\n"
      "v_accvgpr_write_b32 a55, 0\n"
      "v_accvgpr_write_b32 a56, 0\n"
      "v_accvgpr_write_b32 a57, 0\n"
      "v_accvgpr_write_b32 a58, 0\n"
      "v_accvgpr_write_b32 a59, 0\n"
      "v_accvgpr_write_b32 a60, 0\n"
      "v_accvgpr_write_b32 a61, 0\n"
      "v_accvgpr_write_b32 a62, 0\n"
      "v_accvgpr_write_b32 a63, 0\n"
      "v_mov_b32_e64 v217, 0\n"
      "s_mov_b32 s96, 0\n"
      "SLB1_%=:\n"
      "s_mov_b64 s[48:49], %[pk]\n"
      "s_mov_b32 s50, 131072\n"
      "s_add_u32 s97, s96, 0\n"
      "buffer_load_dwordx4 v[236:239], v212, s[48:51], s97 offen offset:0\n"
      "s_waitcnt vmcnt(0)\n"
      "v_mfma_f32_16x16x32_bf16 v[240:243], v[236:239], v[16:19], 0\n"
      "s_nop 3\n"
      "buffer_load_dwordx4 v[236:239], v212, s[48:51], s97 offen offset:64\n"
      "s_waitcnt vmcnt(0)\n"
      "v_mfma_f32_16x16x32_bf16 v[240:243], v[236:239], v[20:23], v[240:243]\n"
      "s_nop 3\n"
      "buffer_load_dwordx4 v[236:239], v212, s[48:51], s97 offen offset:128\n"
      "s_waitcnt vmcnt(0)\n"
      "v_mfma_f32_16x16x32_bf16 v[240:243], v[236:239], v[24:27], v[240:243]\n"
      "s_nop 3\n"
      "buffer_load_dwordx4 v[236:239], v212, s[48:51], s97 offen offset:192\n"
      "s_waitcnt vmcnt(0)\n"
      "v_mfma_f32_16x16x32_bf16 v[240:243], v[236:239], v[28:31], v[240:243]\n"
      "s_nop 3\n"
      "s_add_u32 s97, s96, 4096\n"
      "buffer_load_dwordx4 v[236:239], v212, s[48:51], s97 offen offset:0\n"
      "s_waitcnt vmcnt(0)\n"
      "v_mfma_f32_16x16x32_bf16 v[244:247], v[236:239], v[16:19], 0\n"
      "s_nop 3\n"
      "buffer_load_dwordx4 v[236:239], v212, s[48:51], s97 offen offset:64\n"
      "s_waitcnt vmcnt(0)\n"
      "v_mfma_f32_16x16x32_bf16 v[244:247], v[236:239], v[20:23], v[244:247]\n"
      "s_nop 3\n"
      "buffer_load_dwordx4 v[236:239], v212, s[48:51], s97 offen offset:128\n"
      "s_waitcnt vmcnt(0)\n"
      "v_mfma_f32_16x16x32_bf16 v[244:247], v[236:239], v[24:27], v[244:247]\n"
      "s_nop 3\n"
      "buffer_load_dwordx4 v[236:239], v212, s[48:51], s97 offen offset:192\n"
      "s_waitcnt vmcnt(0)\n"
      "v_mfma_f32_16x16x32_bf16 v[244:247], v[236:239], v[28:31], v[244:247]\n"
      "s_nop 3\n"
      "s_nop 7\n"
      "v_sub_f32 v240, v240, v252\n"
      "v_sub_f32 v241, v241, v252\n"
      "v_sub_f32 v242, v242, v252\n"
      "v_sub_f32 v243, v243, v252\n"
      "v_sub_f32 v244, v244, v252\n"
      "v_sub_f32 v245, v245, v252\n"
      "v_sub_f32 v246, v246, v252\n"
      "v_sub_f32 v247, v247, v252\n"
      "v_exp_f32 v240, v240\n"
      "v_exp_f32 v241, v241\n"
      "v_exp_f32 v242, v242\n"
      "v_exp_f32 v243, v243\n"
      "v_exp_f32 v244, v244\n"
      "v_exp_f32 v245, v245\n"
      "v_exp_f32 v246, v246\n"
      "v_exp_f32 v247, v247\n"
      "s_nop 0\n"
      "v_add_f32 v217, v217, v240\n"
      "v_add_f32 v217, v217, v241\n"
      "v_add_f32 v217, v217, v242\n"
      "v_add_f32 v217, v217, v243\n"
      "v_add_f32 v217, v217, v244\n"
      "v_add_f32_e64 v217, v217, v245\n"
      "v_add_f32_e64 v217, v217, v246\n"
      "v_add_f32_e64 v217, v217, v247\n"
      "v_cvt_pk_bf16_f32 v248, v240, v241\n"
      "v_cvt_pk_bf16_f32 v249, v242, v243\n"
      "v_cvt_pk_bf16_f32 v250, v244, v245\n"
      "v_cvt_pk_bf16_f32 v251, v246, v247\n"
      "s_mov_b64 s[48:49], %[pv]\n"
      "buffer_load_ushort v240, v213, s[48:51], s96 offen offset:0\n"
      "buffer_load_ushort v241, v213, s[48:51], s96 offen offset:256\n"
      "buffer_load_ushort v242, v213, s[48:51], s96 offen offset:512\n"
      "buffer_load_ushort v243, v213, s[48:51], s96 offen offset:768\n"
      "buffer_load_ushort v244, v213, s[48:51], s97 offen offset:0\n"
      "buffer_load_ushort v245, v213, s[48:51], s97 offen offset:256\n"
      "buffer_load_ushort v246, v213, s[48:51], s97 offen offset:512\n"
      "buffer_load_ushort v247, v213, s[48:51], s97 offen offset:768\n"
      "s_waitcnt vmcnt(0)\n"
      "v_lshl_or_b32 v236, v241, 16, v240\n"
      "v_lshl_or_b32 v237, v243, 16, v242\n"
      "v_lshl_or_b32 v238, v245, 16, v244\n"
      "v_lshl_or_b32 v239, v247, 16, v246\n"
      "s_nop 1\n"
      "v_mfma_f32_16x16x32_bf16 a[32:35], v[236:239], v[248:251], a[32:35]\n"
      "s_nop 3\n"
      "buffer_load_ushort v240, v213, s[48:51], s96 offen offset:32\n"
      "buffer_load_ushort v241, v213, s[48:51], s96 offen offset:288\n"
      "buffer_load_ushort v242, v213, s[48:51], s96 offen offset:544\n"
      "buffer_load_ushort v243, v213, s[48:51], s96 offen offset:800\n"
      "buffer_load_ushort v244, v213, s[48:51], s97 offen offset:32\n"
      "buffer_load_ushort v245, v213, s[48:51], s97 offen offset:288\n"
      "buffer_load_ushort v246, v213, s[48:51], s97 offen offset:544\n"
      "buffer_load_ushort v247, v213, s[48:51], s97 offen offset:800\n"
      "s_waitcnt vmcnt(0)\n"
      "v_lshl_or_b32 v236, v241, 16, v240\n"
      "v_lshl_or_b32 v237, v243, 16, v242\n"
      "v_lshl_or_b32 v238, v245, 16, v244\n"
      "v_lshl_or_b32 v239, v247, 16, v246\n"
      "s_nop 1\n"
      "v_mfma_f32_16x16x32_bf16 a[36:39], v[236:239], v[248:251], a[36:39]\n"
      "s_nop 3\n"
      "buffer_load_ushort v240, v213, s[48:51], s96 offen offset:64\n"
      "buffer_load_ushort v241, v213, s[48:51], s96 offen offset:320\n"
      "buffer_load_ushort v242, v213, s[48:51], s96 offen offset:576\n"
      "buffer_load_ushort v243, v213, s[48:51], s96 offen offset:832\n"
      "buffer_load_ushort v244, v213, s[48:51], s97 offen offset:64\n"
      "buffer_load_ushort v245, v213, s[48:51], s97 offen offset:320\n"
      "buffer_load_ushort v246, v213, s[48:51], s97 offen offset:576\n"
      "buffer_load_ushort v247, v213, s[48:51], s97 offen offset:832\n"
      "s_waitcnt vmcnt(0)\n"
      "v_lshl_or_b32 v236, v241, 16, v240\n"
      "v_lshl_or_b32 v237, v243, 16, v242\n"
      "v_lshl_or_b32 v238, v245, 16, v244\n"
      "v_lshl_or_b32 v239, v247, 16, v246\n"
      "s_nop 1\n"
      "v_mfma_f32_16x16x32_bf16 a[40:43], v[236:239], v[248:251], a[40:43]\n"
      "s_nop 3\n"
      "buffer_load_ushort v240, v213, s[48:51], s96 offen offset:96\n"
      "buffer_load_ushort v241, v213, s[48:51], s96 offen offset:352\n"
      "buffer_load_ushort v242, v213, s[48:51], s96 offen offset:608\n"
      "buffer_load_ushort v243, v213, s[48:51], s96 offen offset:864\n"
      "buffer_load_ushort v244, v213, s[48:51], s97 offen offset:96\n"
      "buffer_load_ushort v245, v213, s[48:51], s97 offen offset:352\n"
      "buffer_load_ushort v246, v213, s[48:51], s97 offen offset:608\n"
      "buffer_load_ushort v247, v213, s[48:51], s97 offen offset:864\n"
      "s_waitcnt vmcnt(0)\n"
      "v_lshl_or_b32 v236, v241, 16, v240\n"
      "v_lshl_or_b32 v237, v243, 16, v242\n"
      "v_lshl_or_b32 v238, v245, 16, v244\n"
      "v_lshl_or_b32 v239, v247, 16, v246\n"
      "s_nop 1\n"
      "v_mfma_f32_16x16x32_bf16 a[44:47], v[236:239], v[248:251], a[44:47]\n"
      "s_nop 3\n"
      "buffer_load_ushort v240, v213, s[48:51], s96 offen offset:128\n"
      "buffer_load_ushort v241, v213, s[48:51], s96 offen offset:384\n"
      "buffer_load_ushort v242, v213, s[48:51], s96 offen offset:640\n"
      "buffer_load_ushort v243, v213, s[48:51], s96 offen offset:896\n"
      "buffer_load_ushort v244, v213, s[48:51], s97 offen offset:128\n"
      "buffer_load_ushort v245, v213, s[48:51], s97 offen offset:384\n"
      "buffer_load_ushort v246, v213, s[48:51], s97 offen offset:640\n"
      "buffer_load_ushort v247, v213, s[48:51], s97 offen offset:896\n"
      "s_waitcnt vmcnt(0)\n"
      "v_lshl_or_b32 v236, v241, 16, v240\n"
      "v_lshl_or_b32 v237, v243, 16, v242\n"
      "v_lshl_or_b32 v238, v245, 16, v244\n"
      "v_lshl_or_b32 v239, v247, 16, v246\n"
      "s_nop 1\n"
      "v_mfma_f32_16x16x32_bf16 a[48:51], v[236:239], v[248:251], a[48:51]\n"
      "s_nop 3\n"
      "buffer_load_ushort v240, v213, s[48:51], s96 offen offset:160\n"
      "buffer_load_ushort v241, v213, s[48:51], s96 offen offset:416\n"
      "buffer_load_ushort v242, v213, s[48:51], s96 offen offset:672\n"
      "buffer_load_ushort v243, v213, s[48:51], s96 offen offset:928\n"
      "buffer_load_ushort v244, v213, s[48:51], s97 offen offset:160\n"
      "buffer_load_ushort v245, v213, s[48:51], s97 offen offset:416\n"
      "buffer_load_ushort v246, v213, s[48:51], s97 offen offset:672\n"
      "buffer_load_ushort v247, v213, s[48:51], s97 offen offset:928\n"
      "s_waitcnt vmcnt(0)\n"
      "v_lshl_or_b32 v236, v241, 16, v240\n"
      "v_lshl_or_b32 v237, v243, 16, v242\n"
      "v_lshl_or_b32 v238, v245, 16, v244\n"
      "v_lshl_or_b32 v239, v247, 16, v246\n"
      "s_nop 1\n"
      "v_mfma_f32_16x16x32_bf16 a[52:55], v[236:239], v[248:251], a[52:55]\n"
      "s_nop 3\n"
      "buffer_load_ushort v240, v213, s[48:51], s96 offen offset:192\n"
      "buffer_load_ushort v241, v213, s[48:51], s96 offen offset:448\n"
      "buffer_load_ushort v242, v213, s[48:51], s96 offen offset:704\n"
      "buffer_load_ushort v243, v213, s[48:51], s96 offen offset:960\n"
      "buffer_load_ushort v244, v213, s[48:51], s97 offen offset:192\n"
      "buffer_load_ushort v245, v213, s[48:51], s97 offen offset:448\n"
      "buffer_load_ushort v246, v213, s[48:51], s97 offen offset:704\n"
      "buffer_load_ushort v247, v213, s[48:51], s97 offen offset:960\n"
      "s_waitcnt vmcnt(0)\n"
      "v_lshl_or_b32 v236, v241, 16, v240\n"
      "v_lshl_or_b32 v237, v243, 16, v242\n"
      "v_lshl_or_b32 v238, v245, 16, v244\n"
      "v_lshl_or_b32 v239, v247, 16, v246\n"
      "s_nop 1\n"
      "v_mfma_f32_16x16x32_bf16 a[56:59], v[236:239], v[248:251], a[56:59]\n"
      "s_nop 3\n"
      "buffer_load_ushort v240, v213, s[48:51], s96 offen offset:224\n"
      "buffer_load_ushort v241, v213, s[48:51], s96 offen offset:480\n"
      "buffer_load_ushort v242, v213, s[48:51], s96 offen offset:736\n"
      "buffer_load_ushort v243, v213, s[48:51], s96 offen offset:992\n"
      "buffer_load_ushort v244, v213, s[48:51], s97 offen offset:224\n"
      "buffer_load_ushort v245, v213, s[48:51], s97 offen offset:480\n"
      "buffer_load_ushort v246, v213, s[48:51], s97 offen offset:736\n"
      "buffer_load_ushort v247, v213, s[48:51], s97 offen offset:992\n"
      "s_waitcnt vmcnt(0)\n"
      "v_lshl_or_b32 v236, v241, 16, v240\n"
      "v_lshl_or_b32 v237, v243, 16, v242\n"
      "v_lshl_or_b32 v238, v245, 16, v244\n"
      "v_lshl_or_b32 v239, v247, 16, v246\n"
      "s_nop 1\n"
      "v_mfma_f32_16x16x32_bf16 a[60:63], v[236:239], v[248:251], a[60:63]\n"
      "s_nop 3\n"
      "s_add_u32 s96, s96, 8192\n"
      "s_cmp_lt_u32 s96, 0x20000\n"
      "s_cbranch_scc1 SLB1_%=\n"
      "v_mov_b32 v253, v217\n"
      "s_nop 1\n"
      "v_permlane16_swap_b32 v217, v253\n"
      "v_add_f32 v217, v217, v253\n"
      "v_mov_b32 v253, v217\n"
      "s_nop 1\n"
      "v_permlane32_swap_b32 v217, v253\n"
      "v_add_f32 v217, v217, v253\n"
      "s_nop 3\n"
      "s_branch RET1_%=\n"
      "SLOW2_%=:\n"
      "v_mbcnt_lo_u32_b32 v253, -1, 0\n"
      "v_mbcnt_hi_u32_b32 v253, -1, v253\n"
      "v_and_b32 v212, 15, v253\n"
      "v_lshlrev_b32 v213, 1, v212\n"
      "v_lshlrev_b32 v212, 8, v212\n"
      "v_and_b32 v253, 48, v253\n"
      "v_add_u32_e64 v212, v212, v253\n"
      "v_lshl_add_u32 v213, v253, 6, v213\n"
      "v_mov_b32 v252, 0xff800000\n"
      "s_mov_b32 s96, 0\n"
      "SLA2_%=:\n"
      "s_mov_b64 s[48:49], %[pk]\n"
      "s_mov_b32 s50, 131072\n"
      "s_add_u32 s97, s96, 0\n"
      "buffer_load_dwordx4 v[236:239], v212, s[48:51], s97 offen offset:0\n"
      "s_waitcnt vmcnt(0)\n"
      "v_mfma_f32_16x16x32_bf16 v[240:243], v[236:239], v[32:35], 0\n"
      "s_nop 3\n"
      "buffer_load_dwordx4 v[236:239], v212, s[48:51], s97 offen offset:64\n"
      "s_waitcnt vmcnt(0)\n"
      "v_mfma_f32_16x16x32_bf16 v[240:243], v[236:239], v[36:39], v[240:243]\n"
      "s_nop 3\n"
      "buffer_load_dwordx4 v[236:239], v212, s[48:51], s97 offen offset:128\n"
      "s_waitcnt vmcnt(0)\n"
      "v_mfma_f32_16x16x32_bf16 v[240:243], v[236:239], v[40:43], v[240:243]\n"
      "s_nop 3\n"
      "buffer_load_dwordx4 v[236:239], v212, s[48:51], s97 offen offset:192\n"
      "s_waitcnt vmcnt(0)\n"
      "v_mfma_f32_16x16x32_bf16 v[240:243], v[236:239], v[44:47], v[240:243]\n"
      "s_nop 3\n"
      "s_add_u32 s97, s96, 4096\n"
      "buffer_load_dwordx4 v[236:239], v212, s[48:51], s97 offen offset:0\n"
      "s_waitcnt vmcnt(0)\n"
      "v_mfma_f32_16x16x32_bf16 v[244:247], v[236:239], v[32:35], 0\n"
      "s_nop 3\n"
      "buffer_load_dwordx4 v[236:239], v212, s[48:51], s97 offen offset:64\n"
      "s_waitcnt vmcnt(0)\n"
      "v_mfma_f32_16x16x32_bf16 v[244:247], v[236:239], v[36:39], v[244:247]\n"
      "s_nop 3\n"
      "buffer_load_dwordx4 v[236:239], v212, s[48:51], s97 offen offset:128\n"
      "s_waitcnt vmcnt(0)\n"
      "v_mfma_f32_16x16x32_bf16 v[244:247], v[236:239], v[40:43], v[244:247]\n"
      "s_nop 3\n"
      "buffer_load_dwordx4 v[236:239], v212, s[48:51], s97 offen offset:192\n"
      "s_waitcnt vmcnt(0)\n"
      "v_mfma_f32_16x16x32_bf16 v[244:247], v[236:239], v[44:47], v[244:247]\n"
      "s_nop 3\n"
      "s_nop 7\n"
      "v_max3_f32 v252, v252, v240, v241\n"
      "v_max3_f32 v252, v252, v242, v243\n"
      "v_max3_f32 v252, v252, v244, v245\n"
      "v_max3_f32 v252, v252, v246, v247\n"
      "s_add_u32 s96, s96, 8192\n"
      "s_cmp_lt_u32 s96, 0x20000\n"
      "s_cbranch_scc1 SLA2_%=\n"
      "v_mov_b32 v253, v252\n"
      "s_nop 1\n"
      "v_permlane16_swap_b32 v252, v253\n"
      "v_max_f32 v252, v252, v253\n"
      "v_mov_b32 v253, v252\n"
      "s_nop 1\n"
      "v_permlane32_swap_b32 v252, v253\n"
      "v_max_f32_e64 v252, v252, v253\n"
      "v_accvgpr_write_b32 a64, 0\n"
      "v_accvgpr_write_b32 a65, 0\n"
      "v_accvgpr_write_b32 a66, 0\n"
      "v_accvgpr_write_b32 a67, 0\n"
      "v_accvgpr_write_b32 a68, 0\n"
      "v_accvgpr_write_b32 a69, 0\n"
      "v_accvgpr_write_b32 a70, 0\n"
      "v_accvgpr_write_b32 a71, 0\n"
      "v_accvgpr_write_b32 a72, 0\n"
      "v_accvgpr_write_b32 a73, 0\n"
      "v_accvgpr_write_b32 a74, 0\n"
      "v_accvgpr_write_b32 a75, 0\n"
      "v_accvgpr_write_b32 a76, 0\n"
      "v_accvgpr_write_b32 a77, 0\n"
      "v_accvgpr_write_b32 a78, 0\n"
      "v_accvgpr_write_b32 a79, 0\n"
      "v_accvgpr_write_b32 a80, 0\n"
      "v_accvgpr_write_b32 a81, 0\n"
      "v_accvgpr_write_b32 a82, 0\n"
      "v_accvgpr_write_b32 a83, 0\n"
      "v_accvgpr_write_b32 a84, 0\n"
      "v_accvgpr_write_b32 a85, 0\n"
      "v_accvgpr_write_b32 a86, 0\n"
      "v_accvgpr_write_b32 a87, 0\n"
      "v_accvgpr_write_b32 a88, 0\n"
      "v_accvgpr_write_b32 a89, 0\n"
      "v_accvgpr_write_b32 a90, 0\n"
      "v_accvgpr_write_b32 a91, 0\n"
      "v_accvgpr_write_b32 a92, 0\n"
      "v_accvgpr_write_b32 a93, 0\n"
      "v_accvgpr_write_b32 a94, 0\n"
      "v_accvgpr_write_b32 a95, 0\n"
      "v_mov_b32 v218, 0\n"
      "s_mov_b32 s96, 0\n"
      "SLB2_%=:\n"
      "s_mov_b64 s[48:49], %[pk]\n"
      "s_mov_b32 s50, 131072\n"
      "s_add_u32 s97, s96, 0\n"
      "buffer_load_dwordx4 v[236:239], v212, s[48:51], s97 offen offset:0\n"
      "s_waitcnt vmcnt(0)\n"
      "v_mfma_f32_16x16x32_bf16 v[240:243], v[236:239], v[32:35], 0\n"
      "s_nop 3\n"
      "buffer_load_dwordx4 v[236:239], v212, s[48:51], s97 offen offset:64\n"
      "s_waitcnt vmcnt(0)\n"
      "v_mfma_f32_16x16x32_bf16 v[240:243], v[236:239], v[36:39], v[240:243]\n"
      "s_nop 3\n"
      "buffer_load_dwordx4 v[236:239], v212, s[48:51], s97 offen offset:128\n"
      "s_waitcnt vmcnt(0)\n"
      "v_mfma_f32_16x16x32_bf16 v[240:243], v[236:239], v[40:43], v[240:243]\n"
      "s_nop 3\n"
      "buffer_load_dwordx4 v[236:239], v212, s[48:51], s97 offen offset:192\n"
      "s_waitcnt vmcnt(0)\n"
      "v_mfma_f32_16x16x32_bf16 v[240:243], v[236:239], v[44:47], v[240:243]\n"
      "s_nop 3\n"
      "s_add_u32 s97, s96, 4096\n"
      "buffer_load_dwordx4 v[236:239], v212, s[48:51], s97 offen offset:0\n"
      "s_waitcnt vmcnt(0)\n"
      "v_mfma_f32_16x16x32_bf16 v[244:247], v[236:239], v[32:35], 0\n"
      "s_nop 3\n"
      "buffer_load_dwordx4 v[236:239], v212, s[48:51], s97 offen offset:64\n"
      "s_waitcnt vmcnt(0)\n"
      "v_mfma_f32_16x16x32_bf16 v[244:247], v[236:239], v[36:39], v[244:247]\n"
      "s_nop 3\n"
      "buffer_load_dwordx4 v[236:239], v212, s[48:51], s97 offen offset:128\n"
      "s_waitcnt vmcnt(0)\n"
      "v_mfma_f32_16x16x32_bf16 v[244:247], v[236:239], v[40:43], v[244:247]\n"
      "s_nop 3\n"
      "buffer_load_dwordx4 v[236:239], v212, s[48:51], s97 offen offset:192\n"
      "s_waitcnt vmcnt(0)\n"
      "v_mfma_f32_16x16x32_bf16 v[244:247], v[236:239], v[44:47], v[244:247]\n"
      "s_nop 3\n"
      "s_nop 7\n"
      "v_sub_f32 v240, v240, v252\n"
      "v_sub_f32 v241, v241, v252\n"
      "v_sub_f32 v242, v242, v252\n"
      "v_sub_f32 v243, v243, v252\n"
      "v_sub_f32 v244, v244, v252\n"
      "v_sub_f32 v245, v245, v252\n"
      "v_sub_f32 v246, v246, v252\n"
      "v_sub_f32 v247, v247, v252\n"
      "v_exp_f32 v240, v240\n"
      "v_exp_f32 v241, v241\n"
      "v_exp_f32 v242, v242\n"
      "v_exp_f32 v243, v243\n"
      "v_exp_f32 v244, v244\n"
      "v_exp_f32 v245, v245\n"
      "v_exp_f32 v246, v246\n"
      "v_exp_f32 v247, v247\n"
      "s_nop 0\n"
      "v_add_f32 v218, v218, v240\n"
      "v_add_f32 v218, v218, v241\n"
      "v_add_f32 v218, v218, v242\n"
      "v_add_f32 v218, v218, v243\n"
      "v_add_f32 v218, v218, v244\n"
      "v_add_f32 v218, v218, v245\n"
      "v_add_f32 v218, v218, v246\n"
      "v_add_f32 v218, v218, v247\n"
      "v_cvt_pk_bf16_f32 v248, v240, v241\n"
      "v_cvt_pk_bf16_f32 v249, v242, v243\n"
      "v_cvt_pk_bf16_f32 v250, v244, v245\n"
      "v_cvt_pk_bf16_f32 v251, v246, v247\n"
      "s_mov_b64 s[48:49], %[pv]\n"
      "buffer_load_ushort v240, v213, s[48:51], s96 offen offset:0\n"
      "buffer_load_ushort v241, v213, s[48:51], s96 offen offset:256\n"
      "buffer_load_ushort v242, v213, s[48:51], s96 offen offset:512\n"
      "buffer_load_ushort v243, v213, s[48:51], s96 offen offset:768\n"
      "buffer_load_ushort v244, v213, s[48:51], s97 offen offset:0\n"
      "buffer_load_ushort v245, v213, s[48:51], s97 offen offset:256\n"
      "buffer_load_ushort v246, v213, s[48:51], s97 offen offset:512\n"
      "buffer_load_ushort v247, v213, s[48:51], s97 offen offset:768\n"
      "s_waitcnt vmcnt(0)\n"
      "v_lshl_or_b32 v236, v241, 16, v240\n"
      "v_lshl_or_b32 v237, v243, 16, v242\n"
      "v_lshl_or_b32 v238, v245, 16, v244\n"
      "v_lshl_or_b32 v239, v247, 16, v246\n"
      "s_nop 1\n"
      "v_mfma_f32_16x16x32_bf16 a[64:67], v[236:239], v[248:251], a[64:67]\n"
      "s_nop 3\n"
      "buffer_load_ushort v240, v213, s[48:51], s96 offen offset:32\n"
      "buffer_load_ushort v241, v213, s[48:51], s96 offen offset:288\n"
      "buffer_load_ushort v242, v213, s[48:51], s96 offen offset:544\n"
      "buffer_load_ushort v243, v213, s[48:51], s96 offen offset:800\n"
      "buffer_load_ushort v244, v213, s[48:51], s97 offen offset:32\n"
      "buffer_load_ushort v245, v213, s[48:51], s97 offen offset:288\n"
      "buffer_load_ushort v246, v213, s[48:51], s97 offen offset:544\n"
      "buffer_load_ushort v247, v213, s[48:51], s97 offen offset:800\n"
      "s_waitcnt vmcnt(0)\n"
      "v_lshl_or_b32 v236, v241, 16, v240\n"
      "v_lshl_or_b32 v237, v243, 16, v242\n"
      "v_lshl_or_b32 v238, v245, 16, v244\n"
      "v_lshl_or_b32 v239, v247, 16, v246\n"
      "s_nop 1\n"
      "v_mfma_f32_16x16x32_bf16 a[68:71], v[236:239], v[248:251], a[68:71]\n"
      "s_nop 3\n"
      "buffer_load_ushort v240, v213, s[48:51], s96 offen offset:64\n"
      "buffer_load_ushort v241, v213, s[48:51], s96 offen offset:320\n"
      "buffer_load_ushort v242, v213, s[48:51], s96 offen offset:576\n"
      "buffer_load_ushort v243, v213, s[48:51], s96 offen offset:832\n"
      "buffer_load_ushort v244, v213, s[48:51], s97 offen offset:64\n"
      "buffer_load_ushort v245, v213, s[48:51], s97 offen offset:320\n"
      "buffer_load_ushort v246, v213, s[48:51], s97 offen offset:576\n"
      "buffer_load_ushort v247, v213, s[48:51], s97 offen offset:832\n"
      "s_waitcnt vmcnt(0)\n"
      "v_lshl_or_b32 v236, v241, 16, v240\n"
      "v_lshl_or_b32 v237, v243, 16, v242\n"
      "v_lshl_or_b32 v238, v245, 16, v244\n"
      "v_lshl_or_b32 v239, v247, 16, v246\n"
      "s_nop 1\n"
      "v_mfma_f32_16x16x32_bf16 a[72:75], v[236:239], v[248:251], a[72:75]\n"
      "s_nop 3\n"
      "buffer_load_ushort v240, v213, s[48:51], s96 offen offset:96\n"
      "buffer_load_ushort v241, v213, s[48:51], s96 offen offset:352\n"
      "buffer_load_ushort v242, v213, s[48:51], s96 offen offset:608\n"
      "buffer_load_ushort v243, v213, s[48:51], s96 offen offset:864\n"
      "buffer_load_ushort v244, v213, s[48:51], s97 offen offset:96\n"
      "buffer_load_ushort v245, v213, s[48:51], s97 offen offset:352\n"
      "buffer_load_ushort v246, v213, s[48:51], s97 offen offset:608\n"
      "buffer_load_ushort v247, v213, s[48:51], s97 offen offset:864\n"
      "s_waitcnt vmcnt(0)\n"
      "v_lshl_or_b32 v236, v241, 16, v240\n"
      "v_lshl_or_b32 v237, v243, 16, v242\n"
      "v_lshl_or_b32 v238, v245, 16, v244\n"
      "v_lshl_or_b32 v239, v247, 16, v246\n"
      "s_nop 1\n"
      "v_mfma_f32_16x16x32_bf16 a[76:79], v[236:239], v[248:251], a[76:79]\n"
      "s_nop 3\n"
      "buffer_load_ushort v240, v213, s[48:51], s96 offen offset:128\n"
      "buffer_load_ushort v241, v213, s[48:51], s96 offen offset:384\n"
      "buffer_load_ushort v242, v213, s[48:51], s96 offen offset:640\n"
      "buffer_load_ushort v243, v213, s[48:51], s96 offen offset:896\n"
      "buffer_load_ushort v244, v213, s[48:51], s97 offen offset:128\n"
      "buffer_load_ushort v245, v213, s[48:51], s97 offen offset:384\n"
      "buffer_load_ushort v246, v213, s[48:51], s97 offen offset:640\n"
      "buffer_load_ushort v247, v213, s[48:51], s97 offen offset:896\n"
      "s_waitcnt vmcnt(0)\n"
      "v_lshl_or_b32 v236, v241, 16, v240\n"
      "v_lshl_or_b32 v237, v243, 16, v242\n"
      "v_lshl_or_b32 v238, v245, 16, v244\n"
      "v_lshl_or_b32 v239, v247, 16, v246\n"
      "s_nop 1\n"
      "v_mfma_f32_16x16x32_bf16 a[80:83], v[236:239], v[248:251], a[80:83]\n"
      "s_nop 3\n"
      "buffer_load_ushort v240, v213, s[48:51], s96 offen offset:160\n"
      "buffer_load_ushort v241, v213, s[48:51], s96 offen offset:416\n"
      "buffer_load_ushort v242, v213, s[48:51], s96 offen offset:672\n"
      "buffer_load_ushort v243, v213, s[48:51], s96 offen offset:928\n"
      "buffer_load_ushort v244, v213, s[48:51], s97 offen offset:160\n"
      "buffer_load_ushort v245, v213, s[48:51], s97 offen offset:416\n"
      "buffer_load_ushort v246, v213, s[48:51], s97 offen offset:672\n"
      "buffer_load_ushort v247, v213, s[48:51], s97 offen offset:928\n"
      "s_waitcnt vmcnt(0)\n"
      "v_lshl_or_b32 v236, v241, 16, v240\n"
      "v_lshl_or_b32 v237, v243, 16, v242\n"
      "v_lshl_or_b32 v238, v245, 16, v244\n"
      "v_lshl_or_b32 v239, v247, 16, v246\n"
      "s_nop 1\n"
      "v_mfma_f32_16x16x32_bf16 a[84:87], v[236:239], v[248:251], a[84:87]\n"
      "s_nop 3\n"
      "buffer_load_ushort v240, v213, s[48:51], s96 offen offset:192\n"
      "buffer_load_ushort v241, v213, s[48:51], s96 offen offset:448\n"
      "buffer_load_ushort v242, v213, s[48:51], s96 offen offset:704\n"
      "buffer_load_ushort v243, v213, s[48:51], s96 offen offset:960\n"
      "buffer_load_ushort v244, v213, s[48:51], s97 offen offset:192\n"
      "buffer_load_ushort v245, v213, s[48:51], s97 offen offset:448\n"
      "buffer_load_ushort v246, v213, s[48:51], s97 offen offset:704\n"
      "buffer_load_ushort v247, v213, s[48:51], s97 offen offset:960\n"
      "s_waitcnt vmcnt(0)\n"
      "v_lshl_or_b32 v236, v241, 16, v240\n"
      "v_lshl_or_b32 v237, v243, 16, v242\n"
      "v_lshl_or_b32 v238, v245, 16, v244\n"
      "v_lshl_or_b32 v239, v247, 16, v246\n"
      "s_nop 1\n"
      "v_mfma_f32_16x16x32_bf16 a[88:91], v[236:239], v[248:251], a[88:91]\n"
      "s_nop 3\n"
      "buffer_load_ushort v240, v213, s[48:51], s96 offen offset:224\n"
      "buffer_load_ushort v241, v213, s[48:51], s96 offen offset:480\n"
      "buffer_load_ushort v242, v213, s[48:51], s96 offen offset:736\n"
      "buffer_load_ushort v243, v213, s[48:51], s96 offen offset:992\n"
      "buffer_load_ushort v244, v213, s[48:51], s97 offen offset:224\n"
      "buffer_load_ushort v245, v213, s[48:51], s97 offen offset:480\n"
      "buffer_load_ushort v246, v213, s[48:51], s97 offen offset:736\n"
      "buffer_load_ushort v247, v213, s[48:51], s97 offen offset:992\n"
      "s_waitcnt vmcnt(0)\n"
      "v_lshl_or_b32 v236, v241, 16, v240\n"
      "v_lshl_or_b32 v237, v243, 16, v242\n"
      "v_lshl_or_b32 v238, v245, 16, v244\n"
      "v_lshl_or_b32 v239, v247, 16, v246\n"
      "s_nop 1\n"
      "v_mfma_f32_16x16x32_bf16 a[92:95], v[236:239], v[248:251], a[92:95]\n"
      "s_nop 3\n"
      "s_add_u32 s96, s96, 8192\n"
      "s_cmp_lt_u32 s96, 0x20000\n"
      "s_cbranch_scc1 SLB2_%=\n"
      "v_mov_b32 v253, v218\n"
      "s_nop 1\n"
      "v_permlane16_swap_b32 v218, v253\n"
      "v_add_f32 v218, v218, v253\n"
      "v_mov_b32 v253, v218\n"
      "s_nop 1\n"
      "v_permlane32_swap_b32 v218, v253\n"
      "v_add_f32 v218, v218, v253\n"
      "s_nop 3\n"
      "s_branch RET2_%=\n"
      "SLOW3_%=:\n"
      "v_mbcnt_lo_u32_b32 v253, -1, 0\n"
      "v_mbcnt_hi_u32_b32 v253, -1, v253\n"
      "v_and_b32 v212, 15, v253\n"
      "v_lshlrev_b32 v213, 1, v212\n"
      "v_lshlrev_b32 v212, 8, v212\n"
      "v_and_b32 v253, 48, v253\n"
      "v_add_u32_e64 v212, v212, v253\n"
      "v_lshl_add_u32 v213, v253, 6, v213\n"
      "v_mov_b32 v252, 0xff800000\n"
      "s_mov_b32 s96, 0\n"
      "SLA3_%=:\n"
      "s_mov_b64 s[48:49], %[pk]\n"
      "s_mov_b32 s50, 131072\n"
      "s_add_u32 s97, s96, 0\n"
      "buffer_load_dwordx4 v[236:239], v212, s[48:51], s97 offen offset:0\n"
      "s_waitcnt vmcnt(0)\n"
      "v_mfma_f32_16x16x32_bf16 v[240:243], v[236:239], v[48:51], 0\n"
      "s_nop 3\n"
      "buffer_load_dwordx4 v[236:239], v212, s[48:51], s97 offen offset:64\n"
      "s_waitcnt vmcnt(0)\n"
      "v_mfma_f32_16x16x32_bf16 v[240:243], v[236:239], v[52:55], v[240:243]\n"
      "s_nop 3\n"
      "buffer_load_dwordx4 v[236:239], v212, s[48:51], s97 offen offset:128\n"
      "s_waitcnt vmcnt(0)\n"
      "v_mfma_f32_16x16x32_bf16 v[240:243], v[236:239], v[56:59], v[240:243]\n"
      "s_nop 3\n"
      "buffer_load_dwordx4 v[236:239], v212, s[48:51], s97 offen offset:192\n"
      "s_waitcnt vmcnt(0)\n"
      "v_mfma_f32_16x16x32_bf16 v[240:243], v[236:239], v[60:63], v[240:243]\n"
      "s_nop 3\n"
      "s_add_u32 s97, s96, 4096\n"
      "buffer_load_dwordx4 v[236:239], v212, s[48:51], s97 offen offset:0\n"
      "s_waitcnt vmcnt(0)\n"
      "v_mfma_f32_16x16x32_bf16 v[244:247], v[236:239], v[48:51], 0\n"
      "s_nop 3\n"
      "buffer_load_dwordx4 v[236:239], v212, s[48:51], s97 offen offset:64\n"
      "s_waitcnt vmcnt(0)\n"
      "v_mfma_f32_16x16x32_bf16 v[244:247], v[236:239], v[52:55], v[244:247]\n"
      "s_nop 3\n"
      "buffer_load_dwordx4 v[236:239], v212, s[48:51], s97 offen offset:128\n"
      "s_waitcnt vmcnt(0)\n"
      "v_mfma_f32_16x16x32_bf16 v[244:247], v[236:239], v[56:59], v[244:247]\n"
      "s_nop 3\n"
      "buffer_load_dwordx4 v[236:239], v212, s[48:51], s97 offen offset:192\n"
      "s_waitcnt vmcnt(0)\n"
      "v_mfma_f32_16x16x32_bf16 v[244:247], v[236:239], v[60:63], v[244:247]\n"
      "s_nop 3\n"
      "s_nop 7\n"
      "v_max3_f32 v252, v252, v240, v241\n"
      "v_max3_f32 v252, v252, v242, v243\n"
      "v_max3_f32 v252, v252, v244, v245\n"
      "v_max3_f32 v252, v252, v246, v247\n"
      "s_add_u32 s96, s96, 8192\n"
      "s_cmp_lt_u32 s96, 0x20000\n"
      "s_cbranch_scc1 SLA3_%=\n"
      "v_mov_b32 v253, v252\n"
      "s_nop 1\n"
      "v_permlane16_swap_b32 v252, v253\n"
      "v_max_f32 v252, v252, v253\n"
      "v_mov_b32 v253, v252\n"
      "s_nop 1\n"
      "v_permlane32_swap_b32 v252, v253\n"
      "v_max_f32_e64 v252, v252, v253\n"
      "v_accvgpr_write_b32 a96, 0\n"
      "v_accvgpr_write_b32 a97, 0\n"
      "v_accvgpr_write_b32 a98, 0\n"
      "v_accvgpr_write_b32 a99, 0\n"
      "v_accvgpr_write_b32 a100, 0\n"
      "v_accvgpr_write_b32 a101, 0\n"
      "v_accvgpr_write_b32 a102, 0\n"
      "v_accvgpr_write_b32 a103, 0\n"
      "v_accvgpr_write_b32 a104, 0\n"
      "v_accvgpr_write_b32 a105, 0\n"
      "v_accvgpr_write_b32 a106, 0\n"
      "v_accvgpr_write_b32 a107, 0\n"
      "v_accvgpr_write_b32 a108, 0\n"
      "v_accvgpr_write_b32 a109, 0\n"
      "v_accvgpr_write_b32 a110, 0\n"
      "v_accvgpr_write_b32 a111, 0\n"
      "v_accvgpr_write_b32 a112, 0\n"
      "v_accvgpr_write_b32 a113, 0\n"
      "v_accvgpr_write_b32 a114, 0\n"
      "v_accvgpr_write_b32 a115, 0\n"
      "v_accvgpr_write_b32 a116, 0\n"
      "v_accvgpr_write_b32 a117, 0\n"
      "v_accvgpr_write_b32 a118, 0\n"
      "v_accvgpr_write_b32 a119, 0\n"
      "v_accvgpr_write_b32 a120, 0\n"
      "v_accvgpr_write_b32 a121, 0\n"
      "v_accvgpr_write_b32 a122, 0\n"
      "v_accvgpr_write_b32 a123, 0\n"
      "v_accvgpr_write_b32 a124, 0\n"
      "v_accvgpr_write_b32 a125, 0\n"
      "v_accvgpr_write_b32 a126, 0\n"
      "v_accvgpr_write_b32 a127, 0\n"
      "v_mov_b32_e64 v219, 0\n"
      "s_mov_b32 s96, 0\n"
      "SLB3_%=:\n"
      "s_mov_b64 s[48:49], %[pk]\n"
      "s_mov_b32 s50, 131072\n"
      "s_add_u32 s97, s96, 0\n"
      "buffer_load_dwordx4 v[236:239], v212, s[48:51], s97 offen offset:0\n"
      "s_waitcnt vmcnt(0)\n"
      "v_mfma_f32_16x16x32_bf16 v[240:243], v[236:239], v[48:51], 0\n"
      "s_nop 3\n"
      "buffer_load_dwordx4 v[236:239], v212, s[48:51], s97 offen offset:64\n"
      "s_waitcnt vmcnt(0)\n"
      "v_mfma_f32_16x16x32_bf16 v[240:243], v[236:239], v[52:55], v[240:243]\n"
      "s_nop 3\n"
      "buffer_load_dwordx4 v[236:239], v212, s[48:51], s97 offen offset:128\n"
      "s_waitcnt vmcnt(0)\n"
      "v_mfma_f32_16x16x32_bf16 v[240:243], v[236:239], v[56:59], v[240:243]\n"
      "s_nop 3\n"
      "buffer_load_dwordx4 v[236:239], v212, s[48:51], s97 offen offset:192\n"
      "s_waitcnt vmcnt(0)\n"
      "v_mfma_f32_16x16x32_bf16 v[240:243], v[236:239], v[60:63], v[240:243]\n"
      "s_nop 3\n"
      "s_add_u32 s97, s96, 4096\n"
      "buffer_load_dwordx4 v[236:239], v212, s[48:51], s97 offen offset:0\n"
      "s_waitcnt vmcnt(0)\n"
      "v_mfma_f32_16x16x32_bf16 v[244:247], v[236:239], v[48:51], 0\n"
      "s_nop 3\n"
      "buffer_load_dwordx4 v[236:239], v212, s[48:51], s97 offen offset:64\n"
      "s_waitcnt vmcnt(0)\n"
      "v_mfma_f32_16x16x32_bf16 v[244:247], v[236:239], v[52:55], v[244:247]\n"
      "s_nop 3\n"
      "buffer_load_dwordx4 v[236:239], v212, s[48:51], s97 offen offset:128\n"
      "s_waitcnt vmcnt(0)\n"
      "v_mfma_f32_16x16x32_bf16 v[244:247], v[236:239], v[56:59], v[244:247]\n"
      "s_nop 3\n"
      "buffer_load_dwordx4 v[236:239], v212, s[48:51], s97 offen offset:192\n"
      "s_waitcnt vmcnt(0)\n"
      "v_mfma_f32_16x16x32_bf16 v[244:247], v[236:239], v[60:63], v[244:247]\n"
      "s_nop 3\n"
      "s_nop 7\n"
      "v_sub_f32 v240, v240, v252\n"
      "v_sub_f32 v241, v241, v252\n"
      "v_sub_f32 v242, v242, v252\n"
      "v_sub_f32 v243, v243, v252\n"
      "v_sub_f32 v244, v244, v252\n"
      "v_sub_f32 v245, v245, v252\n"
      "v_sub_f32 v246, v246, v252\n"
      "v_sub_f32 v247, v247, v252\n"
      "v_exp_f32 v240, v240\n"
      "v_exp_f32 v241, v241\n"
      "v_exp_f32 v242, v242\n"
      "v_exp_f32 v243, v243\n"
      "v_exp_f32 v244, v244\n"
      "v_exp_f32 v245, v245\n"
      "v_exp_f32 v246, v246\n"
      "v_exp_f32 v247, v247\n"
      "s_nop 0\n"
      "v_add_f32 v219, v219, v240\n"
      "v_add_f32 v219, v219, v241\n"
      "v_add_f32 v219, v219, v242\n"
      "v_add_f32 v219, v219, v243\n"
      "v_add_f32 v219, v219, v244\n"
      "v_add_f32 v219, v219, v245\n"
      "v_add_f32 v219, v219, v246\n"
      "v_add_f32_e64 v219, v219, v247\n"
      "v_cvt_pk_bf16_f32 v248, v240, v241\n"
      "v_cvt_pk_bf16_f32 v249, v242, v243\n"
      "v_cvt_pk_bf16_f32 v250, v244, v245\n"
      "v_cvt_pk_bf16_f32 v251, v246, v247\n"
      "s_mov_b64 s[48:49], %[pv]\n"
      "buffer_load_ushort v240, v213, s[48:51], s96 offen offset:0\n"
      "buffer_load_ushort v241, v213, s[48:51], s96 offen offset:256\n"
      "buffer_load_ushort v242, v213, s[48:51], s96 offen offset:512\n"
      "buffer_load_ushort v243, v213, s[48:51], s96 offen offset:768\n"
      "buffer_load_ushort v244, v213, s[48:51], s97 offen offset:0\n"
      "buffer_load_ushort v245, v213, s[48:51], s97 offen offset:256\n"
      "buffer_load_ushort v246, v213, s[48:51], s97 offen offset:512\n"
      "buffer_load_ushort v247, v213, s[48:51], s97 offen offset:768\n"
      "s_waitcnt vmcnt(0)\n"
      "v_lshl_or_b32 v236, v241, 16, v240\n"
      "v_lshl_or_b32 v237, v243, 16, v242\n"
      "v_lshl_or_b32 v238, v245, 16, v244\n"
      "v_lshl_or_b32 v239, v247, 16, v246\n"
      "s_nop 1\n"
      "v_mfma_f32_16x16x32_bf16 a[96:99], v[236:239], v[248:251], a[96:99]\n"
      "s_nop 3\n"
      "buffer_load_ushort v240, v213, s[48:51], s96 offen offset:32\n"
      "buffer_load_ushort v241, v213, s[48:51], s96 offen offset:288\n"
      "buffer_load_ushort v242, v213, s[48:51], s96 offen offset:544\n"
      "buffer_load_ushort v243, v213, s[48:51], s96 offen offset:800\n"
      "buffer_load_ushort v244, v213, s[48:51], s97 offen offset:32\n"
      "buffer_load_ushort v245, v213, s[48:51], s97 offen offset:288\n"
      "buffer_load_ushort v246, v213, s[48:51], s97 offen offset:544\n"
      "buffer_load_ushort v247, v213, s[48:51], s97 offen offset:800\n"
      "s_waitcnt vmcnt(0)\n"
      "v_lshl_or_b32 v236, v241, 16, v240\n"
      "v_lshl_or_b32 v237, v243, 16, v242\n"
      "v_lshl_or_b32 v238, v245, 16, v244\n"
      "v_lshl_or_b32 v239, v247, 16, v246\n"
      "s_nop 1\n"
      "v_mfma_f32_16x16x32_bf16 a[100:103], v[236:239], v[248:251], a[100:103]\n"
      "s_nop 3\n"
      "buffer_load_ushort v240, v213, s[48:51], s96 offen offset:64\n"
      "buffer_load_ushort v241, v213, s[48:51], s96 offen offset:320\n"
      "buffer_load_ushort v242, v213, s[48:51], s96 offen offset:576\n"
      "buffer_load_ushort v243, v213, s[48:51], s96 offen offset:832\n"
      "buffer_load_ushort v244, v213, s[48:51], s97 offen offset:64\n"
      "buffer_load_ushort v245, v213, s[48:51], s97 offen offset:320\n"
      "buffer_load_ushort v246, v213, s[48:51], s97 offen offset:576\n"
      "buffer_load_ushort v247, v213, s[48:51], s97 offen offset:832\n"
      "s_waitcnt vmcnt(0)\n"
      "v_lshl_or_b32 v236, v241, 16, v240\n"
      "v_lshl_or_b32 v237, v243, 16, v242\n"
      "v_lshl_or_b32 v238, v245, 16, v244\n"
      "v_lshl_or_b32 v239, v247, 16, v246\n"
      "s_nop 1\n"
      "v_mfma_f32_16x16x32_bf16 a[104:107], v[236:239], v[248:251], a[104:107]\n"
      "s_nop 3\n"
      "buffer_load_ushort v240, v213, s[48:51], s96 offen offset:96\n"
      "buffer_load_ushort v241, v213, s[48:51], s96 offen offset:352\n"
      "buffer_load_ushort v242, v213, s[48:51], s96 offen offset:608\n"
      "buffer_load_ushort v243, v213, s[48:51], s96 offen offset:864\n"
      "buffer_load_ushort v244, v213, s[48:51], s97 offen offset:96\n"
      "buffer_load_ushort v245, v213, s[48:51], s97 offen offset:352\n"
      "buffer_load_ushort v246, v213, s[48:51], s97 offen offset:608\n"
      "buffer_load_ushort v247, v213, s[48:51], s97 offen offset:864\n"
      "s_waitcnt vmcnt(0)\n"
      "v_lshl_or_b32 v236, v241, 16, v240\n"
      "v_lshl_or_b32 v237, v243, 16, v242\n"
      "v_lshl_or_b32 v238, v245, 16, v244\n"
      "v_lshl_or_b32 v239, v247, 16, v246\n"
      "s_nop 1\n"
      "v_mfma_f32_16x16x32_bf16 a[108:111], v[236:239], v[248:251], a[108:111]\n"
      "s_nop 3\n"
      "buffer_load_ushort v240, v213, s[48:51], s96 offen offset:128\n"
      "buffer_load_ushort v241, v213, s[48:51], s96 offen offset:384\n"
      "buffer_load_ushort v242, v213, s[48:51], s96 offen offset:640\n"
      "buffer_load_ushort v243, v213, s[48:51], s96 offen offset:896\n"
      "buffer_load_ushort v244, v213, s[48:51], s97 offen offset:128\n"
      "buffer_load_ushort v245, v213, s[48:51], s97 offen offset:384\n"
      "buffer_load_ushort v246, v213, s[48:51], s97 offen offset:640\n"
      "buffer_load_ushort v247, v213, s[48:51], s97 offen offset:896\n"
      "s_waitcnt vmcnt(0)\n"
      "v_lshl_or_b32 v236, v241, 16, v240\n"
      "v_lshl_or_b32 v237, v243, 16, v242\n"
      "v_lshl_or_b32 v238, v245, 16, v244\n"
      "v_lshl_or_b32 v239, v247, 16, v246\n"
      "s_nop 1\n"
      "v_mfma_f32_16x16x32_bf16 a[112:115], v[236:239], v[248:251], a[112:115]\n"
      "s_nop 3\n"
      "buffer_load_ushort v240, v213, s[48:51], s96 offen offset:160\n"
      "buffer_load_ushort v241, v213, s[48:51], s96 offen offset:416\n"
      "buffer_load_ushort v242, v213, s[48:51], s96 offen offset:672\n"
      "buffer_load_ushort v243, v213, s[48:51], s96 offen offset:928\n"
      "buffer_load_ushort v244, v213, s[48:51], s97 offen offset:160\n"
      "buffer_load_ushort v245, v213, s[48:51], s97 offen offset:416\n"
      "buffer_load_ushort v246, v213, s[48:51], s97 offen offset:672\n"
      "buffer_load_ushort v247, v213, s[48:51], s97 offen offset:928\n"
      "s_waitcnt vmcnt(0)\n"
      "v_lshl_or_b32 v236, v241, 16, v240\n"
      "v_lshl_or_b32 v237, v243, 16, v242\n"
      "v_lshl_or_b32 v238, v245, 16, v244\n"
      "v_lshl_or_b32 v239, v247, 16, v246\n"
      "s_nop 1\n"
      "v_mfma_f32_16x16x32_bf16 a[116:119], v[236:239], v[248:251], a[116:119]\n"
      "s_nop 3\n"
      "buffer_load_ushort v240, v213, s[48:51], s96 offen offset:192\n"
      "buffer_load_ushort v241, v213, s[48:51], s96 offen offset:448\n"
      "buffer_load_ushort v242, v213, s[48:51], s96 offen offset:704\n"
      "buffer_load_ushort v243, v213, s[48:51], s96 offen offset:960\n"
      "buffer_load_ushort v244, v213, s[48:51], s97 offen offset:192\n"
      "buffer_load_ushort v245, v213, s[48:51], s97 offen offset:448\n"
      "buffer_load_ushort v246, v213, s[48:51], s97 offen offset:704\n"
      "buffer_load_ushort v247, v213, s[48:51], s97 offen offset:960\n"
      "s_waitcnt vmcnt(0)\n"
      "v_lshl_or_b32 v236, v241, 16, v240\n"
      "v_lshl_or_b32 v237, v243, 16, v242\n"
      "v_lshl_or_b32 v238, v245, 16, v244\n"
      "v_lshl_or_b32 v239, v247, 16, v246\n"
      "s_nop 1\n"
      "v_mfma_f32_16x16x32_bf16 a[120:123], v[236:239], v[248:251], a[120:123]\n"
      "s_nop 3\n"
      "buffer_load_ushort v240, v213, s[48:51], s96 offen offset:224\n"
      "buffer_load_ushort v241, v213, s[48:51], s96 offen offset:480\n"
      "buffer_load_ushort v242, v213, s[48:51], s96 offen offset:736\n"
      "buffer_load_ushort v243, v213, s[48:51], s96 offen offset:992\n"
      "buffer_load_ushort v244, v213, s[48:51], s97 offen offset:224\n"
      "buffer_load_ushort v245, v213, s[48:51], s97 offen offset:480\n"
      "buffer_load_ushort v246, v213, s[48:51], s97 offen offset:736\n"
      "buffer_load_ushort v247, v213, s[48:51], s97 offen offset:992\n"
      "s_waitcnt vmcnt(0)\n"
      "v_lshl_or_b32 v236, v241, 16, v240\n"
      "v_lshl_or_b32 v237, v243, 16, v242\n"
      "v_lshl_or_b32 v238, v245, 16, v244\n"
      "v_lshl_or_b32 v239, v247, 16, v246\n"
      "s_nop 1\n"
      "v_mfma_f32_16x16x32_bf16 a[124:127], v[236:239], v[248:251], a[124:127]\n"
      "s_nop 3\n"
      "s_add_u32 s96, s96, 8192\n"
      "s_cmp_lt_u32 s96, 0x20000\n"
      "s_cbranch_scc1 SLB3_%=\n"
      "v_mov_b32 v253, v219\n"
      "s_nop 1\n"
      "v_permlane16_swap_b32 v219, v253\n"
      "v_add_f32 v219, v219, v253\n"
      "v_mov_b32 v253, v219\n"
      "s_nop 1\n"
      "v_permlane32_swap_b32 v219, v253\n"
      "v_add_f32 v219, v219, v253\n"
      "s_nop 3\n"
      "s_branch RET3_%=\n"
      "SLOW4_%=:\n"
      "v_mbcnt_lo_u32_b32 v253, -1, 0\n"
      "v_mbcnt_hi_u32_b32 v253, -1, v253\n"
      "v_and_b32 v212, 15, v253\n"
      "v_lshlrev_b32 v213, 1, v212\n"
      "v_lshlrev_b32 v212, 8, v212\n"
      "v_and_b32 v253, 48, v253\n"
      "v_add_u32_e64 v212, v212, v253\n"
      "v_lshl_add_u32 v213, v253, 6, v213\n"
      "v_mov_b32 v252, 0xff800000\n"
      "s_mov_b32 s96, 0\n"
      "SLA4_%=:\n"
      "s_mov_b64 s[48:49], %[pk]\n"
      "s_mov_b32 s50, 131072\n"
      "s_add_u32 s97, s96, 0\n"
      "buffer_load_dwordx4 v[236:239], v212, s[48:51], s97 offen offset:0\n"
      "s_waitcnt vmcnt(0)\n"
      "v_mfma_f32_16x16x32_bf16 v[240:243], v[236:239], v[64:67], 0\n"
      "s_nop 3\n"
      "buffer_load_dwordx4 v[236:239], v212, s[48:51], s97 offen offset:64\n"
      "s_waitcnt vmcnt(0)\n"
      "v_mfma_f32_16x16x32_bf16 v[240:243], v[236:239], v[68:71], v[240:243]\n"
      "s_nop 3\n"
      "buffer_load_dwordx4 v[236:239], v212, s[48:51], s97 offen offset:128\n"
      "s_waitcnt vmcnt(0)\n"
      "v_mfma_f32_16x16x32_bf16 v[240:243], v[236:239], v[72:75], v[240:243]\n"
      "s_nop 3\n"
      "buffer_load_dwordx4 v[236:239], v212, s[48:51], s97 offen offset:192\n"
      "s_waitcnt vmcnt(0)\n"
      "v_mfma_f32_16x16x32_bf16 v[240:243], v[236:239], v[76:79], v[240:243]\n"
      "s_nop 3\n"
      "s_add_u32 s97, s96, 4096\n"
      "buffer_load_dwordx4 v[236:239], v212, s[48:51], s97 offen offset:0\n"
      "s_waitcnt vmcnt(0)\n"
      "v_mfma_f32_16x16x32_bf16 v[244:247], v[236:239], v[64:67], 0\n"
      "s_nop 3\n"
      "buffer_load_dwordx4 v[236:239], v212, s[48:51], s97 offen offset:64\n"
      "s_waitcnt vmcnt(0)\n"
      "v_mfma_f32_16x16x32_bf16 v[244:247], v[236:239], v[68:71], v[244:247]\n"
      "s_nop 3\n"
      "buffer_load_dwordx4 v[236:239], v212, s[48:51], s97 offen offset:128\n"
      "s_waitcnt vmcnt(0)\n"
      "v_mfma_f32_16x16x32_bf16 v[244:247], v[236:239], v[72:75], v[244:247]\n"
      "s_nop 3\n"
      "buffer_load_dwordx4 v[236:239], v212, s[48:51], s97 offen offset:192\n"
      "s_waitcnt vmcnt(0)\n"
      "v_mfma_f32_16x16x32_bf16 v[244:247], v[236:239], v[76:79], v[244:247]\n"
      "s_nop 3\n"
      "s_nop 7\n"
      "v_max3_f32 v252, v252, v240, v241\n"
      "v_max3_f32 v252, v252, v242, v243\n"
      "v_max3_f32 v252, v252, v244, v245\n"
      "v_max3_f32 v252, v252, v246, v247\n"
      "s_add_u32 s96, s96, 8192\n"
      "s_cmp_lt_u32 s96, 0x20000\n"
      "s_cbranch_scc1 SLA4_%=\n"
      "v_mov_b32 v253, v252\n"
      "s_nop 1\n"
      "v_permlane16_swap_b32 v252, v253\n"
      "v_max_f32 v252, v252, v253\n"
      "v_mov_b32 v253, v252\n"
      "s_nop 1\n"
      "v_permlane32_swap_b32 v252, v253\n"
      "v_max_f32_e64 v252, v252, v253\n"
      "v_accvgpr_write_b32 a128, 0\n"
      "v_accvgpr_write_b32 a129, 0\n"
      "v_accvgpr_write_b32 a130, 0\n"
      "v_accvgpr_write_b32 a131, 0\n"
      "v_accvgpr_write_b32 a132, 0\n"
      "v_accvgpr_write_b32 a133, 0\n"
      "v_accvgpr_write_b32 a134, 0\n"
      "v_accvgpr_write_b32 a135, 0\n"
      "v_accvgpr_write_b32 a136, 0\n"
      "v_accvgpr_write_b32 a137, 0\n"
      "v_accvgpr_write_b32 a138, 0\n"
      "v_accvgpr_write_b32 a139, 0\n"
      "v_accvgpr_write_b32 a140, 0\n"
      "v_accvgpr_write_b32 a141, 0\n"
      "v_accvgpr_write_b32 a142, 0\n"
      "v_accvgpr_write_b32 a143, 0\n"
      "v_accvgpr_write_b32 a144, 0\n"
      "v_accvgpr_write_b32 a145, 0\n"
      "v_accvgpr_write_b32 a146, 0\n"
      "v_accvgpr_write_b32 a147, 0\n"
      "v_accvgpr_write_b32 a148, 0\n"
      "v_accvgpr_write_b32 a149, 0\n"
      "v_accvgpr_write_b32 a150, 0\n"
      "v_accvgpr_write_b32 a151, 0\n"
      "v_accvgpr_write_b32 a152, 0\n"
      "v_accvgpr_write_b32 a153, 0\n"
      "v_accvgpr_write_b32 a154, 0\n"
      "v_accvgpr_write_b32 a155, 0\n"
      "v_accvgpr_write_b32 a156, 0\n"
      "v_accvgpr_write_b32 a157, 0\n"
      "v_accvgpr_write_b32 a158, 0\n"
      "v_accvgpr_write_b32 a159, 0\n"
      "v_mov_b32_e64 v220, 0\n"
      "s_mov_b32 s96, 0\n"
      "SLB4_%=:\n"
      "s_mov_b64 s[48:49], %[pk]\n"
      "s_mov_b32 s50, 131072\n"
      "s_add_u32 s97, s96, 0\n"
      "buffer_load_dwordx4 v[236:239], v212, s[48:51], s97 offen offset:0\n"
      "s_waitcnt vmcnt(0)\n"
      "v_mfma_f32_16x16x32_bf16 v[240:243], v[236:239], v[64:67], 0\n"
      "s_nop 3\n"
      "buffer_load_dwordx4 v[236:239], v212, s[48:51], s97 offen offset:64\n"
      "s_waitcnt vmcnt(0)\n"
      "v_mfma_f32_16x16x32_bf16 v[240:243], v[236:239], v[68:71], v[240:243]\n"
      "s_nop 3\n"
      "buffer_load_dwordx4 v[236:239], v212, s[48:51], s97 offen offset:128\n"
      "s_waitcnt vmcnt(0)\n"
      "v_mfma_f32_16x16x32_bf16 v[240:243], v[236:239], v[72:75], v[240:243]\n"
      "s_nop 3\n"
      "buffer_load_dwordx4 v[236:239], v212, s[48:51], s97 offen offset:192\n"
      "s_waitcnt vmcnt(0)\n"
      "v_mfma_f32_16x16x32_bf16 v[240:243], v[236:239], v[76:79], v[240:243]\n"
      "s_nop 3\n"
      "s_add_u32 s97, s96, 4096\n"
      "buffer_load_dwordx4 v[236:239], v212, s[48:51], s97 offen offset:0\n"
      "s_waitcnt vmcnt(0)\n"
      "v_mfma_f32_16x16x32_bf16 v[244:247], v[236:239], v[64:67], 0\n"
      "s_nop 3\n"
      "buffer_load_dwordx4 v[236:239], v212, s[48:51], s97 offen offset:64\n"
      "s_waitcnt vmcnt(0)\n"
      "v_mfma_f32_16x16x32_bf16 v[244:247], v[236:239], v[68:71], v[244:247]\n"
      "s_nop 3\n"
      "buffer_load_dwordx4 v[236:239], v212, s[48:51], s97 offen offset:128\n"
      "s_waitcnt vmcnt(0)\n"
      "v_mfma_f32_16x16x32_bf16 v[244:247], v[236:239], v[72:75], v[244:247]\n"
      "s_nop 3\n"
      "buffer_load_dwordx4 v[236:239], v212, s[48:51], s97 offen offset:192\n"
      "s_waitcnt vmcnt(0)\n"
      "v_mfma_f32_16x16x32_bf16 v[244:247], v[236:239], v[76:79], v[244:247]\n"
      "s_nop 3\n"
      "s_nop 7\n"
      "v_sub_f32 v240, v240, v252\n"
      "v_sub_f32 v241, v241, v252\n"
      "v_sub_f32 v242, v242, v252\n"
      "v_sub_f32 v243, v243, v252\n"
      "v_sub_f32 v244, v244, v252\n"
      "v_sub_f32 v245, v245, v252\n"
      "v_sub_f32 v246, v246, v252\n"
      "v_sub_f32 v247, v247, v252\n"
      "v_exp_f32 v240, v240\n"
      "v_exp_f32 v241, v241\n"
      "v_exp_f32 v242, v242\n"
      "v_exp_f32 v243, v243\n"
      "v_exp_f32 v244, v244\n"
      "v_exp_f32 v245, v245\n"
      "v_exp_f32 v246, v246\n"
      "v_exp_f32 v247, v247\n"
      "s_nop 0\n"
      "v_add_f32 v220, v220, v240\n"
      "v_add_f32 v220, v220, v241\n"
      "v_add_f32 v220, v220, v242\n"
      "v_add_f32 v220, v220, v243\n"
      "v_add_f32 v220, v220, v244\n"
      "v_add_f32 v220, v220, v245\n"
      "v_add_f32 v220, v220, v246\n"
      "v_add_f32_e64 v220, v220, v247\n"
      "v_cvt_pk_bf16_f32 v248, v240, v241\n"
      "v_cvt_pk_bf16_f32 v249, v242, v243\n"
      "v_cvt_pk_bf16_f32 v250, v244, v245\n"
      "v_cvt_pk_bf16_f32 v251, v246, v247\n"
      "s_mov_b64 s[48:49], %[pv]\n"
      "buffer_load_ushort v240, v213, s[48:51], s96 offen offset:0\n"
      "buffer_load_ushort v241, v213, s[48:51], s96 offen offset:256\n"
      "buffer_load_ushort v242, v213, s[48:51], s96 offen offset:512\n"
      "buffer_load_ushort v243, v213, s[48:51], s96 offen offset:768\n"
      "buffer_load_ushort v244, v213, s[48:51], s97 offen offset:0\n"
      "buffer_load_ushort v245, v213, s[48:51], s97 offen offset:256\n"
      "buffer_load_ushort v246, v213, s[48:51], s97 offen offset:512\n"
      "buffer_load_ushort v247, v213, s[48:51], s97 offen offset:768\n"
      "s_waitcnt vmcnt(0)\n"
      "v_lshl_or_b32 v236, v241, 16, v240\n"
      "v_lshl_or_b32 v237, v243, 16, v242\n"
      "v_lshl_or_b32 v238, v245, 16, v244\n"
      "v_lshl_or_b32 v239, v247, 16, v246\n"
      "s_nop 1\n"
      "v_mfma_f32_16x16x32_bf16 a[128:131], v[236:239], v[248:251], a[128:131]\n"
      "s_nop 3\n"
      "buffer_load_ushort v240, v213, s[48:51], s96 offen offset:32\n"
      "buffer_load_ushort v241, v213, s[48:51], s96 offen offset:288\n"
      "buffer_load_ushort v242, v213, s[48:51], s96 offen offset:544\n"
      "buffer_load_ushort v243, v213, s[48:51], s96 offen offset:800\n"
      "buffer_load_ushort v244, v213, s[48:51], s97 offen offset:32\n"
      "buffer_load_ushort v245, v213, s[48:51], s97 offen offset:288\n"
      "buffer_load_ushort v246, v213, s[48:51], s97 offen offset:544\n"
      "buffer_load_ushort v247, v213, s[48:51], s97 offen offset:800\n"
      "s_waitcnt vmcnt(0)\n"
      "v_lshl_or_b32 v236, v241, 16, v240\n"
      "v_lshl_or_b32 v237, v243, 16, v242\n"
      "v_lshl_or_b32 v238, v245, 16, v244\n"
      "v_lshl_or_b32 v239, v247, 16, v246\n"
      "s_nop 1\n"
      "v_mfma_f32_16x16x32_bf16 a[132:135], v[236:239], v[248:251], a[132:135]\n"
      "s_nop 3\n"
      "buffer_load_ushort v240, v213, s[48:51], s96 offen offset:64\n"
      "buffer_load_ushort v241, v213, s[48:51], s96 offen offset:320\n"
      "buffer_load_ushort v242, v213, s[48:51], s96 offen offset:576\n"
      "buffer_load_ushort v243, v213, s[48:51], s96 offen offset:832\n"
      "buffer_load_ushort v244, v213, s[48:51], s97 offen offset:64\n"
      "buffer_load_ushort v245, v213, s[48:51], s97 offen offset:320\n"
      "buffer_load_ushort v246, v213, s[48:51], s97 offen offset:576\n"
      "buffer_load_ushort v247, v213, s[48:51], s97 offen offset:832\n"
      "s_waitcnt vmcnt(0)\n"
      "v_lshl_or_b32 v236, v241, 16, v240\n"
      "v_lshl_or_b32 v237, v243, 16, v242\n"
      "v_lshl_or_b32 v238, v245, 16, v244\n"
      "v_lshl_or_b32 v239, v247, 16, v246\n"
      "s_nop 1\n"
      "v_mfma_f32_16x16x32_bf16 a[136:139], v[236:239], v[248:251], a[136:139]\n"
      "s_nop 3\n"
      "buffer_load_ushort v240, v213, s[48:51], s96 offen offset:96\n"
      "buffer_load_ushort v241, v213, s[48:51], s96 offen offset:352\n"
      "buffer_load_ushort v242, v213, s[48:51], s96 offen offset:608\n"
      "buffer_load_ushort v243, v213, s[48:51], s96 offen offset:864\n"
      "buffer_load_ushort v244, v213, s[48:51], s97 offen offset:96\n"
      "buffer_load_ushort v245, v213, s[48:51], s97 offen offset:352\n"
      "buffer_load_ushort v246, v213, s[48:51], s97 offen offset:608\n"
      "buffer_load_ushort v247, v213, s[48:51], s97 offen offset:864\n"
      "s_waitcnt vmcnt(0)\n"
      "v_lshl_or_b32 v236, v241, 16, v240\n"
      "v_lshl_or_b32 v237, v243, 16, v242\n"
      "v_lshl_or_b32 v238, v245, 16, v244\n"
      "v_lshl_or_b32 v239, v247, 16, v246\n"
      "s_nop 1\n"
      "v_mfma_f32_16x16x32_bf16 a[140:143], v[236:239], v[248:251], a[140:143]\n"
      "s_nop 3\n"
      "buffer_load_ushort v240, v213, s[48:51], s96 offen offset:128\n"
      "buffer_load_ushort v241, v213, s[48:51], s96 offen offset:384\n"
      "buffer_load_ushort v242, v213, s[48:51], s96 offen offset:640\n"
      "buffer_load_ushort v243, v213, s[48:51], s96 offen offset:896\n"
      "buffer_load_ushort v244, v213, s[48:51], s97 offen offset:128\n"
      "buffer_load_ushort v245, v213, s[48:51], s97 offen offset:384\n"
      "buffer_load_ushort v246, v213, s[48:51], s97 offen offset:640\n"
      "buffer_load_ushort v247, v213, s[48:51], s97 offen offset:896\n"
      "s_waitcnt vmcnt(0)\n"
      "v_lshl_or_b32 v236, v241, 16, v240\n"
      "v_lshl_or_b32 v237, v243, 16, v242\n"
      "v_lshl_or_b32 v238, v245, 16, v244\n"
      "v_lshl_or_b32 v239, v247, 16, v246\n"
      "s_nop 1\n"
      "v_mfma_f32_16x16x32_bf16 a[144:147], v[236:239], v[248:251], a[144:147]\n"
      "s_nop 3\n"
      "buffer_load_ushort v240, v213, s[48:51], s96 offen offset:160\n"
      "buffer_load_ushort v241, v213, s[48:51], s96 offen offset:416\n"
      "buffer_load_ushort v242, v213, s[48:51], s96 offen offset:672\n"
      "buffer_load_ushort v243, v213, s[48:51], s96 offen offset:928\n"
      "buffer_load_ushort v244, v213, s[48:51], s97 offen offset:160\n"
      "buffer_load_ushort v245, v213, s[48:51], s97 offen offset:416\n"
      "buffer_load_ushort v246, v213, s[48:51], s97 offen offset:672\n"
      "buffer_load_ushort v247, v213, s[48:51], s97 offen offset:928\n"
      "s_waitcnt vmcnt(0)\n"
      "v_lshl_or_b32 v236, v241, 16, v240\n"
      "v_lshl_or_b32 v237, v243, 16, v242\n"
      "v_lshl_or_b32 v238, v245, 16, v244\n"
      "v_lshl_or_b32 v239, v247, 16, v246\n"
      "s_nop 1\n"
      "v_mfma_f32_16x16x32_bf16 a[148:151], v[236:239], v[248:251], a[148:151]\n"
      "s_nop 3\n"
      "buffer_load_ushort v240, v213, s[48:51], s96 offen offset:192\n"
      "buffer_load_ushort v241, v213, s[48:51], s96 offen offset:448\n"
      "buffer_load_ushort v242, v213, s[48:51], s96 offen offset:704\n"
      "buffer_load_ushort v243, v213, s[48:51], s96 offen offset:960\n"
      "buffer_load_ushort v244, v213, s[48:51], s97 offen offset:192\n"
      "buffer_load_ushort v245, v213, s[48:51], s97 offen offset:448\n"
      "buffer_load_ushort v246, v213, s[48:51], s97 offen offset:704\n"
      "buffer_load_ushort v247, v213, s[48:51], s97 offen offset:960\n"
      "s_waitcnt vmcnt(0)\n"
      "v_lshl_or_b32 v236, v241, 16, v240\n"
      "v_lshl_or_b32 v237, v243, 16, v242\n"
      "v_lshl_or_b32 v238, v245, 16, v244\n"
      "v_lshl_or_b32 v239, v247, 16, v246\n"
      "s_nop 1\n"
      "v_mfma_f32_16x16x32_bf16 a[152:155], v[236:239], v[248:251], a[152:155]\n"
      "s_nop 3\n"
      "buffer_load_ushort v240, v213, s[48:51], s96 offen offset:224\n"
      "buffer_load_ushort v241, v213, s[48:51], s96 offen offset:480\n"
      "buffer_load_ushort v242, v213, s[48:51], s96 offen offset:736\n"
      "buffer_load_ushort v243, v213, s[48:51], s96 offen offset:992\n"
      "buffer_load_ushort v244, v213, s[48:51], s97 offen offset:224\n"
      "buffer_load_ushort v245, v213, s[48:51], s97 offen offset:480\n"
      "buffer_load_ushort v246, v213, s[48:51], s97 offen offset:736\n"
      "buffer_load_ushort v247, v213, s[48:51], s97 offen offset:992\n"
      "s_waitcnt vmcnt(0)\n"
      "v_lshl_or_b32 v236, v241, 16, v240\n"
      "v_lshl_or_b32 v237, v243, 16, v242\n"
      "v_lshl_or_b32 v238, v245, 16, v244\n"
      "v_lshl_or_b32 v239, v247, 16, v246\n"
      "s_nop 1\n"
      "v_mfma_f32_16x16x32_bf16 a[156:159], v[236:239], v[248:251], a[156:159]\n"
      "s_nop 3\n"
      "s_add_u32 s96, s96, 8192\n"
      "s_cmp_lt_u32 s96, 0x20000\n"
      "s_cbranch_scc1 SLB4_%=\n"
      "v_mov_b32 v253, v220\n"
      "s_nop 1\n"
      "v_permlane16_swap_b32 v220, v253\n"
      "v_add_f32 v220, v220, v253\n"
      "v_mov_b32 v253, v220\n"
      "s_nop 1\n"
      "v_permlane32_swap_b32 v220, v253\n"
      "v_add_f32 v220, v220, v253\n"
      "s_nop 3\n"
      "s_branch RET4_%=\n"
      "SLOW5_%=:\n"
      "v_mbcnt_lo_u32_b32 v253, -1, 0\n"
      "v_mbcnt_hi_u32_b32 v253, -1, v253\n"
      "v_and_b32 v212, 15, v253\n"
      "v_lshlrev_b32 v213, 1, v212\n"
      "v_lshlrev_b32 v212, 8, v212\n"
      "v_and_b32 v253, 48, v253\n"
      "v_add_u32_e64 v212, v212, v253\n"
      "v_lshl_add_u32 v213, v253, 6, v213\n"
      "v_mov_b32 v252, 0xff800000\n"
      "s_mov_b32 s96, 0\n"
      "SLA5_%=:\n"
      "s_mov_b64 s[48:49], %[pk]\n"
      "s_mov_b32 s50, 131072\n"
      "s_add_u32 s97, s96, 0\n"
      "buffer_load_dwordx4 v[236:239], v212, s[48:51], s97 offen offset:0\n"
      "s_waitcnt vmcnt(0)\n"
      "v_mfma_f32_16x16x32_bf16 v[240:243], v[236:239], v[80:83], 0\n"
      "s_nop 3\n"
      "buffer_load_dwordx4 v[236:239], v212, s[48:51], s97 offen offset:64\n"
      "s_waitcnt vmcnt(0)\n"
      "v_mfma_f32_16x16x32_bf16 v[240:243], v[236:239], v[84:87], v[240:243]\n"
      "s_nop 3\n"
      "buffer_load_dwordx4 v[236:239], v212, s[48:51], s97 offen offset:128\n"
      "s_waitcnt vmcnt(0)\n"
      "v_mfma_f32_16x16x32_bf16 v[240:243], v[236:239], v[88:91], v[240:243]\n"
      "s_nop 3\n"
      "buffer_load_dwordx4 v[236:239], v212, s[48:51], s97 offen offset:192\n"
      "s_waitcnt vmcnt(0)\n"
      "v_mfma_f32_16x16x32_bf16 v[240:243], v[236:239], v[92:95], v[240:243]\n"
      "s_nop 3\n"
      "s_add_u32 s97, s96, 4096\n"
      "buffer_load_dwordx4 v[236:239], v212, s[48:51], s97 offen offset:0\n"
      "s_waitcnt vmcnt(0)\n"
      "v_mfma_f32_16x16x32_bf16 v[244:247], v[236:239], v[80:83], 0\n"
      "s_nop 3\n"
      "buffer_load_dwordx4 v[236:239], v212, s[48:51], s97 offen offset:64\n"
      "s_waitcnt vmcnt(0)\n"
      "v_mfma_f32_16x16x32_bf16 v[244:247], v[236:239], v[84:87], v[244:247]\n"
      "s_nop 3\n"
      "buffer_load_dwordx4 v[236:239], v212, s[48:51], s97 offen offset:128\n"
      "s_waitcnt vmcnt(0)\n"
      "v_mfma_f32_16x16x32_bf16 v[244:247], v[236:239], v[88:91], v[244:247]\n"
      "s_nop 3\n"
      "buffer_load_dwordx4 v[236:239], v212, s[48:51], s97 offen offset:192\n"
      "s_waitcnt vmcnt(0)\n"
      "v_mfma_f32_16x16x32_bf16 v[244:247], v[236:239], v[92:95], v[244:247]\n"
      "s_nop 3\n"
      "s_nop 7\n"
      "v_max3_f32 v252, v252, v240, v241\n"
      "v_max3_f32 v252, v252, v242, v243\n"
      "v_max3_f32 v252, v252, v244, v245\n"
      "v_max3_f32 v252, v252, v246, v247\n"
      "s_add_u32 s96, s96, 8192\n"
      "s_cmp_lt_u32 s96, 0x20000\n"
      "s_cbranch_scc1 SLA5_%=\n"
      "v_mov_b32 v253, v252\n"
      "s_nop 1\n"
      "v_permlane16_swap_b32 v252, v253\n"
      "v_max_f32 v252, v252, v253\n"
      "v_mov_b32 v253, v252\n"
      "s_nop 1\n"
      "v_permlane32_swap_b32 v252, v253\n"
      "v_max_f32 v252, v252, v253\n"
      "v_mov_b32 v180, 0\n"
      "v_mov_b32 v181, 0\n"
      "v_mov_b32 v182, 0\n"
      "v_mov_b32 v183, 0\n"
      "v_mov_b32 v184, 0\n"
      "v_mov_b32 v185, 0\n"
      "v_mov_b32 v186, 0\n"
      "v_mov_b32 v187, 0\n"
      "v_mov_b32 v188, 0\n"
      "v_mov_b32 v189, 0\n"
      "v_mov_b32 v190, 0\n"
      "v_mov_b32 v191, 0\n"
      "v_mov_b32 v192, 0\n"
      "v_mov_b32 v193, 0\n"
      "v_mov_b32 v194, 0\n"
      "v_mov_b32 v195, 0\n"
      "v_mov_b32 v196, 0\n"
      "v_mov_b32 v197, 0\n"
      "v_mov_b32 v198, 0\n"
      "v_mov_b32 v199, 0\n"
      "v_mov_b32 v200, 0\n"
      "v_mov_b32 v201, 0\n"
      "v_mov_b32 v202, 0\n"
      "v_mov_b32 v203, 0\n"
      "v_mov_b32 v204, 0\n"
      "v_mov_b32 v205, 0\n"
      "v_mov_b32 v206, 0\n"
      "v_mov_b32 v207, 0\n"
      "v_mov_b32 v208, 0\n"
      "v_mov_b32 v209, 0\n"
      "v_mov_b32 v210, 0\n"
      "v_mov_b32 v211, 0\n"
      "v_mov_b32 v221, 0\n"
      "s_mov_b32 s96, 0\n"
      "SLB5_%=:\n"
      "s_mov_b64 s[48:49], %[pk]\n"
      "s_mov_b32 s50, 131072\n"
      "s_add_u32 s97, s96, 0\n"
      "buffer_load_dwordx4 v[236:239], v212, s[48:51], s97 offen offset:0\n"
      "s_waitcnt vmcnt(0)\n"
      "v_mfma_f32_16x16x32_bf16 v[240:243], v[236:239], v[80:83], 0\n"
      "s_nop 3\n"
      "buffer_load_dwordx4 v[236:239], v212, s[48:51], s97 offen offset:64\n"
      "s_waitcnt vmcnt(0)\n"
      "v_mfma_f32_16x16x32_bf16 v[240:243], v[236:239], v[84:87], v[240:243]\n"
      "s_nop 3\n"
      "buffer_load_dwordx4 v[236:239], v212, s[48:51], s97 offen offset:128\n"
      "s_waitcnt vmcnt(0)\n"
      "v_mfma_f32_16x16x32_bf16 v[240:243], v[236:239], v[88:91], v[240:243]\n"
      "s_nop 3\n"
      "buffer_load_dwordx4 v[236:239], v212, s[48:51], s97 offen offset:192\n"
      "s_waitcnt vmcnt(0)\n"
      "v_mfma_f32_16x16x32_bf16 v[240:243], v[236:239], v[92:95], v[240:243]\n"
      "s_nop 3\n"
      "s_add_u32 s97, s96, 4096\n"
      "buffer_load_dwordx4 v[236:239], v212, s[48:51], s97 offen offset:0\n"
      "s_waitcnt vmcnt(0)\n"
      "v_mfma_f32_16x16x32_bf16 v[244:247], v[236:239], v[80:83], 0\n"
      "s_nop 3\n"
      "buffer_load_dwordx4 v[236:239], v212, s[48:51], s97 offen offset:64\n"
      "s_waitcnt vmcnt(0)\n"
      "v_mfma_f32_16x16x32_bf16 v[244:247], v[236:239], v[84:87], v[244:247]\n"
      "s_nop 3\n"
      "buffer_load_dwordx4 v[236:239], v212, s[48:51], s97 offen offset:128\n"
      "s_waitcnt vmcnt(0)\n"
      "v_mfma_f32_16x16x32_bf16 v[244:247], v[236:239], v[88:91], v[244:247]\n"
      "s_nop 3\n"
      "buffer_load_dwordx4 v[236:239], v212, s[48:51], s97 offen offset:192\n"
      "s_waitcnt vmcnt(0)\n"
      "v_mfma_f32_16x16x32_bf16 v[244:247], v[236:239], v[92:95], v[244:247]\n"
      "s_nop 3\n"
      "s_nop 7\n"
      "v_sub_f32 v240, v240, v252\n"
      "v_sub_f32 v241, v241, v252\n"
      "v_sub_f32 v242, v242, v252\n"
      "v_sub_f32 v243, v243, v252\n"
      "v_sub_f32 v244, v244, v252\n"
      "v_sub_f32 v245, v245, v252\n"
      "v_sub_f32 v246, v246, v252\n"
      "v_sub_f32 v247, v247, v252\n"
      "v_exp_f32 v240, v240\n"
      "v_exp_f32 v241, v241\n"
      "v_exp_f32 v242, v242\n"
      "v_exp_f32 v243, v243\n"
      "v_exp_f32 v244, v244\n"
      "v_exp_f32 v245, v245\n"
      "v_exp_f32 v246, v246\n"
      "v_exp_f32 v247, v247\n"
      "s_nop 0\n"
      "v_add_f32 v221, v221, v240\n"
      "v_add_f32 v221, v221, v241\n"
      "v_add_f32 v221, v221, v242\n"
      "v_add_f32 v221, v221, v243\n"
      "v_add_f32 v221, v221, v244\n"
      "v_add_f32_e64 v221, v221, v245\n"
      "v_add_f32_e64 v221, v221, v246\n"
      "v_add_f32_e64 v221, v221, v247\n"
      "v_cvt_pk_bf16_f32 v248, v240, v241\n"
      "v_cvt_pk_bf16_f32 v249, v242, v243\n"
      "v_cvt_pk_bf16_f32 v250, v244, v245\n"
      "v_cvt_pk_bf16_f32 v251, v246, v247\n"
      "s_mov_b64 s[48:49], %[pv]\n"
      "buffer_load_ushort v240, v213, s[48:51], s96 offen offset:0\n"
      "buffer_load_ushort v241, v213, s[48:51], s96 offen offset:256\n"
      "buffer_load_ushort v242, v213, s[48:51], s96 offen offset:512\n"
      "buffer_load_ushort v243, v213, s[48:51], s96 offen offset:768\n"
      "buffer_load_ushort v244, v213, s[48:51], s97 offen offset:0\n"
      "buffer_load_ushort v245, v213, s[48:51], s97 offen offset:256\n"
      "buffer_load_ushort v246, v213, s[48:51], s97 offen offset:512\n"
      "buffer_load_ushort v247, v213, s[48:51], s97 offen offset:768\n"
      "s_waitcnt vmcnt(0)\n"
      "v_lshl_or_b32 v236, v241, 16, v240\n"
      "v_lshl_or_b32 v237, v243, 16, v242\n"
      "v_lshl_or_b32 v238, v245, 16, v244\n"
      "v_lshl_or_b32 v239, v247, 16, v246\n"
      "s_nop 1\n"
      "v_mfma_f32_16x16x32_bf16 v[180:183], v[236:239], v[248:251], v[180:183]\n"
      "s_nop 3\n"
      "buffer_load_ushort v240, v213, s[48:51], s96 offen offset:32\n"
      "buffer_load_ushort v241, v213, s[48:51], s96 offen offset:288\n"
      "buffer_load_ushort v242, v213, s[48:51], s96 offen offset:544\n"
      "buffer_load_ushort v243, v213, s[48:51], s96 offen offset:800\n"
      "buffer_load_ushort v244, v213, s[48:51], s97 offen offset:32\n"
      "buffer_load_ushort v245, v213, s[48:51], s97 offen offset:288\n"
      "buffer_load_ushort v246, v213, s[48:51], s97 offen offset:544\n"
      "buffer_load_ushort v247, v213, s[48:51], s97 offen offset:800\n"
      "s_waitcnt vmcnt(0)\n"
      "v_lshl_or_b32 v236, v241, 16, v240\n"
      "v_lshl_or_b32 v237, v243, 16, v242\n"
      "v_lshl_or_b32 v238, v245, 16, v244\n"
      "v_lshl_or_b32 v239, v247, 16, v246\n"
      "s_nop 1\n"
      "v_mfma_f32_16x16x32_bf16 v[184:187], v[236:239], v[248:251], v[184:187]\n"
      "s_nop 3\n"
      "buffer_load_ushort v240, v213, s[48:51], s96 offen offset:64\n"
      "buffer_load_ushort v241, v213, s[48:51], s96 offen offset:320\n"
      "buffer_load_ushort v242, v213, s[48:51], s96 offen offset:576\n"
      "buffer_load_ushort v243, v213, s[48:51], s96 offen offset:832\n"
      "buffer_load_ushort v244, v213, s[48:51], s97 offen offset:64\n"
      "buffer_load_ushort v245, v213, s[48:51], s97 offen offset:320\n"
      "buffer_load_ushort v246, v213, s[48:51], s97 offen offset:576\n"
      "buffer_load_ushort v247, v213, s[48:51], s97 offen offset:832\n"
      "s_waitcnt vmcnt(0)\n"
      "v_lshl_or_b32 v236, v241, 16, v240\n"
      "v_lshl_or_b32 v237, v243, 16, v242\n"
      "v_lshl_or_b32 v238, v245, 16, v244\n"
      "v_lshl_or_b32 v239, v247, 16, v246\n"
      "s_nop 1\n"
      "v_mfma_f32_16x16x32_bf16 v[188:191], v[236:239], v[248:251], v[188:191]\n"
      "s_nop 3\n"
      "buffer_load_ushort v240, v213, s[48:51], s96 offen offset:96\n"
      "buffer_load_ushort v241, v213, s[48:51], s96 offen offset:352\n"
      "buffer_load_ushort v242, v213, s[48:51], s96 offen offset:608\n"
      "buffer_load_ushort v243, v213, s[48:51], s96 offen offset:864\n"
      "buffer_load_ushort v244, v213, s[48:51], s97 offen offset:96\n"
      "buffer_load_ushort v245, v213, s[48:51], s97 offen offset:352\n"
      "buffer_load_ushort v246, v213, s[48:51], s97 offen offset:608\n"
      "buffer_load_ushort v247, v213, s[48:51], s97 offen offset:864\n"
      "s_waitcnt vmcnt(0)\n"
      "v_lshl_or_b32 v236, v241, 16, v240\n"
      "v_lshl_or_b32 v237, v243, 16, v242\n"
      "v_lshl_or_b32 v238, v245, 16, v244\n"
      "v_lshl_or_b32 v239, v247, 16, v246\n"
      "s_nop 1\n"
      "v_mfma_f32_16x16x32_bf16 v[192:195], v[236:239], v[248:251], v[192:195]\n"
      "s_nop 3\n"
      "buffer_load_ushort v240, v213, s[48:51], s96 offen offset:128\n"
      "buffer_load_ushort v241, v213, s[48:51], s96 offen offset:384\n"
      "buffer_load_ushort v242, v213, s[48:51], s96 offen offset:640\n"
      "buffer_load_ushort v243, v213, s[48:51], s96 offen offset:896\n"
      "buffer_load_ushort v244, v213, s[48:51], s97 offen offset:128\n"
      "buffer_load_ushort v245, v213, s[48:51], s97 offen offset:384\n"
      "buffer_load_ushort v246, v213, s[48:51], s97 offen offset:640\n"
      "buffer_load_ushort v247, v213, s[48:51], s97 offen offset:896\n"
      "s_waitcnt vmcnt(0)\n"
      "v_lshl_or_b32 v236, v241, 16, v240\n"
      "v_lshl_or_b32 v237, v243, 16, v242\n"
      "v_lshl_or_b32 v238, v245, 16, v244\n"
      "v_lshl_or_b32 v239, v247, 16, v246\n"
      "s_nop 1\n"
      "v_mfma_f32_16x16x32_bf16 v[196:199], v[236:239], v[248:251], v[196:199]\n"
      "s_nop 3\n"
      "buffer_load_ushort v240, v213, s[48:51], s96 offen offset:160\n"
      "buffer_load_ushort v241, v213, s[48:51], s96 offen offset:416\n"
      "buffer_load_ushort v242, v213, s[48:51], s96 offen offset:672\n"
      "buffer_load_ushort v243, v213, s[48:51], s96 offen offset:928\n"
      "buffer_load_ushort v244, v213, s[48:51], s97 offen offset:160\n"
      "buffer_load_ushort v245, v213, s[48:51], s97 offen offset:416\n"
      "buffer_load_ushort v246, v213, s[48:51], s97 offen offset:672\n"
      "buffer_load_ushort v247, v213, s[48:51], s97 offen offset:928\n"
      "s_waitcnt vmcnt(0)\n"
      "v_lshl_or_b32 v236, v241, 16, v240\n"
      "v_lshl_or_b32 v237, v243, 16, v242\n"
      "v_lshl_or_b32 v238, v245, 16, v244\n"
      "v_lshl_or_b32 v239, v247, 16, v246\n"
      "s_nop 1\n"
      "v_mfma_f32_16x16x32_bf16 v[200:203], v[236:239], v[248:251], v[200:203]\n"
      "s_nop 3\n"
      "buffer_load_ushort v240, v213, s[48:51], s96 offen offset:192\n"
      "buffer_load_ushort v241, v213, s[48:51], s96 offen offset:448\n"
      "buffer_load_ushort v242, v213, s[48:51], s96 offen offset:704\n"
      "buffer_load_ushort v243, v213, s[48:51], s96 offen offset:960\n"
      "buffer_load_ushort v244, v213, s[48:51], s97 offen offset:192\n"
      "buffer_load_ushort v245, v213, s[48:51], s97 offen offset:448\n"
      "buffer_load_ushort v246, v213, s[48:51], s97 offen offset:704\n"
      "buffer_load_ushort v247, v213, s[48:51], s97 offen offset:960\n"
      "s_waitcnt vmcnt(0)\n"
      "v_lshl_or_b32 v236, v241, 16, v240\n"
      "v_lshl_or_b32 v237, v243, 16, v242\n"
      "v_lshl_or_b32 v238, v245, 16, v244\n"
      "v_lshl_or_b32 v239, v247, 16, v246\n"
      "s_nop 1\n"
      "v_mfma_f32_16x16x32_bf16 v[204:207], v[236:239], v[248:251], v[204:207]\n"
      "s_nop 3\n"
      "buffer_load_ushort v240, v213, s[48:51], s96 offen offset:224\n"
      "buffer_load_ushort v241, v213, s[48:51], s96 offen offset:480\n"
      "buffer_load_ushort v242, v213, s[48:51], s96 offen offset:736\n"
      "buffer_load_ushort v243, v213, s[48:51], s96 offen offset:992\n"
      "buffer_load_ushort v244, v213, s[48:51], s97 offen offset:224\n"
      "buffer_load_ushort v245, v213, s[48:51], s97 offen offset:480\n"
      "buffer_load_ushort v246, v213, s[48:51], s97 offen offset:736\n"
      "buffer_load_ushort v247, v213, s[48:51], s97 offen offset:992\n"
      "s_waitcnt vmcnt(0)\n"
      "v_lshl_or_b32 v236, v241, 16, v240\n"
      "v_lshl_or_b32 v237, v243, 16, v242\n"
      "v_lshl_or_b32 v238, v245, 16, v244\n"
      "v_lshl_or_b32 v239, v247, 16, v246\n"
      "s_nop 1\n"
      "v_mfma_f32_16x16x32_bf16 v[208:211], v[236:239], v[248:251], v[208:211]\n"
      "s_nop 3\n"
      "s_add_u32 s96, s96, 8192\n"
      "s_cmp_lt_u32 s96, 0x20000\n"
      "s_cbranch_scc1 SLB5_%=\n"
      "v_mov_b32 v253, v221\n"
      "s_nop 1\n"
      "v_permlane16_swap_b32 v221, v253\n"
      "v_add_f32 v221, v221, v253\n"
      "v_mov_b32 v253, v221\n"
      "s_nop 1\n"
      "v_permlane32_swap_b32 v221, v253\n"
      "v_add_f32 v221, v221, v253\n"
      "s_nop 3\n"
      "s_branch RET5_%=\n"
      "SLOW6_%=:\n"
      "v_mbcnt_lo_u32_b32 v253, -1, 0\n"
      "v_mbcnt_hi_u32_b32 v253, -1, v253\n"
      "v_and_b32 v212, 15, v253\n"
      "v_lshlrev_b32 v213, 1, v212\n"
      "v_lshlrev_b32 v212, 8, v212\n"
      "v_and_b32 v253, 48, v253\n"
      "v_add_u32_e64 v212, v212, v253\n"
      "v_lshl_add_u32 v213, v253, 6, v213\n"
      "v_mov_b32 v252, 0xff800000\n"
      "s_mov_b32 s96, 0\n"
      "SLA6_%=:\n"
      "s_mov_b64 s[48:49], %[pk]\n"
      "s_mov_b32 s50, 131072\n"
      "s_add_u32 s97, s96, 0\n"
      "buffer_load_dwordx4 v[236:239], v212, s[48:51], s97 offen offset:0\n"
      "s_waitcnt vmcnt(0)\n"
      "v_mfma_f32_16x16x32_bf16 v[240:243], v[236:239], v[96:99], 0\n"
      "s_nop 3\n"
      "buffer_load_dwordx4 v[236:239], v212, s[48:51], s97 offen offset:64\n"
      "s_waitcnt vmcnt(0)\n"
      "v_mfma_f32_16x16x32_bf16 v[240:243], v[236:239], v[100:103], v[240:243]\n"
      "s_nop 3\n"
      "buffer_load_dwordx4 v[236:239], v212, s[48:51], s97 offen offset:128\n"
      "s_waitcnt vmcnt(0)\n"
      "v_mfma_f32_16x16x32_bf16 v[240:243], v[236:239], v[104:107], v[240:243]\n"
      "s_nop 3\n"
      "buffer_load_dwordx4 v[236:239], v212, s[48:51], s97 offen offset:192\n"
      "s_waitcnt vmcnt(0)\n"
      "v_mfma_f32_16x16x32_bf16 v[240:243], v[236:239], v[108:111], v[240:243]\n"
      "s_nop 3\n"
      "s_add_u32 s97, s96, 4096\n"
      "buffer_load_dwordx4 v[236:239], v212, s[48:51], s97 offen offset:0\n"
      "s_waitcnt vmcnt(0)\n"
      "v_mfma_f32_16x16x32_bf16 v[244:247], v[236:239], v[96:99], 0\n"
      "s_nop 3\n"
      "buffer_load_dwordx4 v[236:239], v212, s[48:51], s97 offen offset:64\n"
      "s_waitcnt vmcnt(0)\n"
      "v_mfma_f32_16x16x32_bf16 v[244:247], v[236:239], v[100:103], v[244:247]\n"
      "s_nop 3\n"
      "buffer_load_dwordx4 v[236:239], v212, s[48:51], s97 offen offset:128\n"
      "s_waitcnt vmcnt(0)\n"
      "v_mfma_f32_16x16x32_bf16 v[244:247], v[236:239], v[104:107], v[244:247]\n"
      "s_nop 3\n"
      "buffer_load_dwordx4 v[236:239], v212, s[48:51], s97 offen offset:192\n"
      "s_waitcnt vmcnt(0)\n"
      "v_mfma_f32_16x16x32_bf16 v[244:247], v[236:239], v[108:111], v[244:247]\n"
      "s_nop 3\n"
      "s_nop 7\n"
      "v_max3_f32 v252, v252, v240, v241\n"
      "v_max3_f32 v252, v252, v242, v243\n"
      "v_max3_f32 v252, v252, v244, v245\n"
      "v_max3_f32 v252, v252, v246, v247\n"
      "s_add_u32 s96, s96, 8192\n"
      "s_cmp_lt_u32 s96, 0x20000\n"
      "s_cbranch_scc1 SLA6_%=\n"
      "v_mov_b32 v253, v252\n"
      "s_nop 1\n"
      "v_permlane16_swap_b32 v252, v253\n"
      "v_max_f32 v252, v252, v253\n"
      "v_mov_b32 v253, v252\n"
      "s_nop 1\n"
      "v_permlane32_swap_b32 v252, v253\n"
      "v_max_f32 v252, v252, v253\n"
      "v_mov_b32 v112, 0\n"
      "v_mov_b32 v113, 0\n"
      "v_mov_b32 v114, 0\n"
      "v_mov_b32 v115, 0\n"
      "v_mov_b32 v116, 0\n"
      "v_mov_b32 v117, 0\n"
      "v_mov_b32 v118, 0\n"
      "v_mov_b32 v119, 0\n"
      "v_mov_b32 v120, 0\n"
      "v_mov_b32 v121, 0\n"
      "v_mov_b32 v122, 0\n"
      "v_mov_b32 v123, 0\n"
      "v_mov_b32 v124, 0\n"
      "v_mov_b32 v125, 0\n"
      "v_mov_b32 v126, 0\n"
      "v_mov_b32 v127, 0\n"
      "v_mov_b32 v128, 0\n"
      "v_mov_b32 v129, 0\n"
      "v_mov_b32 v130, 0\n"
      "v_mov_b32 v131, 0\n"
      "v_mov_b32 v132, 0\n"
      "v_mov_b32 v133, 0\n"
      "v_mov_b32 v134, 0\n"
      "v_mov_b32 v135, 0\n"
      "v_mov_b32 v136, 0\n"
      "v_mov_b32 v137, 0\n"
      "v_mov_b32 v138, 0\n"
      "v_mov_b32 v139, 0\n"
      "v_mov_b32 v140, 0\n"
      "v_mov_b32 v141, 0\n"
      "v_mov_b32 v142, 0\n"
      "v_mov_b32 v143, 0\n"
      "v_mov_b32 v222, 0\n"
      "s_mov_b32 s96, 0\n"
      "SLB6_%=:\n"
      "s_mov_b64 s[48:49], %[pk]\n"
      "s_mov_b32 s50, 131072\n"
      "s_add_u32 s97, s96, 0\n"
      "buffer_load_dwordx4 v[236:239], v212, s[48:51], s97 offen offset:0\n"
      "s_waitcnt vmcnt(0)\n"
      "v_mfma_f32_16x16x32_bf16 v[240:243], v[236:239], v[96:99], 0\n"
      "s_nop 3\n"
      "buffer_load_dwordx4 v[236:239], v212, s[48:51], s97 offen offset:64\n"
      "s_waitcnt vmcnt(0)\n"
      "v_mfma_f32_16x16x32_bf16 v[240:243], v[236:239], v[100:103], v[240:243]\n"
      "s_nop 3\n"
      "buffer_load_dwordx4 v[236:239], v212, s[48:51], s97 offen offset:128\n"
      "s_waitcnt vmcnt(0)\n"
      "v_mfma_f32_16x16x32_bf16 v[240:243], v[236:239], v[104:107], v[240:243]\n"
      "s_nop 3\n"
      "buffer_load_dwordx4 v[236:239], v212, s[48:51], s97 offen offset:192\n"
      "s_waitcnt vmcnt(0)\n"
      "v_mfma_f32_16x16x32_bf16 v[240:243], v[236:239], v[108:111], v[240:243]\n"
      "s_nop 3\n"
      "s_add_u32 s97, s96, 4096\n"
      "buffer_load_dwordx4 v[236:239], v212, s[48:51], s97 offen offset:0\n"
      "s_waitcnt vmcnt(0)\n"
      "v_mfma_f32_16x16x32_bf16 v[244:247], v[236:239], v[96:99], 0\n"
      "s_nop 3\n"
      "buffer_load_dwordx4 v[236:239], v212, s[48:51], s97 offen offset:64\n"
      "s_waitcnt vmcnt(0)\n"
      "v_mfma_f32_16x16x32_bf16 v[244:247], v[236:239], v[100:103], v[244:247]\n"
      "s_nop 3\n"
      "buffer_load_dwordx4 v[236:239], v212, s[48:51], s97 offen offset:128\n"
      "s_waitcnt vmcnt(0)\n"
      "v_mfma_f32_16x16x32_bf16 v[244:247], v[236:239], v[104:107], v[244:247]\n"
      "s_nop 3\n"
      "buffer_load_dwordx4 v[236:239], v212, s[48:51], s97 offen offset:192\n"
      "s_waitcnt vmcnt(0)\n"
      "v_mfma_f32_16x16x32_bf16 v[244:247], v[236:239], v[108:111], v[244:247]\n"
      "s_nop 3\n"
      "s_nop 7\n"
      "v_sub_f32 v240, v240, v252\n"
      "v_sub_f32 v241, v241, v252\n"
      "v_sub_f32 v242, v242, v252\n"
      "v_sub_f32 v243, v243, v252\n"
      "v_sub_f32 v244, v244, v252\n"
      "v_sub_f32 v245, v245, v252\n"
      "v_sub_f32 v246, v246, v252\n"
      "v_sub_f32 v247, v247, v252\n"
      "v_exp_f32 v240, v240\n"
      "v_exp_f32 v241, v241\n"
      "v_exp_f32 v242, v242\n"
      "v_exp_f32 v243, v243\n"
      "v_exp_f32 v244, v244\n"
      "v_exp_f32 v245, v245\n"
      "v_exp_f32 v246, v246\n"
      "v_exp_f32 v247, v247\n"
      "s_nop 0\n"
      "v_add_f32 v222, v222, v240\n"
      "v_add_f32 v222, v222, v241\n"
      "v_add_f32 v222, v222, v242\n"
      "v_add_f32 v222, v222, v243\n"
      "v_add_f32 v222, v222, v244\n"
      "v_add_f32_e64 v222, v222, v245\n"
      "v_add_f32_e64 v222, v222, v246\n"
      "v_add_f32_e64 v222, v222, v247\n"
      "v_cvt_pk_bf16_f32 v248, v240, v241\n"
      "v_cvt_pk_bf16_f32 v249, v242, v243\n"
      "v_cvt_pk_bf16_f32 v250, v244, v245\n"
      "v_cvt_pk_bf16_f32 v251, v246, v247\n"
      "s_mov_b64 s[48:49], %[pv]\n"
      "buffer_load_ushort v240, v213, s[48:51], s96 offen offset:0\n"
      "buffer_load_ushort v241, v213, s[48:51], s96 offen offset:256\n"
      "buffer_load_ushort v242, v213, s[48:51], s96 offen offset:512\n"
      "buffer_load_ushort v243, v213, s[48:51], s96 offen offset:768\n"
      "buffer_load_ushort v244, v213, s[48:51], s97 offen offset:0\n"
      "buffer_load_ushort v245, v213, s[48:51], s97 offen offset:256\n"
      "buffer_load_ushort v246, v213, s[48:51], s97 offen offset:512\n"
      "buffer_load_ushort v247, v213, s[48:51], s97 offen offset:768\n"
      "s_waitcnt vmcnt(0)\n"
      "v_lshl_or_b32 v236, v241, 16, v240\n"
      "v_lshl_or_b32 v237, v243, 16, v242\n"
      "v_lshl_or_b32 v238, v245, 16, v244\n"
      "v_lshl_or_b32 v239, v247, 16, v246\n"
      "s_nop 1\n"
      "v_mfma_f32_16x16x32_bf16 v[112:115], v[236:239], v[248:251], v[112:115]\n"
      "s_nop 3\n"
      "buffer_load_ushort v240, v213, s[48:51], s96 offen offset:32\n"
      "buffer_load_ushort v241, v213, s[48:51], s96 offen offset:288\n"
      "buffer_load_ushort v242, v213, s[48:51], s96 offen offset:544\n"
      "buffer_load_ushort v243, v213, s[48:51], s96 offen offset:800\n"
      "buffer_load_ushort v244, v213, s[48:51], s97 offen offset:32\n"
      "buffer_load_ushort v245, v213, s[48:51], s97 offen offset:288\n"
      "buffer_load_ushort v246, v213, s[48:51], s97 offen offset:544\n"
      "buffer_load_ushort v247, v213, s[48:51], s97 offen offset:800\n"
      "s_waitcnt vmcnt(0)\n"
      "v_lshl_or_b32 v236, v241, 16, v240\n"
      "v_lshl_or_b32 v237, v243, 16, v242\n"
      "v_lshl_or_b32 v238, v245, 16, v244\n"
      "v_lshl_or_b32 v239, v247, 16, v246\n"
      "s_nop 1\n"
      "v_mfma_f32_16x16x32_bf16 v[116:119], v[236:239], v[248:251], v[116:119]\n"
      "s_nop 3\n"
      "buffer_load_ushort v240, v213, s[48:51], s96 offen offset:64\n"
      "buffer_load_ushort v241, v213, s[48:51], s96 offen offset:320\n"
      "buffer_load_ushort v242, v213, s[48:51], s96 offen offset:576\n"
      "buffer_load_ushort v243, v213, s[48:51], s96 offen offset:832\n"
      "buffer_load_ushort v244, v213, s[48:51], s97 offen offset:64\n"
      "buffer_load_ushort v245, v213, s[48:51], s97 offen offset:320\n"
      "buffer_load_ushort v246, v213, s[48:51], s97 offen offset:576\n"
      "buffer_load_ushort v247, v213, s[48:51], s97 offen offset:832\n"
      "s_waitcnt vmcnt(0)\n"
      "v_lshl_or_b32 v236, v241, 16, v240\n"
      "v_lshl_or_b32 v237, v243, 16, v242\n"
      "v_lshl_or_b32 v238, v245, 16, v244\n"
      "v_lshl_or_b32 v239, v247, 16, v246\n"
      "s_nop 1\n"
      "v_mfma_f32_16x16x32_bf16 v[120:123], v[236:239], v[248:251], v[120:123]\n"
      "s_nop 3\n"
      "buffer_load_ushort v240, v213, s[48:51], s96 offen offset:96\n"
      "buffer_load_ushort v241, v213, s[48:51], s96 offen offset:352\n"
      "buffer_load_ushort v242, v213, s[48:51], s96 offen offset:608\n"
      "buffer_load_ushort v243, v213, s[48:51], s96 offen offset:864\n"
      "buffer_load_ushort v244, v213, s[48:51], s97 offen offset:96\n"
      "buffer_load_ushort v245, v213, s[48:51], s97 offen offset:352\n"
      "buffer_load_ushort v246, v213, s[48:51], s97 offen offset:608\n"
      "buffer_load_ushort v247, v213, s[48:51], s97 offen offset:864\n"
      "s_waitcnt vmcnt(0)\n"
      "v_lshl_or_b32 v236, v241, 16, v240\n"
      "v_lshl_or_b32 v237, v243, 16, v242\n"
      "v_lshl_or_b32 v238, v245, 16, v244\n"
      "v_lshl_or_b32 v239, v247, 16, v246\n"
      "s_nop 1\n"
      "v_mfma_f32_16x16x32_bf16 v[124:127], v[236:239], v[248:251], v[124:127]\n"
      "s_nop 3\n"
      "buffer_load_ushort v240, v213, s[48:51], s96 offen offset:128\n"
      "buffer_load_ushort v241, v213, s[48:51], s96 offen offset:384\n"
      "buffer_load_ushort v242, v213, s[48:51], s96 offen offset:640\n"
      "buffer_load_ushort v243, v213, s[48:51], s96 offen offset:896\n"
      "buffer_load_ushort v244, v213, s[48:51], s97 offen offset:128\n"
      "buffer_load_ushort v245, v213, s[48:51], s97 offen offset:384\n"
      "buffer_load_ushort v246, v213, s[48:51], s97 offen offset:640\n"
      "buffer_load_ushort v247, v213, s[48:51], s97 offen offset:896\n"
      "s_waitcnt vmcnt(0)\n"
      "v_lshl_or_b32 v236, v241, 16, v240\n"
      "v_lshl_or_b32 v237, v243, 16, v242\n"
      "v_lshl_or_b32 v238, v245, 16, v244\n"
      "v_lshl_or_b32 v239, v247, 16, v246\n"
      "s_nop 1\n"
      "v_mfma_f32_16x16x32_bf16 v[128:131], v[236:239], v[248:251], v[128:131]\n"
      "s_nop 3\n"
      "buffer_load_ushort v240, v213, s[48:51], s96 offen offset:160\n"
      "buffer_load_ushort v241, v213, s[48:51], s96 offen offset:416\n"
      "buffer_load_ushort v242, v213, s[48:51], s96 offen offset:672\n"
      "buffer_load_ushort v243, v213, s[48:51], s96 offen offset:928\n"
      "buffer_load_ushort v244, v213, s[48:51], s97 offen offset:160\n"
      "buffer_load_ushort v245, v213, s[48:51], s97 offen offset:416\n"
      "buffer_load_ushort v246, v213, s[48:51], s97 offen offset:672\n"
      "buffer_load_ushort v247, v213, s[48:51], s97 offen offset:928\n"
      "s_waitcnt vmcnt(0)\n"
      "v_lshl_or_b32 v236, v241, 16, v240\n"
      "v_lshl_or_b32 v237, v243, 16, v242\n"
      "v_lshl_or_b32 v238, v245, 16, v244\n"
      "v_lshl_or_b32 v239, v247, 16, v246\n"
      "s_nop 1\n"
      "v_mfma_f32_16x16x32_bf16 v[132:135], v[236:239], v[248:251], v[132:135]\n"
      "s_nop 3\n"
      "buffer_load_ushort v240, v213, s[48:51], s96 offen offset:192\n"
      "buffer_load_ushort v241, v213, s[48:51], s96 offen offset:448\n"
      "buffer_load_ushort v242, v213, s[48:51], s96 offen offset:704\n"
      "buffer_load_ushort v243, v213, s[48:51], s96 offen offset:960\n"
      "buffer_load_ushort v244, v213, s[48:51], s97 offen offset:192\n"
      "buffer_load_ushort v245, v213, s[48:51], s97 offen offset:448\n"
      "buffer_load_ushort v246, v213, s[48:51], s97 offen offset:704\n"
      "buffer_load_ushort v247, v213, s[48:51], s97 offen offset:960\n"
      "s_waitcnt vmcnt(0)\n"
      "v_lshl_or_b32 v236, v241, 16, v240\n"
      "v_lshl_or_b32 v237, v243, 16, v242\n"
      "v_lshl_or_b32 v238, v245, 16, v244\n"
      "v_lshl_or_b32 v239, v247, 16, v246\n"
      "s_nop 1\n"
      "v_mfma_f32_16x16x32_bf16 v[136:139], v[236:239], v[248:251], v[136:139]\n"
      "s_nop 3\n"
      "buffer_load_ushort v240, v213, s[48:51], s96 offen offset:224\n"
      "buffer_load_ushort v241, v213, s[48:51], s96 offen offset:480\n"
      "buffer_load_ushort v242, v213, s[48:51], s96 offen offset:736\n"
      "buffer_load_ushort v243, v213, s[48:51], s96 offen offset:992\n"
      "buffer_load_ushort v244, v213, s[48:51], s97 offen offset:224\n"
      "buffer_load_ushort v245, v213, s[48:51], s97 offen offset:480\n"
      "buffer_load_ushort v246, v213, s[48:51], s97 offen offset:736\n"
      "buffer_load_ushort v247, v213, s[48:51], s97 offen offset:992\n"
      "s_waitcnt vmcnt(0)\n"
      "v_lshl_or_b32 v236, v241, 16, v240\n"
      "v_lshl_or_b32 v237, v243, 16, v242\n"
      "v_lshl_or_b32 v238, v245, 16, v244\n"
      "v_lshl_or_b32 v239, v247, 16, v246\n"
      "s_nop 1\n"
      "v_mfma_f32_16x16x32_bf16 v[140:143], v[236:239], v[248:251], v[140:143]\n"
      "s_nop 3\n"
      "s_add_u32 s96, s96, 8192\n"
      "s_cmp_lt_u32 s96, 0x20000\n"
      "s_cbranch_scc1 SLB6_%=\n"
      "v_mov_b32 v253, v222\n"
      "s_nop 1\n"
      "v_permlane16_swap_b32 v222, v253\n"
      "v_add_f32 v222, v222, v253\n"
      "v_mov_b32 v253, v222\n"
      "s_nop 1\n"
      "v_permlane32_swap_b32 v222, v253\n"
      "v_add_f32 v222, v222, v253\n"
      "s_nop 3\n"
      "s_branch RET6_%=\n"
      "END_%=:\n"
      :
      : [px] "s"(px), [pwq] "s"(pwq), [pk] "s"(pk), [pv] "s"(pv), [pwo] "s"(pwo), [pout] "s"(pout), [pws] "s"(ws),
        [wid] "s"(wid), [b] "s"(b), [g] "s"(g), [j] "s"(j), [flip] "s"(flip), [rot] "s"(rot), [kr] "s"(kr), [xb] "s"(xb), [xlo] "s"(xlo), [ttab] "s"(ttab), [bid] "s"(bid)
      : "v0","v1","v2","v3","v4","v5","v6","v7","v8","v9","v10","v11","v12","v13","v14","v15","v16","v17","v18","v19","v20","v21","v22","v23","v24","v25","v26","v27","v28","v29","v30","v31","v32","v33","v34","v35","v36","v37","v38","v39","v40","v41","v42","v43","v44","v45","v46","v47","v48","v49","v50","v51","v52","v53","v54","v55","v56","v57","v58","v59","v60","v61","v62","v63","v64","v65","v66","v67","v68","v69","v70","v71","v72","v73","v74","v75","v76","v77","v78","v79","v80","v81","v82","v83","v84","v85","v86","v87","v88","v89","v90","v91","v92","v93","v94","v95","v96","v97","v98","v99","v100","v101","v102","v103","v104","v105","v106","v107","v108","v109","v110","v111","v112","v113","v114","v115","v116","v117","v118","v119","v120","v121","v122","v123","v124","v125","v126","v127","v128","v129","v130","v131","v132","v133","v134","v135","v136","v137","v138","v139","v140","v141","v142","v143","v144","v145","v146","v147","v148","v149","v150","v151","v152","v153","v154","v155","v156","v157","v158","v159","v160","v161","v162","v163","v164","v165","v166","v167","v168","v169","v170","v171","v172","v173","v174","v175","v176","v177","v178","v179","v180","v181","v182","v183","v184","v185","v186","v187","v188","v189","v190","v191","v192","v193","v194","v195","v196","v197","v198","v199","v200","v201","v202","v203","v204","v205","v206","v207","v208","v209","v210","v211","v212","v213","v214","v215","v216","v217","v218","v219","v220","v221","v222","v223","v224","v225","v226","v227","v228","v229","v230","v231","v232","v233","v234","v235","v236","v237","v238","v239","v240","v241","v242","v243","v244","v245","v246","v247","v248","v249","v250","v251","v252","v253","v254","v255","a0","a1","a2","a3","a4","a5","a6","a7","a8","a9","a10","a11","a12","a13","a14","a15","a16","a17","a18","a19","a20","a21","a22","a23","a24","a25","a26","a27","a28","a29","a30","a31","a32","a33","a34","a35","a36","a37","a38","a39","a40","a41","a42","a43","a44","a45","a46","a47","a48","a49","a50","a51","a52","a53","a54","a55","a56","a57","a58","a59","a60","a61","a62","a63","a64","a65","a66","a67","a68","a69","a70","a71","a72","a73","a74","a75","a76","a77","a78","a79","a80","a81","a82","a83","a84","a85","a86","a87","a88","a89","a90","a91","a92","a93","a94","a95","a96","a97","a98","a99","a100","a101","a102","a103","a104","a105","a106","a107","a108","a109","a110","a111","a112","a113","a114","a115","a116","a117","a118","a119","a120","a121","a122","a123","a124","a125","a126","a127","a128","a129","a130","a131","a132","a133","a134","a135","a136","a137","a138","a139","a140","a141","a142","a143","a144","a145","a146","a147","a148","a149","a150","a151","a152","a153","a154","a155","a156","a157","a158","a159","a160","a161","a162","a163","a164","a165","a166","a167","a168","a169","a170","a171","a172","a173","a174","a175","a176","a177","a178","a179","a180","a181","a182","a183","a184","a185","a186","a187","a188","a189","a190","a191","a192","a193","a194","a195","a196","a197","a198","a199","a200","a201","a202","a203","a204","a205","a206","a207","a208","a209","a210","a211","a212","a213","a214","a215","a216","a217","a218","a219","a220","a221","a222","a223","a224","a225","a226","a227","a228","a229","a230","a231","a232","a233","a234","a235","a236","a237","a238","a239","a240","a241","a242","a243","a244","a245","a246","a247","a248","a249","a250","a251","a252","a253","a254","a255","s42","s43","s44","s45","s46","s47","s48","s49","s50","s51","s52","s53","s54","s55","s56","s57","s58","s59","s60","s61","s62","s63","s64","s65","s66","s67","s68","s69","s70","s71","s72","s73","s74","s75","s76","s77","s78","s79","s80","s81","s82","s83","s84","s85","s86","s87","s88","s89","s90","s91","s92","s93","s94","s95","s96","s97","s98","s99","vcc","scc","memory");
}

extern "C" void kernel_launch(void* const* d_in, const int* in_sizes, int n_in, void* d_out, int out_size, void* d_ws, size_t ws_size, hipStream_t stream) {
  (void)hipFuncSetAttribute((const void*)gqa_fused, hipFuncAttributeMaxDynamicSharedMemorySize, 163840);
  hipLaunchKernelGGL(gqa_fused, dim3(256), dim3(256), 163840, stream, (const char*)d_in[0], (const char*)d_in[1], (const char*)d_in[2], (const char*)d_in[3],
                     (const char*)d_in[4], (char*)d_out, (char*)d_ws);
}
```
